```python
import torch

def _softmax(x, axis=-1):
    return torch.softmax(x, dim=axis)
def _gelu(x):
    return torch.nn.functional.gelu(x, approximate="tanh")
def _silu(x):
    return torch.nn.functional.silu(x)
def _sigmoid(x):
    return torch.sigmoid(x)
def _layernorm(x, axis=-1, eps=1e-5):
    m = x.mean(dim=axis, keepdim=True)
    v = x.var(dim=axis, keepdim=True, unbiased=False)
    return (x - m) / torch.sqrt(v + eps)
def _scatter_add(tgt, idx, src, axis=0):
    return tgt.clone().index_add_(axis, idx, src)
def _segment_sum(src, seg, n):
    out = torch.zeros((n,) + src.shape[1:], dtype=src.dtype)
    return out.index_add_(0, seg, src)
def _pad(x, pw):
    flat = tuple(v for pair in reversed(pw) for v in pair)
    return torch.nn.functional.pad(x, flat)

kernel_name = 'gated_mix__compressed_kv_cla__swiglu_ffn__decode'

def setup_inputs():

    _w = (lambda a: (lambda t: t.to(torch.bfloat16) if t.is_floating_point() else t)(torch.as_tensor(a)))
    def _A_setup():
        B, H, Sq, Sk, D, M = 1, 32, 1, 8192, 128, 512
        Dc = 4 * D
        return {
            "x": _w((lambda *s: torch.randn(*s, dtype=torch.bfloat16) * 0.05)(B, Sq, M)),
            "c_kv": _w((lambda *s: torch.randn(*s, dtype=torch.bfloat16) * 0.8)(B, Sk, Dc)),
            "w_q": _w((lambda *s: torch.randn(*s, dtype=torch.bfloat16) * 6.4)(M, H, D) * (M ** -0.5)),
            "w_uk": _w((lambda *s: torch.randn(*s, dtype=torch.bfloat16) * 6.4)(Dc, H, D) * (Dc ** -0.5)),
            "w_uv": _w((lambda *s: torch.randn(*s, dtype=torch.bfloat16) * 0.8)(Dc, H, D) * (Dc ** -0.5)),
            "w_o": _w((lambda *s: torch.randn(*s, dtype=torch.bfloat16) * 0.8)(H, D, M) * ((H * D) ** -0.5)),
        }
    _A_in = _A_setup()

    def _B_setup():
        B, S, D, F = 1, 1, 512, 1376
        return {
            "x": _w((lambda *s: torch.randn(*s, dtype=torch.bfloat16) * 0.05)(B, S, D)),
            "w_gate": _w((lambda *s: torch.randn(*s, dtype=torch.bfloat16) * 0.1)(D, F)),
            "w_up": _w((lambda *s: torch.randn(*s, dtype=torch.bfloat16) * 0.1)(D, F)),
            "w_down": _w((lambda *s: torch.randn(*s, dtype=torch.bfloat16) * 0.1)(F, D)),
        }
    _B_in = _B_setup()

    return {
        "A_x": _A_in["x"],
        "A_c_kv": _A_in["c_kv"],
        "A_w_q": _A_in["w_q"],
        "A_w_uk": _A_in["w_uk"],
        "A_w_uv": _A_in["w_uv"],
        "A_w_o": _A_in["w_o"],
        "B_x": _B_in["x"],
        "B_w_gate": _B_in["w_gate"],
        "B_w_up": _B_in["w_up"],
        "B_w_down": _B_in["w_down"],
        "gate_w": _w((lambda *s: torch.randn(*s, dtype=torch.bfloat16) * 0.05)(512) * 16.0),
    }

def reference(A_x, A_c_kv, A_w_q, A_w_uk, A_w_uv, A_w_o, B_x, B_w_gate, B_w_up, B_w_down, gate_w):
    def _A_ref(x, c_kv, w_q, w_uk, w_uv, w_o):
        B, Sq, M = x.shape[0], x.shape[1], x.shape[2]
        Sk = c_kv.shape[1]
        H, D = w_q.shape[1], w_q.shape[2]
        q = torch.einsum("bqm,mhd->bhqd", x, w_q)
        k = torch.einsum("bkc,chd->bhkd", c_kv, w_uk)
        v = torch.einsum("bkc,chd->bhkd", c_kv, w_uv)
        scores = torch.einsum("bhqd,bhkd->bhqk", q, k) * (D ** -0.5)
        off = Sk - Sq
        qi = torch.arange(Sq)[:, None]
        kj = torch.arange(Sk)[None, :]
        cmask = kj <= (qi + off)
        scores = torch.where(cmask[None, None], scores, -1e9)
        p = _softmax(scores, axis=-1)
        ctx = torch.einsum("bhqk,bhkd->bhqd", p, v)
        return torch.einsum("bhqd,hdm->bqm", ctx, w_o)

    def _B_ref(x, w_gate, w_up, w_down):
        g = _silu(x @ w_gate)
        u = x @ w_up
        return (g * u) @ w_down

    x = A_x
    g = _sigmoid((x * gate_w).sum(-1, keepdims=True))
    a = _A_ref(x, A_c_kv, A_w_q, A_w_uk, A_w_uv, A_w_o)
    b = _B_ref(x, B_w_gate, B_w_up, B_w_down)
    return g * a + (1 - g) * b
```

```cpp
#include <hip/hip_runtime.h>
#include <hip/hip_bf16.h>
#include <stdint.h>

#define NH 32
#define HD 128
#define DM 512
#define DC 512
#define SK 8192
#define FF 1376

typedef unsigned short u16;
typedef __attribute__((ext_vector_type(8))) __bf16 bf16x8;
typedef __attribute__((ext_vector_type(4))) short s16x4;
typedef __attribute__((ext_vector_type(8))) short s16x8;
typedef __attribute__((ext_vector_type(16))) float f32x16;
typedef __attribute__((ext_vector_type(4))) float f32x4;
typedef __attribute__((ext_vector_type(4))) unsigned int u32x4;
typedef __attribute__((ext_vector_type(2))) unsigned int u32x2;

__device__ __forceinline__ float bf2f(u16 v) { return __uint_as_float(((unsigned)v) << 16); }
__device__ __forceinline__ float bflo(unsigned v) { return __uint_as_float(v << 16); }
__device__ __forceinline__ float bfhi(unsigned v) { return __uint_as_float(v & 0xffff0000u); }
__device__ __forceinline__ u16 f2bf(float f) {
    unsigned u = __float_as_uint(f);
    if ((u & 0x7fffffffu) > 0x7f800000u) return (u16)((u >> 16) | 0x40);
    u += 0x7fffu + ((u >> 16) & 1u);
    return (u16)(u >> 16);
}
__device__ __forceinline__ float rbf(float f) { return bf2f(f2bf(f)); }

#define WS_QPART   0
#define WS_GPART   (WS_QPART + 8 * 4096 * 4)
#define WS_UPART   (WS_GPART + 8 * 1408 * 4)
#define WS_QABS    (WS_UPART + 8 * 1408 * 4)
#define WS_BPART   (WS_QABS + 32 * 512 * 2)
#define WS_P       (WS_BPART + 22 * 512 * 4)
#define WS_MLOC    (WS_P + 32 * 8192 * 2)
#define WS_OPART   (WS_MLOC + 256 * 32 * 4)
#define WS_ML      (WS_OPART + 16 * 32 * 512 * 4)
#define WS_OUTP    (WS_ML + 16 * 32 * 2 * 4)
#define WS_END     (WS_OUTP + 256 * 512 * 4)

__device__ __forceinline__ void gemv_tile_64x128(const u16* __restrict__ W, int ldw, int row0, int nrows,
                                                 int col0, int ncols, const float* vs  ,
                                                 float* red  , float* __restrict__ out) {
    const int t = threadIdx.x;
    const int l16 = t & 15;
    const int rg = t >> 4;
    const int cbase = col0 + l16 * 8;
    const bool colok = (l16 * 8) < ncols;
    u32x4 w[4];
#pragma unroll
    for (int i = 0; i < 4; ++i) {
        int r = rg + 16 * i;
        w[i] = (u32x4){0u, 0u, 0u, 0u};
        if (colok && r < nrows) w[i] = *(const u32x4*)(W + (size_t)(row0 + r) * ldw + cbase);
    }
    float acc[8];
#pragma unroll
    for (int j = 0; j < 8; ++j) acc[j] = 0.f;
#pragma unroll
    for (int i = 0; i < 4; ++i) {
        float xv = vs[rg + 16 * i];
#pragma unroll
        for (int j = 0; j < 4; ++j) {
            acc[2 * j] += xv * bflo(w[i][j]);
            acc[2 * j + 1] += xv * bfhi(w[i][j]);
        }
    }
#pragma unroll
    for (int j = 0; j < 8; ++j) {
        acc[j] += __shfl_xor(acc[j], 16);
        acc[j] += __shfl_xor(acc[j], 32);
    }
    const int wave = t >> 6;
    if ((t & 63) < 16) {
        *(f32x4*)(red + wave * 128 + l16 * 8) = (f32x4){acc[0], acc[1], acc[2], acc[3]};
        *(f32x4*)(red + wave * 128 + l16 * 8 + 4) = (f32x4){acc[4], acc[5], acc[6], acc[7]};
    }
    __syncthreads();
    if (t < 128 && t < ncols) {
        out[t] = (red[t] + red[128 + t]) + (red[256 + t] + red[384 + t]);
    }
}

__global__ __launch_bounds__(256) void k1_proj(const u16* __restrict__ x, const u16* __restrict__ wq,
                                               const u16* __restrict__ wg, const u16* __restrict__ wu,
                                               char* __restrict__ ws) {
    __shared__ __attribute__((aligned(16))) float vs[64];
    __shared__ __attribute__((aligned(16))) float red[512];
    const int b = blockIdx.x;
    const int t = threadIdx.x;
    int kc, col0, ncols, ldw;
    const u16* W;
    float* out;
    if (b < 256) {
        kc = b >> 5;
        int h = b & 31;
        col0 = h * 128; ncols = 128; ldw = NH * HD; W = wq;
        out = (float*)(ws + WS_QPART) + kc * 4096 + col0;
    } else {
        int bb = b - 256;
        int which = bb / 88;
        int r = bb % 88;
        kc = r / 11;
        int cc = r % 11;
        col0 = cc * 128; ncols = min(128, FF - col0); ldw = FF; W = which ? wu : wg;
        out = (float*)(ws + (which ? WS_UPART : WS_GPART)) + kc * 1408 + col0;
    }
    if (t < 64) vs[t] = bf2f(x[kc * 64 + t]);
    __syncthreads();
    gemv_tile_64x128(W, ldw, kc * 64, 64, col0, ncols, vs, red, out);
}

__global__ __launch_bounds__(256) void k2_absorb(const u16* __restrict__ wuk, const u16* __restrict__ wd,
                                                 char* __restrict__ ws) {
    __shared__ __attribute__((aligned(16))) float vs[128];
    __shared__ __attribute__((aligned(16))) float red[512];
    const int b = blockIdx.x;
    const int t = threadIdx.x;
    if (b < 256) {
        const int h = b & 31;
        const int c0 = (b >> 5) * 64;
        const int l16 = t & 15, rg = t >> 4;
        u32x4 w[4];
#pragma unroll
        for (int i = 0; i < 4; ++i) {
            int c = c0 + rg + 16 * i;
            w[i] = *(const u32x4*)(wuk + (size_t)c * 4096 + h * 128 + l16 * 8);
        }
        if (t < 128) {
            const float* qp = (const float*)(ws + WS_QPART) + h * 128 + t;
            float s = 0.f;
#pragma unroll
            for (int p = 0; p < 8; ++p) s += qp[p * 4096];
            vs[t] = rbf(s);
        }
        __syncthreads();
        float q8[8];
        *(f32x4*)(q8) = *(const f32x4*)(vs + l16 * 8);
        *(f32x4*)(q8 + 4) = *(const f32x4*)(vs + l16 * 8 + 4);
        const float scale = 0.08838834764831845f;
        u16* qabs = (u16*)(ws + WS_QABS);
#pragma unroll
        for (int i = 0; i < 4; ++i) {
            float d = 0.f;
#pragma unroll
            for (int j = 0; j < 4; ++j) {
                d += q8[2 * j] * bflo(w[i][j]);
                d += q8[2 * j + 1] * bfhi(w[i][j]);
            }
            d += __shfl_xor(d, 1);
            d += __shfl_xor(d, 2);
            d += __shfl_xor(d, 4);
            d += __shfl_xor(d, 8);
            if (l16 == 0) qabs[h * 512 + c0 + rg + 16 * i] = f2bf(d * scale);
        }
    } else {
        const int bb = b - 256;
        const int rc = bb >> 2;
        const int cc = bb & 3;
        const int f0 = rc * 64;
        const int nrows = min(64, FF - f0);
        if (t < 64) {
            float hv = 0.f;
            if (t < nrows) {
                const float* gp = (const float*)(ws + WS_GPART) + f0 + t;
                const float* up = (const float*)(ws + WS_UPART) + f0 + t;
                float gs = 0.f, us = 0.f;
#pragma unroll
                for (int p = 0; p < 8; ++p) { gs += gp[p * 1408]; us += up[p * 1408]; }
                gs = rbf(gs); us = rbf(us);
                float sg = rbf(gs / (1.f + __expf(-gs)));
                hv = rbf(sg * us);
            }
            vs[t] = hv;
        }
        __syncthreads();
        float* out = (float*)(ws + WS_BPART) + rc * 512 + cc * 128;
        gemv_tile_64x128(wd, DM, f0, nrows, cc * 128, 128, vs, red, out);
    }
}

__global__ __launch_bounds__(256) void k3_scores(const u16* __restrict__ ckv, char* __restrict__ ws) {
    __shared__ __attribute__((aligned(16))) float red[4 * 32 * 32];
    const int t = threadIdx.x;
    const int lane = t & 63, w = t >> 6;
    const int key0 = blockIdx.x * 32;
    const int r = lane & 31, h2 = lane >> 5;
    const u16* qabs = (const u16*)(ws + WS_QABS);
    const int coff = 128 * w + 64 * h2;
    const u16* ap = qabs + r * 512 + coff;
    const u16* bp = ckv + (size_t)(key0 + r) * 512 + coff;
    bf16x8 a[8], bq[8];
#pragma unroll
    for (int s = 0; s < 8; ++s) bq[s] = *(const bf16x8*)(bp + 8 * s);
#pragma unroll
    for (int s = 0; s < 8; ++s) a[s] = *(const bf16x8*)(ap + 8 * s);
    f32x16 acc;
#pragma unroll
    for (int i = 0; i < 16; ++i) acc[i] = 0.f;
#pragma unroll
    for (int s = 0; s < 8; ++s) acc = __builtin_amdgcn_mfma_f32_32x32x16_bf16(a[s], bq[s], acc, 0, 0, 0);
#pragma unroll
    for (int i = 0; i < 16; ++i) {
        int head = (i & 3) + 8 * (i >> 2) + 4 * h2;
        red[(w * 32 + head) * 32 + r] = acc[i];
    }
    __syncthreads();
    const int head = t >> 3, kq = (t & 7) * 4;
    f32x4 s4 = *(const f32x4*)(red + head * 32 + kq);
#pragma unroll
    for (int ww = 1; ww < 4; ++ww) s4 += *(const f32x4*)(red + (ww * 32 + head) * 32 + kq);
    float m = fmaxf(fmaxf(s4[0], s4[1]), fmaxf(s4[2], s4[3]));
    m = fmaxf(m, __shfl_xor(m, 1));
    m = fmaxf(m, __shfl_xor(m, 2));
    m = fmaxf(m, __shfl_xor(m, 4));
    unsigned p01 = (unsigned)f2bf(__expf(s4[0] - m)) | ((unsigned)f2bf(__expf(s4[1] - m)) << 16);
    unsigned p23 = (unsigned)f2bf(__expf(s4[2] - m)) | ((unsigned)f2bf(__expf(s4[3] - m)) << 16);
    u16* P = (u16*)(ws + WS_P);
    *(u32x2*)(P + (size_t)head * SK + key0 + kq) = (u32x2){p01, p23};
    if ((t & 7) == 0) ((float*)(ws + WS_MLOC))[blockIdx.x * 32 + head] = m;
}

__global__ __launch_bounds__(256) void k4_pv(const u16* __restrict__ ckv, char* __restrict__ ws) {
    __shared__ __attribute__((aligned(16))) char smem[4 * 8192];
    __shared__ float lred[4 * 2 * 32];
    const int t = threadIdx.x;
    const int lane = t & 63, w = t >> 6;
    const int kc = blockIdx.x & 15, cc = blockIdx.x >> 4;
    const int r = lane & 31, h2 = lane >> 5;
    const int kbase = kc * 512 + w * 128;
    u32x4 cv[8];
    {
        const u16* cp = ckv + (size_t)(kbase + (lane >> 2)) * 512 + cc * 32 + (lane & 3) * 8;
#pragma unroll
        for (int i = 0; i < 8; ++i) cv[i] = *(const u32x4*)(cp + (size_t)i * 16 * 512);
    }
    const u16* P = (const u16*)(ws + WS_P);
    u32x4 pv[8];
    {
        const u16* pp = P + (size_t)r * SK + kbase + 64 * h2;
#pragma unroll
        for (int s = 0; s < 8; ++s) pv[s] = *(const u32x4*)(pp + 8 * s);
    }
    const float* mloc = (const float*)(ws + WS_MLOC) + (size_t)kc * 16 * 32 + r;
    float mv[16];
#pragma unroll
    for (int i = 0; i < 16; ++i) mv[i] = mloc[i * 32];
    float M = mv[0];
#pragma unroll
    for (int i = 1; i < 16; ++i) M = fmaxf(M, mv[i]);
    float f0, f1;
    {
        float m0 = mv[0], m1 = mv[1];
#pragma unroll
        for (int i = 0; i < 16; i += 2) {
            if (i == 4 * w + 2 * h2) { m0 = mv[i]; m1 = mv[i + 1]; }
        }
        f0 = __expf(m0 - M);
        f1 = __expf(m1 - M);
    }
    char* tile = smem + w * 8192;
#pragma unroll
    for (int i = 0; i < 8; ++i) *(u32x4*)(tile + (i * 16 + (lane >> 2)) * 64 + (lane & 3) * 16) = cv[i];
    float lsum = 0.f;
    bf16x8 a[8];
#pragma unroll
    for (int s = 0; s < 8; ++s) {
        float f = (s < 4) ? f0 : f1;
        u32x4 o;
#pragma unroll
        for (int j = 0; j < 4; ++j) {
            u16 lo = f2bf(bflo(pv[s][j]) * f);
            u16 hi = f2bf(bfhi(pv[s][j]) * f);
            lsum += bf2f(lo) + bf2f(hi);
            o[j] = (unsigned)lo | ((unsigned)hi << 16);
        }
        a[s] = __builtin_bit_cast(bf16x8, o);
    }
    __syncthreads();
    f32x16 acc;
#pragma unroll
    for (int i = 0; i < 16; ++i) acc[i] = 0.f;
    {
        const int g = lane >> 4;
        const int q = (lane & 15) >> 2, p = lane & 3;
        const int cb = g & 1;
#pragma unroll
        for (int s = 0; s < 8; ++s) {
            int row = 64 * h2 + 8 * s + q;
            s16x4 t0 = __builtin_amdgcn_ds_read_tr16_b64_v4i16(
                (__attribute__((address_space(3))) s16x4*)(tile + row * 64 + cb * 32 + p * 8));
            s16x4 t1 = __builtin_amdgcn_ds_read_tr16_b64_v4i16(
                (__attribute__((address_space(3))) s16x4*)(tile + (row + 4) * 64 + cb * 32 + p * 8));
            s16x8 bb = {t0[0], t0[1], t0[2], t0[3], t1[0], t1[1], t1[2], t1[3]};
            acc = __builtin_amdgcn_mfma_f32_32x32x16_bf16(a[s], __builtin_bit_cast(bf16x8, bb), acc, 0, 0, 0);
        }
    }
    __syncthreads();
    float* red = (float*)smem;
#pragma unroll
    for (int i = 0; i < 16; ++i) {
        int head = (i & 3) + 8 * (i >> 2) + 4 * h2;
        red[(w * 32 + head) * 32 + r] = acc[i];
    }
    lred[(w * 2 + h2) * 32 + r] = lsum;
    __syncthreads();
    const int head = t >> 3, cq = (t & 7) * 4;
    f32x4 s4 = *(const f32x4*)(red + head * 32 + cq);
#pragma unroll
    for (int ww = 1; ww < 4; ++ww) s4 += *(const f32x4*)(red + (ww * 32 + head) * 32 + cq);
    float* op = (float*)(ws + WS_OPART) + ((size_t)kc * 32 + head) * 512 + cc * 32 + cq;
    *(f32x4*)op = s4;
    if (cc == 0 && t < 32) {
        float L = 0.f;
#pragma unroll
        for (int i = 0; i < 8; ++i) L += lred[i * 32 + t];
        float* ml = (float*)(ws + WS_ML) + (kc * 32 + t) * 2;
        ml[0] = M;
        ml[1] = L;
    }
}

__global__ __launch_bounds__(256) void k5_out(const u16* __restrict__ wuv, const u16* __restrict__ wo,
                                              char* __restrict__ ws) {
    __shared__ __attribute__((aligned(16))) float cc_s[512];
    __shared__ __attribute__((aligned(16))) float red[4 * 512];
    __shared__ __attribute__((aligned(16))) float ctx_s[16];
    const int t = threadIdx.x;
    const int lane = t & 63, w = t >> 6;
    const int h = blockIdx.x & 31, j = blockIdx.x >> 5;
    u32x4 uv[4];
#pragma unroll
    for (int i = 0; i < 4; ++i) {
        int c = (t >> 1) + 128 * i;
        uv[i] = *(const u32x4*)(wuv + (size_t)c * 4096 + h * 128 + j * 16 + (t & 1) * 8);
    }
    u32x4 ov[4];
#pragma unroll
    for (int i = 0; i < 4; ++i) {
        int d = j * 16 + w * 4 + i;
        ov[i] = *(const u32x4*)(wo + ((size_t)h * 128 + d) * 512 + lane * 8);
    }
    const float* ml = (const float*)(ws + WS_ML) + h * 2;
    float mk[16], lk[16];
#pragma unroll
    for (int k = 0; k < 16; ++k) { mk[k] = ml[k * 64]; lk[k] = ml[k * 64 + 1]; }
    float M = mk[0];
#pragma unroll
    for (int k = 1; k < 16; ++k) M = fmaxf(M, mk[k]);
    float L = 0.f;
    float wk[16];
#pragma unroll
    for (int k = 0; k < 16; ++k) { wk[k] = __expf(mk[k] - M); L += wk[k] * lk[k]; }
    const float inv = 1.f / L;
    const float* op = (const float*)(ws + WS_OPART) + (size_t)h * 512 + t * 2;
    float c0 = 0.f, c1 = 0.f;
#pragma unroll
    for (int k = 0; k < 16; ++k) {
        float2 v = *(const float2*)(op + (size_t)k * 32 * 512);
        c0 += wk[k] * v.x;
        c1 += wk[k] * v.y;
    }
    cc_s[t * 2] = c0 * inv;
    cc_s[t * 2 + 1] = c1 * inv;
    __syncthreads();
    float acc[8];
#pragma unroll
    for (int jj = 0; jj < 8; ++jj) acc[jj] = 0.f;
#pragma unroll
    for (int i = 0; i < 4; ++i) {
        float cv = cc_s[(t >> 1) + 128 * i];
#pragma unroll
        for (int jj = 0; jj < 4; ++jj) {
            acc[2 * jj] += cv * bflo(uv[i][jj]);
            acc[2 * jj + 1] += cv * bfhi(uv[i][jj]);
        }
    }
#pragma unroll
    for (int jj = 0; jj < 8; ++jj) {
        acc[jj] += __shfl_xor(acc[jj], 2);
        acc[jj] += __shfl_xor(acc[jj], 4);
        acc[jj] += __shfl_xor(acc[jj], 8);
        acc[jj] += __shfl_xor(acc[jj], 16);
        acc[jj] += __shfl_xor(acc[jj], 32);
    }
    if (lane < 2) {
#pragma unroll
        for (int jj = 0; jj < 8; ++jj) red[w * 16 + lane * 8 + jj] = acc[jj];
    }
    __syncthreads();
    if (t < 16) ctx_s[t] = rbf((red[t] + red[16 + t]) + (red[32 + t] + red[48 + t]));
    __syncthreads();
    float o8[8];
#pragma unroll
    for (int jj = 0; jj < 8; ++jj) o8[jj] = 0.f;
#pragma unroll
    for (int i = 0; i < 4; ++i) {
        float cv = ctx_s[w * 4 + i];
#pragma unroll
        for (int jj = 0; jj < 4; ++jj) {
            o8[2 * jj] += cv * bflo(ov[i][jj]);
            o8[2 * jj + 1] += cv * bfhi(ov[i][jj]);
        }
    }
    *(f32x4*)(red + w * 512 + lane * 8) = (f32x4){o8[0], o8[1], o8[2], o8[3]};
    *(f32x4*)(red + w * 512 + lane * 8 + 4) = (f32x4){o8[4], o8[5], o8[6], o8[7]};
    __syncthreads();
    float* outp = (float*)(ws + WS_OUTP) + (size_t)blockIdx.x * 512;
    {
        int c = t * 2;
        float2 v;
        v.x = (red[c] + red[512 + c]) + (red[1024 + c] + red[1536 + c]);
        v.y = (red[c + 1] + red[512 + c + 1]) + (red[1024 + c + 1] + red[1536 + c + 1]);
        *(float2*)(outp + c) = v;
    }
}

__global__ __launch_bounds__(256) void k6_final(const u16* __restrict__ x, const u16* __restrict__ gw,
                                                const char* __restrict__ ws, u16* __restrict__ out) {
    __shared__ __attribute__((aligned(16))) float reda[64 * 16];
    __shared__ __attribute__((aligned(16))) float redb[64 * 16];
    __shared__ float gsh[4];
    const int t = threadIdx.x;
    const int c0 = blockIdx.x * 16 + (t & 3) * 4;
    const int rg = t >> 2;
    const float* outp = (const float*)(ws + WS_OUTP);
    f32x4 sa = {0.f, 0.f, 0.f, 0.f};
#pragma unroll
    for (int i = 0; i < 4; ++i) sa += *(const f32x4*)(outp + (size_t)(rg + 64 * i) * 512 + c0);
    f32x4 sb = {0.f, 0.f, 0.f, 0.f};
    if (rg < 22) sb = *(const f32x4*)((const float*)(ws + WS_BPART) + rg * 512 + c0);
    *(f32x4*)(reda + rg * 16 + (t & 3) * 4) = sa;
    *(f32x4*)(redb + rg * 16 + (t & 3) * 4) = sb;
    float gp = rbf(bf2f(x[t * 2]) * bf2f(gw[t * 2])) + rbf(bf2f(x[t * 2 + 1]) * bf2f(gw[t * 2 + 1]));
#pragma unroll
    for (int o = 32; o >= 1; o >>= 1) gp += __shfl_xor(gp, o);
    if ((t & 63) == 0) gsh[t >> 6] = gp;
    __syncthreads();
    if (t < 16) {
        float a = 0.f, b = 0.f;
#pragma unroll 8
        for (int i = 0; i < 64; ++i) a += reda[i * 16 + t];
#pragma unroll
        for (int i = 0; i < 22; ++i) b += redb[i * 16 + t];
        float gs = rbf((gsh[0] + gsh[1]) + (gsh[2] + gsh[3]));
        float g = rbf(1.f / (1.f + __expf(-gs)));
        a = rbf(a); b = rbf(b);
        float r = rbf(g * a) + rbf(rbf(1.f - g) * b);
        out[blockIdx.x * 16 + t] = f2bf(r);
    }
}

extern "C" void kernel_launch(void* const* d_in, const int* in_sizes, int n_in,
                              void* d_out, int out_size, void* d_ws, size_t ws_size,
                              hipStream_t stream) {
    const u16* x = (const u16*)d_in[0];
    const u16* ckv = (const u16*)d_in[1];
    const u16* wq = (const u16*)d_in[2];
    const u16* wuk = (const u16*)d_in[3];
    const u16* wuv = (const u16*)d_in[4];
    const u16* wo = (const u16*)d_in[5];
    const u16* wg = (const u16*)d_in[7];
    const u16* wu = (const u16*)d_in[8];
    const u16* wd = (const u16*)d_in[9];
    const u16* gw = (const u16*)d_in[10];
    char* ws = (char*)d_ws;
    u16* out = (u16*)d_out;
    k1_proj<<<256 + 176, 256, 0, stream>>>(x, wq, wg, wu, ws);
    k2_absorb<<<256 + 88, 256, 0, stream>>>(wuk, wd, ws);
    k3_scores<<<256, 256, 0, stream>>>(ckv, ws);
    k4_pv<<<256, 256, 0, stream>>>(ckv, ws);
    k5_out<<<256, 256, 0, stream>>>(wuv, wo, ws);
    k6_final<<<32, 256, 0, stream>>>(x, gw, ws, out);
}
```

```cpp
#include <hip/hip_runtime.h>
#include <hip/hip_bf16.h>
#include <stdint.h>

#define NH 32
#define HD 128
#define DM 512
#define DC 512
#define SK 8192
#define FF 1376

typedef unsigned short u16;
typedef __attribute__((ext_vector_type(8))) __bf16 bf16x8;
typedef __attribute__((ext_vector_type(4))) short s16x4;
typedef __attribute__((ext_vector_type(8))) short s16x8;
typedef __attribute__((ext_vector_type(16))) float f32x16;
typedef __attribute__((ext_vector_type(4))) float f32x4;
typedef __attribute__((ext_vector_type(4))) unsigned int u32x4;
typedef __attribute__((ext_vector_type(2))) unsigned int u32x2;
#define GAS __attribute__((address_space(1)))
typedef GAS unsigned gu32;
typedef GAS unsigned long long gu64;
#define RLX_AGENT __ATOMIC_RELAXED, __HIP_MEMORY_SCOPE_AGENT
#define AUX_SC1 16

__device__ __forceinline__ float bf2f(u16 v) { return __uint_as_float(((unsigned)v) << 16); }
__device__ __forceinline__ float bflo(unsigned v) { return __uint_as_float(v << 16); }
__device__ __forceinline__ float bfhi(unsigned v) { return __uint_as_float(v & 0xffff0000u); }
__device__ __forceinline__ u16 f2bf(float f) {
    unsigned u = __float_as_uint(f);
    if ((u & 0x7fffffffu) > 0x7f800000u) return (u16)((u >> 16) | 0x40);
    u += 0x7fffu + ((u >> 16) & 1u);
    return (u16)(u >> 16);
}
__device__ __forceinline__ float rbf(float f) { return bf2f(f2bf(f)); }

#define WS_QABS    0
#define WS_BPART   (WS_QABS + 32 * 512 * 2)
#define WS_P       (WS_BPART + 172 * 512 * 4)
#define WS_MLOC    (WS_P + 32 * 8192 * 2)
#define WS_OPART   (WS_MLOC + 256 * 32 * 4)
#define WS_ML      (WS_OPART + 16 * 32 * 512 * 4)
#define WS_GRAN    (WS_ML + 16 * 32 * 2 * 4)
#define WS_FLAG    (WS_GRAN + 256 * 512 * 8)
#define WS_END     (WS_FLAG + 256 * 64)

#define SPIN_CAP (1u << 18)

__global__ __launch_bounds__(512, 1) void ka_proj(const u16* __restrict__ x, const u16* __restrict__ wq,
                                                  const u16* __restrict__ wuk, const u16* __restrict__ wg,
                                                  const u16* __restrict__ wu, const u16* __restrict__ wd,
                                                  char* __restrict__ ws) {
    __shared__ __attribute__((aligned(16))) float xs[512];
    __shared__ __attribute__((aligned(16))) float red[8 * 512];
    __shared__ __attribute__((aligned(16))) float red2[16 * 520];
    __shared__ __attribute__((aligned(16))) float qs[128];
    __shared__ __attribute__((aligned(16))) float hs[16];
    const int b = blockIdx.x;
    const int t = threadIdx.x;
    const int lane = t & 63, w = t >> 6;
    const int h = b & 31, c0 = (b >> 5) * 64;
    const int l16 = t & 15, rg = t >> 4;
    const int xcd = b & 7, idx = b >> 3;
    const int fch = xcd * 22 + idx;
    const bool has_ffn = (idx < 22) && (fch < 172);
    const int f0 = fch * 8;

    u16 xv = x[t];
    u32x4 wqv[16];
#pragma unroll
    for (int i = 0; i < 16; ++i) wqv[i] = *(const u32x4*)(wq + (size_t)(rg + 32 * i) * 4096 + h * 128 + l16 * 8);
    u32x4 wkv[2];
#pragma unroll
    for (int i = 0; i < 2; ++i) wkv[i] = *(const u32x4*)(wuk + (size_t)(c0 + rg + 32 * i) * 4096 + h * 128 + l16 * 8);
    u32x4 gv = {0u, 0u, 0u, 0u}, uv = {0u, 0u, 0u, 0u}, dv = {0u, 0u, 0u, 0u};
    if (has_ffn) {
        gv = *(const u32x4*)(wg + (size_t)t * FF + f0);
        uv = *(const u32x4*)(wu + (size_t)t * FF + f0);
        dv = *(const u32x4*)(wd + (size_t)(f0 + w) * 512 + lane * 8);
    }
    {
        *(unsigned long long*)(ws + WS_GRAN + (size_t)b * 4096 + t * 8) = 0ull;
        if (t < 16) *(unsigned*)(ws + WS_FLAG + b * 64 + t * 4) = 0u;
    }
    const float xf = bf2f(xv);
    xs[t] = xf;
    __syncthreads();
    float acc[8];
#pragma unroll
    for (int j = 0; j < 8; ++j) acc[j] = 0.f;
#pragma unroll
    for (int i = 0; i < 16; ++i) {
        float xr = xs[rg + 32 * i];
#pragma unroll
        for (int j = 0; j < 4; ++j) {
            acc[2 * j] += xr * bflo(wqv[i][j]);
            acc[2 * j + 1] += xr * bfhi(wqv[i][j]);
        }
    }
#pragma unroll
    for (int j = 0; j < 8; ++j) {
        acc[j] += __shfl_xor(acc[j], 16);
        acc[j] += __shfl_xor(acc[j], 32);
    }
    if (lane < 16) {
        *(f32x4*)(red + w * 128 + l16 * 8) = (f32x4){acc[0], acc[1], acc[2], acc[3]};
        *(f32x4*)(red + w * 128 + l16 * 8 + 4) = (f32x4){acc[4], acc[5], acc[6], acc[7]};
    }
#pragma unroll
    for (int j = 0; j < 4; ++j) {
        red2[(2 * j) * 520 + t] = xf * bflo(gv[j]);
        red2[(2 * j + 1) * 520 + t] = xf * bfhi(gv[j]);
        red2[(8 + 2 * j) * 520 + t] = xf * bflo(uv[j]);
        red2[(8 + 2 * j + 1) * 520 + t] = xf * bfhi(uv[j]);
    }
    __syncthreads();
    if (t < 128) {
        float s = 0.f;
#pragma unroll
        for (int ww = 0; ww < 8; ++ww) s += red[ww * 128 + t];
        qs[t] = rbf(s);
    }
    {
        float s0 = 0.f, s1 = 0.f;
#pragma unroll
        for (int i = 0; i < 8; ++i) {
            s0 += red2[(2 * w) * 520 + lane + 64 * i];
            s1 += red2[(2 * w + 1) * 520 + lane + 64 * i];
        }
#pragma unroll
        for (int o = 32; o >= 1; o >>= 1) { s0 += __shfl_xor(s0, o); s1 += __shfl_xor(s1, o); }
        if (lane == 0) { hs[2 * w] = s0; hs[2 * w + 1] = s1; }
    }
    __syncthreads();
    {
        float q8[8];
        *(f32x4*)(q8) = *(const f32x4*)(qs + l16 * 8);
        *(f32x4*)(q8 + 4) = *(const f32x4*)(qs + l16 * 8 + 4);
        const float scale = 0.08838834764831845f;
        u16* qabs = (u16*)(ws + WS_QABS);
#pragma unroll
        for (int i = 0; i < 2; ++i) {
            float d = 0.f;
#pragma unroll
            for (int j = 0; j < 4; ++j) {
                d += q8[2 * j] * bflo(wkv[i][j]);
                d += q8[2 * j + 1] * bfhi(wkv[i][j]);
            }
            d += __shfl_xor(d, 1);
            d += __shfl_xor(d, 2);
            d += __shfl_xor(d, 4);
            d += __shfl_xor(d, 8);
            if (l16 == 0) qabs[h * 512 + c0 + rg + 32 * i] = f2bf(d * scale);
        }
    }
    if (has_ffn) {
        float gs = rbf(hs[w]), us = rbf(hs[8 + w]);
        float sg = rbf(gs / (1.f + __expf(-gs)));
        float hv = rbf(sg * us);
        f32x4 o0, o1;
        o0[0] = hv * bflo(dv[0]); o0[1] = hv * bfhi(dv[0]); o0[2] = hv * bflo(dv[1]); o0[3] = hv * bfhi(dv[1]);
        o1[0] = hv * bflo(dv[2]); o1[1] = hv * bfhi(dv[2]); o1[2] = hv * bflo(dv[3]); o1[3] = hv * bfhi(dv[3]);
        *(f32x4*)(red + w * 512 + lane * 8) = o0;
        *(f32x4*)(red + w * 512 + lane * 8 + 4) = o1;
    }
    __syncthreads();
    if (has_ffn) {
        float s = 0.f;
#pragma unroll
        for (int ww = 0; ww < 8; ++ww) s += red[ww * 512 + t];
        ((float*)(ws + WS_BPART))[(size_t)fch * 512 + t] = s;
    }
}

__global__ __launch_bounds__(256, 1) void kb_attn(const u16* __restrict__ ckv, char* __restrict__ ws) {
    __shared__ __attribute__((aligned(16))) char smem[4 * 8192];
    __shared__ __attribute__((aligned(16))) float red[4 * 32 * 32];
    __shared__ float lred[4 * 2 * 32];
    const int t = threadIdx.x;
    const int lane = t & 63, w = t >> 6;
    const int kc = blockIdx.x & 15, mem = blockIdx.x >> 4;
    const int key0 = kc * 512 + mem * 32;
    const int cc = mem;
    const int r = lane & 31, h2 = lane >> 5;
    bf16x8 a[8], bq[8];
    {
        const int coff = 128 * w + 64 * h2;
        const u16* bp = ckv + (size_t)(key0 + r) * 512 + coff;
#pragma unroll
        for (int s = 0; s < 8; ++s) bq[s] = *(const bf16x8*)(bp + 8 * s);
        const u16* ap = (const u16*)(ws + WS_QABS) + r * 512 + coff;
#pragma unroll
        for (int s = 0; s < 8; ++s) a[s] = *(const bf16x8*)(ap + 8 * s);
    }
    const int kbase = kc * 512 + w * 128;
    u32x4 cv[8];
    {
        const u16* cp = ckv + (size_t)(kbase + (lane >> 2)) * 512 + cc * 32 + (lane & 3) * 8;
#pragma unroll
        for (int i = 0; i < 8; ++i) cv[i] = *(const u32x4*)(cp + (size_t)i * 16 * 512);
    }
    __builtin_amdgcn_sched_barrier(0);
    f32x16 acc;
#pragma unroll
    for (int i = 0; i < 16; ++i) acc[i] = 0.f;
#pragma unroll
    for (int s = 0; s < 8; ++s) acc = __builtin_amdgcn_mfma_f32_32x32x16_bf16(a[s], bq[s], acc, 0, 0, 0);
#pragma unroll
    for (int i = 0; i < 16; ++i) {
        int head = (i & 3) + 8 * (i >> 2) + 4 * h2;
        red[(w * 32 + head) * 32 + r] = acc[i];
    }
    __syncthreads();
    {
        const int head = t >> 3, kq = (t & 7) * 4;
        f32x4 s4 = *(const f32x4*)(red + head * 32 + kq);
#pragma unroll
        for (int ww = 1; ww < 4; ++ww) s4 += *(const f32x4*)(red + (ww * 32 + head) * 32 + kq);
        float m = fmaxf(fmaxf(s4[0], s4[1]), fmaxf(s4[2], s4[3]));
        m = fmaxf(m, __shfl_xor(m, 1));
        m = fmaxf(m, __shfl_xor(m, 2));
        m = fmaxf(m, __shfl_xor(m, 4));
        unsigned p01 = (unsigned)f2bf(__expf(s4[0] - m)) | ((unsigned)f2bf(__expf(s4[1] - m)) << 16);
        unsigned p23 = (unsigned)f2bf(__expf(s4[2] - m)) | ((unsigned)f2bf(__expf(s4[3] - m)) << 16);
        gu64* pd = (gu64*)(ws + WS_P + ((size_t)head * SK + key0 + kq) * 2);
        __hip_atomic_store(pd, (unsigned long long)p01 | ((unsigned long long)p23 << 32), RLX_AGENT);
        if ((t & 7) == 0) __hip_atomic_store((gu32*)(ws + WS_MLOC + (blockIdx.x * 32 + head) * 4), __float_as_uint(m), RLX_AGENT);
    }
    asm volatile("s_waitcnt vmcnt(0)" ::: "memory");
    __syncthreads();
    if (t == 0) __hip_atomic_store((gu32*)(ws + WS_FLAG + blockIdx.x * 64), 1u, RLX_AGENT);
    char* tile = smem + w * 8192;
#pragma unroll
    for (int i = 0; i < 8; ++i) *(u32x4*)(tile + (i * 16 + (lane >> 2)) * 64 + (lane & 3) * 16) = cv[i];
    if (w == 0) {
        gu32* fl = (gu32*)(ws + WS_FLAG + (kc + 16 * (lane & 15)) * 64);
        for (unsigned spins = 0; spins < SPIN_CAP; ++spins) {
            unsigned v = __hip_atomic_load(fl, RLX_AGENT);
            if (__all(v == 1u)) break;
            __builtin_amdgcn_s_sleep(1);
        }
    }
    __syncthreads();
    const __amdgpu_buffer_rsrc_t prs = __builtin_amdgcn_make_buffer_rsrc(ws + WS_P, 0, 32 * 8192 * 2, 0x00020000);
    u32x4 pv[8];
#pragma unroll
    for (int s = 0; s < 8; ++s)
        pv[s] = __builtin_amdgcn_raw_buffer_load_b128(prs, (r * SK + kbase + 64 * h2 + 8 * s) * 2, 0, AUX_SC1);
    float mv[16];
#pragma unroll
    for (int i = 0; i < 16; ++i)
        mv[i] = __uint_as_float(__hip_atomic_load((gu32*)(ws + WS_MLOC + ((kc + 16 * i) * 32 + r) * 4), RLX_AGENT));
    float M = mv[0];
#pragma unroll
    for (int i = 1; i < 16; ++i) M = fmaxf(M, mv[i]);
    float f0, f1;
    {
        float m0 = mv[0], m1 = mv[1];
#pragma unroll
        for (int i = 0; i < 16; i += 2) {
            if (i == 4 * w + 2 * h2) { m0 = mv[i]; m1 = mv[i + 1]; }
        }
        f0 = __expf(m0 - M);
        f1 = __expf(m1 - M);
    }
    float lsum = 0.f;
    bf16x8 pa[8];
#pragma unroll
    for (int s = 0; s < 8; ++s) {
        float f = (s < 4) ? f0 : f1;
        u32x4 o;
#pragma unroll
        for (int j = 0; j < 4; ++j) {
            u16 lo = f2bf(bflo(pv[s][j]) * f);
            u16 hi = f2bf(bfhi(pv[s][j]) * f);
            lsum += bf2f(lo) + bf2f(hi);
            o[j] = (unsigned)lo | ((unsigned)hi << 16);
        }
        pa[s] = __builtin_bit_cast(bf16x8, o);
    }
#pragma unroll
    for (int i = 0; i < 16; ++i) acc[i] = 0.f;
    {
        const int g = lane >> 4;
        const int q = (lane & 15) >> 2, p = lane & 3;
        const int cb = g & 1;
#pragma unroll
        for (int s = 0; s < 8; ++s) {
            int row = 64 * h2 + 8 * s + q;
            s16x4 t0 = __builtin_amdgcn_ds_read_tr16_b64_v4i16(
                (__attribute__((address_space(3))) s16x4*)(tile + row * 64 + cb * 32 + p * 8));
            s16x4 t1 = __builtin_amdgcn_ds_read_tr16_b64_v4i16(
                (__attribute__((address_space(3))) s16x4*)(tile + (row + 4) * 64 + cb * 32 + p * 8));
            s16x8 bb = {t0[0], t0[1], t0[2], t0[3], t1[0], t1[1], t1[2], t1[3]};
            acc = __builtin_amdgcn_mfma_f32_32x32x16_bf16(pa[s], __builtin_bit_cast(bf16x8, bb), acc, 0, 0, 0);
        }
    }
#pragma unroll
    for (int i = 0; i < 16; ++i) {
        int head = (i & 3) + 8 * (i >> 2) + 4 * h2;
        red[(w * 32 + head) * 32 + r] = acc[i];
    }
    lred[(w * 2 + h2) * 32 + r] = lsum;
    __syncthreads();
    {
        const int head = t >> 3, cq = (t & 7) * 4;
        f32x4 s4 = *(const f32x4*)(red + head * 32 + cq);
#pragma unroll
        for (int ww = 1; ww < 4; ++ww) s4 += *(const f32x4*)(red + (ww * 32 + head) * 32 + cq);
        float* op = (float*)(ws + WS_OPART) + ((size_t)kc * 32 + head) * 512 + cc * 32 + cq;
        *(f32x4*)op = s4;
    }
    if (cc == 0 && t < 32) {
        float L = 0.f;
#pragma unroll
        for (int i = 0; i < 8; ++i) L += lred[i * 32 + t];
        float* ml = (float*)(ws + WS_ML) + (kc * 32 + t) * 2;
        ml[0] = M;
        ml[1] = L;
    }
}

__global__ __launch_bounds__(256, 1) void kc_out(const u16* __restrict__ x, const u16* __restrict__ gw,
                                                 const u16* __restrict__ wuv, const u16* __restrict__ wo,
                                                 char* __restrict__ ws, u16* __restrict__ out) {
    __shared__ __attribute__((aligned(16))) float cc_s[512];
    __shared__ __attribute__((aligned(16))) float red[4 * 512];
    __shared__ __attribute__((aligned(16))) float ctx_s[16];
    __shared__ float fin[4 * 5];
    const int t = threadIdx.x;
    const int lane = t & 63, w = t >> 6;
    const int b = blockIdx.x;
    const int h = b & 31, j = b >> 5;
    u32x4 uv[4];
#pragma unroll
    for (int i = 0; i < 4; ++i) {
        int c = (t >> 1) + 128 * i;
        uv[i] = *(const u32x4*)(wuv + (size_t)c * 4096 + h * 128 + j * 16 + (t & 1) * 8);
    }
    u32x4 ov[4];
#pragma unroll
    for (int i = 0; i < 4; ++i) {
        int d = j * 16 + w * 4 + i;
        ov[i] = *(const u32x4*)(wo + ((size_t)h * 128 + d) * 512 + lane * 8);
    }
    const float* ml = (const float*)(ws + WS_ML) + h * 2;
    float mk[16], lk[16];
#pragma unroll
    for (int k = 0; k < 16; ++k) { float2 v = *(const float2*)(ml + k * 64); mk[k] = v.x; lk[k] = v.y; }
    const float* op = (const float*)(ws + WS_OPART) + (size_t)h * 512 + t * 2;
    float2 ovv[16];
#pragma unroll
    for (int k = 0; k < 16; ++k) ovv[k] = *(const float2*)(op + (size_t)k * 32 * 512);
    float2 bp = {0.f, 0.f};
    if (t < 172) bp = *(const float2*)((const float*)(ws + WS_BPART) + (size_t)t * 512 + 2 * b);
    unsigned x2 = *(const unsigned*)(x + 2 * t);
    unsigned g2 = *(const unsigned*)(gw + 2 * t);
    __builtin_amdgcn_sched_barrier(0);

    float M = mk[0];
#pragma unroll
    for (int k = 1; k < 16; ++k) M = fmaxf(M, mk[k]);
    float L = 0.f;
    float wk[16];
#pragma unroll
    for (int k = 0; k < 16; ++k) { wk[k] = __expf(mk[k] - M); L += wk[k] * lk[k]; }
    const float inv = 1.f / L;
    float c0 = 0.f, c1 = 0.f;
#pragma unroll
    for (int k = 0; k < 16; ++k) { c0 += wk[k] * ovv[k].x; c1 += wk[k] * ovv[k].y; }
    cc_s[t * 2] = c0 * inv;
    cc_s[t * 2 + 1] = c1 * inv;
    __syncthreads();
    float acc[8];
#pragma unroll
    for (int jj = 0; jj < 8; ++jj) acc[jj] = 0.f;
#pragma unroll
    for (int i = 0; i < 4; ++i) {
        float cv = cc_s[(t >> 1) + 128 * i];
#pragma unroll
        for (int jj = 0; jj < 4; ++jj) {
            acc[2 * jj] += cv * bflo(uv[i][jj]);
            acc[2 * jj + 1] += cv * bfhi(uv[i][jj]);
        }
    }
#pragma unroll
    for (int jj = 0; jj < 8; ++jj) {
        acc[jj] += __shfl_xor(acc[jj], 2);
        acc[jj] += __shfl_xor(acc[jj], 4);
        acc[jj] += __shfl_xor(acc[jj], 8);
        acc[jj] += __shfl_xor(acc[jj], 16);
        acc[jj] += __shfl_xor(acc[jj], 32);
    }
    if (lane < 2) {
#pragma unroll
        for (int jj = 0; jj < 8; ++jj) red[w * 16 + lane * 8 + jj] = acc[jj];
    }
    __syncthreads();
    if (t < 16) ctx_s[t] = rbf((red[t] + red[16 + t]) + (red[32 + t] + red[48 + t]));
    __syncthreads();
    float o8[8];
#pragma unroll
    for (int jj = 0; jj < 8; ++jj) o8[jj] = 0.f;
#pragma unroll
    for (int i = 0; i < 4; ++i) {
        float cv = ctx_s[w * 4 + i];
#pragma unroll
        for (int jj = 0; jj < 4; ++jj) {
            o8[2 * jj] += cv * bflo(ov[i][jj]);
            o8[2 * jj + 1] += cv * bfhi(ov[i][jj]);
        }
    }
    *(f32x4*)(red + w * 512 + lane * 8) = (f32x4){o8[0], o8[1], o8[2], o8[3]};
    *(f32x4*)(red + w * 512 + lane * 8 + 4) = (f32x4){o8[4], o8[5], o8[6], o8[7]};
    __syncthreads();
    {
        int c = t * 2;
        float vx = (red[c] + red[512 + c]) + (red[1024 + c] + red[1536 + c]);
        float vy = (red[c + 1] + red[512 + c + 1]) + (red[1024 + c + 1] + red[1536 + c + 1]);
        gu64* g = (gu64*)(ws + WS_GRAN + (size_t)b * 4096 + c * 8);
        __hip_atomic_store(g, (1ull << 32) | __float_as_uint(vx), RLX_AGENT);
        __hip_atomic_store(g + 1, (1ull << 32) | __float_as_uint(vy), RLX_AGENT);
    }
    float gp = rbf(bflo(x2) * bflo(g2)) + rbf(bfhi(x2) * bfhi(g2));
    float ax = 0.f, ay = 0.f;
    {
        gu64* g = (gu64*)(ws + WS_GRAN + (size_t)t * 4096 + (2 * b) * 8);
        for (unsigned spins = 0; spins < SPIN_CAP; ++spins) {
            unsigned long long v0 = __hip_atomic_load(g, RLX_AGENT);
            unsigned long long v1 = __hip_atomic_load(g + 1, RLX_AGENT);
            ax = __uint_as_float((unsigned)v0);
            ay = __uint_as_float((unsigned)v1);
            bool ok = ((v0 >> 32) == 1ull) && ((v1 >> 32) == 1ull);
            if (__all(ok)) break;
            __builtin_amdgcn_s_sleep(1);
        }
    }
    float bx = bp.x, by = bp.y;
#pragma unroll
    for (int o = 32; o >= 1; o >>= 1) {
        ax += __shfl_xor(ax, o); ay += __shfl_xor(ay, o);
        bx += __shfl_xor(bx, o); by += __shfl_xor(by, o);
        gp += __shfl_xor(gp, o);
    }
    if (lane == 0) { fin[w * 5] = ax; fin[w * 5 + 1] = ay; fin[w * 5 + 2] = bx; fin[w * 5 + 3] = by; fin[w * 5 + 4] = gp; }
    __syncthreads();
    if (t < 2) {
        float a = (fin[t] + fin[5 + t]) + (fin[10 + t] + fin[15 + t]);
        float bb = (fin[2 + t] + fin[7 + t]) + (fin[12 + t] + fin[17 + t]);
        float gs = rbf((fin[4] + fin[9]) + (fin[14] + fin[19]));
        float g = rbf(1.f / (1.f + __expf(-gs)));
        a = rbf(a); bb = rbf(bb);
        float rr = rbf(g * a) + rbf(rbf(1.f - g) * bb);
        out[2 * b + t] = f2bf(rr);
    }
}

extern "C" void kernel_launch(void* const* d_in, const int* in_sizes, int n_in,
                              void* d_out, int out_size, void* d_ws, size_t ws_size,
                              hipStream_t stream) {
    const u16* x = (const u16*)d_in[0];
    const u16* ckv = (const u16*)d_in[1];
    const u16* wq = (const u16*)d_in[2];
    const u16* wuk = (const u16*)d_in[3];
    const u16* wuv = (const u16*)d_in[4];
    const u16* wo = (const u16*)d_in[5];
    const u16* wg = (const u16*)d_in[7];
    const u16* wu = (const u16*)d_in[8];
    const u16* wd = (const u16*)d_in[9];
    const u16* gw = (const u16*)d_in[10];
    char* ws = (char*)d_ws;
    u16* out = (u16*)d_out;
    ka_proj<<<256, 512, 0, stream>>>(x, wq, wuk, wg, wu, wd, ws);
    kb_attn<<<256, 256, 0, stream>>>(ckv, ws);
    kc_out<<<256, 256, 0, stream>>>(x, gw, wuv, wo, ws, out);
}
```

```cpp
#include <hip/hip_runtime.h>
#include <hip/hip_bf16.h>
#include <stdint.h>

#define NH 32
#define HD 128
#define DM 512
#define DC 512
#define SK 8192
#define FF 1376

typedef unsigned short u16;
typedef unsigned long long u64;
typedef __attribute__((ext_vector_type(8))) __bf16 bf16x8;
typedef __attribute__((ext_vector_type(2))) __bf16 bf16x2;
typedef __attribute__((ext_vector_type(4))) short s16x4;
typedef __attribute__((ext_vector_type(8))) short s16x8;
typedef __attribute__((ext_vector_type(16))) float f32x16;
typedef __attribute__((ext_vector_type(4))) float f32x4;
typedef __attribute__((ext_vector_type(2))) float f32x2;
typedef __attribute__((ext_vector_type(4))) unsigned int u32x4;
typedef __attribute__((ext_vector_type(2))) unsigned int u32x2;
#define GAS __attribute__((address_space(1)))
typedef GAS unsigned gu32;
typedef GAS u64 gu64;
#define RLX_AGENT __ATOMIC_RELAXED, __HIP_MEMORY_SCOPE_AGENT
#define AUX_SC1 16

__device__ __forceinline__ float bf2f(u16 v) { return __uint_as_float(((unsigned)v) << 16); }
__device__ __forceinline__ float bflo(unsigned v) { return __uint_as_float(v << 16); }
__device__ __forceinline__ float bfhi(unsigned v) { return __uint_as_float(v & 0xffff0000u); }
__device__ __forceinline__ unsigned pk2(float a, float b) {
    bf16x2 v = {(__bf16)a, (__bf16)b};
    return __builtin_bit_cast(unsigned, v);
}
__device__ __forceinline__ u16 f2bf(float f) { __bf16 v = (__bf16)f; return __builtin_bit_cast(u16, v); }
__device__ __forceinline__ float rbf(float f) { return bf2f(f2bf(f)); }

#define WS_EPOCH   0
#define WS_F2      256
#define WS_F3      (WS_F2 + 2048)
#define WS_F4      (WS_F3 + 2048)
#define WS_QG      (WS_F4 + 2048)
#define WS_QABS    (WS_QG + 4096 * 16)
#define WS_GB      (WS_QABS + 32 * 512 * 2)
#define WS_P       (WS_GB + 172 * 512 * 4)
#define WS_MLOC    (WS_P + 32 * 8192 * 2)
#define WS_OPART   (WS_MLOC + 256 * 32 * 8)
#define WS_ML      (WS_OPART + 16 * 32 * 512 * 4)
#define WS_GA      (WS_ML + 16 * 32 * 8)
#define WS_END     (WS_GA + 256 * 512 * 8)

#define SPIN_CAP (1u << 17)
#define STAMP(i)

__device__ __forceinline__ void drain() { asm volatile("s_waitcnt vmcnt(0)" ::: "memory"); }

__global__ __launch_bounds__(576, 1) void mega(const u16* __restrict__ x, const u16* __restrict__ ckv,
                                               const u16* __restrict__ wq, const u16* __restrict__ wuk,
                                               const u16* __restrict__ wuv, const u16* __restrict__ wo,
                                               const u16* __restrict__ wg, const u16* __restrict__ wu,
                                               const u16* __restrict__ wd, const u16* __restrict__ gw,
                                               char* __restrict__ ws, u16* __restrict__ out) {
    __shared__ __attribute__((aligned(16))) char tile[512 * 64];
    __shared__ __attribute__((aligned(16))) float red[8 * 1024];
    __shared__ __attribute__((aligned(16))) float pad_[6 * 1024];
    __shared__ __attribute__((aligned(16))) float sm1[8 * 16];
    __shared__ __attribute__((aligned(16))) float sm2[8 * 16];
    __shared__ __attribute__((aligned(16))) float qs[128];
    __shared__ __attribute__((aligned(16))) float qa[64];
    __shared__ __attribute__((aligned(16))) float fac[16 * 32];
    __shared__ __attribute__((aligned(16))) float wks[16];
    __shared__ __attribute__((aligned(16))) float cc_s[512];
    __shared__ __attribute__((aligned(16))) float ctx_s[16];
    __shared__ __attribute__((aligned(16))) float fin[9 * 8];

    const int b = blockIdx.x;
    const int t = threadIdx.x;
    const int lane = t & 63, w = t >> 6;
    const bool comm = (w == 8);
    const int h = b & 31, j = b >> 5;
    const int kc = b & 15, mem = b >> 4;
    const int key0 = kc * 512 + mem * 32;
    const int r = lane & 31, h2 = lane >> 5;
    const int fidx = b >> 3;
    const int fch = (b & 7) * 22 + fidx;
    const bool has_ffn = (fidx < 22) && (fch < 172);
    const int f0 = fch * 8;
    pad_[t * 10] = 0.f;
    STAMP(0);

    u32x4 wqv[2], wkv[2], gv = {0u, 0u, 0u, 0u}, uv_ = {0u, 0u, 0u, 0u}, dv = {0u, 0u, 0u, 0u};
    bf16x8 bq[4];
    u32x4 cv[4], uvv[2], ovv[2];
    float xr0 = 0.f, xr1 = 0.f, xt = 0.f;
    unsigned tag = 0;
    if (!comm) {
        const int row = t >> 1;
        xr0 = bf2f(x[row]); xr1 = bf2f(x[row + 256]); xt = bf2f(x[t]);
#pragma unroll
        for (int i = 0; i < 2; ++i)
            wqv[i] = *(const u32x4*)(wq + (size_t)(row + 256 * i) * 4096 + h * 128 + j * 16 + (t & 1) * 8);
        if (has_ffn) {
            gv = *(const u32x4*)(wg + (size_t)t * FF + f0);
            uv_ = *(const u32x4*)(wu + (size_t)t * FF + f0);
            dv = *(const u32x4*)(wd + (size_t)(f0 + w) * 512 + lane * 8);
        }
#pragma unroll
        for (int i = 0; i < 2; ++i)
            wkv[i] = *(const u32x4*)(wuk + (size_t)(j * 64 + (t >> 4) + 32 * i) * 4096 + h * 128 + (t & 15) * 8);
        {
            const u16* bp = ckv + (size_t)(key0 + r) * 512 + 64 * w + 32 * h2;
#pragma unroll
            for (int s = 0; s < 4; ++s) bq[s] = *(const bf16x8*)(bp + 8 * s);
        }
        {
            const u16* cp = ckv + (size_t)(kc * 512 + (t >> 2)) * 512 + mem * 32 + (t & 3) * 8;
#pragma unroll
            for (int i = 0; i < 4; ++i) cv[i] = *(const u32x4*)(cp + (size_t)i * 128 * 512);
        }
#pragma unroll
        for (int i = 0; i < 2; ++i)
            uvv[i] = *(const u32x4*)(wuv + (size_t)(row + 256 * i) * 4096 + h * 128 + j * 16 + (t & 1) * 8);
#pragma unroll
        for (int i = 0; i < 2; ++i)
            ovv[i] = *(const u32x4*)(wo + ((size_t)h * 128 + j * 16 + w * 2 + i) * 512 + lane * 8);
        __hip_atomic_store((gu64*)(ws + WS_GA + (size_t)b * 4096 + t * 8), 0ull, RLX_AGENT);
        __builtin_amdgcn_sched_barrier(0);

        float acc[8];
#pragma unroll
        for (int jj = 0; jj < 4; ++jj) {
            acc[2 * jj] = xr0 * bflo(wqv[0][jj]) + xr1 * bflo(wqv[1][jj]);
            acc[2 * jj + 1] = xr0 * bfhi(wqv[0][jj]) + xr1 * bfhi(wqv[1][jj]);
        }
        {
            bool up = lane & 2;
            float s[4];
#pragma unroll
            for (int k = 0; k < 4; ++k) { float send = up ? acc[k] : acc[k + 4]; float keep = up ? acc[k + 4] : acc[k]; s[k] = keep + __shfl_xor(send, 2); }
            up = lane & 4;
            float s2[2];
#pragma unroll
            for (int k = 0; k < 2; ++k) { float send = up ? s[k] : s[k + 2]; float keep = up ? s[k + 2] : s[k]; s2[k] = keep + __shfl_xor(send, 4); }
            up = lane & 8;
            float send = up ? s2[0] : s2[1]; float keep = up ? s2[1] : s2[0];
            float v = keep + __shfl_xor(send, 8);
            v += __shfl_xor(v, 16);
            v += __shfl_xor(v, 32);
            if (lane < 16) {
                int d = (lane & 1) * 8 + ((lane >> 1) & 1) * 4 + ((lane >> 2) & 1) * 2 + ((lane >> 3) & 1);
                sm1[w * 16 + d] = v;
            }
        }
        {
            float p[16];
#pragma unroll
            for (int jj = 0; jj < 4; ++jj) {
                p[2 * jj] = xt * bflo(gv[jj]); p[2 * jj + 1] = xt * bfhi(gv[jj]);
                p[8 + 2 * jj] = xt * bflo(uv_[jj]); p[8 + 2 * jj + 1] = xt * bfhi(uv_[jj]);
            }
            bool up = lane & 1;
            float s8[8];
#pragma unroll
            for (int k = 0; k < 8; ++k) { float send = up ? p[k] : p[k + 8]; float keep = up ? p[k + 8] : p[k]; s8[k] = keep + __shfl_xor(send, 1); }
            up = lane & 2;
            float s4[4];
#pragma unroll
            for (int k = 0; k < 4; ++k) { float send = up ? s8[k] : s8[k + 4]; float keep = up ? s8[k + 4] : s8[k]; s4[k] = keep + __shfl_xor(send, 2); }
            up = lane & 4;
            float s2[2];
#pragma unroll
            for (int k = 0; k < 2; ++k) { float send = up ? s4[k] : s4[k + 2]; float keep = up ? s4[k + 2] : s4[k]; s2[k] = keep + __shfl_xor(send, 4); }
            up = lane & 8;
            float send = up ? s2[0] : s2[1]; float keep = up ? s2[1] : s2[0];
            float v = keep + __shfl_xor(send, 8);
            v += __shfl_xor(v, 16);
            v += __shfl_xor(v, 32);
            if (lane < 16) {
                int idx = (lane & 1) * 8 + ((lane >> 1) & 1) * 4 + ((lane >> 2) & 1) * 2 + ((lane >> 3) & 1);
                sm2[w * 16 + idx] = v;
            }
        }
    } else {
        unsigned e = __hip_atomic_load((gu32*)(ws + WS_EPOCH), RLX_AGENT);
        tag = e + 1u;
    }
    STAMP(1);
    __syncthreads();
    if (comm) {
        if (lane < 16) {
            float s = 0.f;
#pragma unroll
            for (int ww = 0; ww < 8; ++ww) s += sm1[ww * 16 + lane];
            unsigned v = __float_as_uint(rbf(s));
            gu64* g = (gu64*)(ws + WS_QG + (size_t)(h * 128 + j * 16 + lane) * 16);
            __hip_atomic_store(g, ((u64)tag << 32) | v, RLX_AGENT);
            __hip_atomic_store(g + 1, ((u64)(~tag) << 32) | v, RLX_AGENT);
        }
        gu64* g0 = (gu64*)(ws + WS_QG + (size_t)(h * 128 + lane) * 16);
        gu64* g1 = g0 + 128;
        for (unsigned spins = 0; spins < SPIN_CAP; ++spins) {
            u64 a0 = __hip_atomic_load(g0, RLX_AGENT), a1 = __hip_atomic_load(g0 + 1, RLX_AGENT);
            u64 b0 = __hip_atomic_load(g1, RLX_AGENT), b1 = __hip_atomic_load(g1 + 1, RLX_AGENT);
            bool ok = ((unsigned)(a0 >> 32) == tag) && ((unsigned)(a1 >> 32) == ~tag) &&
                      ((unsigned)(b0 >> 32) == tag) && ((unsigned)(b1 >> 32) == ~tag);
            if (__all(ok)) {
                qs[lane] = __uint_as_float((unsigned)a0);
                qs[lane + 64] = __uint_as_float((unsigned)b0);
                break;
            }
            __builtin_amdgcn_s_sleep(1);
        }
    } else if (has_ffn) {
        float gs = 0.f, us = 0.f;
#pragma unroll
        for (int ww = 0; ww < 8; ++ww) { gs += sm2[ww * 16 + w]; us += sm2[ww * 16 + 8 + w]; }
        gs = rbf(gs); us = rbf(us);
        float sg = rbf(gs / (1.f + __expf(-gs)));
        float hv = rbf(sg * us);
        f32x4 o0, o1;
        o0[0] = hv * bflo(dv[0]); o0[1] = hv * bfhi(dv[0]); o0[2] = hv * bflo(dv[1]); o0[3] = hv * bfhi(dv[1]);
        o1[0] = hv * bflo(dv[2]); o1[1] = hv * bfhi(dv[2]); o1[2] = hv * bflo(dv[3]); o1[3] = hv * bfhi(dv[3]);
        *(f32x4*)(red + w * 512 + lane * 8) = o0;
        *(f32x4*)(red + w * 512 + lane * 8 + 4) = o1;
    }
    STAMP(2);
    __syncthreads();
    if (!comm) {
        const int l16 = t & 15;
        f32x4 qa0 = *(const f32x4*)(qs + l16 * 8), qa1 = *(const f32x4*)(qs + l16 * 8 + 4);
#pragma unroll
        for (int i = 0; i < 2; ++i) {
            float d = qa0[0] * bflo(wkv[i][0]) + qa0[1] * bfhi(wkv[i][0]) + qa0[2] * bflo(wkv[i][1]) + qa0[3] * bfhi(wkv[i][1]);
            d += qa1[0] * bflo(wkv[i][2]) + qa1[1] * bfhi(wkv[i][2]) + qa1[2] * bflo(wkv[i][3]) + qa1[3] * bfhi(wkv[i][3]);
            d += __shfl_xor(d, 1);
            d += __shfl_xor(d, 2);
            d += __shfl_xor(d, 4);
            d += __shfl_xor(d, 8);
            if (l16 == 0) qa[(t >> 4) + 32 * i] = d * 0.08838834764831845f;
        }
        if (has_ffn) {
            float s = 0.f;
#pragma unroll
            for (int ww = 0; ww < 8; ++ww) s += red[ww * 512 + t];
            __hip_atomic_store((gu32*)(ws + WS_GB + ((size_t)fch * 512 + t) * 4), __float_as_uint(s), RLX_AGENT);
        }
    }
    __syncthreads();
    if (comm) {
        if (lane < 16) {
            f32x4 v = *(const f32x4*)(qa + lane * 4);
            u64 pkd = (u64)pk2(v[0], v[1]) | ((u64)pk2(v[2], v[3]) << 32);
            __hip_atomic_store((gu64*)(ws + WS_QABS + (size_t)(h * 512 + j * 64 + lane * 4) * 2), pkd, RLX_AGENT);
        }
        drain();
        if (lane == 0) __hip_atomic_store((gu64*)(ws + WS_F2 + b * 8), ((u64)(~tag) << 32) | tag, RLX_AGENT);
        const u64 want = ((u64)(~tag) << 32) | tag;
        gu64* f = (gu64*)(ws + WS_F2 + lane * 8);
        for (unsigned spins = 0; spins < SPIN_CAP; ++spins) {
            u64 a0 = __hip_atomic_load(f, RLX_AGENT), a1 = __hip_atomic_load(f + 64, RLX_AGENT);
            u64 a2 = __hip_atomic_load(f + 128, RLX_AGENT), a3 = __hip_atomic_load(f + 192, RLX_AGENT);
            if (__all(a0 == want && a1 == want && a2 == want && a3 == want)) break;
            __builtin_amdgcn_s_sleep(1);
        }
    } else {
#pragma unroll
        for (int i = 0; i < 4; ++i) *(u32x4*)(tile + (i * 128 + (t >> 2)) * 64 + (t & 3) * 16) = cv[i];
    }
    STAMP(3);
    __syncthreads();
    if (!comm) {
        const __amdgpu_buffer_rsrc_t qrs = __builtin_amdgcn_make_buffer_rsrc(ws + WS_QABS, 0, 32 * 512 * 2, 0x00020000);
        u32x4 av[4];
#pragma unroll
        for (int s = 0; s < 4; ++s)
            av[s] = __builtin_amdgcn_raw_buffer_load_b128(qrs, (r * 512 + 64 * w + 32 * h2 + 8 * s) * 2, 0, AUX_SC1);
        f32x16 acc;
#pragma unroll
        for (int i = 0; i < 16; ++i) acc[i] = 0.f;
#pragma unroll
        for (int s = 0; s < 4; ++s)
            acc = __builtin_amdgcn_mfma_f32_32x32x16_bf16(__builtin_bit_cast(bf16x8, av[s]), bq[s], acc, 0, 0, 0);
#pragma unroll
        for (int i = 0; i < 16; ++i) {
            int head = (i & 3) + 8 * (i >> 2) + 4 * h2;
            red[(w * 32 + head) * 32 + r] = acc[i];
        }
    }
    STAMP(4);
    __syncthreads();
    if (!comm) {
        const int head = t >> 4, kq = (t & 15) * 2;
        f32x2 s2 = *(const f32x2*)(red + head * 32 + kq);
#pragma unroll
        for (int ww = 1; ww < 8; ++ww) s2 += *(const f32x2*)(red + (ww * 32 + head) * 32 + kq);
        float m = fmaxf(s2[0], s2[1]);
        m = fmaxf(m, __shfl_xor(m, 1));
        m = fmaxf(m, __shfl_xor(m, 2));
        m = fmaxf(m, __shfl_xor(m, 4));
        m = fmaxf(m, __shfl_xor(m, 8));
        unsigned p01 = pk2(__expf(s2[0] - m), __expf(s2[1] - m));
        float l = bflo(p01) + bfhi(p01);
        l += __shfl_xor(l, 1);
        l += __shfl_xor(l, 2);
        l += __shfl_xor(l, 4);
        l += __shfl_xor(l, 8);
        __hip_atomic_store((gu32*)(ws + WS_P + ((size_t)head * SK + key0 + kq) * 2), p01, RLX_AGENT);
        if ((t & 15) == 0)
            __hip_atomic_store((gu64*)(ws + WS_MLOC + (size_t)(b * 32 + head) * 8),
                               (u64)__float_as_uint(m) | ((u64)__float_as_uint(l) << 32), RLX_AGENT);
        drain();
    }
    __syncthreads();
    float Mk = 0.f, Lk = 0.f;
    if (comm) {
        const u64 want = ((u64)(~tag) << 32) | tag;
        if (lane == 0) __hip_atomic_store((gu64*)(ws + WS_F3 + (kc * 16 + mem) * 8), want, RLX_AGENT);
        gu64* f = (gu64*)(ws + WS_F3 + (kc * 16 + (lane & 15)) * 8);
        for (unsigned spins = 0; spins < SPIN_CAP; ++spins) {
            u64 a0 = __hip_atomic_load(f, RLX_AGENT);
            if (__all(a0 == want)) break;
            __builtin_amdgcn_s_sleep(1);
        }
        float mv[8], lv[8];
#pragma unroll
        for (int i = 0; i < 8; ++i) {
            u64 v = __hip_atomic_load((gu64*)(ws + WS_MLOC + (size_t)((kc + 16 * (8 * h2 + i)) * 32 + r) * 8), RLX_AGENT);
            mv[i] = __uint_as_float((unsigned)v); lv[i] = __uint_as_float((unsigned)(v >> 32));
        }
        float M = mv[0];
#pragma unroll
        for (int i = 1; i < 8; ++i) M = fmaxf(M, mv[i]);
        M = fmaxf(M, __shfl_xor(M, 32));
        float L = 0.f;
#pragma unroll
        for (int i = 0; i < 8; ++i) {
            float f_ = __expf(mv[i] - M);
            fac[(8 * h2 + i) * 32 + r] = f_;
            L += f_ * lv[i];
        }
        L += __shfl_xor(L, 32);
        Mk = M; Lk = L;
    }
    STAMP(5);
    __syncthreads();
    if (!comm) {
        const __amdgpu_buffer_rsrc_t prs = __builtin_amdgcn_make_buffer_rsrc(ws + WS_P, 0, 32 * 8192 * 2, 0x00020000);
        u32x4 pv[4];
#pragma unroll
        for (int s = 0; s < 4; ++s)
            pv[s] = __builtin_amdgcn_raw_buffer_load_b128(prs, (r * SK + kc * 512 + w * 64 + 16 * s + 8 * h2) * 2, 0, AUX_SC1);
        const int g = lane >> 4;
        const int q = (lane & 15) >> 2, p = lane & 3;
        const int cb = g & 1;
        f32x16 tot;
#pragma unroll
        for (int mm = 0; mm < 2; ++mm) {
            f32x16 acc;
#pragma unroll
            for (int i = 0; i < 16; ++i) acc[i] = 0.f;
#pragma unroll
            for (int ss = 0; ss < 2; ++ss) {
                int s = mm * 2 + ss;
                int row = w * 64 + 16 * s + 8 * h2 + q;
                s16x4 t0 = __builtin_amdgcn_ds_read_tr16_b64_v4i16(
                    (__attribute__((address_space(3))) s16x4*)(tile + row * 64 + cb * 32 + p * 8));
                s16x4 t1 = __builtin_amdgcn_ds_read_tr16_b64_v4i16(
                    (__attribute__((address_space(3))) s16x4*)(tile + (row + 4) * 64 + cb * 32 + p * 8));
                s16x8 bb = {t0[0], t0[1], t0[2], t0[3], t1[0], t1[1], t1[2], t1[3]};
                acc = __builtin_amdgcn_mfma_f32_32x32x16_bf16(__builtin_bit_cast(bf16x8, pv[s]), __builtin_bit_cast(bf16x8, bb), acc, 0, 0, 0);
            }
            const float* fp = fac + (2 * w + mm) * 32 + 4 * h2;
#pragma unroll
            for (int qd = 0; qd < 4; ++qd) {
                f32x4 f4 = *(const f32x4*)(fp + 8 * qd);
#pragma unroll
                for (int e = 0; e < 4; ++e) {
                    if (mm == 0) tot[qd * 4 + e] = f4[e] * acc[qd * 4 + e];
                    else tot[qd * 4 + e] += f4[e] * acc[qd * 4 + e];
                }
            }
        }
#pragma unroll
        for (int i = 0; i < 16; ++i) {
            int head = (i & 3) + 8 * (i >> 2) + 4 * h2;
            red[(w * 32 + head) * 32 + r] = tot[i];
        }
    }
    STAMP(6);
    __syncthreads();
    if (!comm) {
        const int head = t >> 4, cq = (t & 15) * 2;
        f32x2 s2 = *(const f32x2*)(red + head * 32 + cq);
#pragma unroll
        for (int ww = 1; ww < 8; ++ww) s2 += *(const f32x2*)(red + (ww * 32 + head) * 32 + cq);
        __hip_atomic_store((gu64*)(ws + WS_OPART + (((size_t)kc * 32 + head) * 512 + mem * 32 + cq) * 4),
                           (u64)__float_as_uint(s2[0]) | ((u64)__float_as_uint(s2[1]) << 32), RLX_AGENT);
        drain();
    } else {
        if (mem == 0 && lane < 32)
            __hip_atomic_store((gu64*)(ws + WS_ML + (size_t)(r * 16 + kc) * 8),
                               (u64)__float_as_uint(Mk) | ((u64)__float_as_uint(Lk) << 32), RLX_AGENT);
        drain();
    }
    __syncthreads();
    if (comm) {
        const u64 want = ((u64)(~tag) << 32) | tag;
        if (lane == 0) __hip_atomic_store((gu64*)(ws + WS_F4 + b * 8), want, RLX_AGENT);
        gu64* f = (gu64*)(ws + WS_F4 + lane * 8);
        for (unsigned spins = 0; spins < SPIN_CAP; ++spins) {
            u64 a0 = __hip_atomic_load(f, RLX_AGENT), a1 = __hip_atomic_load(f + 64, RLX_AGENT);
            u64 a2 = __hip_atomic_load(f + 128, RLX_AGENT), a3 = __hip_atomic_load(f + 192, RLX_AGENT);
            if (__all(a0 == want && a1 == want && a2 == want && a3 == want)) break;
            __builtin_amdgcn_s_sleep(1);
        }
        u64 v = __hip_atomic_load((gu64*)(ws + WS_ML + (size_t)(h * 16 + (lane & 15)) * 8), RLX_AGENT);
        float mk = __uint_as_float((unsigned)v), lk = __uint_as_float((unsigned)(v >> 32));
        float M = mk;
        M = fmaxf(M, __shfl_xor(M, 1)); M = fmaxf(M, __shfl_xor(M, 2));
        M = fmaxf(M, __shfl_xor(M, 4)); M = fmaxf(M, __shfl_xor(M, 8));
        float wk = __expf(mk - M);
        float L = wk * lk;
        L += __shfl_xor(L, 1); L += __shfl_xor(L, 2); L += __shfl_xor(L, 4); L += __shfl_xor(L, 8);
        if (lane < 16) wks[lane] = wk / L;
    }
    STAMP(7);
    __syncthreads();
    float bx = 0.f, by = 0.f;
    if (!comm) {
        float ov[16];
#pragma unroll
        for (int k = 0; k < 16; ++k)
            ov[k] = __uint_as_float(__hip_atomic_load((gu32*)(ws + WS_OPART + (((size_t)k * 32 + h) * 512 + t) * 4), RLX_AGENT));
        if (t < 172) {
            u64 v = __hip_atomic_load((gu64*)(ws + WS_GB + ((size_t)t * 512 + 2 * b) * 4), RLX_AGENT);
            bx = __uint_as_float((unsigned)v); by = __uint_as_float((unsigned)(v >> 32));
        }
        float c0 = 0.f;
#pragma unroll
        for (int k = 0; k < 16; ++k) c0 += wks[k] * ov[k];
        cc_s[t] = c0;
    }
    __syncthreads();
    if (!comm) {
        const int row = t >> 1;
        float c0 = cc_s[row], c1 = cc_s[row + 256];
        float acc[8];
#pragma unroll
        for (int jj = 0; jj < 4; ++jj) {
            acc[2 * jj] = c0 * bflo(uvv[0][jj]) + c1 * bflo(uvv[1][jj]);
            acc[2 * jj + 1] = c0 * bfhi(uvv[0][jj]) + c1 * bfhi(uvv[1][jj]);
        }
        bool up = lane & 2;
        float s[4];
#pragma unroll
        for (int k = 0; k < 4; ++k) { float send = up ? acc[k] : acc[k + 4]; float keep = up ? acc[k + 4] : acc[k]; s[k] = keep + __shfl_xor(send, 2); }
        up = lane & 4;
        float s2[2];
#pragma unroll
        for (int k = 0; k < 2; ++k) { float send = up ? s[k] : s[k + 2]; float keep = up ? s[k + 2] : s[k]; s2[k] = keep + __shfl_xor(send, 4); }
        up = lane & 8;
        float send = up ? s2[0] : s2[1]; float keep = up ? s2[1] : s2[0];
        float v = keep + __shfl_xor(send, 8);
        v += __shfl_xor(v, 16);
        v += __shfl_xor(v, 32);
        if (lane < 16) {
            int d = (lane & 1) * 8 + ((lane >> 1) & 1) * 4 + ((lane >> 2) & 1) * 2 + ((lane >> 3) & 1);
            sm1[w * 16 + d] = v;
        }
    }
    __syncthreads();
    if (!comm) {
        float c0 = 0.f, c1 = 0.f;
#pragma unroll
        for (int ww = 0; ww < 8; ++ww) { c0 += sm1[ww * 16 + 2 * w]; c1 += sm1[ww * 16 + 2 * w + 1]; }
        c0 = rbf(c0); c1 = rbf(c1);
        f32x4 o0, o1;
        o0[0] = c0 * bflo(ovv[0][0]) + c1 * bflo(ovv[1][0]); o0[1] = c0 * bfhi(ovv[0][0]) + c1 * bfhi(ovv[1][0]);
        o0[2] = c0 * bflo(ovv[0][1]) + c1 * bflo(ovv[1][1]); o0[3] = c0 * bfhi(ovv[0][1]) + c1 * bfhi(ovv[1][1]);
        o1[0] = c0 * bflo(ovv[0][2]) + c1 * bflo(ovv[1][2]); o1[1] = c0 * bfhi(ovv[0][2]) + c1 * bfhi(ovv[1][2]);
        o1[2] = c0 * bflo(ovv[0][3]) + c1 * bflo(ovv[1][3]); o1[3] = c0 * bfhi(ovv[0][3]) + c1 * bfhi(ovv[1][3]);
        *(f32x4*)(red + w * 512 + lane * 8) = o0;
        *(f32x4*)(red + w * 512 + lane * 8 + 4) = o1;
    }
    __syncthreads();
    STAMP(8);
    float ax = 0.f, ay = 0.f, gp = 0.f;
    if (!comm) {
        float s = 0.f;
#pragma unroll
        for (int ww = 0; ww < 8; ++ww) s += red[ww * 512 + t];
        __hip_atomic_store((gu64*)(ws + WS_GA + (size_t)b * 4096 + t * 8), (1ull << 32) | __float_as_uint(s), RLX_AGENT);
        gp = rbf(xt * bf2f(gw[t]));
        if (t < 256) {
            gu64* g = (gu64*)(ws + WS_GA + (size_t)t * 4096 + (2 * b) * 8);
            for (unsigned spins = 0; spins < SPIN_CAP; ++spins) {
                u64 v0 = __hip_atomic_load(g, RLX_AGENT);
                u64 v1 = __hip_atomic_load(g + 1, RLX_AGENT);
                ax = __uint_as_float((unsigned)v0);
                ay = __uint_as_float((unsigned)v1);
                if (__all(((v0 >> 32) == 1ull) && ((v1 >> 32) == 1ull))) break;
                __builtin_amdgcn_s_sleep(1);
            }
        }
#pragma unroll
        for (int o = 32; o >= 1; o >>= 1) {
            ax += __shfl_xor(ax, o); ay += __shfl_xor(ay, o);
            bx += __shfl_xor(bx, o); by += __shfl_xor(by, o);
            gp += __shfl_xor(gp, o);
        }
        if (lane == 0) { fin[w * 8] = ax; fin[w * 8 + 1] = ay; fin[w * 8 + 2] = bx; fin[w * 8 + 3] = by; fin[w * 8 + 4] = gp; }
    }
    __syncthreads();
    if (t < 2) {
        float a = 0.f, bb = 0.f, gs = 0.f;
#pragma unroll
        for (int ww = 0; ww < 8; ++ww) { a += fin[ww * 8 + t]; bb += fin[ww * 8 + 2 + t]; gs += fin[ww * 8 + 4]; }
        gs = rbf(gs);
        float g = rbf(1.f / (1.f + __expf(-gs)));
        a = rbf(a); bb = rbf(bb);
        float rr = rbf(g * a) + rbf(rbf(1.f - g) * bb);
        out[2 * b + t] = f2bf(rr);
    }
    if (comm && b == 0 && lane == 0) __hip_atomic_store((gu32*)(ws + WS_EPOCH), tag, RLX_AGENT);
    STAMP(9);
}

extern "C" void kernel_launch(void* const* d_in, const int* in_sizes, int n_in,
                              void* d_out, int out_size, void* d_ws, size_t ws_size,
                              hipStream_t stream) {
    const u16* x = (const u16*)d_in[0];
    const u16* ckv = (const u16*)d_in[1];
    const u16* wq = (const u16*)d_in[2];
    const u16* wuk = (const u16*)d_in[3];
    const u16* wuv = (const u16*)d_in[4];
    const u16* wo = (const u16*)d_in[5];
    const u16* wg = (const u16*)d_in[7];
    const u16* wu = (const u16*)d_in[8];
    const u16* wd = (const u16*)d_in[9];
    const u16* gw = (const u16*)d_in[10];
    mega<<<256, 576, 0, stream>>>(x, ckv, wq, wuk, wuv, wo, wg, wu, wd, gw, (char*)d_ws, (u16*)d_out);
}
```

```cpp
#include <hip/hip_runtime.h>
#include <hip/hip_bf16.h>
#include <stdint.h>

#define SK 8192
#define FF 1376

typedef unsigned short u16;
typedef unsigned long long u64;
typedef __attribute__((ext_vector_type(8))) __bf16 bf16x8;
typedef __attribute__((ext_vector_type(2))) __bf16 bf16x2;
typedef __attribute__((ext_vector_type(4))) short s16x4;
typedef __attribute__((ext_vector_type(8))) short s16x8;
typedef __attribute__((ext_vector_type(16))) float f32x16;
typedef __attribute__((ext_vector_type(4))) float f32x4;
typedef __attribute__((ext_vector_type(2))) float f32x2;
typedef __attribute__((ext_vector_type(4))) unsigned int u32x4;
typedef __attribute__((ext_vector_type(2))) unsigned int u32x2;
#define GAS __attribute__((address_space(1)))
typedef GAS unsigned gu32;
typedef GAS u64 gu64;
#define RLX_AGENT __ATOMIC_RELAXED, __HIP_MEMORY_SCOPE_AGENT
#define SC1 16
#define POISON 0xFFFFFFFFu

__device__ __forceinline__ float bf2f(u16 v) { return __uint_as_float(((unsigned)v) << 16); }
__device__ __forceinline__ float bflo(unsigned v) { return __uint_as_float(v << 16); }
__device__ __forceinline__ float bfhi(unsigned v) { return __uint_as_float(v & 0xffff0000u); }
__device__ __forceinline__ float asf(unsigned v) { return __uint_as_float(v); }
__device__ __forceinline__ unsigned asu(float v) { return __float_as_uint(v); }
__device__ __forceinline__ unsigned pk2(float a, float b) {
    bf16x2 v = {(__bf16)a, (__bf16)b};
    return __builtin_bit_cast(unsigned, v);
}
__device__ __forceinline__ u16 f2bf(float f) { __bf16 v = (__bf16)f; return __builtin_bit_cast(u16, v); }
__device__ __forceinline__ float rbf(float f) { return bf2f(f2bf(f)); }
__device__ __forceinline__ unsigned umax(unsigned a, unsigned b) { return a > b ? a : b; }
__device__ __forceinline__ unsigned max4(u32x4 v) { return umax(umax(v[0], v[1]), umax(v[2], v[3])); }

#define NREP 32
#define WS_EPOCH   0
#define WS_F0      256
#define WS_QG      (WS_F0 + NREP * 2048)
#define WS_QABS    (WS_QG + 256 * 1024)
#define WS_P       (WS_QABS + 8 * 32768)
#define WS_MLOC    (WS_P + 16 * 32 * 512 * 2)
#define WS_OPART   (WS_MLOC + 16 * 32 * 16 * 8)
#define WS_ML      (WS_OPART + 16 * 32 * 512 * 4)
#define WS_GA      (WS_ML + 32 * 16 * 8)
#define WS_GB      (WS_GA + 256 * 2048)
#define WS_END     (WS_GB + 172 * 2048)

#define SPIN_CAP (1u << 17)
#define STAMP(i)
#define NBAR 8

__device__ __forceinline__ void lgkm0() { asm volatile("s_waitcnt lgkmcnt(0)" ::: "memory"); }
__device__ __forceinline__ void vm0() { asm volatile("s_waitcnt vmcnt(0)" ::: "memory"); }
__device__ __forceinline__ void bar() { asm volatile("s_waitcnt lgkmcnt(0)\n\ts_barrier" ::: "memory"); }
typedef volatile __attribute__((address_space(3))) unsigned lds_u32;
__device__ __forceinline__ void lds_wait(lds_u32* p, unsigned target) {
    for (unsigned s = 0; s < SPIN_CAP; ++s) {
        if (*p >= target) break;
        __builtin_amdgcn_s_sleep(1);
    }
    asm volatile("" ::: "memory");
}

__global__ __launch_bounds__(640, 1) void mega(const u16* __restrict__ x, const u16* __restrict__ ckv,
                                               const u16* __restrict__ wq, const u16* __restrict__ wuk,
                                               const u16* __restrict__ wuv, const u16* __restrict__ wo,
                                               const u16* __restrict__ wg, const u16* __restrict__ wu,
                                               const u16* __restrict__ wd, const u16* __restrict__ gw,
                                               char* __restrict__ ws, u16* __restrict__ out) {
    __shared__ __attribute__((aligned(16))) char tile[512 * 64];
    __shared__ __attribute__((aligned(16))) float red1[8 * 1024];
    __shared__ __attribute__((aligned(16))) float red2[8 * 1024];
    __shared__ __attribute__((aligned(16))) float redf[8 * 512];
    __shared__ __attribute__((aligned(16))) float redo[8 * 512];
    __shared__ __attribute__((aligned(16))) float sm1[8 * 128];
    __shared__ __attribute__((aligned(16))) float sm2[8 * 16];
    __shared__ __attribute__((aligned(16))) float sm3[8 * 16];
    __shared__ __attribute__((aligned(16))) float qs[128];
    __shared__ __attribute__((aligned(16))) float fac[16 * 32];
    __shared__ __attribute__((aligned(16))) float Mws[32 * 8];
    __shared__ __attribute__((aligned(16))) float Lws[32 * 8];
    __shared__ __attribute__((aligned(16))) float cpart[4 * 512];
    __shared__ __attribute__((aligned(16))) float fin[8 * 32];
    __shared__ __attribute__((aligned(16))) float fing[8];
    __shared__ unsigned flg[4];

    const int b = blockIdx.x;
    const int t = threadIdx.x;
    const int lane = t & 63, w = t >> 6;
    const int h = b & 31, j = b >> 5;
    const int kc = b & 15, mem = b >> 4;
    const int key0 = kc * 512 + mem * 32;
    const int r = lane & 31, h2 = lane >> 5;
    const int fidx = b >> 3;
    const int fch = (b & 7) * 22 + fidx;
    const bool has_ffn = (fidx < 22) && (fch < 172);
    const int f0 = fch * 8;
    const bool cons = b >= 224;
    const __amdgpu_buffer_rsrc_t rs = __builtin_amdgcn_make_buffer_rsrc(ws, 0, WS_END, 0x00020000);
    lds_u32* vflg = (lds_u32*)flg;
    STAMP(0);

    if (w >= 8) {
        const unsigned e = __hip_atomic_load((gu32*)(ws + WS_EPOCH), RLX_AGENT);
        if (w == 9) {
            const u32x4 P4 = {POISON, POISON, POISON, POISON};
            const u32x2 P2 = {POISON, POISON};
            __builtin_amdgcn_raw_buffer_store_b128(P4, rs, WS_QABS + (((lane >> 3) * 8 + j) * 32 + h) * 128 + (lane & 7) * 16, 0, SC1);
#pragma unroll
            for (int i = 0; i < 2; ++i)
                __builtin_amdgcn_raw_buffer_store_b128(P4, rs, WS_P + ((kc * 32 + i * 16 + (lane >> 2)) * 512 + mem * 32) * 2 + (lane & 3) * 16, 0, SC1);
            if (lane < 32) __builtin_amdgcn_raw_buffer_store_b64(P2, rs, WS_MLOC + ((kc * 32 + lane) * 16 + mem) * 8, 0, SC1);
#pragma unroll
            for (int i = 0; i < 4; ++i)
                __builtin_amdgcn_raw_buffer_store_b128(P4, rs, WS_OPART + ((kc * 32 + i * 8 + (lane >> 3)) * 512 + mem * 32) * 4 + (lane & 7) * 16, 0, SC1);
            if (mem == 0 && lane < 32) __builtin_amdgcn_raw_buffer_store_b64(P2, rs, WS_ML + (lane * 16 + kc) * 8, 0, SC1);
#pragma unroll
            for (int i = 0; i < 2; ++i)
                __builtin_amdgcn_raw_buffer_store_b128(P4, rs, WS_GA + b * 2048 + i * 1024 + lane * 16, 0, SC1);
            if (has_ffn) {
#pragma unroll
                for (int i = 0; i < 2; ++i)
                    __builtin_amdgcn_raw_buffer_store_b128(P4, rs, WS_GB + fch * 2048 + i * 1024 + lane * 16, 0, SC1);
            }
        }
        bar();
        const unsigned tag = e + 1u;
        if (w == 8) {
            lds_wait(vflg + 0, 8);
            STAMP(1);
            f32x2 s = *(const f32x2*)(sm1 + 2 * lane);
#pragma unroll
            for (int ww = 1; ww < 8; ++ww) s += *(const f32x2*)(sm1 + ww * 128 + 2 * lane);
            u32x4 g = {tag, asu(s[0]), asu(s[1]), ~tag};
            __builtin_amdgcn_raw_buffer_store_b128(g, rs, WS_QG + ((h * 8 + j) * 64 + lane) * 16, 0, SC1);
            for (unsigned spins = 0; spins < SPIN_CAP; ++spins) {
            asm volatile("" ::: "memory");
                u32x4 v[8];
#pragma unroll
                for (int jj = 0; jj < 8; ++jj)
                    v[jj] = __builtin_amdgcn_raw_buffer_load_b128(rs, WS_QG + ((h * 8 + jj) * 64 + lane) * 16, 0, SC1);
                unsigned bad = 0;
                float q0 = 0.f, q1 = 0.f;
#pragma unroll
                for (int jj = 0; jj < 8; ++jj) {
                    bad |= (v[jj][0] ^ tag) | (v[jj][3] ^ ~tag);
                    q0 += asf(v[jj][1]); q1 += asf(v[jj][2]);
                }
                if (__all(bad == 0) || spins == SPIN_CAP - 1) {
                    f32x2 qq = {rbf(q0), rbf(q1)};
                    *(f32x2*)(qs + 2 * lane) = qq;
                    break;
                }
                __builtin_amdgcn_s_sleep(1);
            }
            lgkm0();
            if (lane == 0) vflg[1] = 1u;
            STAMP(2);
        } else {
            vm0();
            const u64 want = ((u64)(~tag) << 32) | tag;
            if (lane < NREP) __hip_atomic_store((gu64*)(ws + WS_F0 + lane * 2048 + b * 8), want, RLX_AGENT);
            STAMP(1);
            const int fo = WS_F0 + (b & (NREP - 1)) * 2048 + lane * 16;
            for (unsigned spins = 0; spins < SPIN_CAP; ++spins) {
            asm volatile("" ::: "memory");
                u32x4 a0 = __builtin_amdgcn_raw_buffer_load_b128(rs, fo, 0, SC1);
                u32x4 a1 = __builtin_amdgcn_raw_buffer_load_b128(rs, fo + 1024, 0, SC1);
                unsigned bad = (a0[0] ^ tag) | (a0[1] ^ ~tag) | (a0[2] ^ tag) | (a0[3] ^ ~tag) |
                               (a1[0] ^ tag) | (a1[1] ^ ~tag) | (a1[2] ^ tag) | (a1[3] ^ ~tag);
                if (__all(bad == 0)) break;
                __builtin_amdgcn_s_sleep(1);
            }
            if (lane == 0) vflg[2] = 1u;
            STAMP(2);
            unsigned pf[14];
            {
                const int rr = lane >> 2, sg = lane & 3;
                const int colb = (b & 7) * 352 + sg * 64;
                const char* pg = (const char*)wg + (size_t)((b >> 3) * 16 + rr) * (FF * 2);
                const char* pu = (const char*)wu + (size_t)((b >> 3) * 16 + rr) * (FF * 2);
                int c0 = colb, c1 = colb + 256 - (sg < 2 ? 0 : 256);
                if (c0 > FF * 2 - 4) c0 = FF * 2 - 4;
                if (c1 > FF * 2 - 4) c1 = FF * 2 - 4;
                pf[0] = *(const unsigned*)(pg + c0); pf[1] = *(const unsigned*)(pu + c0);
                pf[2] = *(const unsigned*)(pg + c1); pf[3] = *(const unsigned*)(pu + c1);
                const int fr = has_ffn ? f0 : 0;
#pragma unroll
                for (int i = 0; i < 2; ++i) pf[4 + i] = *(const unsigned*)((const char*)wd + (size_t)fr * 1024 + i * 4096 + lane * 64);
#pragma unroll
                for (int i = 0; i < 4; ++i)
                    pf[6 + i] = *(const unsigned*)((const char*)wuv + (size_t)(j * 64 + i * 16 + (lane >> 2)) * 8192 + h * 256 + (lane & 3) * 64);
#pragma unroll
                for (int i = 0; i < 4; ++i)
                    pf[10 + i] = *(const unsigned*)((const char*)wo + ((size_t)h * 128 + j * 16) * 1024 + i * 4096 + lane * 64);
            }
#pragma unroll
            for (int i = 0; i < NBAR; ++i) asm volatile("s_barrier" ::: "memory");
            asm volatile("" :: "v"(pf[0]), "v"(pf[1]), "v"(pf[2]), "v"(pf[3]), "v"(pf[4]), "v"(pf[5]), "v"(pf[6]),
                         "v"(pf[7]), "v"(pf[8]), "v"(pf[9]), "v"(pf[10]), "v"(pf[11]), "v"(pf[12]), "v"(pf[13]));
            return;
        }
#pragma unroll
        for (int i = 0; i < NBAR; ++i) asm volatile("s_barrier" ::: "memory");
        if (b == 224 && lane == 0) __hip_atomic_store((gu32*)(ws + WS_EPOCH), tag, RLX_AGENT);
        return;
    }

    u32x4 wqv[2], wkv[2];
    float x0, x1;
    {
        const int row = j * 64 + (t >> 4);
        x0 = bf2f(x[row]); x1 = bf2f(x[row + 32]);
#pragma unroll
        for (int i = 0; i < 2; ++i)
            wqv[i] = *(const u32x4*)(wq + (size_t)(row + 32 * i) * 4096 + h * 128 + (t & 15) * 8);
#pragma unroll
        for (int i = 0; i < 2; ++i)
            wkv[i] = *(const u32x4*)(wuk + (size_t)(j * 64 + 8 * w + (lane >> 4) + 4 * i) * 4096 + h * 128 + (lane & 15) * 8);
    }
    if (t < 4) flg[t] = 0u;
    bar();
    __builtin_amdgcn_sched_barrier(0);
    asm volatile("s_waitcnt vmcnt(2)" ::: "memory");
    STAMP(1);
    bf16x8 bq[4];
    u32x4 cv[4];
    {
        const u16* bp = ckv + (size_t)(key0 + r) * 512 + 64 * w + 32 * h2;
#pragma unroll
        for (int s = 0; s < 4; ++s) bq[s] = *(const bf16x8*)(bp + 8 * s);
        const u16* cp = ckv + (size_t)(kc * 512 + (t >> 2)) * 512 + mem * 32 + (t & 3) * 8;
#pragma unroll
        for (int i = 0; i < 4; ++i) cv[i] = *(const u32x4*)(cp + (size_t)i * 128 * 512);
    }
    __builtin_amdgcn_sched_barrier(0);
    {
        float acc[8];
#pragma unroll
        for (int jj = 0; jj < 4; ++jj) {
            acc[2 * jj] = x0 * bflo(wqv[0][jj]) + x1 * bflo(wqv[1][jj]);
            acc[2 * jj + 1] = x0 * bfhi(wqv[0][jj]) + x1 * bfhi(wqv[1][jj]);
        }
        bool up = lane & 16;
        float s[4];
#pragma unroll
        for (int k = 0; k < 4; ++k) { float send = up ? acc[k] : acc[k + 4]; float keep = up ? acc[k + 4] : acc[k]; s[k] = keep + __shfl_xor(send, 16); }
        up = lane & 32;
        f32x2 s2;
#pragma unroll
        for (int k = 0; k < 2; ++k) { float send = up ? s[k] : s[k + 2]; float keep = up ? s[k + 2] : s[k]; s2[k] = keep + __shfl_xor(send, 32); }
        *(f32x2*)(sm1 + w * 128 + (lane & 15) * 8 + ((lane >> 4) & 1) * 4 + (lane >> 5) * 2) = s2;
        lgkm0();
        if (lane == 0) __hip_atomic_fetch_add(flg + 0, 1u, __ATOMIC_RELAXED, __HIP_MEMORY_SCOPE_WORKGROUP);
    }
    lds_wait(vflg + 1, 1);
    STAMP(2);
    {
        const int l16 = lane & 15;
        f32x4 qa0 = *(const f32x4*)(qs + l16 * 8), qa1 = *(const f32x4*)(qs + l16 * 8 + 4);
        float d[2];
#pragma unroll
        for (int i = 0; i < 2; ++i) {
            d[i] = qa0[0] * bflo(wkv[i][0]) + qa0[1] * bfhi(wkv[i][0]) + qa0[2] * bflo(wkv[i][1]) + qa0[3] * bfhi(wkv[i][1]);
            d[i] += qa1[0] * bflo(wkv[i][2]) + qa1[1] * bfhi(wkv[i][2]) + qa1[2] * bflo(wkv[i][3]) + qa1[3] * bfhi(wkv[i][3]);
        }
        bool up = lane & 1;
        float send = up ? d[0] : d[1], keep = up ? d[1] : d[0];
        float v = keep + __shfl_xor(send, 1);
        v += __shfl_xor(v, 2);
        v += __shfl_xor(v, 4);
        v += __shfl_xor(v, 8);
        v *= 0.08838834764831845f;
        const int k = lane & 3;
        const int ca = 2 * k, cb = 2 * k + 1;
        float va = __shfl(v, (ca & 3) * 16 + (ca >> 2));
        float vb = __shfl(v, (cb & 3) * 16 + (cb >> 2));
        if (lane < 32)
            __builtin_amdgcn_raw_buffer_store_b32(pk2(va, vb), rs, WS_QABS + (((lane >> 2) * 8 + j) * 32 + h) * 128 + 16 * w + 4 * k, 0, SC1);
    }
#pragma unroll
    for (int i = 0; i < 4; ++i) *(u32x4*)(tile + (i * 128 + (t >> 2)) * 64 + (t & 3) * 16) = cv[i];
    lds_wait(vflg + 2, 1);
    STAMP(3);
    {
        u32x4 av[4];
        const int qo = WS_QABS + (((b & 7) * 8 + w) * 32 + r) * 128 + 64 * h2;
        for (unsigned spins = 0; spins < SPIN_CAP; ++spins) {
            asm volatile("" ::: "memory");
#pragma unroll
            for (int s = 0; s < 4; ++s) av[s] = __builtin_amdgcn_raw_buffer_load_b128(rs, qo + 16 * s, 0, SC1);
            unsigned m = umax(umax(max4(av[0]), max4(av[1])), umax(max4(av[2]), max4(av[3])));
            if (__all(m != POISON)) break;
            __builtin_amdgcn_s_sleep(1);
        }
        STAMP(4);
        f32x16 acc;
#pragma unroll
        for (int i = 0; i < 16; ++i) acc[i] = 0.f;
#pragma unroll
        for (int s = 0; s < 4; ++s)
            acc = __builtin_amdgcn_mfma_f32_32x32x16_bf16(__builtin_bit_cast(bf16x8, av[s]), bq[s], acc, 0, 0, 0);
#pragma unroll
        for (int i = 0; i < 16; ++i) {
            int head = (i & 3) + 8 * (i >> 2) + 4 * h2;
            red1[(w * 32 + head) * 32 + r] = acc[i];
        }
    }
    bar();
    {
        const int head = t >> 4, kq = (t & 15) * 2;
        f32x2 s2 = *(const f32x2*)(red1 + head * 32 + kq);
#pragma unroll
        for (int ww = 1; ww < 8; ++ww) s2 += *(const f32x2*)(red1 + (ww * 32 + head) * 32 + kq);
        float m = fmaxf(s2[0], s2[1]);
        m = fmaxf(m, __shfl_xor(m, 1));
        m = fmaxf(m, __shfl_xor(m, 2));
        m = fmaxf(m, __shfl_xor(m, 4));
        m = fmaxf(m, __shfl_xor(m, 8));
        unsigned p01 = pk2(__expf(s2[0] - m), __expf(s2[1] - m));
        float l = bflo(p01) + bfhi(p01);
        l += __shfl_xor(l, 1);
        l += __shfl_xor(l, 2);
        l += __shfl_xor(l, 4);
        l += __shfl_xor(l, 8);
        __builtin_amdgcn_raw_buffer_store_b32(p01, rs, WS_P + ((kc * 32 + head) * 512 + mem * 32 + kq) * 2, 0, SC1);
        if ((t & 15) == 0) {
            u32x2 ml = {asu(m), asu(l)};
            __builtin_amdgcn_raw_buffer_store_b64(ml, rs, WS_MLOC + ((kc * 32 + head) * 16 + mem) * 8, 0, SC1);
        }
    }
    STAMP(5);
    {
        u32x4 pv[4], mlv;
        const int po = WS_P + ((kc * 32 + r) * 512 + 64 * w + 8 * h2) * 2;
        const int mo = WS_MLOC + ((kc * 32 + r) * 16 + 2 * w) * 8;
        for (unsigned spins = 0; spins < SPIN_CAP; ++spins) {
            asm volatile("" ::: "memory");
#pragma unroll
            for (int s = 0; s < 4; ++s) pv[s] = __builtin_amdgcn_raw_buffer_load_b128(rs, po + 32 * s, 0, SC1);
            mlv = __builtin_amdgcn_raw_buffer_load_b128(rs, mo, 0, SC1);
            unsigned m = umax(umax(max4(pv[0]), max4(pv[1])), umax(max4(pv[2]), max4(pv[3])));
            m = umax(m, max4(mlv));
            if (__all(m != POISON)) break;
            __builtin_amdgcn_s_sleep(1);
        }
        STAMP(6);
        {
            float m0 = asf(mlv[0]), l0 = asf(mlv[1]), m1 = asf(mlv[2]), l1 = asf(mlv[3]);
            float Mw = fmaxf(m0, m1);
            float f0_ = __expf(m0 - Mw), f1_ = __expf(m1 - Mw);
            if (lane < 32) {
                fac[(2 * w) * 32 + r] = f0_; fac[(2 * w + 1) * 32 + r] = f1_;
                Mws[r * 8 + w] = Mw; Lws[r * 8 + w] = f0_ * l0 + f1_ * l1;
            }
        }
        const int g = lane >> 4;
        const int q = (lane & 15) >> 2, p = lane & 3;
        const int cb = g & 1;
        f32x16 tot;
#pragma unroll
        for (int mm = 0; mm < 2; ++mm) {
            f32x16 acc;
#pragma unroll
            for (int i = 0; i < 16; ++i) acc[i] = 0.f;
#pragma unroll
            for (int ss = 0; ss < 2; ++ss) {
                int s = mm * 2 + ss;
                int row = w * 64 + 16 * s + 8 * h2 + q;
                s16x4 t0 = __builtin_amdgcn_ds_read_tr16_b64_v4i16(
                    (__attribute__((address_space(3))) s16x4*)(tile + row * 64 + cb * 32 + p * 8));
                s16x4 t1 = __builtin_amdgcn_ds_read_tr16_b64_v4i16(
                    (__attribute__((address_space(3))) s16x4*)(tile + (row + 4) * 64 + cb * 32 + p * 8));
                s16x8 bb = {t0[0], t0[1], t0[2], t0[3], t1[0], t1[1], t1[2], t1[3]};
                acc = __builtin_amdgcn_mfma_f32_32x32x16_bf16(__builtin_bit_cast(bf16x8, pv[s]), __builtin_bit_cast(bf16x8, bb), acc, 0, 0, 0);
            }
            const float* fp = fac + (2 * w + mm) * 32 + 4 * h2;
#pragma unroll
            for (int qd = 0; qd < 4; ++qd) {
                f32x4 f4 = *(const f32x4*)(fp + 8 * qd);
#pragma unroll
                for (int e = 0; e < 4; ++e) {
                    if (mm == 0) tot[qd * 4 + e] = f4[e] * acc[qd * 4 + e];
                    else tot[qd * 4 + e] += f4[e] * acc[qd * 4 + e];
                }
            }
        }
#pragma unroll
        for (int i = 0; i < 16; ++i) {
            int head = (i & 3) + 8 * (i >> 2) + 4 * h2;
            red2[(w * 32 + head) * 32 + r] = tot[i];
        }
    }
    bar();
    {
        const int head = t >> 4, cq = (t & 15) * 2;
        f32x4 ma = *(const f32x4*)(Mws + head * 8), mb = *(const f32x4*)(Mws + head * 8 + 4);
        float M = fmaxf(fmaxf(fmaxf(ma[0], ma[1]), fmaxf(ma[2], ma[3])), fmaxf(fmaxf(mb[0], mb[1]), fmaxf(mb[2], mb[3])));
        float ew[8];
#pragma unroll
        for (int ww = 0; ww < 4; ++ww) { ew[ww] = __expf(ma[ww] - M); ew[ww + 4] = __expf(mb[ww] - M); }
        f32x2 s2 = {0.f, 0.f};
#pragma unroll
        for (int ww = 0; ww < 8; ++ww) s2 += ew[ww] * *(const f32x2*)(red2 + (ww * 32 + head) * 32 + cq);
        u32x2 o = {asu(s2[0]), asu(s2[1])};
        __builtin_amdgcn_raw_buffer_store_b64(o, rs, WS_OPART + ((kc * 32 + head) * 512 + mem * 32 + cq) * 4, 0, SC1);
        if (mem == 0 && (t & 15) == 0) {
            f32x4 la = *(const f32x4*)(Lws + head * 8), lb = *(const f32x4*)(Lws + head * 8 + 4);
            float L = 0.f;
#pragma unroll
            for (int ww = 0; ww < 4; ++ww) L += ew[ww] * la[ww] + ew[ww + 4] * lb[ww];
            u32x2 ml = {asu(M), asu(L)};
            __builtin_amdgcn_raw_buffer_store_b64(ml, rs, WS_ML + (head * 16 + kc) * 8, 0, SC1);
        }
    }
    STAMP(7);
    u32x4 uvv[2], ovv[2];
    const float xt = bf2f(x[t]);
    {
        u32x4 gv = {0u, 0u, 0u, 0u}, uv_ = {0u, 0u, 0u, 0u}, dv = {0u, 0u, 0u, 0u};
        if (has_ffn) {
            gv = *(const u32x4*)(wg + (size_t)t * FF + f0);
            uv_ = *(const u32x4*)(wu + (size_t)t * FF + f0);
            dv = *(const u32x4*)(wd + (size_t)(f0 + w) * 512 + lane * 8);
        }
#pragma unroll
        for (int i = 0; i < 2; ++i)
            uvv[i] = *(const u32x4*)(wuv + (size_t)((t >> 1) + 256 * i) * 4096 + h * 128 + j * 16 + (t & 1) * 8);
#pragma unroll
        for (int i = 0; i < 2; ++i)
            ovv[i] = *(const u32x4*)(wo + ((size_t)h * 128 + j * 16 + w * 2 + i) * 512 + lane * 8);
        float p[16];
#pragma unroll
        for (int jj = 0; jj < 4; ++jj) {
            p[2 * jj] = xt * bflo(gv[jj]); p[2 * jj + 1] = xt * bfhi(gv[jj]);
            p[8 + 2 * jj] = xt * bflo(uv_[jj]); p[8 + 2 * jj + 1] = xt * bfhi(uv_[jj]);
        }
        bool up = lane & 1;
        float s8[8];
#pragma unroll
        for (int k = 0; k < 8; ++k) { float send = up ? p[k] : p[k + 8]; float keep = up ? p[k + 8] : p[k]; s8[k] = keep + __shfl_xor(send, 1); }
        up = lane & 2;
        float s4[4];
#pragma unroll
        for (int k = 0; k < 4; ++k) { float send = up ? s8[k] : s8[k + 4]; float keep = up ? s8[k + 4] : s8[k]; s4[k] = keep + __shfl_xor(send, 2); }
        up = lane & 4;
        float s2[2];
#pragma unroll
        for (int k = 0; k < 2; ++k) { float send = up ? s4[k] : s4[k + 2]; float keep = up ? s4[k + 2] : s4[k]; s2[k] = keep + __shfl_xor(send, 4); }
        up = lane & 8;
        float send = up ? s2[0] : s2[1]; float keep = up ? s2[1] : s2[0];
        float v = keep + __shfl_xor(send, 8);
        v += __shfl_xor(v, 16);
        v += __shfl_xor(v, 32);
        if (lane < 16) {
            int idx = (lane & 1) * 8 + ((lane >> 1) & 1) * 4 + ((lane >> 2) & 1) * 2 + ((lane >> 3) & 1);
            sm2[w * 16 + idx] = v;
        }
        bar();
        float gs = 0.f, us = 0.f;
#pragma unroll
        for (int ww = 0; ww < 8; ++ww) { gs += sm2[ww * 16 + w]; us += sm2[ww * 16 + 8 + w]; }
        gs = rbf(gs); us = rbf(us);
        float sg = rbf(gs / (1.f + __expf(-gs)));
        float hv = rbf(sg * us);
        f32x4 o0, o1;
        o0[0] = hv * bflo(dv[0]); o0[1] = hv * bfhi(dv[0]); o0[2] = hv * bflo(dv[1]); o0[3] = hv * bfhi(dv[1]);
        o1[0] = hv * bflo(dv[2]); o1[1] = hv * bfhi(dv[2]); o1[2] = hv * bflo(dv[3]); o1[3] = hv * bfhi(dv[3]);
        *(f32x4*)(redf + w * 512 + lane * 8) = o0;
        *(f32x4*)(redf + w * 512 + lane * 8 + 4) = o1;
        bar();
        if (has_ffn) {
            float s = 0.f;
#pragma unroll
            for (int ww = 0; ww < 8; ++ww) s += redf[ww * 512 + t];
            __builtin_amdgcn_raw_buffer_store_b32(asu(s), rs, WS_GB + fch * 2048 + t * 4, 0, SC1);
        }
    }
    STAMP(8);
    {
        const int qd = t >> 7;
        u32x4 ov[4];
        u32x2 ml;
        const int oo = WS_OPART + ((qd * 4 * 32 + h) * 512 + (t & 127) * 4) * 4;
        for (unsigned spins = 0; spins < SPIN_CAP; ++spins) {
            asm volatile("" ::: "memory");
#pragma unroll
            for (int i = 0; i < 4; ++i) ov[i] = __builtin_amdgcn_raw_buffer_load_b128(rs, oo + i * 65536, 0, SC1);
            ml = __builtin_amdgcn_raw_buffer_load_b64(rs, WS_ML + (h * 16 + (lane & 15)) * 8, 0, SC1);
            unsigned m = umax(umax(max4(ov[0]), max4(ov[1])), umax(max4(ov[2]), max4(ov[3])));
            m = umax(m, umax(ml[0], ml[1]));
            if (__all(m != POISON)) break;
            __builtin_amdgcn_s_sleep(1);
        }
        STAMP(9);
        float mk = asf(ml[0]), lk = asf(ml[1]);
        float M = mk;
        M = fmaxf(M, __shfl_xor(M, 1)); M = fmaxf(M, __shfl_xor(M, 2));
        M = fmaxf(M, __shfl_xor(M, 4)); M = fmaxf(M, __shfl_xor(M, 8));
        float wk = __expf(mk - M);
        float L = wk * lk;
        L += __shfl_xor(L, 1); L += __shfl_xor(L, 2); L += __shfl_xor(L, 4); L += __shfl_xor(L, 8);
        wk = wk / L;
        f32x4 c4 = {0.f, 0.f, 0.f, 0.f};
#pragma unroll
        for (int i = 0; i < 4; ++i) {
            float wi = __shfl(wk, qd * 4 + i);
            c4[0] += wi * asf(ov[i][0]); c4[1] += wi * asf(ov[i][1]); c4[2] += wi * asf(ov[i][2]); c4[3] += wi * asf(ov[i][3]);
        }
        *(f32x4*)(cpart + qd * 512 + (t & 127) * 4) = c4;
    }
    bar();
    {
        const int row = t >> 1;
        float c0 = (cpart[row] + cpart[512 + row]) + (cpart[1024 + row] + cpart[1536 + row]);
        float c1 = (cpart[row + 256] + cpart[768 + row]) + (cpart[1280 + row] + cpart[1792 + row]);
        float acc[8];
#pragma unroll
        for (int jj = 0; jj < 4; ++jj) {
            acc[2 * jj] = c0 * bflo(uvv[0][jj]) + c1 * bflo(uvv[1][jj]);
            acc[2 * jj + 1] = c0 * bfhi(uvv[0][jj]) + c1 * bfhi(uvv[1][jj]);
        }
        bool up = lane & 2;
        float s[4];
#pragma unroll
        for (int k = 0; k < 4; ++k) { float send = up ? acc[k] : acc[k + 4]; float keep = up ? acc[k + 4] : acc[k]; s[k] = keep + __shfl_xor(send, 2); }
        up = lane & 4;
        float s2[2];
#pragma unroll
        for (int k = 0; k < 2; ++k) { float send = up ? s[k] : s[k + 2]; float keep = up ? s[k + 2] : s[k]; s2[k] = keep + __shfl_xor(send, 4); }
        up = lane & 8;
        float send = up ? s2[0] : s2[1]; float keep = up ? s2[1] : s2[0];
        float v = keep + __shfl_xor(send, 8);
        v += __shfl_xor(v, 16);
        v += __shfl_xor(v, 32);
        if (lane < 16) {
            int d = (lane & 1) * 8 + ((lane >> 1) & 1) * 4 + ((lane >> 2) & 1) * 2 + ((lane >> 3) & 1);
            sm3[w * 16 + d] = v;
        }
    }
    bar();
    {
        float c0 = 0.f, c1 = 0.f;
#pragma unroll
        for (int ww = 0; ww < 8; ++ww) { c0 += sm3[ww * 16 + 2 * w]; c1 += sm3[ww * 16 + 2 * w + 1]; }
        c0 = rbf(c0); c1 = rbf(c1);
        f32x4 o0, o1;
        o0[0] = c0 * bflo(ovv[0][0]) + c1 * bflo(ovv[1][0]); o0[1] = c0 * bfhi(ovv[0][0]) + c1 * bfhi(ovv[1][0]);
        o0[2] = c0 * bflo(ovv[0][1]) + c1 * bflo(ovv[1][1]); o0[3] = c0 * bfhi(ovv[0][1]) + c1 * bfhi(ovv[1][1]);
        o1[0] = c0 * bflo(ovv[0][2]) + c1 * bflo(ovv[1][2]); o1[1] = c0 * bfhi(ovv[0][2]) + c1 * bfhi(ovv[1][2]);
        o1[2] = c0 * bflo(ovv[0][3]) + c1 * bflo(ovv[1][3]); o1[3] = c0 * bfhi(ovv[0][3]) + c1 * bfhi(ovv[1][3]);
        *(f32x4*)(redo + w * 512 + lane * 8) = o0;
        *(f32x4*)(redo + w * 512 + lane * 8 + 4) = o1;
    }
    bar();
    {
        float s = 0.f;
#pragma unroll
        for (int ww = 0; ww < 8; ++ww) s += redo[ww * 512 + t];
        __builtin_amdgcn_raw_buffer_store_b32(asu(s), rs, WS_GA + b * 2048 + t * 4, 0, SC1);
    }
    STAMP(10);
    if (cons) {
        const int k = b - 224;
        const int p = t >> 1, hf = t & 1;
        float gp = rbf(xt * bf2f(gw[t]));
        const int ao = WS_GA + p * 2048 + k * 64 + hf * 32;
        const int bo = WS_GB + (p < 172 ? p : 171) * 2048 + k * 64 + hf * 32;
        u32x4 a0, a1, b0, b1;
        for (unsigned spins = 0; spins < SPIN_CAP; ++spins) {
            asm volatile("" ::: "memory");
            a0 = __builtin_amdgcn_raw_buffer_load_b128(rs, ao, 0, SC1);
            a1 = __builtin_amdgcn_raw_buffer_load_b128(rs, ao + 16, 0, SC1);
            b0 = __builtin_amdgcn_raw_buffer_load_b128(rs, bo, 0, SC1);
            b1 = __builtin_amdgcn_raw_buffer_load_b128(rs, bo + 16, 0, SC1);
            unsigned m = umax(umax(max4(a0), max4(a1)), umax(max4(b0), max4(b1)));
            if (__all(m != POISON)) break;
            __builtin_amdgcn_s_sleep(1);
        }
        STAMP(11);
        float v[16];
        const float bz = p < 172 ? 1.f : 0.f;
#pragma unroll
        for (int i = 0; i < 4; ++i) {
            v[i] = asf(a0[i]); v[4 + i] = asf(a1[i]);
            v[8 + i] = p < 172 ? asf(b0[i]) : 0.f; v[12 + i] = p < 172 ? asf(b1[i]) : 0.f;
        }
        (void)bz;
        bool up = lane & 2;
        float s8[8];
#pragma unroll
        for (int q = 0; q < 8; ++q) { float send = up ? v[q] : v[q + 8]; float keep = up ? v[q + 8] : v[q]; s8[q] = keep + __shfl_xor(send, 2); }
        up = lane & 4;
        float s4[4];
#pragma unroll
        for (int q = 0; q < 4; ++q) { float send = up ? s8[q] : s8[q + 4]; float keep = up ? s8[q + 4] : s8[q]; s4[q] = keep + __shfl_xor(send, 4); }
        up = lane & 8;
        float s2[2];
#pragma unroll
        for (int q = 0; q < 2; ++q) { float send = up ? s4[q] : s4[q + 2]; float keep = up ? s4[q + 2] : s4[q]; s2[q] = keep + __shfl_xor(send, 8); }
        up = lane & 16;
        float send = up ? s2[0] : s2[1]; float keep = up ? s2[1] : s2[0];
        float vv = keep + __shfl_xor(send, 16);
        vv += __shfl_xor(vv, 32);
#pragma unroll
        for (int o = 32; o >= 1; o >>= 1) gp += __shfl_xor(gp, o);
        if (lane < 32) {
            int idx = ((lane >> 1) & 1) * 16 + (lane & 1) * 8 + ((lane >> 2) & 1) * 4 + ((lane >> 3) & 1) * 2 + ((lane >> 4) & 1);
            fin[w * 32 + idx] = vv;
        }
        if (lane == 0) fing[w] = gp;
    }
    bar();
    if (cons && t < 16) {
        float a = 0.f, bb = 0.f, gs = 0.f;
#pragma unroll
        for (int ww = 0; ww < 8; ++ww) { a += fin[ww * 32 + t]; bb += fin[ww * 32 + 16 + t]; gs += fing[ww]; }
        gs = rbf(gs);
        float g = rbf(1.f / (1.f + __expf(-gs)));
        a = rbf(a); bb = rbf(bb);
        float rr = rbf(g * a) + rbf(rbf(1.f - g) * bb);
        out[(b - 224) * 16 + t] = f2bf(rr);
    }
    STAMP(12);
}

extern "C" void kernel_launch(void* const* d_in, const int* in_sizes, int n_in,
                              void* d_out, int out_size, void* d_ws, size_t ws_size,
                              hipStream_t stream) {
    const u16* x = (const u16*)d_in[0];
    const u16* ckv = (const u16*)d_in[1];
    const u16* wq = (const u16*)d_in[2];
    const u16* wuk = (const u16*)d_in[3];
    const u16* wuv = (const u16*)d_in[4];
    const u16* wo = (const u16*)d_in[5];
    const u16* wg = (const u16*)d_in[7];
    const u16* wu = (const u16*)d_in[8];
    const u16* wd = (const u16*)d_in[9];
    const u16* gw = (const u16*)d_in[10];
    mega<<<256, 640, 0, stream>>>(x, ckv, wq, wuk, wuv, wo, wg, wu, wd, gw, (char*)d_ws, (u16*)d_out);
}
```

```cpp
#include <hip/hip_runtime.h>
#include <hip/hip_bf16.h>
#include <stdint.h>

#define SK 8192
#define FF 1376

typedef unsigned short u16;
typedef unsigned long long u64;
typedef __attribute__((ext_vector_type(8))) __bf16 bf16x8;
typedef __attribute__((ext_vector_type(2))) __bf16 bf16x2;
typedef __attribute__((ext_vector_type(4))) short s16x4;
typedef __attribute__((ext_vector_type(8))) short s16x8;
typedef __attribute__((ext_vector_type(16))) float f32x16;
typedef __attribute__((ext_vector_type(4))) float f32x4;
typedef __attribute__((ext_vector_type(2))) float f32x2;
typedef __attribute__((ext_vector_type(4))) unsigned int u32x4;
typedef __attribute__((ext_vector_type(2))) unsigned int u32x2;
#define GAS __attribute__((address_space(1)))
typedef GAS unsigned gu32;
typedef GAS u64 gu64;
#define RLX_AGENT __ATOMIC_RELAXED, __HIP_MEMORY_SCOPE_AGENT
#define SC1 16
#define POISON 0xFFFFFFFFu

__device__ __forceinline__ float bf2f(u16 v) { return __uint_as_float(((unsigned)v) << 16); }
__device__ __forceinline__ float bflo(unsigned v) { return __uint_as_float(v << 16); }
__device__ __forceinline__ float bfhi(unsigned v) { return __uint_as_float(v & 0xffff0000u); }
__device__ __forceinline__ float asf(unsigned v) { return __uint_as_float(v); }
__device__ __forceinline__ unsigned asu(float v) { return __float_as_uint(v); }
__device__ __forceinline__ unsigned pk2(float a, float b) {
    bf16x2 v = {(__bf16)a, (__bf16)b};
    return __builtin_bit_cast(unsigned, v);
}
__device__ __forceinline__ u16 f2bf(float f) { __bf16 v = (__bf16)f; return __builtin_bit_cast(u16, v); }
__device__ __forceinline__ float rbf(float f) { return bf2f(f2bf(f)); }
__device__ __forceinline__ unsigned umax(unsigned a, unsigned b) { return a > b ? a : b; }
__device__ __forceinline__ unsigned max4(u32x4 v) { return umax(umax(v[0], v[1]), umax(v[2], v[3])); }

#define NREP 32
#define WS_EPOCH   0
#define WS_F0      256
#define WS_QG      (WS_F0 + NREP * 2048)
#define WS_QABS    (WS_QG + 256 * 1024)
#define WS_P       (WS_QABS + 8 * 32768)
#define WS_MLOC    (WS_P + 16 * 32 * 512 * 2)
#define WS_OPART   (WS_MLOC + 16 * 32 * 16 * 8)
#define WS_ML      (WS_OPART + 16 * 32 * 512 * 4)
#define WS_GA      (WS_ML + 32 * 16 * 8)
#define WS_GB      (WS_GA + 256 * 2048)
#define WS_END     (WS_GB + 172 * 2048)

#define SPIN_CAP (1u << 17)
#define STAMP(i)
#define NBAR 8

__device__ __forceinline__ void lgkm0() { asm volatile("s_waitcnt lgkmcnt(0)" ::: "memory"); }
__device__ __forceinline__ void vm0() { asm volatile("s_waitcnt vmcnt(0)" ::: "memory"); }
__device__ __forceinline__ void bar() { asm volatile("s_waitcnt lgkmcnt(0)\n\ts_barrier" ::: "memory"); }
typedef volatile __attribute__((address_space(3))) unsigned lds_u32;
__device__ __forceinline__ void lds_wait(lds_u32* p, unsigned target) {
    for (unsigned s = 0; s < SPIN_CAP; ++s) {
        if (*p >= target) break;
        __builtin_amdgcn_s_sleep(1);
    }
    asm volatile("" ::: "memory");
}

__global__ __launch_bounds__(640, 1) void mega(const u16* __restrict__ x, const u16* __restrict__ ckv,
                                               const u16* __restrict__ wq, const u16* __restrict__ wuk,
                                               const u16* __restrict__ wuv, const u16* __restrict__ wo,
                                               const u16* __restrict__ wg, const u16* __restrict__ wu,
                                               const u16* __restrict__ wd, const u16* __restrict__ gw,
                                               char* __restrict__ ws, u16* __restrict__ out) {
    __shared__ __attribute__((aligned(16))) char tile[512 * 64];
    __shared__ __attribute__((aligned(16))) float red1[8 * 1024];
    __shared__ __attribute__((aligned(16))) float red2[8 * 1024];
    __shared__ __attribute__((aligned(16))) float redf[8 * 512];
    __shared__ __attribute__((aligned(16))) float redo[8 * 512];
    __shared__ __attribute__((aligned(16))) float sm1[8 * 128];
    __shared__ __attribute__((aligned(16))) float sm2[8 * 16];
    __shared__ __attribute__((aligned(16))) float sm3[8 * 16];
    __shared__ __attribute__((aligned(16))) float qs[128];
    __shared__ __attribute__((aligned(16))) float fac[16 * 32];
    __shared__ __attribute__((aligned(16))) float Mws[32 * 8];
    __shared__ __attribute__((aligned(16))) float Lws[32 * 8];
    __shared__ __attribute__((aligned(16))) float cpart[4 * 512];
    __shared__ __attribute__((aligned(16))) float fin[8 * 32];
    __shared__ __attribute__((aligned(16))) float fing[8];
    __shared__ unsigned flg[4];

    const int b = blockIdx.x;
    const int t = threadIdx.x;
    const int lane = t & 63, w = t >> 6;
    const int h = b & 31, j = b >> 5;
    const int kc = b & 15, mem = b >> 4;
    const int key0 = kc * 512 + mem * 32;
    const int r = lane & 31, h2 = lane >> 5;
    const int fidx = b >> 3;
    const int fch = (b & 7) * 22 + fidx;
    const bool has_ffn = (fidx < 22) && (fch < 172);
    const int f0 = fch * 8;
    const bool cons = b >= 224;
    const __amdgpu_buffer_rsrc_t rs = __builtin_amdgcn_make_buffer_rsrc(ws, 0, WS_END, 0x00020000);
    lds_u32* vflg = (lds_u32*)flg;
    STAMP(0);

    if (w >= 8) {
        const unsigned e = __hip_atomic_load((gu32*)(ws + WS_EPOCH), RLX_AGENT);
        if (w == 9) {
            const u32x4 P4 = {POISON, POISON, POISON, POISON};
            const u32x2 P2 = {POISON, POISON};
            __builtin_amdgcn_raw_buffer_store_b128(P4, rs, WS_QABS + (lane >> 3) * 32768 + ((j * 8 + (lane & 7)) * 32 + h) * 16, 0, SC1);
#pragma unroll
            for (int i = 0; i < 2; ++i)
                __builtin_amdgcn_raw_buffer_store_b128(P4, rs, WS_P + ((kc * 32 + mem * 2 + i) * 64 + lane) * 16, 0, SC1);
            if (lane < 32) __builtin_amdgcn_raw_buffer_store_b64(P2, rs, WS_MLOC + ((kc * 8 + (mem >> 1)) * 32 + lane) * 16 + (mem & 1) * 8, 0, SC1);
#pragma unroll
            for (int i = 0; i < 4; ++i)
                __builtin_amdgcn_raw_buffer_store_b128(P4, rs, WS_OPART + ((kc * 32 + i * 8 + (lane >> 3)) * 512 + mem * 32) * 4 + (lane & 7) * 16, 0, SC1);
            if (mem == 0 && lane < 32) __builtin_amdgcn_raw_buffer_store_b64(P2, rs, WS_ML + (lane * 16 + kc) * 8, 0, SC1);
#pragma unroll
            for (int i = 0; i < 2; ++i)
                __builtin_amdgcn_raw_buffer_store_b128(P4, rs, WS_GA + b * 2048 + i * 1024 + lane * 16, 0, SC1);
            if (has_ffn) {
#pragma unroll
                for (int i = 0; i < 2; ++i)
                    __builtin_amdgcn_raw_buffer_store_b128(P4, rs, WS_GB + fch * 2048 + i * 1024 + lane * 16, 0, SC1);
            }
        }
        bar();
        const unsigned tag = e + 1u;
        if (w == 8) {
            lds_wait(vflg + 0, 8);
            STAMP(1);
            f32x2 s = *(const f32x2*)(sm1 + 2 * lane);
#pragma unroll
            for (int ww = 1; ww < 8; ++ww) s += *(const f32x2*)(sm1 + ww * 128 + 2 * lane);
            u32x4 g = {tag, asu(s[0]), asu(s[1]), ~tag};
            __builtin_amdgcn_raw_buffer_store_b128(g, rs, WS_QG + ((h * 8 + j) * 64 + lane) * 16, 0, SC1);
            for (unsigned spins = 0; spins < SPIN_CAP; ++spins) {
            asm volatile("" ::: "memory");
                u32x4 v[8];
#pragma unroll
                for (int jj = 0; jj < 8; ++jj)
                    v[jj] = __builtin_amdgcn_raw_buffer_load_b128(rs, WS_QG + ((h * 8 + jj) * 64 + lane) * 16, 0, SC1);
                unsigned bad = 0;
                float q0 = 0.f, q1 = 0.f;
#pragma unroll
                for (int jj = 0; jj < 8; ++jj) {
                    bad |= (v[jj][0] ^ tag) | (v[jj][3] ^ ~tag);
                    q0 += asf(v[jj][1]); q1 += asf(v[jj][2]);
                }
                if (__all(bad == 0) || spins == SPIN_CAP - 1) {
                    f32x2 qq = {rbf(q0), rbf(q1)};
                    *(f32x2*)(qs + 2 * lane) = qq;
                    break;
                }
                __builtin_amdgcn_s_sleep(1);
            }
            lgkm0();
            if (lane == 0) vflg[1] = 1u;
            STAMP(2);
        } else {
            vm0();
            const u64 want = ((u64)(~tag) << 32) | tag;
            if (lane < NREP) __hip_atomic_store((gu64*)(ws + WS_F0 + lane * 2048 + b * 8), want, RLX_AGENT);
            STAMP(1);
            const int fo = WS_F0 + (b & (NREP - 1)) * 2048 + lane * 16;
            for (unsigned spins = 0; spins < SPIN_CAP; ++spins) {
            asm volatile("" ::: "memory");
                u32x4 a0 = __builtin_amdgcn_raw_buffer_load_b128(rs, fo, 0, SC1);
                u32x4 a1 = __builtin_amdgcn_raw_buffer_load_b128(rs, fo + 1024, 0, SC1);
                unsigned bad = (a0[0] ^ tag) | (a0[1] ^ ~tag) | (a0[2] ^ tag) | (a0[3] ^ ~tag) |
                               (a1[0] ^ tag) | (a1[1] ^ ~tag) | (a1[2] ^ tag) | (a1[3] ^ ~tag);
                if (__all(bad == 0)) break;
                __builtin_amdgcn_s_sleep(1);
            }
            if (lane == 0) vflg[2] = 1u;
            STAMP(2);
            unsigned pf[14];
            {
                const int rr = lane >> 2, sg = lane & 3;
                const int colb = (b & 7) * 352 + sg * 64;
                const char* pg = (const char*)wg + (size_t)((b >> 3) * 16 + rr) * (FF * 2);
                const char* pu = (const char*)wu + (size_t)((b >> 3) * 16 + rr) * (FF * 2);
                int c0 = colb, c1 = colb + 256 - (sg < 2 ? 0 : 256);
                if (c0 > FF * 2 - 4) c0 = FF * 2 - 4;
                if (c1 > FF * 2 - 4) c1 = FF * 2 - 4;
                pf[0] = *(const unsigned*)(pg + c0); pf[1] = *(const unsigned*)(pu + c0);
                pf[2] = *(const unsigned*)(pg + c1); pf[3] = *(const unsigned*)(pu + c1);
                const int fr = has_ffn ? f0 : 0;
#pragma unroll
                for (int i = 0; i < 2; ++i) pf[4 + i] = *(const unsigned*)((const char*)wd + (size_t)fr * 1024 + i * 4096 + lane * 64);
#pragma unroll
                for (int i = 0; i < 4; ++i)
                    pf[6 + i] = *(const unsigned*)((const char*)wuv + (size_t)(j * 64 + i * 16 + (lane >> 2)) * 8192 + h * 256 + (lane & 3) * 64);
#pragma unroll
                for (int i = 0; i < 4; ++i)
                    pf[10 + i] = *(const unsigned*)((const char*)wo + ((size_t)h * 128 + j * 16) * 1024 + i * 4096 + lane * 64);
            }
#pragma unroll
            for (int i = 0; i < NBAR; ++i) asm volatile("s_barrier" ::: "memory");
            asm volatile("" :: "v"(pf[0]), "v"(pf[1]), "v"(pf[2]), "v"(pf[3]), "v"(pf[4]), "v"(pf[5]), "v"(pf[6]),
                         "v"(pf[7]), "v"(pf[8]), "v"(pf[9]), "v"(pf[10]), "v"(pf[11]), "v"(pf[12]), "v"(pf[13]));
            return;
        }
#pragma unroll
        for (int i = 0; i < NBAR; ++i) asm volatile("s_barrier" ::: "memory");
        if (b == 224 && lane == 0) __hip_atomic_store((gu32*)(ws + WS_EPOCH), tag, RLX_AGENT);
        return;
    }

    u32x4 wqv[2], wkv[2];
    float x0, x1;
    {
        const int row = j * 64 + (t >> 4);
        x0 = bf2f(x[row]); x1 = bf2f(x[row + 32]);
#pragma unroll
        for (int i = 0; i < 2; ++i)
            wqv[i] = *(const u32x4*)(wq + (size_t)(row + 32 * i) * 4096 + h * 128 + (t & 15) * 8);
#pragma unroll
        for (int i = 0; i < 2; ++i)
            wkv[i] = *(const u32x4*)(wuk + (size_t)(j * 64 + 8 * w + (lane >> 4) + 4 * i) * 4096 + h * 128 + (lane & 15) * 8);
    }
    if (t < 4) flg[t] = 0u;
    bar();
    __builtin_amdgcn_sched_barrier(0);
    asm volatile("s_waitcnt vmcnt(2)" ::: "memory");
    STAMP(1);
    bf16x8 bq[4];
    u32x4 cv[4];
    {
        const u16* bp = ckv + (size_t)(key0 + r) * 512 + 64 * w + 8 * h2;
#pragma unroll
        for (int s = 0; s < 4; ++s) bq[s] = *(const bf16x8*)(bp + 16 * s);
        const u16* cp = ckv + (size_t)(kc * 512 + (t >> 2)) * 512 + mem * 32 + (t & 3) * 8;
#pragma unroll
        for (int i = 0; i < 4; ++i) cv[i] = *(const u32x4*)(cp + (size_t)i * 128 * 512);
    }
    __builtin_amdgcn_sched_barrier(0);
    {
        float acc[8];
#pragma unroll
        for (int jj = 0; jj < 4; ++jj) {
            acc[2 * jj] = x0 * bflo(wqv[0][jj]) + x1 * bflo(wqv[1][jj]);
            acc[2 * jj + 1] = x0 * bfhi(wqv[0][jj]) + x1 * bfhi(wqv[1][jj]);
        }
        bool up = lane & 16;
        float s[4];
#pragma unroll
        for (int k = 0; k < 4; ++k) { float send = up ? acc[k] : acc[k + 4]; float keep = up ? acc[k + 4] : acc[k]; s[k] = keep + __shfl_xor(send, 16); }
        up = lane & 32;
        f32x2 s2;
#pragma unroll
        for (int k = 0; k < 2; ++k) { float send = up ? s[k] : s[k + 2]; float keep = up ? s[k + 2] : s[k]; s2[k] = keep + __shfl_xor(send, 32); }
        *(f32x2*)(sm1 + w * 128 + (lane & 15) * 8 + ((lane >> 4) & 1) * 4 + (lane >> 5) * 2) = s2;
        lgkm0();
        if (lane == 0) __hip_atomic_fetch_add(flg + 0, 1u, __ATOMIC_RELAXED, __HIP_MEMORY_SCOPE_WORKGROUP);
    }
    lds_wait(vflg + 1, 1);
    STAMP(2);
    {
        const int l16 = lane & 15;
        f32x4 qa0 = *(const f32x4*)(qs + l16 * 8), qa1 = *(const f32x4*)(qs + l16 * 8 + 4);
        float d[2];
#pragma unroll
        for (int i = 0; i < 2; ++i) {
            d[i] = qa0[0] * bflo(wkv[i][0]) + qa0[1] * bfhi(wkv[i][0]) + qa0[2] * bflo(wkv[i][1]) + qa0[3] * bfhi(wkv[i][1]);
            d[i] += qa1[0] * bflo(wkv[i][2]) + qa1[1] * bfhi(wkv[i][2]) + qa1[2] * bflo(wkv[i][3]) + qa1[3] * bfhi(wkv[i][3]);
        }
        bool up = lane & 1;
        float send = up ? d[0] : d[1], keep = up ? d[1] : d[0];
        float v = keep + __shfl_xor(send, 1);
        v += __shfl_xor(v, 2);
        v += __shfl_xor(v, 4);
        v += __shfl_xor(v, 8);
        v *= 0.08838834764831845f;
        float vv[8];
#pragma unroll
        for (int k = 0; k < 8; ++k) vv[k] = __shfl(v, (k & 3) * 16 + (k >> 2));
        u32x4 o = {pk2(vv[0], vv[1]), pk2(vv[2], vv[3]), pk2(vv[4], vv[5]), pk2(vv[6], vv[7])};
        if (lane < 8)
            __builtin_amdgcn_raw_buffer_store_b128(o, rs, WS_QABS + lane * 32768 + ((j * 8 + w) * 32 + h) * 16, 0, SC1);
    }
#pragma unroll
    for (int i = 0; i < 4; ++i) *(u32x4*)(tile + (i * 128 + (t >> 2)) * 64 + (t & 3) * 16) = cv[i];
    lds_wait(vflg + 2, 1);
    STAMP(3);
    {
        u32x4 av[4];
        const int qo = WS_QABS + (b & 7) * 32768 + (w * 256 + lane) * 16;
        for (unsigned spins = 0; spins < SPIN_CAP; ++spins) {
            asm volatile("" ::: "memory");
#pragma unroll
            for (int s = 0; s < 4; ++s) av[s] = __builtin_amdgcn_raw_buffer_load_b128(rs, qo + 1024 * s, 0, SC1);
            unsigned m = umax(umax(max4(av[0]), max4(av[1])), umax(max4(av[2]), max4(av[3])));
            if (__all(m != POISON)) break;
            __builtin_amdgcn_s_sleep(1);
        }
        STAMP(4);
        f32x16 acc;
#pragma unroll
        for (int i = 0; i < 16; ++i) acc[i] = 0.f;
#pragma unroll
        for (int s = 0; s < 4; ++s)
            acc = __builtin_amdgcn_mfma_f32_32x32x16_bf16(__builtin_bit_cast(bf16x8, av[s]), bq[s], acc, 0, 0, 0);
#pragma unroll
        for (int i = 0; i < 16; ++i) {
            int head = (i & 3) + 8 * (i >> 2) + 4 * h2;
            red1[(w * 32 + head) * 32 + r] = acc[i];
        }
    }
    bar();
    {
        const int head = t >> 4, kq = (t & 15) * 2;
        f32x2 s2 = *(const f32x2*)(red1 + head * 32 + kq);
#pragma unroll
        for (int ww = 1; ww < 8; ++ww) s2 += *(const f32x2*)(red1 + (ww * 32 + head) * 32 + kq);
        float m = fmaxf(s2[0], s2[1]);
        m = fmaxf(m, __shfl_xor(m, 1));
        m = fmaxf(m, __shfl_xor(m, 2));
        m = fmaxf(m, __shfl_xor(m, 4));
        m = fmaxf(m, __shfl_xor(m, 8));
        unsigned p01 = pk2(__expf(s2[0] - m), __expf(s2[1] - m));
        float l = bflo(p01) + bfhi(p01);
        l += __shfl_xor(l, 1);
        l += __shfl_xor(l, 2);
        l += __shfl_xor(l, 4);
        l += __shfl_xor(l, 8);
        u32x4 pp = {p01, (unsigned)__shfl_down((int)p01, 1), (unsigned)__shfl_down((int)p01, 2), (unsigned)__shfl_down((int)p01, 3)};
        if ((t & 3) == 0)
            __builtin_amdgcn_raw_buffer_store_b128(pp, rs, WS_P + ((kc * 32 + mem * 2) * 64 + ((t & 15) >> 2) * 32 + head) * 16, 0, SC1);
        if ((t & 15) == 0) {
            u32x2 ml = {asu(m), asu(l)};
            __builtin_amdgcn_raw_buffer_store_b64(ml, rs, WS_MLOC + ((kc * 8 + (mem >> 1)) * 32 + head) * 16 + (mem & 1) * 8, 0, SC1);
        }
    }
    STAMP(5);
    {
        u32x4 pv[4], mlv;
        const int po = WS_P + ((kc * 32 + w * 4) * 64 + lane) * 16;
        const int mo = WS_MLOC + ((kc * 8 + w) * 32 + r) * 16;
        for (unsigned spins = 0; spins < SPIN_CAP; ++spins) {
            asm volatile("" ::: "memory");
#pragma unroll
            for (int s = 0; s < 4; ++s) pv[s] = __builtin_amdgcn_raw_buffer_load_b128(rs, po + 1024 * s, 0, SC1);
            mlv = __builtin_amdgcn_raw_buffer_load_b128(rs, mo, 0, SC1);
            unsigned m = umax(umax(max4(pv[0]), max4(pv[1])), umax(max4(pv[2]), max4(pv[3])));
            m = umax(m, max4(mlv));
            if (__all(m != POISON)) break;
            __builtin_amdgcn_s_sleep(1);
        }
        STAMP(6);
        {
            float m0 = asf(mlv[0]), l0 = asf(mlv[1]), m1 = asf(mlv[2]), l1 = asf(mlv[3]);
            float Mw = fmaxf(m0, m1);
            float f0_ = __expf(m0 - Mw), f1_ = __expf(m1 - Mw);
            if (lane < 32) {
                fac[(2 * w) * 32 + r] = f0_; fac[(2 * w + 1) * 32 + r] = f1_;
                Mws[r * 8 + w] = Mw; Lws[r * 8 + w] = f0_ * l0 + f1_ * l1;
            }
        }
        const int g = lane >> 4;
        const int q = (lane & 15) >> 2, p = lane & 3;
        const int cb = g & 1;
        f32x16 tot;
#pragma unroll
        for (int mm = 0; mm < 2; ++mm) {
            f32x16 acc;
#pragma unroll
            for (int i = 0; i < 16; ++i) acc[i] = 0.f;
#pragma unroll
            for (int ss = 0; ss < 2; ++ss) {
                int s = mm * 2 + ss;
                int row = w * 64 + 16 * s + 8 * h2 + q;
                s16x4 t0 = __builtin_amdgcn_ds_read_tr16_b64_v4i16(
                    (__attribute__((address_space(3))) s16x4*)(tile + row * 64 + cb * 32 + p * 8));
                s16x4 t1 = __builtin_amdgcn_ds_read_tr16_b64_v4i16(
                    (__attribute__((address_space(3))) s16x4*)(tile + (row + 4) * 64 + cb * 32 + p * 8));
                s16x8 bb = {t0[0], t0[1], t0[2], t0[3], t1[0], t1[1], t1[2], t1[3]};
                acc = __builtin_amdgcn_mfma_f32_32x32x16_bf16(__builtin_bit_cast(bf16x8, pv[s]), __builtin_bit_cast(bf16x8, bb), acc, 0, 0, 0);
            }
            const float* fp = fac + (2 * w + mm) * 32 + 4 * h2;
#pragma unroll
            for (int qd = 0; qd < 4; ++qd) {
                f32x4 f4 = *(const f32x4*)(fp + 8 * qd);
#pragma unroll
                for (int e = 0; e < 4; ++e) {
                    if (mm == 0) tot[qd * 4 + e] = f4[e] * acc[qd * 4 + e];
                    else tot[qd * 4 + e] += f4[e] * acc[qd * 4 + e];
                }
            }
        }
#pragma unroll
        for (int i = 0; i < 16; ++i) {
            int head = (i & 3) + 8 * (i >> 2) + 4 * h2;
            red2[(w * 32 + head) * 32 + r] = tot[i];
        }
    }
    bar();
    {
        const int head = t >> 4, cq = (t & 15) * 2;
        f32x4 ma = *(const f32x4*)(Mws + head * 8), mb = *(const f32x4*)(Mws + head * 8 + 4);
        float M = fmaxf(fmaxf(fmaxf(ma[0], ma[1]), fmaxf(ma[2], ma[3])), fmaxf(fmaxf(mb[0], mb[1]), fmaxf(mb[2], mb[3])));
        float ew[8];
#pragma unroll
        for (int ww = 0; ww < 4; ++ww) { ew[ww] = __expf(ma[ww] - M); ew[ww + 4] = __expf(mb[ww] - M); }
        f32x2 s2 = {0.f, 0.f};
#pragma unroll
        for (int ww = 0; ww < 8; ++ww) s2 += ew[ww] * *(const f32x2*)(red2 + (ww * 32 + head) * 32 + cq);
        u32x4 o = {asu(s2[0]), asu(s2[1]), asu(__shfl_down(s2[0], 1)), asu(__shfl_down(s2[1], 1))};
        if ((t & 1) == 0)
            __builtin_amdgcn_raw_buffer_store_b128(o, rs, WS_OPART + ((kc * 32 + head) * 512 + mem * 32 + cq) * 4, 0, SC1);
        if (mem == 0 && (t & 15) == 0) {
            f32x4 la = *(const f32x4*)(Lws + head * 8), lb = *(const f32x4*)(Lws + head * 8 + 4);
            float L = 0.f;
#pragma unroll
            for (int ww = 0; ww < 4; ++ww) L += ew[ww] * la[ww] + ew[ww + 4] * lb[ww];
            u32x2 ml = {asu(M), asu(L)};
            __builtin_amdgcn_raw_buffer_store_b64(ml, rs, WS_ML + (head * 16 + kc) * 8, 0, SC1);
        }
    }
    STAMP(7);
    u32x4 uvv[2], ovv[2];
    const float xt = bf2f(x[t]);
    {
        u32x4 gv = {0u, 0u, 0u, 0u}, uv_ = {0u, 0u, 0u, 0u}, dv = {0u, 0u, 0u, 0u};
        if (has_ffn) {
            gv = *(const u32x4*)(wg + (size_t)t * FF + f0);
            uv_ = *(const u32x4*)(wu + (size_t)t * FF + f0);
            dv = *(const u32x4*)(wd + (size_t)(f0 + w) * 512 + lane * 8);
        }
#pragma unroll
        for (int i = 0; i < 2; ++i)
            uvv[i] = *(const u32x4*)(wuv + (size_t)((t >> 1) + 256 * i) * 4096 + h * 128 + j * 16 + (t & 1) * 8);
#pragma unroll
        for (int i = 0; i < 2; ++i)
            ovv[i] = *(const u32x4*)(wo + ((size_t)h * 128 + j * 16 + w * 2 + i) * 512 + lane * 8);
        float p[16];
#pragma unroll
        for (int jj = 0; jj < 4; ++jj) {
            p[2 * jj] = xt * bflo(gv[jj]); p[2 * jj + 1] = xt * bfhi(gv[jj]);
            p[8 + 2 * jj] = xt * bflo(uv_[jj]); p[8 + 2 * jj + 1] = xt * bfhi(uv_[jj]);
        }
        bool up = lane & 1;
        float s8[8];
#pragma unroll
        for (int k = 0; k < 8; ++k) { float send = up ? p[k] : p[k + 8]; float keep = up ? p[k + 8] : p[k]; s8[k] = keep + __shfl_xor(send, 1); }
        up = lane & 2;
        float s4[4];
#pragma unroll
        for (int k = 0; k < 4; ++k) { float send = up ? s8[k] : s8[k + 4]; float keep = up ? s8[k + 4] : s8[k]; s4[k] = keep + __shfl_xor(send, 2); }
        up = lane & 4;
        float s2[2];
#pragma unroll
        for (int k = 0; k < 2; ++k) { float send = up ? s4[k] : s4[k + 2]; float keep = up ? s4[k + 2] : s4[k]; s2[k] = keep + __shfl_xor(send, 4); }
        up = lane & 8;
        float send = up ? s2[0] : s2[1]; float keep = up ? s2[1] : s2[0];
        float v = keep + __shfl_xor(send, 8);
        v += __shfl_xor(v, 16);
        v += __shfl_xor(v, 32);
        if (lane < 16) {
            int idx = (lane & 1) * 8 + ((lane >> 1) & 1) * 4 + ((lane >> 2) & 1) * 2 + ((lane >> 3) & 1);
            sm2[w * 16 + idx] = v;
        }
        bar();
        float gs = 0.f, us = 0.f;
#pragma unroll
        for (int ww = 0; ww < 8; ++ww) { gs += sm2[ww * 16 + w]; us += sm2[ww * 16 + 8 + w]; }
        gs = rbf(gs); us = rbf(us);
        float sg = rbf(gs / (1.f + __expf(-gs)));
        float hv = rbf(sg * us);
        f32x4 o0, o1;
        o0[0] = hv * bflo(dv[0]); o0[1] = hv * bfhi(dv[0]); o0[2] = hv * bflo(dv[1]); o0[3] = hv * bfhi(dv[1]);
        o1[0] = hv * bflo(dv[2]); o1[1] = hv * bfhi(dv[2]); o1[2] = hv * bflo(dv[3]); o1[3] = hv * bfhi(dv[3]);
        *(f32x4*)(redf + w * 512 + lane * 8) = o0;
        *(f32x4*)(redf + w * 512 + lane * 8 + 4) = o1;
        bar();
        if (has_ffn && t < 128) {
            f32x4 s = *(const f32x4*)(redf + t * 4);
#pragma unroll
            for (int ww = 1; ww < 8; ++ww) s += *(const f32x4*)(redf + ww * 512 + t * 4);
            __builtin_amdgcn_raw_buffer_store_b128(__builtin_bit_cast(u32x4, s), rs, WS_GB + fch * 2048 + t * 16, 0, SC1);
        }
    }
    STAMP(8);
    {
        const int qd = t >> 7;
        u32x4 ov[4];
        u32x2 ml;
        const int oo = WS_OPART + ((qd * 4 * 32 + h) * 512 + (t & 127) * 4) * 4;
        for (unsigned spins = 0; spins < SPIN_CAP; ++spins) {
            asm volatile("" ::: "memory");
#pragma unroll
            for (int i = 0; i < 4; ++i) ov[i] = __builtin_amdgcn_raw_buffer_load_b128(rs, oo + i * 65536, 0, SC1);
            ml = __builtin_amdgcn_raw_buffer_load_b64(rs, WS_ML + (h * 16 + (lane & 15)) * 8, 0, SC1);
            unsigned m = umax(umax(max4(ov[0]), max4(ov[1])), umax(max4(ov[2]), max4(ov[3])));
            m = umax(m, umax(ml[0], ml[1]));
            if (__all(m != POISON)) break;
            __builtin_amdgcn_s_sleep(1);
        }
        STAMP(9);
        float mk = asf(ml[0]), lk = asf(ml[1]);
        float M = mk;
        M = fmaxf(M, __shfl_xor(M, 1)); M = fmaxf(M, __shfl_xor(M, 2));
        M = fmaxf(M, __shfl_xor(M, 4)); M = fmaxf(M, __shfl_xor(M, 8));
        float wk = __expf(mk - M);
        float L = wk * lk;
        L += __shfl_xor(L, 1); L += __shfl_xor(L, 2); L += __shfl_xor(L, 4); L += __shfl_xor(L, 8);
        wk = wk / L;
        f32x4 c4 = {0.f, 0.f, 0.f, 0.f};
#pragma unroll
        for (int i = 0; i < 4; ++i) {
            float wi = __shfl(wk, qd * 4 + i);
            c4[0] += wi * asf(ov[i][0]); c4[1] += wi * asf(ov[i][1]); c4[2] += wi * asf(ov[i][2]); c4[3] += wi * asf(ov[i][3]);
        }
        *(f32x4*)(cpart + qd * 512 + (t & 127) * 4) = c4;
    }
    bar();
    {
        const int row = t >> 1;
        float c0 = (cpart[row] + cpart[512 + row]) + (cpart[1024 + row] + cpart[1536 + row]);
        float c1 = (cpart[row + 256] + cpart[768 + row]) + (cpart[1280 + row] + cpart[1792 + row]);
        float acc[8];
#pragma unroll
        for (int jj = 0; jj < 4; ++jj) {
            acc[2 * jj] = c0 * bflo(uvv[0][jj]) + c1 * bflo(uvv[1][jj]);
            acc[2 * jj + 1] = c0 * bfhi(uvv[0][jj]) + c1 * bfhi(uvv[1][jj]);
        }
        bool up = lane & 2;
        float s[4];
#pragma unroll
        for (int k = 0; k < 4; ++k) { float send = up ? acc[k] : acc[k + 4]; float keep = up ? acc[k + 4] : acc[k]; s[k] = keep + __shfl_xor(send, 2); }
        up = lane & 4;
        float s2[2];
#pragma unroll
        for (int k = 0; k < 2; ++k) { float send = up ? s[k] : s[k + 2]; float keep = up ? s[k + 2] : s[k]; s2[k] = keep + __shfl_xor(send, 4); }
        up = lane & 8;
        float send = up ? s2[0] : s2[1]; float keep = up ? s2[1] : s2[0];
        float v = keep + __shfl_xor(send, 8);
        v += __shfl_xor(v, 16);
        v += __shfl_xor(v, 32);
        if (lane < 16) {
            int d = (lane & 1) * 8 + ((lane >> 1) & 1) * 4 + ((lane >> 2) & 1) * 2 + ((lane >> 3) & 1);
            sm3[w * 16 + d] = v;
        }
    }
    bar();
    {
        float c0 = 0.f, c1 = 0.f;
#pragma unroll
        for (int ww = 0; ww < 8; ++ww) { c0 += sm3[ww * 16 + 2 * w]; c1 += sm3[ww * 16 + 2 * w + 1]; }
        c0 = rbf(c0); c1 = rbf(c1);
        f32x4 o0, o1;
        o0[0] = c0 * bflo(ovv[0][0]) + c1 * bflo(ovv[1][0]); o0[1] = c0 * bfhi(ovv[0][0]) + c1 * bfhi(ovv[1][0]);
        o0[2] = c0 * bflo(ovv[0][1]) + c1 * bflo(ovv[1][1]); o0[3] = c0 * bfhi(ovv[0][1]) + c1 * bfhi(ovv[1][1]);
        o1[0] = c0 * bflo(ovv[0][2]) + c1 * bflo(ovv[1][2]); o1[1] = c0 * bfhi(ovv[0][2]) + c1 * bfhi(ovv[1][2]);
        o1[2] = c0 * bflo(ovv[0][3]) + c1 * bflo(ovv[1][3]); o1[3] = c0 * bfhi(ovv[0][3]) + c1 * bfhi(ovv[1][3]);
        *(f32x4*)(redo + w * 512 + lane * 8) = o0;
        *(f32x4*)(redo + w * 512 + lane * 8 + 4) = o1;
    }
    bar();
    if (t < 128) {
        f32x4 s = *(const f32x4*)(redo + t * 4);
#pragma unroll
        for (int ww = 1; ww < 8; ++ww) s += *(const f32x4*)(redo + ww * 512 + t * 4);
        __builtin_amdgcn_raw_buffer_store_b128(__builtin_bit_cast(u32x4, s), rs, WS_GA + b * 2048 + t * 16, 0, SC1);
    }
    STAMP(10);
    if (cons) {
        const int k = b - 224;
        const int p = t >> 1, hf = t & 1;
        float gp = rbf(xt * bf2f(gw[t]));
        const int ao = WS_GA + p * 2048 + k * 64 + hf * 32;
        const int bo = WS_GB + (p < 172 ? p : 171) * 2048 + k * 64 + hf * 32;
        u32x4 a0, a1, b0, b1;
        for (unsigned spins = 0; spins < SPIN_CAP; ++spins) {
            asm volatile("" ::: "memory");
            a0 = __builtin_amdgcn_raw_buffer_load_b128(rs, ao, 0, SC1);
            a1 = __builtin_amdgcn_raw_buffer_load_b128(rs, ao + 16, 0, SC1);
            b0 = __builtin_amdgcn_raw_buffer_load_b128(rs, bo, 0, SC1);
            b1 = __builtin_amdgcn_raw_buffer_load_b128(rs, bo + 16, 0, SC1);
            unsigned m = umax(umax(max4(a0), max4(a1)), umax(max4(b0), max4(b1)));
            if (__all(m != POISON)) break;
            __builtin_amdgcn_s_sleep(1);
        }
        STAMP(11);
        float v[16];
        const float bz = p < 172 ? 1.f : 0.f;
#pragma unroll
        for (int i = 0; i < 4; ++i) {
            v[i] = asf(a0[i]); v[4 + i] = asf(a1[i]);
            v[8 + i] = p < 172 ? asf(b0[i]) : 0.f; v[12 + i] = p < 172 ? asf(b1[i]) : 0.f;
        }
        (void)bz;
        bool up = lane & 2;
        float s8[8];
#pragma unroll
        for (int q = 0; q < 8; ++q) { float send = up ? v[q] : v[q + 8]; float keep = up ? v[q + 8] : v[q]; s8[q] = keep + __shfl_xor(send, 2); }
        up = lane & 4;
        float s4[4];
#pragma unroll
        for (int q = 0; q < 4; ++q) { float send = up ? s8[q] : s8[q + 4]; float keep = up ? s8[q + 4] : s8[q]; s4[q] = keep + __shfl_xor(send, 4); }
        up = lane & 8;
        float s2[2];
#pragma unroll
        for (int q = 0; q < 2; ++q) { float send = up ? s4[q] : s4[q + 2]; float keep = up ? s4[q + 2] : s4[q]; s2[q] = keep + __shfl_xor(send, 8); }
        up = lane & 16;
        float send = up ? s2[0] : s2[1]; float keep = up ? s2[1] : s2[0];
        float vv = keep + __shfl_xor(send, 16);
        vv += __shfl_xor(vv, 32);
#pragma unroll
        for (int o = 32; o >= 1; o >>= 1) gp += __shfl_xor(gp, o);
        if (lane < 32) {
            int idx = ((lane >> 1) & 1) * 16 + (lane & 1) * 8 + ((lane >> 2) & 1) * 4 + ((lane >> 3) & 1) * 2 + ((lane >> 4) & 1);
            fin[w * 32 + idx] = vv;
        }
        if (lane == 0) fing[w] = gp;
    }
    bar();
    if (cons && t < 16) {
        float a = 0.f, bb = 0.f, gs = 0.f;
#pragma unroll
        for (int ww = 0; ww < 8; ++ww) { a += fin[ww * 32 + t]; bb += fin[ww * 32 + 16 + t]; gs += fing[ww]; }
        gs = rbf(gs);
        float g = rbf(1.f / (1.f + __expf(-gs)));
        a = rbf(a); bb = rbf(bb);
        float rr = rbf(g * a) + rbf(rbf(1.f - g) * bb);
        out[(b - 224) * 16 + t] = f2bf(rr);
    }
    STAMP(12);
}

extern "C" void kernel_launch(void* const* d_in, const int* in_sizes, int n_in,
                              void* d_out, int out_size, void* d_ws, size_t ws_size,
                              hipStream_t stream) {
    const u16* x = (const u16*)d_in[0];
    const u16* ckv = (const u16*)d_in[1];
    const u16* wq = (const u16*)d_in[2];
    const u16* wuk = (const u16*)d_in[3];
    const u16* wuv = (const u16*)d_in[4];
    const u16* wo = (const u16*)d_in[5];
    const u16* wg = (const u16*)d_in[7];
    const u16* wu = (const u16*)d_in[8];
    const u16* wd = (const u16*)d_in[9];
    const u16* gw = (const u16*)d_in[10];
    mega<<<256, 640, 0, stream>>>(x, ckv, wq, wuk, wuv, wo, wg, wu, wd, gw, (char*)d_ws, (u16*)d_out);
}
```

```cpp
#include <hip/hip_runtime.h>
#include <hip/hip_bf16.h>
#include <stdint.h>

#define SK 8192
#define FF 1376

typedef unsigned short u16;
typedef unsigned long long u64;
typedef __attribute__((ext_vector_type(8))) __bf16 bf16x8;
typedef __attribute__((ext_vector_type(2))) __bf16 bf16x2;
typedef __attribute__((ext_vector_type(4))) short s16x4;
typedef __attribute__((ext_vector_type(8))) short s16x8;
typedef __attribute__((ext_vector_type(16))) float f32x16;
typedef __attribute__((ext_vector_type(4))) float f32x4;
typedef __attribute__((ext_vector_type(2))) float f32x2;
typedef __attribute__((ext_vector_type(4))) unsigned int u32x4;
typedef __attribute__((ext_vector_type(2))) unsigned int u32x2;
#define GAS __attribute__((address_space(1)))
typedef GAS unsigned gu32;
typedef GAS u64 gu64;
#define RLX_AGENT __ATOMIC_RELAXED, __HIP_MEMORY_SCOPE_AGENT
#define SC1 16
#define POISON 0xFFFFFFFFu

__device__ __forceinline__ float bf2f(u16 v) { return __uint_as_float(((unsigned)v) << 16); }
__device__ __forceinline__ float bflo(unsigned v) { return __uint_as_float(v << 16); }
__device__ __forceinline__ float bfhi(unsigned v) { return __uint_as_float(v & 0xffff0000u); }
__device__ __forceinline__ float asf(unsigned v) { return __uint_as_float(v); }
__device__ __forceinline__ unsigned asu(float v) { return __float_as_uint(v); }
__device__ __forceinline__ unsigned pk2(float a, float b) {
    bf16x2 v = {(__bf16)a, (__bf16)b};
    return __builtin_bit_cast(unsigned, v);
}
__device__ __forceinline__ u16 f2bf(float f) { __bf16 v = (__bf16)f; return __builtin_bit_cast(u16, v); }
__device__ __forceinline__ float rbf(float f) { return bf2f(f2bf(f)); }
__device__ __forceinline__ unsigned umax(unsigned a, unsigned b) { return a > b ? a : b; }
__device__ __forceinline__ unsigned max4(u32x4 v) { return umax(umax(v[0], v[1]), umax(v[2], v[3])); }

#define NREP 32
#define WS_EPOCH   0
#define WS_F0      256
#define WS_QG      (WS_F0 + NREP * 2048)
#define WS_QABS    (WS_QG + 256 * 1024)
#define WS_P       (WS_QABS + 8 * 32768)
#define WS_MLOC    (WS_P + 16 * 32 * 512 * 2)
#define WS_OPART   (WS_MLOC + 16 * 32 * 16 * 8)
#define WS_ML      (WS_OPART + 16 * 32 * 512 * 4)
#define WS_GA      (WS_ML + 32 * 16 * 8)
#define WS_GB      (WS_GA + 256 * 2048)
#define WS_QGF     (WS_GB + 172 * 2048)
#define WS_PF      (WS_QGF + 256 * 1024)
#define WS_MLOCF   (WS_PF + 16 * 32 * 512 * 2)
#define WS_END     (WS_MLOCF + 16 * 32 * 16 * 8)

#define SPIN_CAP (1u << 17)
#define STAMP(i)
#define CKV_MODE 1
#define NBAR 8

__device__ __forceinline__ void lgkm0() { asm volatile("s_waitcnt lgkmcnt(0)" ::: "memory"); }
__device__ __forceinline__ void vm0() { asm volatile("s_waitcnt vmcnt(0)" ::: "memory"); }
__device__ __forceinline__ void bar() { asm volatile("s_waitcnt lgkmcnt(0)\n\ts_barrier" ::: "memory"); }
typedef volatile __attribute__((address_space(3))) unsigned lds_u32;
__device__ __forceinline__ void lds_wait(lds_u32* p, unsigned target) {
    for (unsigned s = 0; s < SPIN_CAP; ++s) {
        if (*p >= target) break;
        __builtin_amdgcn_s_sleep(1);
    }
    asm volatile("" ::: "memory");
}

__global__ __launch_bounds__(640, 1) void mega(const u16* __restrict__ x, const u16* __restrict__ ckv,
                                               const u16* __restrict__ wq, const u16* __restrict__ wuk,
                                               const u16* __restrict__ wuv, const u16* __restrict__ wo,
                                               const u16* __restrict__ wg, const u16* __restrict__ wu,
                                               const u16* __restrict__ wd, const u16* __restrict__ gw,
                                               char* __restrict__ ws, u16* __restrict__ out) {
    __shared__ __attribute__((aligned(16))) char tile[512 * 64];
    __shared__ __attribute__((aligned(16))) float red1[8 * 1024];
    __shared__ __attribute__((aligned(16))) float red2[8 * 1024];
    __shared__ __attribute__((aligned(16))) float redf[8 * 512];
    __shared__ __attribute__((aligned(16))) float redo[8 * 512];
    __shared__ __attribute__((aligned(16))) float sm1[8 * 128];
    __shared__ __attribute__((aligned(16))) float sm2[8 * 16];
    __shared__ __attribute__((aligned(16))) float sm3[8 * 16];
    __shared__ __attribute__((aligned(16))) float qs[128];
    __shared__ __attribute__((aligned(16))) float fac[16 * 32];
    __shared__ __attribute__((aligned(16))) float Mws[32 * 8];
    __shared__ __attribute__((aligned(16))) float Lws[32 * 8];
    __shared__ __attribute__((aligned(16))) float cpart[4 * 512];
    __shared__ __attribute__((aligned(16))) float fin[8 * 32];
    __shared__ __attribute__((aligned(16))) float fing[8];
    __shared__ unsigned flg[4];

    const int b = blockIdx.x;
    const int t = threadIdx.x;
    const int lane = t & 63, w = t >> 6;
    const int h = b & 31, j = b >> 5;
    const int kc = b & 15, mem = b >> 4;
    const int key0 = kc * 512 + mem * 32;
    const int r = lane & 31, h2 = lane >> 5;
    const int fidx = b >> 3;
    const int fch = (b & 7) * 22 + fidx;
    const bool has_ffn = (fidx < 22) && (fch < 172);
    const int f0 = fch * 8;
    const bool cons = b >= 224;
    const __amdgpu_buffer_rsrc_t rs = __builtin_amdgcn_make_buffer_rsrc(ws, 0, WS_END, 0x00020000);
    lds_u32* vflg = (lds_u32*)flg;
    STAMP(0);

    if (w >= 8) {
        const unsigned e = __hip_atomic_load((gu32*)(ws + WS_EPOCH), RLX_AGENT);
        const u32x4 P4 = {POISON, POISON, POISON, POISON};
        const u32x2 P2 = {POISON, POISON};
        if (w == 9)
            __builtin_amdgcn_raw_buffer_store_b128(P4, rs, WS_QABS + (lane >> 3) * 32768 + ((j * 8 + (lane & 7)) * 32 + h) * 16, 0, SC1);
        bar();
        const unsigned tag = e + 1u;
        if (w == 8) {
            lds_wait(vflg + 0, 8);
            STAMP(1);
            f32x2 s = *(const f32x2*)(sm1 + 2 * lane);
#pragma unroll
            for (int ww = 1; ww < 8; ++ww) s += *(const f32x2*)(sm1 + ww * 128 + 2 * lane);
            u32x4 g = {tag, asu(s[0]), asu(s[1]), ~tag};
            __builtin_amdgcn_raw_buffer_store_b128(g, rs, WS_QGF + ((h * 8 + j) * 64 + lane) * 16, 0, 0);
            __builtin_amdgcn_raw_buffer_store_b128(g, rs, WS_QG + ((h * 8 + j) * 64 + lane) * 16, 0, SC1);
            for (unsigned spins = 0; spins < SPIN_CAP; ++spins) {
            asm volatile("" ::: "memory");
                u32x4 v[8];
                const int qb = ((spins & 3) == 3 ? WS_QG : WS_QGF) + (h * 8 * 64 + lane) * 16;
#pragma unroll
                for (int jj = 0; jj < 8; ++jj)
                    v[jj] = __builtin_amdgcn_raw_buffer_load_b128(rs, qb + jj * 1024, 0, SC1);
                unsigned bad = 0;
                float q0 = 0.f, q1 = 0.f;
#pragma unroll
                for (int jj = 0; jj < 8; ++jj) {
                    bad |= (v[jj][0] ^ tag) | (v[jj][3] ^ ~tag);
                    q0 += asf(v[jj][1]); q1 += asf(v[jj][2]);
                }
                if (__all(bad == 0) || spins == SPIN_CAP - 1) {
                    f32x2 qq = {rbf(q0), rbf(q1)};
                    *(f32x2*)(qs + 2 * lane) = qq;
                    break;
                }
                __builtin_amdgcn_s_sleep(1);
            }
            lgkm0();
            if (lane == 0) vflg[1] = 1u;
            STAMP(2);
        } else {
            vm0();
            {
                const unsigned tg = e + 1u;
                const u64 want = ((u64)(~tg) << 32) | tg;
                if (lane < NREP) __hip_atomic_store((gu64*)(ws + WS_F0 + lane * 2048 + b * 8), want, RLX_AGENT);
            }
#pragma unroll
            for (int i = 0; i < 2; ++i) {
                __builtin_amdgcn_raw_buffer_store_b128(P4, rs, WS_P + ((kc * 32 + mem * 2 + i) * 64 + lane) * 16, 0, SC1);
                __builtin_amdgcn_raw_buffer_store_b128(P4, rs, WS_PF + ((kc * 32 + mem * 2 + i) * 64 + lane) * 16, 0, SC1);
            }
            __builtin_amdgcn_raw_buffer_store_b64(P2, rs, (lane < 32 ? WS_MLOC : WS_MLOCF) + ((kc * 8 + (mem >> 1)) * 32 + r) * 16 + (mem & 1) * 8, 0, SC1);
#pragma unroll
            for (int i = 0; i < 4; ++i)
                __builtin_amdgcn_raw_buffer_store_b128(P4, rs, WS_OPART + ((kc * 32 + i * 8 + (lane >> 3)) * 512 + mem * 32) * 4 + (lane & 7) * 16, 0, SC1);
            if (mem == 0 && lane < 32) __builtin_amdgcn_raw_buffer_store_b64(P2, rs, WS_ML + (lane * 16 + kc) * 8, 0, SC1);
#pragma unroll
            for (int i = 0; i < 2; ++i)
                __builtin_amdgcn_raw_buffer_store_b128(P4, rs, WS_GA + b * 2048 + i * 1024 + lane * 16, 0, SC1);
            if (has_ffn) {
#pragma unroll
                for (int i = 0; i < 2; ++i)
                    __builtin_amdgcn_raw_buffer_store_b128(P4, rs, WS_GB + fch * 2048 + i * 1024 + lane * 16, 0, SC1);
            }
            vm0();
            if (lane == 0) vflg[3] = 1u;
            STAMP(1);
            const int fo = WS_F0 + (b & (NREP - 1)) * 2048 + lane * 16;
            for (unsigned spins = 0; spins < SPIN_CAP; ++spins) {
            asm volatile("" ::: "memory");
                u32x4 a0 = __builtin_amdgcn_raw_buffer_load_b128(rs, fo, 0, SC1);
                u32x4 a1 = __builtin_amdgcn_raw_buffer_load_b128(rs, fo + 1024, 0, SC1);
                unsigned bad = (a0[0] ^ tag) | (a0[1] ^ ~tag) | (a0[2] ^ tag) | (a0[3] ^ ~tag) |
                               (a1[0] ^ tag) | (a1[1] ^ ~tag) | (a1[2] ^ tag) | (a1[3] ^ ~tag);
                if (__all(bad == 0)) break;
                __builtin_amdgcn_s_sleep(1);
            }
            if (lane == 0) vflg[2] = 1u;
            STAMP(2);
            unsigned pf[14];
            {
                const int rr = lane >> 2, sg = lane & 3;
                const int colb = (b & 7) * 352 + sg * 64;
                const char* pg = (const char*)wg + (size_t)((b >> 3) * 16 + rr) * (FF * 2);
                const char* pu = (const char*)wu + (size_t)((b >> 3) * 16 + rr) * (FF * 2);
                int c0 = colb, c1 = colb + 256 - (sg < 2 ? 0 : 256);
                if (c0 > FF * 2 - 4) c0 = FF * 2 - 4;
                if (c1 > FF * 2 - 4) c1 = FF * 2 - 4;
                pf[0] = *(const unsigned*)(pg + c0); pf[1] = *(const unsigned*)(pu + c0);
                pf[2] = *(const unsigned*)(pg + c1); pf[3] = *(const unsigned*)(pu + c1);
                const int fr = has_ffn ? f0 : 0;
#pragma unroll
                for (int i = 0; i < 2; ++i) pf[4 + i] = *(const unsigned*)((const char*)wd + (size_t)fr * 1024 + i * 4096 + lane * 64);
#pragma unroll
                for (int i = 0; i < 4; ++i)
                    pf[6 + i] = *(const unsigned*)((const char*)wuv + (size_t)(j * 64 + i * 16 + (lane >> 2)) * 8192 + h * 256 + (lane & 3) * 64);
#pragma unroll
                for (int i = 0; i < 4; ++i)
                    pf[10 + i] = *(const unsigned*)((const char*)wo + ((size_t)h * 128 + j * 16) * 1024 + i * 4096 + lane * 64);
            }
#pragma unroll
            for (int i = 0; i < NBAR; ++i) asm volatile("s_barrier" ::: "memory");
            asm volatile("" :: "v"(pf[0]), "v"(pf[1]), "v"(pf[2]), "v"(pf[3]), "v"(pf[4]), "v"(pf[5]), "v"(pf[6]),
                         "v"(pf[7]), "v"(pf[8]), "v"(pf[9]), "v"(pf[10]), "v"(pf[11]), "v"(pf[12]), "v"(pf[13]));
            return;
        }
#pragma unroll
        for (int i = 0; i < NBAR; ++i) asm volatile("s_barrier" ::: "memory");
        if (b == 224 && lane == 0) __hip_atomic_store((gu32*)(ws + WS_EPOCH), tag, RLX_AGENT);
        return;
    }

    u32x4 wqv[2], wkv[2];
    float x0, x1;
    {
        const int row = j * 64 + (t >> 4);
        x0 = bf2f(x[row]); x1 = bf2f(x[row + 32]);
#pragma unroll
        for (int i = 0; i < 2; ++i)
            wqv[i] = *(const u32x4*)(wq + (size_t)(row + 32 * i) * 4096 + h * 128 + (t & 15) * 8);
#pragma unroll
        for (int i = 0; i < 2; ++i)
            wkv[i] = *(const u32x4*)(wuk + (size_t)(j * 64 + 8 * w + (lane >> 4) + 4 * i) * 4096 + h * 128 + (lane & 15) * 8);
    }
    if (t < 4) flg[t] = 0u;
    bar();
    __builtin_amdgcn_sched_barrier(0);
    asm volatile("s_waitcnt vmcnt(2)" ::: "memory");
    STAMP(1);
    bf16x8 bq[4];
    u32x4 cv[4];
    {
        float acc[8];
#pragma unroll
        for (int jj = 0; jj < 4; ++jj) {
            acc[2 * jj] = x0 * bflo(wqv[0][jj]) + x1 * bflo(wqv[1][jj]);
            acc[2 * jj + 1] = x0 * bfhi(wqv[0][jj]) + x1 * bfhi(wqv[1][jj]);
        }
        bool up = lane & 16;
        float s[4];
#pragma unroll
        for (int k = 0; k < 4; ++k) { float send = up ? acc[k] : acc[k + 4]; float keep = up ? acc[k + 4] : acc[k]; s[k] = keep + __shfl_xor(send, 16); }
        up = lane & 32;
        f32x2 s2;
#pragma unroll
        for (int k = 0; k < 2; ++k) { float send = up ? s[k] : s[k + 2]; float keep = up ? s[k + 2] : s[k]; s2[k] = keep + __shfl_xor(send, 32); }
        *(f32x2*)(sm1 + w * 128 + (lane & 15) * 8 + ((lane >> 4) & 1) * 4 + (lane >> 5) * 2) = s2;
        lgkm0();
        if (lane == 0) __hip_atomic_fetch_add(flg + 0, 1u, __ATOMIC_RELAXED, __HIP_MEMORY_SCOPE_WORKGROUP);
    }
    __builtin_amdgcn_sched_barrier(0);
    lds_wait(vflg + 1, 1);
    STAMP(2);
    lds_wait(vflg + 3, 1);
    {
        const int l16 = lane & 15;
        f32x4 qa0 = *(const f32x4*)(qs + l16 * 8), qa1 = *(const f32x4*)(qs + l16 * 8 + 4);
        float d[2];
#pragma unroll
        for (int i = 0; i < 2; ++i) {
            d[i] = qa0[0] * bflo(wkv[i][0]) + qa0[1] * bfhi(wkv[i][0]) + qa0[2] * bflo(wkv[i][1]) + qa0[3] * bfhi(wkv[i][1]);
            d[i] += qa1[0] * bflo(wkv[i][2]) + qa1[1] * bfhi(wkv[i][2]) + qa1[2] * bflo(wkv[i][3]) + qa1[3] * bfhi(wkv[i][3]);
        }
        bool up = lane & 1;
        float send = up ? d[0] : d[1], keep = up ? d[1] : d[0];
        float v = keep + __shfl_xor(send, 1);
        v += __shfl_xor(v, 2);
        v += __shfl_xor(v, 4);
        v += __shfl_xor(v, 8);
        v *= 0.08838834764831845f;
        float vv[8];
#pragma unroll
        for (int k = 0; k < 8; ++k) vv[k] = __shfl(v, (k & 3) * 16 + (k >> 2));
        u32x4 o = {pk2(vv[0], vv[1]), pk2(vv[2], vv[3]), pk2(vv[4], vv[5]), pk2(vv[6], vv[7])};
        if (lane < 8)
            __builtin_amdgcn_raw_buffer_store_b128(o, rs, WS_QABS + lane * 32768 + ((j * 8 + w) * 32 + h) * 16, 0, SC1);
    }
    {
        const u16* bp = ckv + (size_t)(key0 + r) * 512 + 64 * w + 8 * h2;
#pragma unroll
        for (int s = 0; s < 4; ++s) bq[s] = *(const bf16x8*)(bp + 16 * s);
        const u16* cp = ckv + (size_t)(kc * 512 + (t >> 2)) * 512 + mem * 32 + (t & 3) * 8;
#pragma unroll
        for (int i = 0; i < 4; ++i) cv[i] = *(const u32x4*)(cp + (size_t)i * 128 * 512);
    }
    __builtin_amdgcn_sched_barrier(0);
#pragma unroll
    for (int i = 0; i < 4; ++i) *(u32x4*)(tile + (i * 128 + (t >> 2)) * 64 + (t & 3) * 16) = cv[i];
    lds_wait(vflg + 2, 1);
    STAMP(3);
    {
        u32x4 av[4];
        const int qo = WS_QABS + (b & 7) * 32768 + (w * 256 + lane) * 16;
        for (unsigned spins = 0; spins < SPIN_CAP; ++spins) {
            asm volatile("" ::: "memory");
#pragma unroll
            for (int s = 0; s < 4; ++s) av[s] = __builtin_amdgcn_raw_buffer_load_b128(rs, qo + 1024 * s, 0, SC1);
            unsigned m = umax(umax(max4(av[0]), max4(av[1])), umax(max4(av[2]), max4(av[3])));
            if (__all(m != POISON)) break;
            __builtin_amdgcn_s_sleep(1);
        }
        STAMP(4);
        f32x16 acc;
#pragma unroll
        for (int i = 0; i < 16; ++i) acc[i] = 0.f;
#pragma unroll
        for (int s = 0; s < 4; ++s)
            acc = __builtin_amdgcn_mfma_f32_32x32x16_bf16(__builtin_bit_cast(bf16x8, av[s]), bq[s], acc, 0, 0, 0);
#pragma unroll
        for (int i = 0; i < 16; ++i) {
            int head = (i & 3) + 8 * (i >> 2) + 4 * h2;
            red1[(w * 32 + head) * 32 + r] = acc[i];
        }
    }
    bar();
    {
        const int head = t >> 4, kq = (t & 15) * 2;
        f32x2 s2 = *(const f32x2*)(red1 + head * 32 + kq);
#pragma unroll
        for (int ww = 1; ww < 8; ++ww) s2 += *(const f32x2*)(red1 + (ww * 32 + head) * 32 + kq);
        float m = fmaxf(s2[0], s2[1]);
        m = fmaxf(m, __shfl_xor(m, 1));
        m = fmaxf(m, __shfl_xor(m, 2));
        m = fmaxf(m, __shfl_xor(m, 4));
        m = fmaxf(m, __shfl_xor(m, 8));
        unsigned p01 = pk2(__expf(s2[0] - m), __expf(s2[1] - m));
        float l = bflo(p01) + bfhi(p01);
        l += __shfl_xor(l, 1);
        l += __shfl_xor(l, 2);
        l += __shfl_xor(l, 4);
        l += __shfl_xor(l, 8);
        u32x4 pp = {p01, (unsigned)__shfl_down((int)p01, 1), (unsigned)__shfl_down((int)p01, 2), (unsigned)__shfl_down((int)p01, 3)};
        u32x2 ml = {asu(m), asu(l)};
        if ((t & 3) == 0)
            __builtin_amdgcn_raw_buffer_store_b128(pp, rs, WS_PF + ((kc * 32 + mem * 2) * 64 + ((t & 15) >> 2) * 32 + head) * 16, 0, 0);
        if ((t & 15) == 0)
            __builtin_amdgcn_raw_buffer_store_b64(ml, rs, WS_MLOCF + ((kc * 8 + (mem >> 1)) * 32 + head) * 16 + (mem & 1) * 8, 0, 0);
        if ((t & 3) == 0)
            __builtin_amdgcn_raw_buffer_store_b128(pp, rs, WS_P + ((kc * 32 + mem * 2) * 64 + ((t & 15) >> 2) * 32 + head) * 16, 0, SC1);
        if ((t & 15) == 0)
            __builtin_amdgcn_raw_buffer_store_b64(ml, rs, WS_MLOC + ((kc * 8 + (mem >> 1)) * 32 + head) * 16 + (mem & 1) * 8, 0, SC1);
    }
    STAMP(5);
    {
        u32x4 pv[4], mlv;
        for (unsigned spins = 0; spins < SPIN_CAP; ++spins) {
            asm volatile("" ::: "memory");
            const bool slow = (spins & 3) == 3;
            const int po = (slow ? WS_P : WS_PF) + ((kc * 32 + w * 4) * 64 + lane) * 16;
            const int mo = (slow ? WS_MLOC : WS_MLOCF) + ((kc * 8 + w) * 32 + r) * 16;
#pragma unroll
            for (int s = 0; s < 4; ++s) pv[s] = __builtin_amdgcn_raw_buffer_load_b128(rs, po + 1024 * s, 0, SC1);
            mlv = __builtin_amdgcn_raw_buffer_load_b128(rs, mo, 0, SC1);
            unsigned m = umax(umax(max4(pv[0]), max4(pv[1])), umax(max4(pv[2]), max4(pv[3])));
            m = umax(m, max4(mlv));
            if (__all(m != POISON)) break;
            __builtin_amdgcn_s_sleep(1);
        }
        STAMP(6);
        {
            float m0 = asf(mlv[0]), l0 = asf(mlv[1]), m1 = asf(mlv[2]), l1 = asf(mlv[3]);
            float Mw = fmaxf(m0, m1);
            float f0_ = __expf(m0 - Mw), f1_ = __expf(m1 - Mw);
            if (lane < 32) {
                fac[(2 * w) * 32 + r] = f0_; fac[(2 * w + 1) * 32 + r] = f1_;
                Mws[r * 8 + w] = Mw; Lws[r * 8 + w] = f0_ * l0 + f1_ * l1;
            }
        }
        const int g = lane >> 4;
        const int q = (lane & 15) >> 2, p = lane & 3;
        const int cb = g & 1;
        f32x16 tot;
#pragma unroll
        for (int mm = 0; mm < 2; ++mm) {
            f32x16 acc;
#pragma unroll
            for (int i = 0; i < 16; ++i) acc[i] = 0.f;
#pragma unroll
            for (int ss = 0; ss < 2; ++ss) {
                int s = mm * 2 + ss;
                int row = w * 64 + 16 * s + 8 * h2 + q;
                s16x4 t0 = __builtin_amdgcn_ds_read_tr16_b64_v4i16(
                    (__attribute__((address_space(3))) s16x4*)(tile + row * 64 + cb * 32 + p * 8));
                s16x4 t1 = __builtin_amdgcn_ds_read_tr16_b64_v4i16(
                    (__attribute__((address_space(3))) s16x4*)(tile + (row + 4) * 64 + cb * 32 + p * 8));
                s16x8 bb = {t0[0], t0[1], t0[2], t0[3], t1[0], t1[1], t1[2], t1[3]};
                acc = __builtin_amdgcn_mfma_f32_32x32x16_bf16(__builtin_bit_cast(bf16x8, pv[s]), __builtin_bit_cast(bf16x8, bb), acc, 0, 0, 0);
            }
            const float* fp = fac + (2 * w + mm) * 32 + 4 * h2;
#pragma unroll
            for (int qd = 0; qd < 4; ++qd) {
                f32x4 f4 = *(const f32x4*)(fp + 8 * qd);
#pragma unroll
                for (int e = 0; e < 4; ++e) {
                    if (mm == 0) tot[qd * 4 + e] = f4[e] * acc[qd * 4 + e];
                    else tot[qd * 4 + e] += f4[e] * acc[qd * 4 + e];
                }
            }
        }
#pragma unroll
        for (int i = 0; i < 16; ++i) {
            int head = (i & 3) + 8 * (i >> 2) + 4 * h2;
            red2[(w * 32 + head) * 32 + r] = tot[i];
        }
    }
    bar();
    {
        const int head = t >> 4, cq = (t & 15) * 2;
        f32x4 ma = *(const f32x4*)(Mws + head * 8), mb = *(const f32x4*)(Mws + head * 8 + 4);
        float M = fmaxf(fmaxf(fmaxf(ma[0], ma[1]), fmaxf(ma[2], ma[3])), fmaxf(fmaxf(mb[0], mb[1]), fmaxf(mb[2], mb[3])));
        float ew[8];
#pragma unroll
        for (int ww = 0; ww < 4; ++ww) { ew[ww] = __expf(ma[ww] - M); ew[ww + 4] = __expf(mb[ww] - M); }
        f32x2 s2 = {0.f, 0.f};
#pragma unroll
        for (int ww = 0; ww < 8; ++ww) s2 += ew[ww] * *(const f32x2*)(red2 + (ww * 32 + head) * 32 + cq);
        u32x4 o = {asu(s2[0]), asu(s2[1]), asu(__shfl_down(s2[0], 1)), asu(__shfl_down(s2[1], 1))};
        if ((t & 1) == 0)
            __builtin_amdgcn_raw_buffer_store_b128(o, rs, WS_OPART + ((kc * 32 + head) * 512 + mem * 32 + cq) * 4, 0, SC1);
        if (mem == 0 && (t & 15) == 0) {
            f32x4 la = *(const f32x4*)(Lws + head * 8), lb = *(const f32x4*)(Lws + head * 8 + 4);
            float L = 0.f;
#pragma unroll
            for (int ww = 0; ww < 4; ++ww) L += ew[ww] * la[ww] + ew[ww + 4] * lb[ww];
            u32x2 ml = {asu(M), asu(L)};
            __builtin_amdgcn_raw_buffer_store_b64(ml, rs, WS_ML + (head * 16 + kc) * 8, 0, SC1);
        }
    }
    STAMP(7);
    u32x4 uvv[2], ovv[2];
    const float xt = bf2f(x[t]);
    {
        u32x4 gv = {0u, 0u, 0u, 0u}, uv_ = {0u, 0u, 0u, 0u}, dv = {0u, 0u, 0u, 0u};
        if (has_ffn) {
            gv = *(const u32x4*)(wg + (size_t)t * FF + f0);
            uv_ = *(const u32x4*)(wu + (size_t)t * FF + f0);
            dv = *(const u32x4*)(wd + (size_t)(f0 + w) * 512 + lane * 8);
        }
#pragma unroll
        for (int i = 0; i < 2; ++i)
            uvv[i] = *(const u32x4*)(wuv + (size_t)((t >> 1) + 256 * i) * 4096 + h * 128 + j * 16 + (t & 1) * 8);
#pragma unroll
        for (int i = 0; i < 2; ++i)
            ovv[i] = *(const u32x4*)(wo + ((size_t)h * 128 + j * 16 + w * 2 + i) * 512 + lane * 8);
        float p[16];
#pragma unroll
        for (int jj = 0; jj < 4; ++jj) {
            p[2 * jj] = xt * bflo(gv[jj]); p[2 * jj + 1] = xt * bfhi(gv[jj]);
            p[8 + 2 * jj] = xt * bflo(uv_[jj]); p[8 + 2 * jj + 1] = xt * bfhi(uv_[jj]);
        }
        bool up = lane & 1;
        float s8[8];
#pragma unroll
        for (int k = 0; k < 8; ++k) { float send = up ? p[k] : p[k + 8]; float keep = up ? p[k + 8] : p[k]; s8[k] = keep + __shfl_xor(send, 1); }
        up = lane & 2;
        float s4[4];
#pragma unroll
        for (int k = 0; k < 4; ++k) { float send = up ? s8[k] : s8[k + 4]; float keep = up ? s8[k + 4] : s8[k]; s4[k] = keep + __shfl_xor(send, 2); }
        up = lane & 4;
        float s2[2];
#pragma unroll
        for (int k = 0; k < 2; ++k) { float send = up ? s4[k] : s4[k + 2]; float keep = up ? s4[k + 2] : s4[k]; s2[k] = keep + __shfl_xor(send, 4); }
        up = lane & 8;
        float send = up ? s2[0] : s2[1]; float keep = up ? s2[1] : s2[0];
        float v = keep + __shfl_xor(send, 8);
        v += __shfl_xor(v, 16);
        v += __shfl_xor(v, 32);
        if (lane < 16) {
            int idx = (lane & 1) * 8 + ((lane >> 1) & 1) * 4 + ((lane >> 2) & 1) * 2 + ((lane >> 3) & 1);
            sm2[w * 16 + idx] = v;
        }
        bar();
        float gs = 0.f, us = 0.f;
#pragma unroll
        for (int ww = 0; ww < 8; ++ww) { gs += sm2[ww * 16 + w]; us += sm2[ww * 16 + 8 + w]; }
        gs = rbf(gs); us = rbf(us);
        float sg = rbf(gs / (1.f + __expf(-gs)));
        float hv = rbf(sg * us);
        f32x4 o0, o1;
        o0[0] = hv * bflo(dv[0]); o0[1] = hv * bfhi(dv[0]); o0[2] = hv * bflo(dv[1]); o0[3] = hv * bfhi(dv[1]);
        o1[0] = hv * bflo(dv[2]); o1[1] = hv * bfhi(dv[2]); o1[2] = hv * bflo(dv[3]); o1[3] = hv * bfhi(dv[3]);
        *(f32x4*)(redf + w * 512 + lane * 8) = o0;
        *(f32x4*)(redf + w * 512 + lane * 8 + 4) = o1;
        bar();
        if (has_ffn && t < 128) {
            f32x4 s = *(const f32x4*)(redf + t * 4);
#pragma unroll
            for (int ww = 1; ww < 8; ++ww) s += *(const f32x4*)(redf + ww * 512 + t * 4);
            __builtin_amdgcn_raw_buffer_store_b128(__builtin_bit_cast(u32x4, s), rs, WS_GB + fch * 2048 + t * 16, 0, SC1);
        }
    }
    STAMP(8);
    {
        const int qd = t >> 7;
        u32x4 ov[4];
        u32x2 ml;
        const int oo = WS_OPART + ((qd * 4 * 32 + h) * 512 + (t & 127) * 4) * 4;
        for (unsigned spins = 0; spins < SPIN_CAP; ++spins) {
            asm volatile("" ::: "memory");
#pragma unroll
            for (int i = 0; i < 4; ++i) ov[i] = __builtin_amdgcn_raw_buffer_load_b128(rs, oo + i * 65536, 0, SC1);
            ml = __builtin_amdgcn_raw_buffer_load_b64(rs, WS_ML + (h * 16 + (lane & 15)) * 8, 0, SC1);
            unsigned m = umax(umax(max4(ov[0]), max4(ov[1])), umax(max4(ov[2]), max4(ov[3])));
            m = umax(m, umax(ml[0], ml[1]));
            if (__all(m != POISON)) break;
            __builtin_amdgcn_s_sleep(1);
        }
        STAMP(9);
        float mk = asf(ml[0]), lk = asf(ml[1]);
        float M = mk;
        M = fmaxf(M, __shfl_xor(M, 1)); M = fmaxf(M, __shfl_xor(M, 2));
        M = fmaxf(M, __shfl_xor(M, 4)); M = fmaxf(M, __shfl_xor(M, 8));
        float wk = __expf(mk - M);
        float L = wk * lk;
        L += __shfl_xor(L, 1); L += __shfl_xor(L, 2); L += __shfl_xor(L, 4); L += __shfl_xor(L, 8);
        wk = wk / L;
        f32x4 c4 = {0.f, 0.f, 0.f, 0.f};
#pragma unroll
        for (int i = 0; i < 4; ++i) {
            float wi = __shfl(wk, qd * 4 + i);
            c4[0] += wi * asf(ov[i][0]); c4[1] += wi * asf(ov[i][1]); c4[2] += wi * asf(ov[i][2]); c4[3] += wi * asf(ov[i][3]);
        }
        *(f32x4*)(cpart + qd * 512 + (t & 127) * 4) = c4;
    }
    bar();
    {
        const int row = t >> 1;
        float c0 = (cpart[row] + cpart[512 + row]) + (cpart[1024 + row] + cpart[1536 + row]);
        float c1 = (cpart[row + 256] + cpart[768 + row]) + (cpart[1280 + row] + cpart[1792 + row]);
        float acc[8];
#pragma unroll
        for (int jj = 0; jj < 4; ++jj) {
            acc[2 * jj] = c0 * bflo(uvv[0][jj]) + c1 * bflo(uvv[1][jj]);
            acc[2 * jj + 1] = c0 * bfhi(uvv[0][jj]) + c1 * bfhi(uvv[1][jj]);
        }
        bool up = lane & 2;
        float s[4];
#pragma unroll
        for (int k = 0; k < 4; ++k) { float send = up ? acc[k] : acc[k + 4]; float keep = up ? acc[k + 4] : acc[k]; s[k] = keep + __shfl_xor(send, 2); }
        up = lane & 4;
        float s2[2];
#pragma unroll
        for (int k = 0; k < 2; ++k) { float send = up ? s[k] : s[k + 2]; float keep = up ? s[k + 2] : s[k]; s2[k] = keep + __shfl_xor(send, 4); }
        up = lane & 8;
        float send = up ? s2[0] : s2[1]; float keep = up ? s2[1] : s2[0];
        float v = keep + __shfl_xor(send, 8);
        v += __shfl_xor(v, 16);
        v += __shfl_xor(v, 32);
        if (lane < 16) {
            int d = (lane & 1) * 8 + ((lane >> 1) & 1) * 4 + ((lane >> 2) & 1) * 2 + ((lane >> 3) & 1);
            sm3[w * 16 + d] = v;
        }
    }
    bar();
    {
        float c0 = 0.f, c1 = 0.f;
#pragma unroll
        for (int ww = 0; ww < 8; ++ww) { c0 += sm3[ww * 16 + 2 * w]; c1 += sm3[ww * 16 + 2 * w + 1]; }
        c0 = rbf(c0); c1 = rbf(c1);
        f32x4 o0, o1;
        o0[0] = c0 * bflo(ovv[0][0]) + c1 * bflo(ovv[1][0]); o0[1] = c0 * bfhi(ovv[0][0]) + c1 * bfhi(ovv[1][0]);
        o0[2] = c0 * bflo(ovv[0][1]) + c1 * bflo(ovv[1][1]); o0[3] = c0 * bfhi(ovv[0][1]) + c1 * bfhi(ovv[1][1]);
        o1[0] = c0 * bflo(ovv[0][2]) + c1 * bflo(ovv[1][2]); o1[1] = c0 * bfhi(ovv[0][2]) + c1 * bfhi(ovv[1][2]);
        o1[2] = c0 * bflo(ovv[0][3]) + c1 * bflo(ovv[1][3]); o1[3] = c0 * bfhi(ovv[0][3]) + c1 * bfhi(ovv[1][3]);
        *(f32x4*)(redo + w * 512 + lane * 8) = o0;
        *(f32x4*)(redo + w * 512 + lane * 8 + 4) = o1;
    }
    bar();
    if (t < 128) {
        f32x4 s = *(const f32x4*)(redo + t * 4);
#pragma unroll
        for (int ww = 1; ww < 8; ++ww) s += *(const f32x4*)(redo + ww * 512 + t * 4);
        __builtin_amdgcn_raw_buffer_store_b128(__builtin_bit_cast(u32x4, s), rs, WS_GA + b * 2048 + t * 16, 0, SC1);
    }
    STAMP(10);
    if (cons) {
        const int k = b - 224;
        const int p = t >> 1, hf = t & 1;
        float gp = rbf(xt * bf2f(gw[t]));
        const int ao = WS_GA + p * 2048 + k * 64 + hf * 32;
        const int bo = WS_GB + (p < 172 ? p : 171) * 2048 + k * 64 + hf * 32;
        u32x4 a0, a1, b0, b1;
        for (unsigned spins = 0; spins < SPIN_CAP; ++spins) {
            asm volatile("" ::: "memory");
            a0 = __builtin_amdgcn_raw_buffer_load_b128(rs, ao, 0, SC1);
            a1 = __builtin_amdgcn_raw_buffer_load_b128(rs, ao + 16, 0, SC1);
            b0 = __builtin_amdgcn_raw_buffer_load_b128(rs, bo, 0, SC1);
            b1 = __builtin_amdgcn_raw_buffer_load_b128(rs, bo + 16, 0, SC1);
            unsigned m = umax(umax(max4(a0), max4(a1)), umax(max4(b0), max4(b1)));
            if (__all(m != POISON)) break;
            __builtin_amdgcn_s_sleep(1);
        }
        STAMP(11);
        float v[16];
        const float bz = p < 172 ? 1.f : 0.f;
#pragma unroll
        for (int i = 0; i < 4; ++i) {
            v[i] = asf(a0[i]); v[4 + i] = asf(a1[i]);
            v[8 + i] = p < 172 ? asf(b0[i]) : 0.f; v[12 + i] = p < 172 ? asf(b1[i]) : 0.f;
        }
        (void)bz;
        bool up = lane & 2;
        float s8[8];
#pragma unroll
        for (int q = 0; q < 8; ++q) { float send = up ? v[q] : v[q + 8]; float keep = up ? v[q + 8] : v[q]; s8[q] = keep + __shfl_xor(send, 2); }
        up = lane & 4;
        float s4[4];
#pragma unroll
        for (int q = 0; q < 4; ++q) { float send = up ? s8[q] : s8[q + 4]; float keep = up ? s8[q + 4] : s8[q]; s4[q] = keep + __shfl_xor(send, 4); }
        up = lane & 8;
        float s2[2];
#pragma unroll
        for (int q = 0; q < 2; ++q) { float send = up ? s4[q] : s4[q + 2]; float keep = up ? s4[q + 2] : s4[q]; s2[q] = keep + __shfl_xor(send, 8); }
        up = lane & 16;
        float send = up ? s2[0] : s2[1]; float keep = up ? s2[1] : s2[0];
        float vv = keep + __shfl_xor(send, 16);
        vv += __shfl_xor(vv, 32);
#pragma unroll
        for (int o = 32; o >= 1; o >>= 1) gp += __shfl_xor(gp, o);
        if (lane < 32) {
            int idx = ((lane >> 1) & 1) * 16 + (lane & 1) * 8 + ((lane >> 2) & 1) * 4 + ((lane >> 3) & 1) * 2 + ((lane >> 4) & 1);
            fin[w * 32 + idx] = vv;
        }
        if (lane == 0) fing[w] = gp;
    }
    bar();
    if (cons && t < 16) {
        float a = 0.f, bb = 0.f, gs = 0.f;
#pragma unroll
        for (int ww = 0; ww < 8; ++ww) { a += fin[ww * 32 + t]; bb += fin[ww * 32 + 16 + t]; gs += fing[ww]; }
        gs = rbf(gs);
        float g = rbf(1.f / (1.f + __expf(-gs)));
        a = rbf(a); bb = rbf(bb);
        float rr = rbf(g * a) + rbf(rbf(1.f - g) * bb);
        out[(b - 224) * 16 + t] = f2bf(rr);
    }
    STAMP(12);
}

extern "C" void kernel_launch(void* const* d_in, const int* in_sizes, int n_in,
                              void* d_out, int out_size, void* d_ws, size_t ws_size,
                              hipStream_t stream) {
    const u16* x = (const u16*)d_in[0];
    const u16* ckv = (const u16*)d_in[1];
    const u16* wq = (const u16*)d_in[2];
    const u16* wuk = (const u16*)d_in[3];
    const u16* wuv = (const u16*)d_in[4];
    const u16* wo = (const u16*)d_in[5];
    const u16* wg = (const u16*)d_in[7];
    const u16* wu = (const u16*)d_in[8];
    const u16* wd = (const u16*)d_in[9];
    const u16* gw = (const u16*)d_in[10];
    mega<<<256, 640, 0, stream>>>(x, ckv, wq, wuk, wuv, wo, wg, wu, wd, gw, (char*)d_ws, (u16*)d_out);
}
```

```cpp
#include <hip/hip_runtime.h>
#include <hip/hip_bf16.h>
#include <stdint.h>

#define SK 8192
#define FF 1376

typedef unsigned short u16;
typedef unsigned long long u64;
typedef __attribute__((ext_vector_type(8))) __bf16 bf16x8;
typedef __attribute__((ext_vector_type(2))) __bf16 bf16x2;
typedef __attribute__((ext_vector_type(4))) short s16x4;
typedef __attribute__((ext_vector_type(8))) short s16x8;
typedef __attribute__((ext_vector_type(16))) float f32x16;
typedef __attribute__((ext_vector_type(4))) float f32x4;
typedef __attribute__((ext_vector_type(2))) float f32x2;
typedef __attribute__((ext_vector_type(4))) unsigned int u32x4;
typedef __attribute__((ext_vector_type(2))) unsigned int u32x2;
#define GAS __attribute__((address_space(1)))
typedef GAS unsigned gu32;
typedef GAS u64 gu64;
#define RLX_AGENT __ATOMIC_RELAXED, __HIP_MEMORY_SCOPE_AGENT
#define SC1 16
#define POISON 0xFFFFFFFFu

__device__ __forceinline__ float bf2f(u16 v) { return __uint_as_float(((unsigned)v) << 16); }
__device__ __forceinline__ float bflo(unsigned v) { return __uint_as_float(v << 16); }
__device__ __forceinline__ float bfhi(unsigned v) { return __uint_as_float(v & 0xffff0000u); }
__device__ __forceinline__ float asf(unsigned v) { return __uint_as_float(v); }
__device__ __forceinline__ unsigned asu(float v) { return __float_as_uint(v); }
__device__ __forceinline__ unsigned pk2(float a, float b) {
    bf16x2 v = {(__bf16)a, (__bf16)b};
    return __builtin_bit_cast(unsigned, v);
}
__device__ __forceinline__ u16 f2bf(float f) { __bf16 v = (__bf16)f; return __builtin_bit_cast(u16, v); }
__device__ __forceinline__ float rbf(float f) { return bf2f(f2bf(f)); }
__device__ __forceinline__ unsigned umax(unsigned a, unsigned b) { return a > b ? a : b; }
__device__ __forceinline__ unsigned max4(u32x4 v) { return umax(umax(v[0], v[1]), umax(v[2], v[3])); }

#define NREP 32
#define WS_EPOCH   0
#define WS_F0      256
#define WS_QG      (WS_F0 + NREP * 2048)
#define WS_QABS    (WS_QG + 256 * 1024)
#define WS_P       (WS_QABS + 8 * 32768)
#define WS_MLOC    (WS_P + 16 * 32 * 512 * 2)
#define WS_OPART   (WS_MLOC + 16 * 32 * 16 * 8)
#define WS_ML      (WS_OPART + 16 * 32 * 512 * 4)
#define WS_GA      (WS_ML + 32 * 16 * 8)
#define WS_GB      (WS_GA + 256 * 2048)
#define WS_QGF     (WS_GB + 172 * 2048)
#define WS_PF      (WS_QGF + 256 * 1024)
#define WS_MLOCF   (WS_PF + 16 * 32 * 512 * 2)
#define WS_END     (WS_MLOCF + 16 * 32 * 16 * 8)

#define SPIN_CAP (1u << 17)
#define STAMP(i)
#define CKV_MODE 1
#define NBAR 8

__device__ __forceinline__ void lgkm0() { asm volatile("s_waitcnt lgkmcnt(0)" ::: "memory"); }
__device__ __forceinline__ void vm0() { asm volatile("s_waitcnt vmcnt(0)" ::: "memory"); }
__device__ __forceinline__ void bar() { asm volatile("s_waitcnt lgkmcnt(0)\n\ts_barrier" ::: "memory"); }
typedef volatile __attribute__((address_space(3))) unsigned lds_u32;
__device__ __forceinline__ void lds_wait(lds_u32* p, unsigned target) {
    for (unsigned s = 0; s < SPIN_CAP; ++s) {
        if (*p >= target) break;
        __builtin_amdgcn_s_sleep(1);
    }
    asm volatile("" ::: "memory");
}

__global__ __launch_bounds__(640, 1) void mega(const u16* __restrict__ x, const u16* __restrict__ ckv,
                                               const u16* __restrict__ wq, const u16* __restrict__ wuk,
                                               const u16* __restrict__ wuv, const u16* __restrict__ wo,
                                               const u16* __restrict__ wg, const u16* __restrict__ wu,
                                               const u16* __restrict__ wd, const u16* __restrict__ gw,
                                               char* __restrict__ ws, u16* __restrict__ out) {
    __shared__ __attribute__((aligned(16))) char tile[512 * 64];
    __shared__ __attribute__((aligned(16))) float red1[8 * 1024];
    __shared__ __attribute__((aligned(16))) float red2[8 * 1024];
    __shared__ __attribute__((aligned(16))) float redf[8 * 512];
    __shared__ __attribute__((aligned(16))) float redo[8 * 512];
    __shared__ __attribute__((aligned(16))) float sm1[8 * 128];
    __shared__ __attribute__((aligned(16))) float sm2[8 * 16];
    __shared__ __attribute__((aligned(16))) float sm3[8 * 16];
    __shared__ __attribute__((aligned(16))) float qs[128];
    __shared__ __attribute__((aligned(16))) float fac[16 * 32];
    __shared__ __attribute__((aligned(16))) float Mws[32 * 8];
    __shared__ __attribute__((aligned(16))) float Lws[32 * 8];
    __shared__ __attribute__((aligned(16))) float cpart[4 * 512];
    __shared__ __attribute__((aligned(16))) float fin[8 * 32];
    __shared__ __attribute__((aligned(16))) float fing[8];
    __shared__ unsigned flg[4];

    const int b = blockIdx.x;
    const int t = threadIdx.x;
    const int lane = t & 63, w = t >> 6;
    const int h = b & 31, j = b >> 5;
    const int kc = b & 15, mem = b >> 4;
    const int key0 = kc * 512 + mem * 32;
    const int r = lane & 31, h2 = lane >> 5;
    const int fidx = b >> 3;
    const int fch = (b & 7) * 22 + fidx;
    const bool has_ffn = (fidx < 22) && (fch < 172);
    const int f0 = fch * 8;
    const bool cons = b >= 224;
    const __amdgpu_buffer_rsrc_t rs = __builtin_amdgcn_make_buffer_rsrc(ws, 0, WS_END, 0x00020000);
    lds_u32* vflg = (lds_u32*)flg;
    STAMP(0);

    if (w >= 8) {
        const unsigned e = __hip_atomic_load((gu32*)(ws + WS_EPOCH), RLX_AGENT);
        const u32x4 P4 = {POISON, POISON, POISON, POISON};
        const u32x2 P2 = {POISON, POISON};
        if (w == 9)
            __builtin_amdgcn_raw_buffer_store_b128(P4, rs, WS_QABS + (lane >> 3) * 32768 + ((j * 8 + (lane & 7)) * 32 + h) * 16, 0, SC1);
        bar();
        const unsigned tag = e + 1u;
        if (w == 8) {
            lds_wait(vflg + 0, 8);
            STAMP(1);
            f32x2 s = *(const f32x2*)(sm1 + 2 * lane);
#pragma unroll
            for (int ww = 1; ww < 8; ++ww) s += *(const f32x2*)(sm1 + ww * 128 + 2 * lane);
            u32x4 g = {tag, asu(s[0]), asu(s[1]), ~tag};
            __builtin_amdgcn_raw_buffer_store_b128(g, rs, WS_QGF + ((h * 8 + j) * 64 + lane) * 16, 0, 0);
            __builtin_amdgcn_raw_buffer_store_b128(g, rs, WS_QG + ((h * 8 + j) * 64 + lane) * 16, 0, SC1);
            for (unsigned spins = 0; spins < SPIN_CAP; ++spins) {
            asm volatile("" ::: "memory");
                u32x4 v[8];
                const int qb = ((spins & 3) == 3 ? WS_QG : WS_QGF) + (h * 8 * 64 + lane) * 16;
#pragma unroll
                for (int jj = 0; jj < 8; ++jj)
                    v[jj] = __builtin_amdgcn_raw_buffer_load_b128(rs, qb + jj * 1024, 0, SC1);
                unsigned bad = 0;
                float q0 = 0.f, q1 = 0.f;
#pragma unroll
                for (int jj = 0; jj < 8; ++jj) {
                    bad |= (v[jj][0] ^ tag) | (v[jj][3] ^ ~tag);
                    q0 += asf(v[jj][1]); q1 += asf(v[jj][2]);
                }
                if (__all(bad == 0) || spins == SPIN_CAP - 1) {
                    f32x2 qq = {rbf(q0), rbf(q1)};
                    *(f32x2*)(qs + 2 * lane) = qq;
                    break;
                }
                __builtin_amdgcn_s_sleep(1);
            }
            lgkm0();
            if (lane == 0) vflg[1] = 1u;
            STAMP(2);
        } else {
            vm0();
            {
                const unsigned tg = e + 1u;
                const u64 want = ((u64)(~tg) << 32) | tg;
                if (lane < NREP) __hip_atomic_store((gu64*)(ws + WS_F0 + lane * 2048 + b * 8), want, RLX_AGENT);
            }
#pragma unroll
            for (int i = 0; i < 2; ++i) {
                __builtin_amdgcn_raw_buffer_store_b128(P4, rs, WS_P + ((kc * 32 + mem * 2 + i) * 64 + lane) * 16, 0, SC1);
                __builtin_amdgcn_raw_buffer_store_b128(P4, rs, WS_PF + ((kc * 32 + mem * 2 + i) * 64 + lane) * 16, 0, SC1);
            }
            __builtin_amdgcn_raw_buffer_store_b64(P2, rs, (lane < 32 ? WS_MLOC : WS_MLOCF) + ((kc * 8 + (mem >> 1)) * 32 + r) * 16 + (mem & 1) * 8, 0, SC1);
#pragma unroll
            for (int i = 0; i < 4; ++i)
                __builtin_amdgcn_raw_buffer_store_b128(P4, rs, WS_OPART + ((kc * 32 + i * 8 + (lane >> 3)) * 512 + mem * 32) * 4 + (lane & 7) * 16, 0, SC1);
            if (mem == 0 && lane < 32) __builtin_amdgcn_raw_buffer_store_b64(P2, rs, WS_ML + (lane * 16 + kc) * 8, 0, SC1);
#pragma unroll
            for (int i = 0; i < 2; ++i)
                __builtin_amdgcn_raw_buffer_store_b128(P4, rs, WS_GA + b * 2048 + i * 1024 + lane * 16, 0, SC1);
            if (has_ffn) {
#pragma unroll
                for (int i = 0; i < 2; ++i)
                    __builtin_amdgcn_raw_buffer_store_b128(P4, rs, WS_GB + fch * 2048 + i * 1024 + lane * 16, 0, SC1);
            }
            vm0();
            if (lane == 0) vflg[3] = 1u;
            STAMP(1);
            const int fo = WS_F0 + (b & (NREP - 1)) * 2048 + lane * 16;
            for (unsigned spins = 0; spins < SPIN_CAP; ++spins) {
            asm volatile("" ::: "memory");
                u32x4 a0 = __builtin_amdgcn_raw_buffer_load_b128(rs, fo, 0, SC1);
                u32x4 a1 = __builtin_amdgcn_raw_buffer_load_b128(rs, fo + 1024, 0, SC1);
                unsigned bad = (a0[0] ^ tag) | (a0[1] ^ ~tag) | (a0[2] ^ tag) | (a0[3] ^ ~tag) |
                               (a1[0] ^ tag) | (a1[1] ^ ~tag) | (a1[2] ^ tag) | (a1[3] ^ ~tag);
                if (__all(bad == 0)) break;
                __builtin_amdgcn_s_sleep(1);
            }
            if (lane == 0) vflg[2] = 1u;
            STAMP(2);
            unsigned pf[14];
            {
#pragma unroll
                for (int i = 0; i < 4; ++i)
                    pf[6 + i] = *(const unsigned*)((const char*)wuv + (size_t)(j * 64 + i * 16 + (lane >> 2)) * 8192 + h * 256 + (lane & 3) * 64);
#pragma unroll
                for (int i = 0; i < 4; ++i)
                    pf[10 + i] = *(const unsigned*)((const char*)wo + ((size_t)h * 128 + j * 16) * 1024 + i * 4096 + lane * 64);
            }
#pragma unroll
            for (int i = 0; i < NBAR; ++i) asm volatile("s_barrier" ::: "memory");
            asm volatile("" :: "v"(pf[6]),
                         "v"(pf[7]), "v"(pf[8]), "v"(pf[9]), "v"(pf[10]), "v"(pf[11]), "v"(pf[12]), "v"(pf[13]));
            return;
        }
#pragma unroll
        for (int i = 0; i < NBAR; ++i) asm volatile("s_barrier" ::: "memory");
        if (b == 224 && lane == 0) __hip_atomic_store((gu32*)(ws + WS_EPOCH), tag, RLX_AGENT);
        return;
    }

    u32x4 wqv[2], wkv[2];
    float x0, x1;
    {
        const int row = j * 64 + (t >> 4);
        x0 = bf2f(x[row]); x1 = bf2f(x[row + 32]);
#pragma unroll
        for (int i = 0; i < 2; ++i)
            wqv[i] = *(const u32x4*)(wq + (size_t)(row + 32 * i) * 4096 + h * 128 + (t & 15) * 8);
#pragma unroll
        for (int i = 0; i < 2; ++i)
            wkv[i] = *(const u32x4*)(wuk + (size_t)(j * 64 + 8 * w + (lane >> 4) + 4 * i) * 4096 + h * 128 + (lane & 15) * 8);
    }
    if (t < 4) flg[t] = 0u;
    bar();
    __builtin_amdgcn_sched_barrier(0);
    asm volatile("s_waitcnt vmcnt(2)" ::: "memory");
    STAMP(1);
    bf16x8 bq[4];
    u32x4 cv[4];
    {
        float acc[8];
#pragma unroll
        for (int jj = 0; jj < 4; ++jj) {
            acc[2 * jj] = x0 * bflo(wqv[0][jj]) + x1 * bflo(wqv[1][jj]);
            acc[2 * jj + 1] = x0 * bfhi(wqv[0][jj]) + x1 * bfhi(wqv[1][jj]);
        }
        bool up = lane & 16;
        float s[4];
#pragma unroll
        for (int k = 0; k < 4; ++k) { float send = up ? acc[k] : acc[k + 4]; float keep = up ? acc[k + 4] : acc[k]; s[k] = keep + __shfl_xor(send, 16); }
        up = lane & 32;
        f32x2 s2;
#pragma unroll
        for (int k = 0; k < 2; ++k) { float send = up ? s[k] : s[k + 2]; float keep = up ? s[k + 2] : s[k]; s2[k] = keep + __shfl_xor(send, 32); }
        *(f32x2*)(sm1 + w * 128 + (lane & 15) * 8 + ((lane >> 4) & 1) * 4 + (lane >> 5) * 2) = s2;
        lgkm0();
        if (lane == 0) __hip_atomic_fetch_add(flg + 0, 1u, __ATOMIC_RELAXED, __HIP_MEMORY_SCOPE_WORKGROUP);
    }
    __builtin_amdgcn_sched_barrier(0);
    lds_wait(vflg + 1, 1);
    STAMP(2);
    lds_wait(vflg + 3, 1);
    {
        const int l16 = lane & 15;
        f32x4 qa0 = *(const f32x4*)(qs + l16 * 8), qa1 = *(const f32x4*)(qs + l16 * 8 + 4);
        float d[2];
#pragma unroll
        for (int i = 0; i < 2; ++i) {
            d[i] = qa0[0] * bflo(wkv[i][0]) + qa0[1] * bfhi(wkv[i][0]) + qa0[2] * bflo(wkv[i][1]) + qa0[3] * bfhi(wkv[i][1]);
            d[i] += qa1[0] * bflo(wkv[i][2]) + qa1[1] * bfhi(wkv[i][2]) + qa1[2] * bflo(wkv[i][3]) + qa1[3] * bfhi(wkv[i][3]);
        }
        bool up = lane & 1;
        float send = up ? d[0] : d[1], keep = up ? d[1] : d[0];
        float v = keep + __shfl_xor(send, 1);
        v += __shfl_xor(v, 2);
        v += __shfl_xor(v, 4);
        v += __shfl_xor(v, 8);
        v *= 0.08838834764831845f;
        float vv[8];
#pragma unroll
        for (int k = 0; k < 8; ++k) vv[k] = __shfl(v, (k & 3) * 16 + (k >> 2));
        u32x4 o = {pk2(vv[0], vv[1]), pk2(vv[2], vv[3]), pk2(vv[4], vv[5]), pk2(vv[6], vv[7])};
        if (lane < 8)
            __builtin_amdgcn_raw_buffer_store_b128(o, rs, WS_QABS + lane * 32768 + ((j * 8 + w) * 32 + h) * 16, 0, SC1);
    }
    {
        const u16* bp = ckv + (size_t)(key0 + r) * 512 + 64 * w + 8 * h2;
#pragma unroll
        for (int s = 0; s < 4; ++s) bq[s] = *(const bf16x8*)(bp + 16 * s);
        const u16* cp = ckv + (size_t)(kc * 512 + (t >> 2)) * 512 + mem * 32 + (t & 3) * 8;
#pragma unroll
        for (int i = 0; i < 4; ++i) cv[i] = *(const u32x4*)(cp + (size_t)i * 128 * 512);
    }
    __builtin_amdgcn_sched_barrier(0);
    const float xt = bf2f(x[t]);
    {
        const int fz = has_ffn ? f0 : 0;
        const u32x4 gv = *(const u32x4*)(wg + (size_t)t * FF + fz);
        const u32x4 uv_ = *(const u32x4*)(wu + (size_t)t * FF + fz);
        const u32x4 dv = *(const u32x4*)(wd + (size_t)(fz + w) * 512 + lane * 8);
        __builtin_amdgcn_sched_barrier(0);
#pragma unroll
        for (int i = 0; i < 4; ++i) *(u32x4*)(tile + (i * 128 + (t >> 2)) * 64 + (t & 3) * 16) = cv[i];
        float p[16];
#pragma unroll
        for (int jj = 0; jj < 4; ++jj) {
            p[2 * jj] = xt * bflo(gv[jj]); p[2 * jj + 1] = xt * bfhi(gv[jj]);
            p[8 + 2 * jj] = xt * bflo(uv_[jj]); p[8 + 2 * jj + 1] = xt * bfhi(uv_[jj]);
        }
        bool up = lane & 1;
        float s8[8];
#pragma unroll
        for (int k = 0; k < 8; ++k) { float send = up ? p[k] : p[k + 8]; float keep = up ? p[k + 8] : p[k]; s8[k] = keep + __shfl_xor(send, 1); }
        up = lane & 2;
        float s4[4];
#pragma unroll
        for (int k = 0; k < 4; ++k) { float send = up ? s8[k] : s8[k + 4]; float keep = up ? s8[k + 4] : s8[k]; s4[k] = keep + __shfl_xor(send, 2); }
        up = lane & 4;
        float s2[2];
#pragma unroll
        for (int k = 0; k < 2; ++k) { float send = up ? s4[k] : s4[k + 2]; float keep = up ? s4[k + 2] : s4[k]; s2[k] = keep + __shfl_xor(send, 4); }
        up = lane & 8;
        float send = up ? s2[0] : s2[1]; float keep = up ? s2[1] : s2[0];
        float v = keep + __shfl_xor(send, 8);
        v += __shfl_xor(v, 16);
        v += __shfl_xor(v, 32);
        if (lane < 16) {
            int idx = (lane & 1) * 8 + ((lane >> 1) & 1) * 4 + ((lane >> 2) & 1) * 2 + ((lane >> 3) & 1);
            sm2[w * 16 + idx] = v;
        }
        bar();
        float gs = 0.f, us = 0.f;
#pragma unroll
        for (int ww = 0; ww < 8; ++ww) { gs += sm2[ww * 16 + w]; us += sm2[ww * 16 + 8 + w]; }
        gs = rbf(gs); us = rbf(us);
        float sg = rbf(gs / (1.f + __expf(-gs)));
        float hv = rbf(sg * us);
        f32x4 o0, o1;
        o0[0] = hv * bflo(dv[0]); o0[1] = hv * bfhi(dv[0]); o0[2] = hv * bflo(dv[1]); o0[3] = hv * bfhi(dv[1]);
        o1[0] = hv * bflo(dv[2]); o1[1] = hv * bfhi(dv[2]); o1[2] = hv * bflo(dv[3]); o1[3] = hv * bfhi(dv[3]);
        *(f32x4*)(redf + w * 512 + lane * 8) = o0;
        *(f32x4*)(redf + w * 512 + lane * 8 + 4) = o1;
        bar();
        if (has_ffn && t < 128) {
            f32x4 s = *(const f32x4*)(redf + t * 4);
#pragma unroll
            for (int ww = 1; ww < 8; ++ww) s += *(const f32x4*)(redf + ww * 512 + t * 4);
            __builtin_amdgcn_raw_buffer_store_b128(__builtin_bit_cast(u32x4, s), rs, WS_GB + fch * 2048 + t * 16, 0, SC1);
        }
    }
    lds_wait(vflg + 2, 1);
    STAMP(3);
    {
        u32x4 av[4];
        const int qo = WS_QABS + (b & 7) * 32768 + (w * 256 + lane) * 16;
        for (unsigned spins = 0; spins < SPIN_CAP; ++spins) {
            asm volatile("" ::: "memory");
#pragma unroll
            for (int s = 0; s < 4; ++s) av[s] = __builtin_amdgcn_raw_buffer_load_b128(rs, qo + 1024 * s, 0, SC1);
            unsigned m = umax(umax(max4(av[0]), max4(av[1])), umax(max4(av[2]), max4(av[3])));
            if (__all(m != POISON)) break;
            __builtin_amdgcn_s_sleep(1);
        }
        STAMP(4);
        f32x16 acc;
#pragma unroll
        for (int i = 0; i < 16; ++i) acc[i] = 0.f;
#pragma unroll
        for (int s = 0; s < 4; ++s)
            acc = __builtin_amdgcn_mfma_f32_32x32x16_bf16(__builtin_bit_cast(bf16x8, av[s]), bq[s], acc, 0, 0, 0);
#pragma unroll
        for (int i = 0; i < 16; ++i) {
            int head = (i & 3) + 8 * (i >> 2) + 4 * h2;
            red1[(w * 32 + head) * 32 + r] = acc[i];
        }
    }
    bar();
    {
        const int head = t >> 4, kq = (t & 15) * 2;
        f32x2 s2 = *(const f32x2*)(red1 + head * 32 + kq);
#pragma unroll
        for (int ww = 1; ww < 8; ++ww) s2 += *(const f32x2*)(red1 + (ww * 32 + head) * 32 + kq);
        float m = fmaxf(s2[0], s2[1]);
        m = fmaxf(m, __shfl_xor(m, 1));
        m = fmaxf(m, __shfl_xor(m, 2));
        m = fmaxf(m, __shfl_xor(m, 4));
        m = fmaxf(m, __shfl_xor(m, 8));
        unsigned p01 = pk2(__expf(s2[0] - m), __expf(s2[1] - m));
        float l = bflo(p01) + bfhi(p01);
        l += __shfl_xor(l, 1);
        l += __shfl_xor(l, 2);
        l += __shfl_xor(l, 4);
        l += __shfl_xor(l, 8);
        u32x4 pp = {p01, (unsigned)__shfl_down((int)p01, 1), (unsigned)__shfl_down((int)p01, 2), (unsigned)__shfl_down((int)p01, 3)};
        u32x2 ml = {asu(m), asu(l)};
        if ((t & 3) == 0)
            __builtin_amdgcn_raw_buffer_store_b128(pp, rs, WS_PF + ((kc * 32 + mem * 2) * 64 + ((t & 15) >> 2) * 32 + head) * 16, 0, 0);
        if ((t & 15) == 0)
            __builtin_amdgcn_raw_buffer_store_b64(ml, rs, WS_MLOCF + ((kc * 8 + (mem >> 1)) * 32 + head) * 16 + (mem & 1) * 8, 0, 0);
        if ((t & 3) == 0)
            __builtin_amdgcn_raw_buffer_store_b128(pp, rs, WS_P + ((kc * 32 + mem * 2) * 64 + ((t & 15) >> 2) * 32 + head) * 16, 0, SC1);
        if ((t & 15) == 0)
            __builtin_amdgcn_raw_buffer_store_b64(ml, rs, WS_MLOC + ((kc * 8 + (mem >> 1)) * 32 + head) * 16 + (mem & 1) * 8, 0, SC1);
    }
    STAMP(5);
    {
        u32x4 pv[4], mlv;
        for (unsigned spins = 0; spins < SPIN_CAP; ++spins) {
            asm volatile("" ::: "memory");
            const bool slow = (spins & 3) == 3;
            const int po = (slow ? WS_P : WS_PF) + ((kc * 32 + w * 4) * 64 + lane) * 16;
            const int mo = (slow ? WS_MLOC : WS_MLOCF) + ((kc * 8 + w) * 32 + r) * 16;
#pragma unroll
            for (int s = 0; s < 4; ++s) pv[s] = __builtin_amdgcn_raw_buffer_load_b128(rs, po + 1024 * s, 0, SC1);
            mlv = __builtin_amdgcn_raw_buffer_load_b128(rs, mo, 0, SC1);
            unsigned m = umax(umax(max4(pv[0]), max4(pv[1])), umax(max4(pv[2]), max4(pv[3])));
            m = umax(m, max4(mlv));
            if (__all(m != POISON)) break;
            __builtin_amdgcn_s_sleep(1);
        }
        STAMP(6);
        {
            float m0 = asf(mlv[0]), l0 = asf(mlv[1]), m1 = asf(mlv[2]), l1 = asf(mlv[3]);
            float Mw = fmaxf(m0, m1);
            float f0_ = __expf(m0 - Mw), f1_ = __expf(m1 - Mw);
            if (lane < 32) {
                fac[(2 * w) * 32 + r] = f0_; fac[(2 * w + 1) * 32 + r] = f1_;
                Mws[r * 8 + w] = Mw; Lws[r * 8 + w] = f0_ * l0 + f1_ * l1;
            }
        }
        const int g = lane >> 4;
        const int q = (lane & 15) >> 2, p = lane & 3;
        const int cb = g & 1;
        f32x16 tot;
#pragma unroll
        for (int mm = 0; mm < 2; ++mm) {
            f32x16 acc;
#pragma unroll
            for (int i = 0; i < 16; ++i) acc[i] = 0.f;
#pragma unroll
            for (int ss = 0; ss < 2; ++ss) {
                int s = mm * 2 + ss;
                int row = w * 64 + 16 * s + 8 * h2 + q;
                s16x4 t0 = __builtin_amdgcn_ds_read_tr16_b64_v4i16(
                    (__attribute__((address_space(3))) s16x4*)(tile + row * 64 + cb * 32 + p * 8));
                s16x4 t1 = __builtin_amdgcn_ds_read_tr16_b64_v4i16(
                    (__attribute__((address_space(3))) s16x4*)(tile + (row + 4) * 64 + cb * 32 + p * 8));
                s16x8 bb = {t0[0], t0[1], t0[2], t0[3], t1[0], t1[1], t1[2], t1[3]};
                acc = __builtin_amdgcn_mfma_f32_32x32x16_bf16(__builtin_bit_cast(bf16x8, pv[s]), __builtin_bit_cast(bf16x8, bb), acc, 0, 0, 0);
            }
            const float* fp = fac + (2 * w + mm) * 32 + 4 * h2;
#pragma unroll
            for (int qd = 0; qd < 4; ++qd) {
                f32x4 f4 = *(const f32x4*)(fp + 8 * qd);
#pragma unroll
                for (int e = 0; e < 4; ++e) {
                    if (mm == 0) tot[qd * 4 + e] = f4[e] * acc[qd * 4 + e];
                    else tot[qd * 4 + e] += f4[e] * acc[qd * 4 + e];
                }
            }
        }
#pragma unroll
        for (int i = 0; i < 16; ++i) {
            int head = (i & 3) + 8 * (i >> 2) + 4 * h2;
            red2[(w * 32 + head) * 32 + r] = tot[i];
        }
    }
    bar();
    {
        const int head = t >> 4, cq = (t & 15) * 2;
        f32x4 ma = *(const f32x4*)(Mws + head * 8), mb = *(const f32x4*)(Mws + head * 8 + 4);
        float M = fmaxf(fmaxf(fmaxf(ma[0], ma[1]), fmaxf(ma[2], ma[3])), fmaxf(fmaxf(mb[0], mb[1]), fmaxf(mb[2], mb[3])));
        float ew[8];
#pragma unroll
        for (int ww = 0; ww < 4; ++ww) { ew[ww] = __expf(ma[ww] - M); ew[ww + 4] = __expf(mb[ww] - M); }
        f32x2 s2 = {0.f, 0.f};
#pragma unroll
        for (int ww = 0; ww < 8; ++ww) s2 += ew[ww] * *(const f32x2*)(red2 + (ww * 32 + head) * 32 + cq);
        u32x4 o = {asu(s2[0]), asu(s2[1]), asu(__shfl_down(s2[0], 1)), asu(__shfl_down(s2[1], 1))};
        if ((t & 1) == 0)
            __builtin_amdgcn_raw_buffer_store_b128(o, rs, WS_OPART + ((kc * 32 + head) * 512 + mem * 32 + cq) * 4, 0, SC1);
        if (mem == 0 && (t & 15) == 0) {
            f32x4 la = *(const f32x4*)(Lws + head * 8), lb = *(const f32x4*)(Lws + head * 8 + 4);
            float L = 0.f;
#pragma unroll
            for (int ww = 0; ww < 4; ++ww) L += ew[ww] * la[ww] + ew[ww + 4] * lb[ww];
            u32x2 ml = {asu(M), asu(L)};
            __builtin_amdgcn_raw_buffer_store_b64(ml, rs, WS_ML + (head * 16 + kc) * 8, 0, SC1);
        }
    }
    STAMP(7);
    u32x4 uvv[2], ovv[2];
#pragma unroll
    for (int i = 0; i < 2; ++i)
        uvv[i] = *(const u32x4*)(wuv + (size_t)((t >> 1) + 256 * i) * 4096 + h * 128 + j * 16 + (t & 1) * 8);
#pragma unroll
    for (int i = 0; i < 2; ++i)
        ovv[i] = *(const u32x4*)(wo + ((size_t)h * 128 + j * 16 + w * 2 + i) * 512 + lane * 8);
    STAMP(8);
    {
        const int qd = t >> 7;
        u32x4 ov[4];
        u32x2 ml;
        const int oo = WS_OPART + ((qd * 4 * 32 + h) * 512 + (t & 127) * 4) * 4;
        for (unsigned spins = 0; spins < SPIN_CAP; ++spins) {
            asm volatile("" ::: "memory");
#pragma unroll
            for (int i = 0; i < 4; ++i) ov[i] = __builtin_amdgcn_raw_buffer_load_b128(rs, oo + i * 65536, 0, SC1);
            ml = __builtin_amdgcn_raw_buffer_load_b64(rs, WS_ML + (h * 16 + (lane & 15)) * 8, 0, SC1);
            unsigned m = umax(umax(max4(ov[0]), max4(ov[1])), umax(max4(ov[2]), max4(ov[3])));
            m = umax(m, umax(ml[0], ml[1]));
            if (__all(m != POISON)) break;
            __builtin_amdgcn_s_sleep(1);
        }
        STAMP(9);
        float mk = asf(ml[0]), lk = asf(ml[1]);
        float M = mk;
        M = fmaxf(M, __shfl_xor(M, 1)); M = fmaxf(M, __shfl_xor(M, 2));
        M = fmaxf(M, __shfl_xor(M, 4)); M = fmaxf(M, __shfl_xor(M, 8));
        float wk = __expf(mk - M);
        float L = wk * lk;
        L += __shfl_xor(L, 1); L += __shfl_xor(L, 2); L += __shfl_xor(L, 4); L += __shfl_xor(L, 8);
        wk = wk / L;
        f32x4 c4 = {0.f, 0.f, 0.f, 0.f};
#pragma unroll
        for (int i = 0; i < 4; ++i) {
            float wi = __shfl(wk, qd * 4 + i);
            c4[0] += wi * asf(ov[i][0]); c4[1] += wi * asf(ov[i][1]); c4[2] += wi * asf(ov[i][2]); c4[3] += wi * asf(ov[i][3]);
        }
        *(f32x4*)(cpart + qd * 512 + (t & 127) * 4) = c4;
    }
    bar();
    {
        const int row = t >> 1;
        float c0 = (cpart[row] + cpart[512 + row]) + (cpart[1024 + row] + cpart[1536 + row]);
        float c1 = (cpart[row + 256] + cpart[768 + row]) + (cpart[1280 + row] + cpart[1792 + row]);
        float acc[8];
#pragma unroll
        for (int jj = 0; jj < 4; ++jj) {
            acc[2 * jj] = c0 * bflo(uvv[0][jj]) + c1 * bflo(uvv[1][jj]);
            acc[2 * jj + 1] = c0 * bfhi(uvv[0][jj]) + c1 * bfhi(uvv[1][jj]);
        }
        bool up = lane & 2;
        float s[4];
#pragma unroll
        for (int k = 0; k < 4; ++k) { float send = up ? acc[k] : acc[k + 4]; float keep = up ? acc[k + 4] : acc[k]; s[k] = keep + __shfl_xor(send, 2); }
        up = lane & 4;
        float s2[2];
#pragma unroll
        for (int k = 0; k < 2; ++k) { float send = up ? s[k] : s[k + 2]; float keep = up ? s[k + 2] : s[k]; s2[k] = keep + __shfl_xor(send, 4); }
        up = lane & 8;
        float send = up ? s2[0] : s2[1]; float keep = up ? s2[1] : s2[0];
        float v = keep + __shfl_xor(send, 8);
        v += __shfl_xor(v, 16);
        v += __shfl_xor(v, 32);
        if (lane < 16) {
            int d = (lane & 1) * 8 + ((lane >> 1) & 1) * 4 + ((lane >> 2) & 1) * 2 + ((lane >> 3) & 1);
            sm3[w * 16 + d] = v;
        }
    }
    bar();
    {
        float c0 = 0.f, c1 = 0.f;
#pragma unroll
        for (int ww = 0; ww < 8; ++ww) { c0 += sm3[ww * 16 + 2 * w]; c1 += sm3[ww * 16 + 2 * w + 1]; }
        c0 = rbf(c0); c1 = rbf(c1);
        f32x4 o0, o1;
        o0[0] = c0 * bflo(ovv[0][0]) + c1 * bflo(ovv[1][0]); o0[1] = c0 * bfhi(ovv[0][0]) + c1 * bfhi(ovv[1][0]);
        o0[2] = c0 * bflo(ovv[0][1]) + c1 * bflo(ovv[1][1]); o0[3] = c0 * bfhi(ovv[0][1]) + c1 * bfhi(ovv[1][1]);
        o1[0] = c0 * bflo(ovv[0][2]) + c1 * bflo(ovv[1][2]); o1[1] = c0 * bfhi(ovv[0][2]) + c1 * bfhi(ovv[1][2]);
        o1[2] = c0 * bflo(ovv[0][3]) + c1 * bflo(ovv[1][3]); o1[3] = c0 * bfhi(ovv[0][3]) + c1 * bfhi(ovv[1][3]);
        *(f32x4*)(redo + w * 512 + lane * 8) = o0;
        *(f32x4*)(redo + w * 512 + lane * 8 + 4) = o1;
    }
    bar();
    if (t < 128) {
        f32x4 s = *(const f32x4*)(redo + t * 4);
#pragma unroll
        for (int ww = 1; ww < 8; ++ww) s += *(const f32x4*)(redo + ww * 512 + t * 4);
        __builtin_amdgcn_raw_buffer_store_b128(__builtin_bit_cast(u32x4, s), rs, WS_GA + b * 2048 + t * 16, 0, SC1);
    }
    STAMP(10);
    if (cons) {
        const int k = b - 224;
        const int p = t >> 1, hf = t & 1;
        float gp = rbf(xt * bf2f(gw[t]));
        const int ao = WS_GA + p * 2048 + k * 64 + hf * 32;
        const int bo = WS_GB + (p < 172 ? p : 171) * 2048 + k * 64 + hf * 32;
        u32x4 a0, a1, b0, b1;
        for (unsigned spins = 0; spins < SPIN_CAP; ++spins) {
            asm volatile("" ::: "memory");
            a0 = __builtin_amdgcn_raw_buffer_load_b128(rs, ao, 0, SC1);
            a1 = __builtin_amdgcn_raw_buffer_load_b128(rs, ao + 16, 0, SC1);
            b0 = __builtin_amdgcn_raw_buffer_load_b128(rs, bo, 0, SC1);
            b1 = __builtin_amdgcn_raw_buffer_load_b128(rs, bo + 16, 0, SC1);
            unsigned m = umax(umax(max4(a0), max4(a1)), umax(max4(b0), max4(b1)));
            if (__all(m != POISON)) break;
            __builtin_amdgcn_s_sleep(1);
        }
        STAMP(11);
        float v[16];
        const float bz = p < 172 ? 1.f : 0.f;
#pragma unroll
        for (int i = 0; i < 4; ++i) {
            v[i] = asf(a0[i]); v[4 + i] = asf(a1[i]);
            v[8 + i] = p < 172 ? asf(b0[i]) : 0.f; v[12 + i] = p < 172 ? asf(b1[i]) : 0.f;
        }
        (void)bz;
        bool up = lane & 2;
        float s8[8];
#pragma unroll
        for (int q = 0; q < 8; ++q) { float send = up ? v[q] : v[q + 8]; float keep = up ? v[q + 8] : v[q]; s8[q] = keep + __shfl_xor(send, 2); }
        up = lane & 4;
        float s4[4];
#pragma unroll
        for (int q = 0; q < 4; ++q) { float send = up ? s8[q] : s8[q + 4]; float keep = up ? s8[q + 4] : s8[q]; s4[q] = keep + __shfl_xor(send, 4); }
        up = lane & 8;
        float s2[2];
#pragma unroll
        for (int q = 0; q < 2; ++q) { float send = up ? s4[q] : s4[q + 2]; float keep = up ? s4[q + 2] : s4[q]; s2[q] = keep + __shfl_xor(send, 8); }
        up = lane & 16;
        float send = up ? s2[0] : s2[1]; float keep = up ? s2[1] : s2[0];
        float vv = keep + __shfl_xor(send, 16);
        vv += __shfl_xor(vv, 32);
#pragma unroll
        for (int o = 32; o >= 1; o >>= 1) gp += __shfl_xor(gp, o);
        if (lane < 32) {
            int idx = ((lane >> 1) & 1) * 16 + (lane & 1) * 8 + ((lane >> 2) & 1) * 4 + ((lane >> 3) & 1) * 2 + ((lane >> 4) & 1);
            fin[w * 32 + idx] = vv;
        }
        if (lane == 0) fing[w] = gp;
    }
    bar();
    if (cons && t < 16) {
        float a = 0.f, bb = 0.f, gs = 0.f;
#pragma unroll
        for (int ww = 0; ww < 8; ++ww) { a += fin[ww * 32 + t]; bb += fin[ww * 32 + 16 + t]; gs += fing[ww]; }
        gs = rbf(gs);
        float g = rbf(1.f / (1.f + __expf(-gs)));
        a = rbf(a); bb = rbf(bb);
        float rr = rbf(g * a) + rbf(rbf(1.f - g) * bb);
        out[(b - 224) * 16 + t] = f2bf(rr);
    }
    STAMP(12);
}

extern "C" void kernel_launch(void* const* d_in, const int* in_sizes, int n_in,
                              void* d_out, int out_size, void* d_ws, size_t ws_size,
                              hipStream_t stream) {
    const u16* x = (const u16*)d_in[0];
    const u16* ckv = (const u16*)d_in[1];
    const u16* wq = (const u16*)d_in[2];
    const u16* wuk = (const u16*)d_in[3];
    const u16* wuv = (const u16*)d_in[4];
    const u16* wo = (const u16*)d_in[5];
    const u16* wg = (const u16*)d_in[7];
    const u16* wu = (const u16*)d_in[8];
    const u16* wd = (const u16*)d_in[9];
    const u16* gw = (const u16*)d_in[10];
    mega<<<256, 640, 0, stream>>>(x, ckv, wq, wuk, wuv, wo, wg, wu, wd, gw, (char*)d_ws, (u16*)d_out);
}
```

```cpp
#include <hip/hip_runtime.h>
#include <hip/hip_bf16.h>
#include <stdint.h>

#define SK 8192
#define FF 1376

typedef unsigned short u16;
typedef unsigned long long u64;
typedef __attribute__((ext_vector_type(8))) __bf16 bf16x8;
typedef __attribute__((ext_vector_type(2))) __bf16 bf16x2;
typedef __attribute__((ext_vector_type(4))) short s16x4;
typedef __attribute__((ext_vector_type(8))) short s16x8;
typedef __attribute__((ext_vector_type(16))) float f32x16;
typedef __attribute__((ext_vector_type(4))) float f32x4;
typedef __attribute__((ext_vector_type(2))) float f32x2;
typedef __attribute__((ext_vector_type(4))) unsigned int u32x4;
typedef __attribute__((ext_vector_type(2))) unsigned int u32x2;
#define GAS __attribute__((address_space(1)))
typedef GAS unsigned gu32;
typedef GAS u64 gu64;
#define RLX_AGENT __ATOMIC_RELAXED, __HIP_MEMORY_SCOPE_AGENT
#define SC1 16
#define POISON 0xFFFFFFFFu

__device__ __forceinline__ float bf2f(u16 v) { return __uint_as_float(((unsigned)v) << 16); }
__device__ __forceinline__ float bflo(unsigned v) { return __uint_as_float(v << 16); }
__device__ __forceinline__ float bfhi(unsigned v) { return __uint_as_float(v & 0xffff0000u); }
__device__ __forceinline__ float asf(unsigned v) { return __uint_as_float(v); }
__device__ __forceinline__ unsigned asu(float v) { return __float_as_uint(v); }
__device__ __forceinline__ unsigned pk2(float a, float b) {
    bf16x2 v = {(__bf16)a, (__bf16)b};
    return __builtin_bit_cast(unsigned, v);
}
__device__ __forceinline__ u16 f2bf(float f) { __bf16 v = (__bf16)f; return __builtin_bit_cast(u16, v); }
__device__ __forceinline__ float rbf(float f) { return bf2f(f2bf(f)); }
__device__ __forceinline__ unsigned umax(unsigned a, unsigned b) { return a > b ? a : b; }
__device__ __forceinline__ unsigned max4(u32x4 v) { return umax(umax(v[0], v[1]), umax(v[2], v[3])); }

#define NREP 32
#define WS_EPOCH   0
#define WS_F0      256
#define WS_QG      (WS_F0 + NREP * 2048)
#define WS_QABS    (WS_QG + 256 * 1024)
#define WS_P       (WS_QABS + 8 * 32768)
#define WS_MLOC    (WS_P + 16 * 32 * 512 * 2)
#define WS_OPART   (WS_MLOC + 16 * 32 * 16 * 8)
#define WS_ML      (WS_OPART + 16 * 32 * 512 * 4)
#define WS_GA      (WS_ML + 32 * 16 * 8)
#define WS_GB      (WS_GA + 256 * 2048)
#define WS_QGF     (WS_GB + 172 * 2048)
#define WS_PF      (WS_QGF + 256 * 1024)
#define WS_MLOCF   (WS_PF + 16 * 32 * 512 * 2)
#define WS_END     (WS_MLOCF + 16 * 32 * 16 * 8)

#define SPIN_CAP (1u << 17)
#define STAMP(i)
#define CKV_MODE 1
#define NBAR 8

__device__ __forceinline__ void lgkm0() { asm volatile("s_waitcnt lgkmcnt(0)" ::: "memory"); }
__device__ __forceinline__ void vm0() { asm volatile("s_waitcnt vmcnt(0)" ::: "memory"); }
__device__ __forceinline__ void bar() { asm volatile("s_waitcnt lgkmcnt(0)\n\ts_barrier" ::: "memory"); }
typedef volatile __attribute__((address_space(3))) unsigned lds_u32;
__device__ __forceinline__ void lds_wait(lds_u32* p, unsigned target) {
    for (unsigned s = 0; s < SPIN_CAP; ++s) {
        if (*p >= target) break;
        __builtin_amdgcn_s_sleep(1);
    }
    asm volatile("" ::: "memory");
}

__global__ __launch_bounds__(640, 1) void mega(const u16* __restrict__ x, const u16* __restrict__ ckv,
                                               const u16* __restrict__ wq, const u16* __restrict__ wuk,
                                               const u16* __restrict__ wuv, const u16* __restrict__ wo,
                                               const u16* __restrict__ wg, const u16* __restrict__ wu,
                                               const u16* __restrict__ wd, const u16* __restrict__ gw,
                                               char* __restrict__ ws, u16* __restrict__ out) {
    __shared__ __attribute__((aligned(16))) char tile[512 * 64];
    __shared__ __attribute__((aligned(16))) float red1[8 * 1024];
    __shared__ __attribute__((aligned(16))) float red2[8 * 1024];
    __shared__ __attribute__((aligned(16))) float redf[8 * 512];
    __shared__ __attribute__((aligned(16))) float redo[8 * 512];
    __shared__ __attribute__((aligned(16))) float sm1[8 * 128];
    __shared__ __attribute__((aligned(16))) float sm2[8 * 16];
    __shared__ __attribute__((aligned(16))) float sm3[8 * 16];
    __shared__ __attribute__((aligned(16))) float qs[128];
    __shared__ __attribute__((aligned(16))) float fac[16 * 32];
    __shared__ __attribute__((aligned(16))) float Mws[32 * 8];
    __shared__ __attribute__((aligned(16))) float Lws[32 * 8];
    __shared__ __attribute__((aligned(16))) float cpart[4 * 512];
    __shared__ __attribute__((aligned(16))) float fin[8 * 32];
    __shared__ __attribute__((aligned(16))) float fing[8];
    __shared__ unsigned flg[4];

    const int b = blockIdx.x;
    const int t = threadIdx.x;
    const int lane = t & 63, w = t >> 6;
    const int h = b & 31, j = b >> 5;
    const int kc = b & 15, mem = b >> 4;
    const int key0 = kc * 512 + mem * 32;
    const int r = lane & 31, h2 = lane >> 5;
    const int fidx = b >> 3;
    const int fch = (b & 7) * 22 + fidx;
    const bool has_ffn = (fidx < 22) && (fch < 172);
    const int f0 = fch * 8;
    const bool cons = b >= 224;
    const __amdgpu_buffer_rsrc_t rs = __builtin_amdgcn_make_buffer_rsrc(ws, 0, WS_END, 0x00020000);
    lds_u32* vflg = (lds_u32*)flg;
    STAMP(0);

    if (w >= 8) {
        const unsigned e = __hip_atomic_load((gu32*)(ws + WS_EPOCH), RLX_AGENT);
        const u32x4 P4 = {POISON, POISON, POISON, POISON};
        const u32x2 P2 = {POISON, POISON};
        if (w == 9)
            __builtin_amdgcn_raw_buffer_store_b128(P4, rs, WS_QABS + (lane >> 3) * 32768 + ((j * 8 + (lane & 7)) * 32 + h) * 16, 0, SC1);
        bar();
        const unsigned tag = e + 1u;
        if (w == 8) {
            lds_wait(vflg + 0, 8);
            STAMP(1);
            f32x2 s = *(const f32x2*)(sm1 + 2 * lane);
#pragma unroll
            for (int ww = 1; ww < 8; ++ww) s += *(const f32x2*)(sm1 + ww * 128 + 2 * lane);
            u32x4 g = {tag, asu(s[0]), asu(s[1]), ~tag};
            __builtin_amdgcn_raw_buffer_store_b128(g, rs, WS_QGF + ((h * 8 + j) * 64 + lane) * 16, 0, 0);
            __builtin_amdgcn_raw_buffer_store_b128(g, rs, WS_QG + ((h * 8 + j) * 64 + lane) * 16, 0, SC1);
            for (unsigned spins = 0; spins < SPIN_CAP; ++spins) {
            asm volatile("" ::: "memory");
                u32x4 v[8];
                const int qb = ((spins & 3) == 3 ? WS_QG : WS_QGF) + (h * 8 * 64 + lane) * 16;
#pragma unroll
                for (int jj = 0; jj < 8; ++jj)
                    v[jj] = __builtin_amdgcn_raw_buffer_load_b128(rs, qb + jj * 1024, 0, SC1);
                unsigned bad = 0;
                float q0 = 0.f, q1 = 0.f;
#pragma unroll
                for (int jj = 0; jj < 8; ++jj) {
                    bad |= (v[jj][0] ^ tag) | (v[jj][3] ^ ~tag);
                    q0 += asf(v[jj][1]); q1 += asf(v[jj][2]);
                }
                if (__all(bad == 0) || spins == SPIN_CAP - 1) {
                    f32x2 qq = {rbf(q0), rbf(q1)};
                    *(f32x2*)(qs + 2 * lane) = qq;
                    break;
                }
                __builtin_amdgcn_s_sleep(1);
            }
            lgkm0();
            if (lane == 0) vflg[1] = 1u;
            STAMP(2);
        } else {
            vm0();
            {
                const unsigned tg = e + 1u;
                const u64 want = ((u64)(~tg) << 32) | tg;
                if (lane < NREP) __hip_atomic_store((gu64*)(ws + WS_F0 + lane * 2048 + b * 8), want, RLX_AGENT);
            }
#pragma unroll
            for (int i = 0; i < 2; ++i) {
                __builtin_amdgcn_raw_buffer_store_b128(P4, rs, WS_P + ((kc * 32 + mem * 2 + i) * 64 + lane) * 16, 0, SC1);
                __builtin_amdgcn_raw_buffer_store_b128(P4, rs, WS_PF + ((kc * 32 + mem * 2 + i) * 64 + lane) * 16, 0, SC1);
            }
            __builtin_amdgcn_raw_buffer_store_b64(P2, rs, (lane < 32 ? WS_MLOC : WS_MLOCF) + ((kc * 8 + (mem >> 1)) * 32 + r) * 16 + (mem & 1) * 8, 0, SC1);
#pragma unroll
            for (int i = 0; i < 4; ++i)
                __builtin_amdgcn_raw_buffer_store_b128(P4, rs, WS_OPART + ((kc * 32 + i * 8 + (lane >> 3)) * 512 + mem * 32) * 4 + (lane & 7) * 16, 0, SC1);
            if (mem == 0 && lane < 32) __builtin_amdgcn_raw_buffer_store_b64(P2, rs, WS_ML + (lane * 16 + kc) * 8, 0, SC1);
#pragma unroll
            for (int i = 0; i < 2; ++i)
                __builtin_amdgcn_raw_buffer_store_b128(P4, rs, WS_GA + b * 2048 + i * 1024 + lane * 16, 0, SC1);
            if (has_ffn) {
#pragma unroll
                for (int i = 0; i < 2; ++i)
                    __builtin_amdgcn_raw_buffer_store_b128(P4, rs, WS_GB + fch * 2048 + i * 1024 + lane * 16, 0, SC1);
            }
            vm0();
            if (lane == 0) vflg[3] = 1u;
            STAMP(1);
            const int fo = WS_F0 + (b & (NREP - 1)) * 2048 + lane * 16;
            for (unsigned spins = 0; spins < SPIN_CAP; ++spins) {
            asm volatile("" ::: "memory");
                u32x4 a0 = __builtin_amdgcn_raw_buffer_load_b128(rs, fo, 0, SC1);
                u32x4 a1 = __builtin_amdgcn_raw_buffer_load_b128(rs, fo + 1024, 0, SC1);
                unsigned bad = (a0[0] ^ tag) | (a0[1] ^ ~tag) | (a0[2] ^ tag) | (a0[3] ^ ~tag) |
                               (a1[0] ^ tag) | (a1[1] ^ ~tag) | (a1[2] ^ tag) | (a1[3] ^ ~tag);
                if (__all(bad == 0)) break;
                __builtin_amdgcn_s_sleep(1);
            }
            if (lane == 0) vflg[2] = 1u;
            STAMP(2);
            unsigned pf[14];
            {
                const int rr = lane >> 2, sg = lane & 3;
                const int colb = (b & 7) * 352 + sg * 64;
                const char* pg = (const char*)wg + (size_t)((b >> 3) * 16 + rr) * (FF * 2);
                const char* pu = (const char*)wu + (size_t)((b >> 3) * 16 + rr) * (FF * 2);
                int c0 = colb, c1 = colb + 256 - (sg < 2 ? 0 : 256);
                if (c0 > FF * 2 - 4) c0 = FF * 2 - 4;
                if (c1 > FF * 2 - 4) c1 = FF * 2 - 4;
                pf[0] = *(const unsigned*)(pg + c0); pf[1] = *(const unsigned*)(pu + c0);
                pf[2] = *(const unsigned*)(pg + c1); pf[3] = *(const unsigned*)(pu + c1);
                const int fr = has_ffn ? f0 : 0;
#pragma unroll
                for (int i = 0; i < 2; ++i) pf[4 + i] = *(const unsigned*)((const char*)wd + (size_t)fr * 1024 + i * 4096 + lane * 64);
#pragma unroll
                for (int i = 0; i < 4; ++i)
                    pf[6 + i] = *(const unsigned*)((const char*)wuv + (size_t)(j * 64 + i * 16 + (lane >> 2)) * 8192 + h * 256 + (lane & 3) * 64);
#pragma unroll
                for (int i = 0; i < 4; ++i)
                    pf[10 + i] = *(const unsigned*)((const char*)wo + ((size_t)h * 128 + j * 16) * 1024 + i * 4096 + lane * 64);
            }
#pragma unroll
            for (int i = 0; i < NBAR; ++i) asm volatile("s_barrier" ::: "memory");
            asm volatile("" :: "v"(pf[0]), "v"(pf[1]), "v"(pf[2]), "v"(pf[3]), "v"(pf[4]), "v"(pf[5]), "v"(pf[6]),
                         "v"(pf[7]), "v"(pf[8]), "v"(pf[9]), "v"(pf[10]), "v"(pf[11]), "v"(pf[12]), "v"(pf[13]));
            return;
        }
#pragma unroll
        for (int i = 0; i < NBAR; ++i) asm volatile("s_barrier" ::: "memory");
        if (b == 224 && lane == 0) __hip_atomic_store((gu32*)(ws + WS_EPOCH), tag, RLX_AGENT);
        return;
    }

    u32x4 wqv[2], wkv[2];
    float x0, x1;
    {
        const int row = j * 64 + (t >> 4);
        x0 = bf2f(x[row]); x1 = bf2f(x[row + 32]);
#pragma unroll
        for (int i = 0; i < 2; ++i)
            wqv[i] = *(const u32x4*)(wq + (size_t)(row + 32 * i) * 4096 + h * 128 + (t & 15) * 8);
#pragma unroll
        for (int i = 0; i < 2; ++i)
            wkv[i] = *(const u32x4*)(wuk + (size_t)(j * 64 + 8 * w + (lane >> 4) + 4 * i) * 4096 + h * 128 + (lane & 15) * 8);
    }
    if (t < 4) flg[t] = 0u;
    bar();
    __builtin_amdgcn_sched_barrier(0);
    asm volatile("s_waitcnt vmcnt(2)" ::: "memory");
    STAMP(1);
    bf16x8 bq[4];
    u32x4 cv[4];
    {
        float acc[8];
#pragma unroll
        for (int jj = 0; jj < 4; ++jj) {
            acc[2 * jj] = x0 * bflo(wqv[0][jj]) + x1 * bflo(wqv[1][jj]);
            acc[2 * jj + 1] = x0 * bfhi(wqv[0][jj]) + x1 * bfhi(wqv[1][jj]);
        }
        bool up = lane & 16;
        float s[4];
#pragma unroll
        for (int k = 0; k < 4; ++k) { float send = up ? acc[k] : acc[k + 4]; float keep = up ? acc[k + 4] : acc[k]; s[k] = keep + __shfl_xor(send, 16); }
        up = lane & 32;
        f32x2 s2;
#pragma unroll
        for (int k = 0; k < 2; ++k) { float send = up ? s[k] : s[k + 2]; float keep = up ? s[k + 2] : s[k]; s2[k] = keep + __shfl_xor(send, 32); }
        *(f32x2*)(sm1 + w * 128 + (lane & 15) * 8 + ((lane >> 4) & 1) * 4 + (lane >> 5) * 2) = s2;
        lgkm0();
        if (lane == 0) __hip_atomic_fetch_add(flg + 0, 1u, __ATOMIC_RELAXED, __HIP_MEMORY_SCOPE_WORKGROUP);
    }
    __builtin_amdgcn_sched_barrier(0);
    lds_wait(vflg + 1, 1);
    STAMP(2);
    lds_wait(vflg + 3, 1);
    {
        const int l16 = lane & 15;
        f32x4 qa0 = *(const f32x4*)(qs + l16 * 8), qa1 = *(const f32x4*)(qs + l16 * 8 + 4);
        float d[2];
#pragma unroll
        for (int i = 0; i < 2; ++i) {
            d[i] = qa0[0] * bflo(wkv[i][0]) + qa0[1] * bfhi(wkv[i][0]) + qa0[2] * bflo(wkv[i][1]) + qa0[3] * bfhi(wkv[i][1]);
            d[i] += qa1[0] * bflo(wkv[i][2]) + qa1[1] * bfhi(wkv[i][2]) + qa1[2] * bflo(wkv[i][3]) + qa1[3] * bfhi(wkv[i][3]);
        }
        bool up = lane & 1;
        float send = up ? d[0] : d[1], keep = up ? d[1] : d[0];
        float v = keep + __shfl_xor(send, 1);
        v += __shfl_xor(v, 2);
        v += __shfl_xor(v, 4);
        v += __shfl_xor(v, 8);
        v *= 0.08838834764831845f;
        float vv[8];
#pragma unroll
        for (int k = 0; k < 8; ++k) vv[k] = __shfl(v, (k & 3) * 16 + (k >> 2));
        u32x4 o = {pk2(vv[0], vv[1]), pk2(vv[2], vv[3]), pk2(vv[4], vv[5]), pk2(vv[6], vv[7])};
        if (lane < 8)
            __builtin_amdgcn_raw_buffer_store_b128(o, rs, WS_QABS + lane * 32768 + ((j * 8 + w) * 32 + h) * 16, 0, SC1);
    }
    {
        const u16* bp = ckv + (size_t)(key0 + r) * 512 + 64 * w + 8 * h2;
#pragma unroll
        for (int s = 0; s < 4; ++s) bq[s] = *(const bf16x8*)(bp + 16 * s);
        const u16* cp = ckv + (size_t)(kc * 512 + (t >> 2)) * 512 + mem * 32 + (t & 3) * 8;
#pragma unroll
        for (int i = 0; i < 4; ++i) cv[i] = *(const u32x4*)(cp + (size_t)i * 128 * 512);
    }
    __builtin_amdgcn_sched_barrier(0);
#pragma unroll
    for (int i = 0; i < 4; ++i) *(u32x4*)(tile + (i * 128 + (t >> 2)) * 64 + (t & 3) * 16) = cv[i];
    lds_wait(vflg + 2, 1);
    STAMP(3);
    {
        u32x4 av[4];
        const int qo = WS_QABS + (b & 7) * 32768 + (w * 256 + lane) * 16;
        for (unsigned spins = 0; spins < SPIN_CAP; ++spins) {
            asm volatile("" ::: "memory");
#pragma unroll
            for (int s = 0; s < 4; ++s) av[s] = __builtin_amdgcn_raw_buffer_load_b128(rs, qo + 1024 * s, 0, SC1);
            unsigned m = umax(umax(max4(av[0]), max4(av[1])), umax(max4(av[2]), max4(av[3])));
            if (__all(m != POISON)) break;
            __builtin_amdgcn_s_sleep(1);
        }
        STAMP(4);
        f32x16 acc;
#pragma unroll
        for (int i = 0; i < 16; ++i) acc[i] = 0.f;
#pragma unroll
        for (int s = 0; s < 4; ++s)
            acc = __builtin_amdgcn_mfma_f32_32x32x16_bf16(__builtin_bit_cast(bf16x8, av[s]), bq[s], acc, 0, 0, 0);
#pragma unroll
        for (int i = 0; i < 16; ++i) {
            int head = (i & 3) + 8 * (i >> 2) + 4 * h2;
            red1[(w * 32 + head) * 32 + r] = acc[i];
        }
    }
    bar();
    {
        const int head = t >> 4, kq = (t & 15) * 2;
        f32x2 s2 = *(const f32x2*)(red1 + head * 32 + kq);
#pragma unroll
        for (int ww = 1; ww < 8; ++ww) s2 += *(const f32x2*)(red1 + (ww * 32 + head) * 32 + kq);
        float m = fmaxf(s2[0], s2[1]);
        m = fmaxf(m, __shfl_xor(m, 1));
        m = fmaxf(m, __shfl_xor(m, 2));
        m = fmaxf(m, __shfl_xor(m, 4));
        m = fmaxf(m, __shfl_xor(m, 8));
        unsigned p01 = pk2(__expf(s2[0] - m), __expf(s2[1] - m));
        float l = bflo(p01) + bfhi(p01);
        l += __shfl_xor(l, 1);
        l += __shfl_xor(l, 2);
        l += __shfl_xor(l, 4);
        l += __shfl_xor(l, 8);
        u32x4 pp = {p01, (unsigned)__shfl_down((int)p01, 1), (unsigned)__shfl_down((int)p01, 2), (unsigned)__shfl_down((int)p01, 3)};
        u32x2 ml = {asu(m), asu(l)};
        if ((t & 3) == 0)
            __builtin_amdgcn_raw_buffer_store_b128(pp, rs, WS_PF + ((kc * 32 + mem * 2) * 64 + ((t & 15) >> 2) * 32 + head) * 16, 0, 0);
        if ((t & 15) == 0)
            __builtin_amdgcn_raw_buffer_store_b64(ml, rs, WS_MLOCF + ((kc * 8 + (mem >> 1)) * 32 + head) * 16 + (mem & 1) * 8, 0, 0);
        if ((t & 3) == 0)
            __builtin_amdgcn_raw_buffer_store_b128(pp, rs, WS_P + ((kc * 32 + mem * 2) * 64 + ((t & 15) >> 2) * 32 + head) * 16, 0, SC1);
        if ((t & 15) == 0)
            __builtin_amdgcn_raw_buffer_store_b64(ml, rs, WS_MLOC + ((kc * 8 + (mem >> 1)) * 32 + head) * 16 + (mem & 1) * 8, 0, SC1);
    }
    STAMP(5);
    u32x4 uvv[2], ovv[2];
    u32x4 gv, uv_, dv;
    float xt;
    {
        u32x4 pv[4], mlv;
        for (unsigned spins = 0; spins < SPIN_CAP; ++spins) {
            asm volatile("" ::: "memory");
            const bool slow = (spins & 3) == 3;
            const int po = (slow ? WS_P : WS_PF) + ((kc * 32 + w * 4) * 64 + lane) * 16;
            const int mo = (slow ? WS_MLOC : WS_MLOCF) + ((kc * 8 + w) * 32 + r) * 16;
#pragma unroll
            for (int s = 0; s < 4; ++s) pv[s] = __builtin_amdgcn_raw_buffer_load_b128(rs, po + 1024 * s, 0, SC1);
            mlv = __builtin_amdgcn_raw_buffer_load_b128(rs, mo, 0, SC1);
            unsigned m = umax(umax(max4(pv[0]), max4(pv[1])), umax(max4(pv[2]), max4(pv[3])));
            m = umax(m, max4(mlv));
            if (__all(m != POISON)) break;
            __builtin_amdgcn_s_sleep(1);
        }
        STAMP(6);
        xt = bf2f(x[t]);
#pragma unroll
        for (int i = 0; i < 2; ++i)
            uvv[i] = *(const u32x4*)(wuv + (size_t)((t >> 1) + 256 * i) * 4096 + h * 128 + j * 16 + (t & 1) * 8);
#pragma unroll
        for (int i = 0; i < 2; ++i)
            ovv[i] = *(const u32x4*)(wo + ((size_t)h * 128 + j * 16 + w * 2 + i) * 512 + lane * 8);
        {
            const int fz = has_ffn ? f0 : (b & 7) * 176;
            gv = *(const u32x4*)(wg + (size_t)t * FF + fz);
            uv_ = *(const u32x4*)(wu + (size_t)t * FF + fz);
            dv = *(const u32x4*)(wd + (size_t)(fz + w) * 512 + lane * 8);
        }
        __builtin_amdgcn_sched_barrier(0);
        {
            float m0 = asf(mlv[0]), l0 = asf(mlv[1]), m1 = asf(mlv[2]), l1 = asf(mlv[3]);
            float Mw = fmaxf(m0, m1);
            float f0_ = __expf(m0 - Mw), f1_ = __expf(m1 - Mw);
            if (lane < 32) {
                fac[(2 * w) * 32 + r] = f0_; fac[(2 * w + 1) * 32 + r] = f1_;
                Mws[r * 8 + w] = Mw; Lws[r * 8 + w] = f0_ * l0 + f1_ * l1;
            }
        }
        const int g = lane >> 4;
        const int q = (lane & 15) >> 2, p = lane & 3;
        const int cb = g & 1;
        f32x16 tot;
#pragma unroll
        for (int mm = 0; mm < 2; ++mm) {
            f32x16 acc;
#pragma unroll
            for (int i = 0; i < 16; ++i) acc[i] = 0.f;
#pragma unroll
            for (int ss = 0; ss < 2; ++ss) {
                int s = mm * 2 + ss;
                int row = w * 64 + 16 * s + 8 * h2 + q;
                s16x4 t0 = __builtin_amdgcn_ds_read_tr16_b64_v4i16(
                    (__attribute__((address_space(3))) s16x4*)(tile + row * 64 + cb * 32 + p * 8));
                s16x4 t1 = __builtin_amdgcn_ds_read_tr16_b64_v4i16(
                    (__attribute__((address_space(3))) s16x4*)(tile + (row + 4) * 64 + cb * 32 + p * 8));
                s16x8 bb = {t0[0], t0[1], t0[2], t0[3], t1[0], t1[1], t1[2], t1[3]};
                acc = __builtin_amdgcn_mfma_f32_32x32x16_bf16(__builtin_bit_cast(bf16x8, pv[s]), __builtin_bit_cast(bf16x8, bb), acc, 0, 0, 0);
            }
            const float* fp = fac + (2 * w + mm) * 32 + 4 * h2;
#pragma unroll
            for (int qd = 0; qd < 4; ++qd) {
                f32x4 f4 = *(const f32x4*)(fp + 8 * qd);
#pragma unroll
                for (int e = 0; e < 4; ++e) {
                    if (mm == 0) tot[qd * 4 + e] = f4[e] * acc[qd * 4 + e];
                    else tot[qd * 4 + e] += f4[e] * acc[qd * 4 + e];
                }
            }
        }
#pragma unroll
        for (int i = 0; i < 16; ++i) {
            int head = (i & 3) + 8 * (i >> 2) + 4 * h2;
            red2[(w * 32 + head) * 32 + r] = tot[i];
        }
    }
    bar();
    {
        const int head = t >> 4, cq = (t & 15) * 2;
        f32x4 ma = *(const f32x4*)(Mws + head * 8), mb = *(const f32x4*)(Mws + head * 8 + 4);
        float M = fmaxf(fmaxf(fmaxf(ma[0], ma[1]), fmaxf(ma[2], ma[3])), fmaxf(fmaxf(mb[0], mb[1]), fmaxf(mb[2], mb[3])));
        float ew[8];
#pragma unroll
        for (int ww = 0; ww < 4; ++ww) { ew[ww] = __expf(ma[ww] - M); ew[ww + 4] = __expf(mb[ww] - M); }
        f32x2 s2 = {0.f, 0.f};
#pragma unroll
        for (int ww = 0; ww < 8; ++ww) s2 += ew[ww] * *(const f32x2*)(red2 + (ww * 32 + head) * 32 + cq);
        u32x4 o = {asu(s2[0]), asu(s2[1]), asu(__shfl_down(s2[0], 1)), asu(__shfl_down(s2[1], 1))};
        if ((t & 1) == 0)
            __builtin_amdgcn_raw_buffer_store_b128(o, rs, WS_OPART + ((kc * 32 + head) * 512 + mem * 32 + cq) * 4, 0, SC1);
        if (mem == 0 && (t & 15) == 0) {
            f32x4 la = *(const f32x4*)(Lws + head * 8), lb = *(const f32x4*)(Lws + head * 8 + 4);
            float L = 0.f;
#pragma unroll
            for (int ww = 0; ww < 4; ++ww) L += ew[ww] * la[ww] + ew[ww + 4] * lb[ww];
            u32x2 ml = {asu(M), asu(L)};
            __builtin_amdgcn_raw_buffer_store_b64(ml, rs, WS_ML + (head * 16 + kc) * 8, 0, SC1);
        }
    }
    STAMP(7);
    {
        float p[16];
#pragma unroll
        for (int jj = 0; jj < 4; ++jj) {
            p[2 * jj] = xt * bflo(gv[jj]); p[2 * jj + 1] = xt * bfhi(gv[jj]);
            p[8 + 2 * jj] = xt * bflo(uv_[jj]); p[8 + 2 * jj + 1] = xt * bfhi(uv_[jj]);
        }
        bool up = lane & 1;
        float s8[8];
#pragma unroll
        for (int k = 0; k < 8; ++k) { float send = up ? p[k] : p[k + 8]; float keep = up ? p[k + 8] : p[k]; s8[k] = keep + __shfl_xor(send, 1); }
        up = lane & 2;
        float s4[4];
#pragma unroll
        for (int k = 0; k < 4; ++k) { float send = up ? s8[k] : s8[k + 4]; float keep = up ? s8[k + 4] : s8[k]; s4[k] = keep + __shfl_xor(send, 2); }
        up = lane & 4;
        float s2[2];
#pragma unroll
        for (int k = 0; k < 2; ++k) { float send = up ? s4[k] : s4[k + 2]; float keep = up ? s4[k + 2] : s4[k]; s2[k] = keep + __shfl_xor(send, 4); }
        up = lane & 8;
        float send = up ? s2[0] : s2[1]; float keep = up ? s2[1] : s2[0];
        float v = keep + __shfl_xor(send, 8);
        v += __shfl_xor(v, 16);
        v += __shfl_xor(v, 32);
        if (lane < 16) {
            int idx = (lane & 1) * 8 + ((lane >> 1) & 1) * 4 + ((lane >> 2) & 1) * 2 + ((lane >> 3) & 1);
            sm2[w * 16 + idx] = v;
        }
        bar();
        float gs = 0.f, us = 0.f;
#pragma unroll
        for (int ww = 0; ww < 8; ++ww) { gs += sm2[ww * 16 + w]; us += sm2[ww * 16 + 8 + w]; }
        gs = rbf(gs); us = rbf(us);
        float sg = rbf(gs / (1.f + __expf(-gs)));
        float hv = rbf(sg * us);
        f32x4 o0, o1;
        o0[0] = hv * bflo(dv[0]); o0[1] = hv * bfhi(dv[0]); o0[2] = hv * bflo(dv[1]); o0[3] = hv * bfhi(dv[1]);
        o1[0] = hv * bflo(dv[2]); o1[1] = hv * bfhi(dv[2]); o1[2] = hv * bflo(dv[3]); o1[3] = hv * bfhi(dv[3]);
        *(f32x4*)(redf + w * 512 + lane * 8) = o0;
        *(f32x4*)(redf + w * 512 + lane * 8 + 4) = o1;
        bar();
        if (has_ffn && t < 128) {
            f32x4 s = *(const f32x4*)(redf + t * 4);
#pragma unroll
            for (int ww = 1; ww < 8; ++ww) s += *(const f32x4*)(redf + ww * 512 + t * 4);
            __builtin_amdgcn_raw_buffer_store_b128(__builtin_bit_cast(u32x4, s), rs, WS_GB + fch * 2048 + t * 16, 0, SC1);
        }
    }
    STAMP(8);
    {
        const int qd = t >> 7;
        u32x4 ov[4];
        u32x2 ml;
        const int oo = WS_OPART + ((qd * 4 * 32 + h) * 512 + (t & 127) * 4) * 4;
        for (unsigned spins = 0; spins < SPIN_CAP; ++spins) {
            asm volatile("" ::: "memory");
#pragma unroll
            for (int i = 0; i < 4; ++i) ov[i] = __builtin_amdgcn_raw_buffer_load_b128(rs, oo + i * 65536, 0, SC1);
            ml = __builtin_amdgcn_raw_buffer_load_b64(rs, WS_ML + (h * 16 + (lane & 15)) * 8, 0, SC1);
            unsigned m = umax(umax(max4(ov[0]), max4(ov[1])), umax(max4(ov[2]), max4(ov[3])));
            m = umax(m, umax(ml[0], ml[1]));
            if (__all(m != POISON)) break;
            __builtin_amdgcn_s_sleep(1);
        }
        STAMP(9);
        float mk = asf(ml[0]), lk = asf(ml[1]);
        float M = mk;
        M = fmaxf(M, __shfl_xor(M, 1)); M = fmaxf(M, __shfl_xor(M, 2));
        M = fmaxf(M, __shfl_xor(M, 4)); M = fmaxf(M, __shfl_xor(M, 8));
        float wk = __expf(mk - M);
        float L = wk * lk;
        L += __shfl_xor(L, 1); L += __shfl_xor(L, 2); L += __shfl_xor(L, 4); L += __shfl_xor(L, 8);
        wk = wk / L;
        f32x4 c4 = {0.f, 0.f, 0.f, 0.f};
#pragma unroll
        for (int i = 0; i < 4; ++i) {
            float wi = __shfl(wk, qd * 4 + i);
            c4[0] += wi * asf(ov[i][0]); c4[1] += wi * asf(ov[i][1]); c4[2] += wi * asf(ov[i][2]); c4[3] += wi * asf(ov[i][3]);
        }
        *(f32x4*)(cpart + qd * 512 + (t & 127) * 4) = c4;
    }
    bar();
    {
        const int row = t >> 1;
        float c0 = (cpart[row] + cpart[512 + row]) + (cpart[1024 + row] + cpart[1536 + row]);
        float c1 = (cpart[row + 256] + cpart[768 + row]) + (cpart[1280 + row] + cpart[1792 + row]);
        float acc[8];
#pragma unroll
        for (int jj = 0; jj < 4; ++jj) {
            acc[2 * jj] = c0 * bflo(uvv[0][jj]) + c1 * bflo(uvv[1][jj]);
            acc[2 * jj + 1] = c0 * bfhi(uvv[0][jj]) + c1 * bfhi(uvv[1][jj]);
        }
        bool up = lane & 2;
        float s[4];
#pragma unroll
        for (int k = 0; k < 4; ++k) { float send = up ? acc[k] : acc[k + 4]; float keep = up ? acc[k + 4] : acc[k]; s[k] = keep + __shfl_xor(send, 2); }
        up = lane & 4;
        float s2[2];
#pragma unroll
        for (int k = 0; k < 2; ++k) { float send = up ? s[k] : s[k + 2]; float keep = up ? s[k + 2] : s[k]; s2[k] = keep + __shfl_xor(send, 4); }
        up = lane & 8;
        float send = up ? s2[0] : s2[1]; float keep = up ? s2[1] : s2[0];
        float v = keep + __shfl_xor(send, 8);
        v += __shfl_xor(v, 16);
        v += __shfl_xor(v, 32);
        if (lane < 16) {
            int d = (lane & 1) * 8 + ((lane >> 1) & 1) * 4 + ((lane >> 2) & 1) * 2 + ((lane >> 3) & 1);
            sm3[w * 16 + d] = v;
        }
    }
    bar();
    {
        float c0 = 0.f, c1 = 0.f;
#pragma unroll
        for (int ww = 0; ww < 8; ++ww) { c0 += sm3[ww * 16 + 2 * w]; c1 += sm3[ww * 16 + 2 * w + 1]; }
        c0 = rbf(c0); c1 = rbf(c1);
        f32x4 o0, o1;
        o0[0] = c0 * bflo(ovv[0][0]) + c1 * bflo(ovv[1][0]); o0[1] = c0 * bfhi(ovv[0][0]) + c1 * bfhi(ovv[1][0]);
        o0[2] = c0 * bflo(ovv[0][1]) + c1 * bflo(ovv[1][1]); o0[3] = c0 * bfhi(ovv[0][1]) + c1 * bfhi(ovv[1][1]);
        o1[0] = c0 * bflo(ovv[0][2]) + c1 * bflo(ovv[1][2]); o1[1] = c0 * bfhi(ovv[0][2]) + c1 * bfhi(ovv[1][2]);
        o1[2] = c0 * bflo(ovv[0][3]) + c1 * bflo(ovv[1][3]); o1[3] = c0 * bfhi(ovv[0][3]) + c1 * bfhi(ovv[1][3]);
        *(f32x4*)(redo + w * 512 + lane * 8) = o0;
        *(f32x4*)(redo + w * 512 + lane * 8 + 4) = o1;
    }
    bar();
    if (t < 128) {
        f32x4 s = *(const f32x4*)(redo + t * 4);
#pragma unroll
        for (int ww = 1; ww < 8; ++ww) s += *(const f32x4*)(redo + ww * 512 + t * 4);
        __builtin_amdgcn_raw_buffer_store_b128(__builtin_bit_cast(u32x4, s), rs, WS_GA + b * 2048 + t * 16, 0, SC1);
    }
    STAMP(10);
    if (cons) {
        const int k = b - 224;
        const int p = t >> 1, hf = t & 1;
        float gp = rbf(xt * bf2f(gw[t]));
        const int ao = WS_GA + p * 2048 + k * 64 + hf * 32;
        const int bo = WS_GB + (p < 172 ? p : 171) * 2048 + k * 64 + hf * 32;
        u32x4 a0, a1, b0, b1;
        for (unsigned spins = 0; spins < SPIN_CAP; ++spins) {
            asm volatile("" ::: "memory");
            a0 = __builtin_amdgcn_raw_buffer_load_b128(rs, ao, 0, SC1);
            a1 = __builtin_amdgcn_raw_buffer_load_b128(rs, ao + 16, 0, SC1);
            b0 = __builtin_amdgcn_raw_buffer_load_b128(rs, bo, 0, SC1);
            b1 = __builtin_amdgcn_raw_buffer_load_b128(rs, bo + 16, 0, SC1);
            unsigned m = umax(umax(max4(a0), max4(a1)), umax(max4(b0), max4(b1)));
            if (__all(m != POISON)) break;
            __builtin_amdgcn_s_sleep(1);
        }
        STAMP(11);
        float v[16];
        const float bz = p < 172 ? 1.f : 0.f;
#pragma unroll
        for (int i = 0; i < 4; ++i) {
            v[i] = asf(a0[i]); v[4 + i] = asf(a1[i]);
            v[8 + i] = p < 172 ? asf(b0[i]) : 0.f; v[12 + i] = p < 172 ? asf(b1[i]) : 0.f;
        }
        (void)bz;
        bool up = lane & 2;
        float s8[8];
#pragma unroll
        for (int q = 0; q < 8; ++q) { float send = up ? v[q] : v[q + 8]; float keep = up ? v[q + 8] : v[q]; s8[q] = keep + __shfl_xor(send, 2); }
        up = lane & 4;
        float s4[4];
#pragma unroll
        for (int q = 0; q < 4; ++q) { float send = up ? s8[q] : s8[q + 4]; float keep = up ? s8[q + 4] : s8[q]; s4[q] = keep + __shfl_xor(send, 4); }
        up = lane & 8;
        float s2[2];
#pragma unroll
        for (int q = 0; q < 2; ++q) { float send = up ? s4[q] : s4[q + 2]; float keep = up ? s4[q + 2] : s4[q]; s2[q] = keep + __shfl_xor(send, 8); }
        up = lane & 16;
        float send = up ? s2[0] : s2[1]; float keep = up ? s2[1] : s2[0];
        float vv = keep + __shfl_xor(send, 16);
        vv += __shfl_xor(vv, 32);
#pragma unroll
        for (int o = 32; o >= 1; o >>= 1) gp += __shfl_xor(gp, o);
        if (lane < 32) {
            int idx = ((lane >> 1) & 1) * 16 + (lane & 1) * 8 + ((lane >> 2) & 1) * 4 + ((lane >> 3) & 1) * 2 + ((lane >> 4) & 1);
            fin[w * 32 + idx] = vv;
        }
        if (lane == 0) fing[w] = gp;
    }
    bar();
    if (cons && t < 16) {
        float a = 0.f, bb = 0.f, gs = 0.f;
#pragma unroll
        for (int ww = 0; ww < 8; ++ww) { a += fin[ww * 32 + t]; bb += fin[ww * 32 + 16 + t]; gs += fing[ww]; }
        gs = rbf(gs);
        float g = rbf(1.f / (1.f + __expf(-gs)));
        a = rbf(a); bb = rbf(bb);
        float rr = rbf(g * a) + rbf(rbf(1.f - g) * bb);
        out[(b - 224) * 16 + t] = f2bf(rr);
    }
    STAMP(12);
}

extern "C" void kernel_launch(void* const* d_in, const int* in_sizes, int n_in,
                              void* d_out, int out_size, void* d_ws, size_t ws_size,
                              hipStream_t stream) {
    const u16* x = (const u16*)d_in[0];
    const u16* ckv = (const u16*)d_in[1];
    const u16* wq = (const u16*)d_in[2];
    const u16* wuk = (const u16*)d_in[3];
    const u16* wuv = (const u16*)d_in[4];
    const u16* wo = (const u16*)d_in[5];
    const u16* wg = (const u16*)d_in[7];
    const u16* wu = (const u16*)d_in[8];
    const u16* wd = (const u16*)d_in[9];
    const u16* gw = (const u16*)d_in[10];
    mega<<<256, 640, 0, stream>>>(x, ckv, wq, wuk, wuv, wo, wg, wu, wd, gw, (char*)d_ws, (u16*)d_out);
}
```

```cpp
#include <hip/hip_runtime.h>
#include <hip/hip_bf16.h>
#include <stdint.h>

#define SK 8192
#define FF 1376

typedef unsigned short u16;
typedef unsigned long long u64;
typedef __attribute__((ext_vector_type(8))) __bf16 bf16x8;
typedef __attribute__((ext_vector_type(2))) __bf16 bf16x2;
typedef __attribute__((ext_vector_type(4))) short s16x4;
typedef __attribute__((ext_vector_type(8))) short s16x8;
typedef __attribute__((ext_vector_type(16))) float f32x16;
typedef __attribute__((ext_vector_type(4))) float f32x4;
typedef __attribute__((ext_vector_type(2))) float f32x2;
typedef __attribute__((ext_vector_type(4))) unsigned int u32x4;
typedef __attribute__((ext_vector_type(2))) unsigned int u32x2;
#define GAS __attribute__((address_space(1)))
typedef GAS unsigned gu32;
typedef GAS u64 gu64;
#define RLX_AGENT __ATOMIC_RELAXED, __HIP_MEMORY_SCOPE_AGENT
#define SC1 16
#define POISON 0xFFFFFFFFu

__device__ __forceinline__ float bf2f(u16 v) { return __uint_as_float(((unsigned)v) << 16); }
__device__ __forceinline__ float bflo(unsigned v) { return __uint_as_float(v << 16); }
__device__ __forceinline__ float bfhi(unsigned v) { return __uint_as_float(v & 0xffff0000u); }
__device__ __forceinline__ float asf(unsigned v) { return __uint_as_float(v); }
__device__ __forceinline__ unsigned asu(float v) { return __float_as_uint(v); }
__device__ __forceinline__ unsigned pk2(float a, float b) {
    bf16x2 v = {(__bf16)a, (__bf16)b};
    return __builtin_bit_cast(unsigned, v);
}
__device__ __forceinline__ u16 f2bf(float f) { __bf16 v = (__bf16)f; return __builtin_bit_cast(u16, v); }
__device__ __forceinline__ float rbf(float f) { return bf2f(f2bf(f)); }
__device__ __forceinline__ unsigned umax(unsigned a, unsigned b) { return a > b ? a : b; }
__device__ __forceinline__ unsigned max4(u32x4 v) { return umax(umax(v[0], v[1]), umax(v[2], v[3])); }

#define NREP 32
#define WS_EPOCH   0
#define WS_F0      256
#define WS_QG      (WS_F0 + NREP * 2048)
#define WS_QABS    (WS_QG + 256 * 1024)
#define WS_P       (WS_QABS + 8 * 32768)
#define WS_MLOC    (WS_P + 16 * 32 * 512 * 2)
#define WS_OPART   (WS_MLOC + 16 * 32 * 16 * 8)
#define WS_ML      (WS_OPART + 16 * 32 * 512 * 4)
#define WS_GA      (WS_ML + 32 * 16 * 8)
#define WS_GB      (WS_GA + 256 * 2048)
#define WS_QGF     (WS_GB + 172 * 2048)
#define WS_PF      (WS_QGF + 256 * 1024)
#define WS_MLOCF   (WS_PF + 16 * 32 * 512 * 2)
#define WS_END     (WS_MLOCF + 16 * 32 * 16 * 8)

#define SPIN_CAP (1u << 17)
#define STAMP(i)
#define CKV_MODE 1
#define NBAR 8

__device__ __forceinline__ void lgkm0() { asm volatile("s_waitcnt lgkmcnt(0)" ::: "memory"); }
__device__ __forceinline__ void vm0() { asm volatile("s_waitcnt vmcnt(0)" ::: "memory"); }
__device__ __forceinline__ void bar() { asm volatile("s_waitcnt lgkmcnt(0)\n\ts_barrier" ::: "memory"); }
typedef volatile __attribute__((address_space(3))) unsigned lds_u32;
__device__ __forceinline__ void lds_wait(lds_u32* p, unsigned target) {
    for (unsigned s = 0; s < SPIN_CAP; ++s) {
        if (*p >= target) break;
        __builtin_amdgcn_s_sleep(1);
    }
    asm volatile("" ::: "memory");
}

__global__ __launch_bounds__(576, 1) void mega(const u16* __restrict__ x, const u16* __restrict__ ckv,
                                               const u16* __restrict__ wq, const u16* __restrict__ wuk,
                                               const u16* __restrict__ wuv, const u16* __restrict__ wo,
                                               const u16* __restrict__ wg, const u16* __restrict__ wu,
                                               const u16* __restrict__ wd, const u16* __restrict__ gw,
                                               char* __restrict__ ws, u16* __restrict__ out) {
    __shared__ __attribute__((aligned(16))) char tile[512 * 64];
    __shared__ __attribute__((aligned(16))) float red1[8 * 1024];
    __shared__ __attribute__((aligned(16))) float red2[8 * 1024];
    __shared__ __attribute__((aligned(16))) float redf[8 * 512];
    __shared__ __attribute__((aligned(16))) float redo[8 * 512];
    __shared__ __attribute__((aligned(16))) float sm1[8 * 128];
    __shared__ __attribute__((aligned(16))) float sm2[8 * 16];
    __shared__ __attribute__((aligned(16))) float sm3[8 * 16];
    __shared__ __attribute__((aligned(16))) float xs[512];
    __shared__ __attribute__((aligned(16))) float fac[16 * 32];
    __shared__ __attribute__((aligned(16))) float Mws[32 * 8];
    __shared__ __attribute__((aligned(16))) float Lws[32 * 8];
    __shared__ __attribute__((aligned(16))) float cpart[4 * 512];
    __shared__ __attribute__((aligned(16))) float fin[8 * 32];
    __shared__ __attribute__((aligned(16))) float fing[8];
    __shared__ unsigned flg[4];

    const int b = blockIdx.x;
    const int t = threadIdx.x;
    const int lane = t & 63, w = t >> 6;
    const int h = b & 31, j = b >> 5;
    const int kc = b & 15, mem = b >> 4;
    const int key0 = kc * 512 + mem * 32;
    const int r = lane & 31, h2 = lane >> 5;
    const int fidx = b >> 3;
    const int fch = (b & 7) * 22 + fidx;
    const bool has_ffn = (fidx < 22) && (fch < 172);
    const int f0 = fch * 8;
    const bool cons = b >= 224;
    const __amdgpu_buffer_rsrc_t rs = __builtin_amdgcn_make_buffer_rsrc(ws, 0, WS_END, 0x00020000);
    lds_u32* vflg = (lds_u32*)flg;
    STAMP(0);

    if (w >= 8) {
        const unsigned e = __hip_atomic_load((gu32*)(ws + WS_EPOCH), RLX_AGENT);
        const u32x4 P4 = {POISON, POISON, POISON, POISON};
        const u32x2 P2 = {POISON, POISON};
        __builtin_amdgcn_raw_buffer_store_b128(P4, rs, WS_QABS + (lane >> 3) * 32768 + ((j * 8 + (lane & 7)) * 32 + h) * 16, 0, SC1);
        const unsigned tag = e + 1u;
        {
            vm0();
            {
                const unsigned tg = e + 1u;
                const u64 want = ((u64)(~tg) << 32) | tg;
                if (lane < NREP) __hip_atomic_store((gu64*)(ws + WS_F0 + lane * 2048 + b * 8), want, RLX_AGENT);
            }
#pragma unroll
            for (int i = 0; i < 2; ++i) {
                __builtin_amdgcn_raw_buffer_store_b128(P4, rs, WS_P + ((kc * 32 + mem * 2 + i) * 64 + lane) * 16, 0, SC1);
                __builtin_amdgcn_raw_buffer_store_b128(P4, rs, WS_PF + ((kc * 32 + mem * 2 + i) * 64 + lane) * 16, 0, SC1);
            }
            __builtin_amdgcn_raw_buffer_store_b64(P2, rs, (lane < 32 ? WS_MLOC : WS_MLOCF) + ((kc * 8 + (mem >> 1)) * 32 + r) * 16 + (mem & 1) * 8, 0, SC1);
#pragma unroll
            for (int i = 0; i < 4; ++i)
                __builtin_amdgcn_raw_buffer_store_b128(P4, rs, WS_OPART + ((kc * 32 + i * 8 + (lane >> 3)) * 512 + mem * 32) * 4 + (lane & 7) * 16, 0, SC1);
            if (mem == 0 && lane < 32) __builtin_amdgcn_raw_buffer_store_b64(P2, rs, WS_ML + (lane * 16 + kc) * 8, 0, SC1);
#pragma unroll
            for (int i = 0; i < 2; ++i)
                __builtin_amdgcn_raw_buffer_store_b128(P4, rs, WS_GA + b * 2048 + i * 1024 + lane * 16, 0, SC1);
            if (has_ffn) {
#pragma unroll
                for (int i = 0; i < 2; ++i)
                    __builtin_amdgcn_raw_buffer_store_b128(P4, rs, WS_GB + fch * 2048 + i * 1024 + lane * 16, 0, SC1);
            }
            bar();
            vm0();
            if (lane == 0) vflg[3] = 1u;
            STAMP(1);
            const int fo = WS_F0 + (b & (NREP - 1)) * 2048 + lane * 16;
            for (unsigned spins = 0; spins < SPIN_CAP; ++spins) {
            asm volatile("" ::: "memory");
                u32x4 a0 = __builtin_amdgcn_raw_buffer_load_b128(rs, fo, 0, SC1);
                u32x4 a1 = __builtin_amdgcn_raw_buffer_load_b128(rs, fo + 1024, 0, SC1);
                unsigned bad = (a0[0] ^ tag) | (a0[1] ^ ~tag) | (a0[2] ^ tag) | (a0[3] ^ ~tag) |
                               (a1[0] ^ tag) | (a1[1] ^ ~tag) | (a1[2] ^ tag) | (a1[3] ^ ~tag);
                if (__all(bad == 0)) break;
                __builtin_amdgcn_s_sleep(1);
            }
            if (lane == 0) vflg[2] = 1u;
            STAMP(2);
            unsigned pf[14];
            {
                const int rr = lane >> 2, sg = lane & 3;
                const int colb = (b & 7) * 352 + sg * 64;
                const char* pg = (const char*)wg + (size_t)((b >> 3) * 16 + rr) * (FF * 2);
                const char* pu = (const char*)wu + (size_t)((b >> 3) * 16 + rr) * (FF * 2);
                int c0 = colb, c1 = colb + 256 - (sg < 2 ? 0 : 256);
                if (c0 > FF * 2 - 4) c0 = FF * 2 - 4;
                if (c1 > FF * 2 - 4) c1 = FF * 2 - 4;
                pf[0] = *(const unsigned*)(pg + c0); pf[1] = *(const unsigned*)(pu + c0);
                pf[2] = *(const unsigned*)(pg + c1); pf[3] = *(const unsigned*)(pu + c1);
                const int fr = has_ffn ? f0 : 0;
#pragma unroll
                for (int i = 0; i < 2; ++i) pf[4 + i] = *(const unsigned*)((const char*)wd + (size_t)fr * 1024 + i * 4096 + lane * 64);
#pragma unroll
                for (int i = 0; i < 4; ++i)
                    pf[6 + i] = *(const unsigned*)((const char*)wuv + (size_t)(j * 64 + i * 16 + (lane >> 2)) * 8192 + h * 256 + (lane & 3) * 64);
#pragma unroll
                for (int i = 0; i < 4; ++i)
                    pf[10 + i] = *(const unsigned*)((const char*)wo + ((size_t)h * 128 + j * 16) * 1024 + i * 4096 + lane * 64);
            }
#pragma unroll
            for (int i = 0; i < NBAR; ++i) asm volatile("s_barrier" ::: "memory");
            asm volatile("" :: "v"(pf[0]), "v"(pf[1]), "v"(pf[2]), "v"(pf[3]), "v"(pf[4]), "v"(pf[5]), "v"(pf[6]),
                         "v"(pf[7]), "v"(pf[8]), "v"(pf[9]), "v"(pf[10]), "v"(pf[11]), "v"(pf[12]), "v"(pf[13]));
            if (b == 224 && lane == 0) __hip_atomic_store((gu32*)(ws + WS_EPOCH), tag, RLX_AGENT);
            return;
        }
    }

    u32x4 wqv[16], wkv[2];
    bf16x8 bq[4];
    u32x4 cv[4];
    const float xt = bf2f(x[t]);
    {
#pragma unroll
        for (int i = 0; i < 16; ++i)
            wqv[i] = *(const u32x4*)(wq + (size_t)((t >> 4) + 32 * i) * 4096 + h * 128 + (t & 15) * 8);
#pragma unroll
        for (int i = 0; i < 2; ++i)
            wkv[i] = *(const u32x4*)(wuk + (size_t)(j * 64 + 8 * w + (lane >> 4) + 4 * i) * 4096 + h * 128 + (lane & 15) * 8);
        const u16* bp = ckv + (size_t)(key0 + r) * 512 + 64 * w + 8 * h2;
#pragma unroll
        for (int s = 0; s < 4; ++s) bq[s] = *(const bf16x8*)(bp + 16 * s);
        const u16* cp = ckv + (size_t)(kc * 512 + (t >> 2)) * 512 + mem * 32 + (t & 3) * 8;
#pragma unroll
        for (int i = 0; i < 4; ++i) cv[i] = *(const u32x4*)(cp + (size_t)i * 128 * 512);
    }
    __builtin_amdgcn_sched_barrier(0);
    if (t < 4) flg[t] = 0u;
    xs[(t & 31) * 16 + (t >> 5)] = xt;
    bar();
    STAMP(1);
    {
        float acc[8];
#pragma unroll
        for (int k = 0; k < 8; ++k) acc[k] = 0.f;
#pragma unroll
        for (int i4 = 0; i4 < 4; ++i4) {
            f32x4 xv = *(const f32x4*)(xs + (t >> 4) * 16 + i4 * 4);
#pragma unroll
            for (int ii = 0; ii < 4; ++ii) {
                const int i = i4 * 4 + ii;
#pragma unroll
                for (int jj = 0; jj < 4; ++jj) {
                    acc[2 * jj] += xv[ii] * bflo(wqv[i][jj]);
                    acc[2 * jj + 1] += xv[ii] * bfhi(wqv[i][jj]);
                }
            }
        }
        bool up = lane & 16;
        float s[4];
#pragma unroll
        for (int k = 0; k < 4; ++k) { float send = up ? acc[k] : acc[k + 4]; float keep = up ? acc[k + 4] : acc[k]; s[k] = keep + __shfl_xor(send, 16); }
        up = lane & 32;
        f32x2 s2;
#pragma unroll
        for (int k = 0; k < 2; ++k) { float send = up ? s[k] : s[k + 2]; float keep = up ? s[k + 2] : s[k]; s2[k] = keep + __shfl_xor(send, 32); }
        *(f32x2*)(sm1 + w * 128 + (lane & 15) * 8 + ((lane >> 4) & 1) * 4 + (lane >> 5) * 2) = s2;
        lgkm0();
        if (lane == 0) __hip_atomic_fetch_add(flg + 0, 1u, __ATOMIC_RELAXED, __HIP_MEMORY_SCOPE_WORKGROUP);
    }
    lds_wait(vflg + 0, 8);
    STAMP(2);
    lds_wait(vflg + 3, 1);
    {
        const int l16 = lane & 15;
        f32x4 qa0 = *(const f32x4*)(sm1 + l16 * 8), qa1 = *(const f32x4*)(sm1 + l16 * 8 + 4);
#pragma unroll
        for (int ww = 1; ww < 8; ++ww) { qa0 += *(const f32x4*)(sm1 + ww * 128 + l16 * 8); qa1 += *(const f32x4*)(sm1 + ww * 128 + l16 * 8 + 4); }
#pragma unroll
        for (int k = 0; k < 4; ++k) { qa0[k] = rbf(qa0[k]); qa1[k] = rbf(qa1[k]); }
        float d[2];
#pragma unroll
        for (int i = 0; i < 2; ++i) {
            d[i] = qa0[0] * bflo(wkv[i][0]) + qa0[1] * bfhi(wkv[i][0]) + qa0[2] * bflo(wkv[i][1]) + qa0[3] * bfhi(wkv[i][1]);
            d[i] += qa1[0] * bflo(wkv[i][2]) + qa1[1] * bfhi(wkv[i][2]) + qa1[2] * bflo(wkv[i][3]) + qa1[3] * bfhi(wkv[i][3]);
        }
        bool up = lane & 1;
        float send = up ? d[0] : d[1], keep = up ? d[1] : d[0];
        float v = keep + __shfl_xor(send, 1);
        v += __shfl_xor(v, 2);
        v += __shfl_xor(v, 4);
        v += __shfl_xor(v, 8);
        v *= 0.08838834764831845f;
        float vv[8];
#pragma unroll
        for (int k = 0; k < 8; ++k) vv[k] = __shfl(v, (k & 3) * 16 + (k >> 2));
        u32x4 o = {pk2(vv[0], vv[1]), pk2(vv[2], vv[3]), pk2(vv[4], vv[5]), pk2(vv[6], vv[7])};
        if (lane < 8)
            __builtin_amdgcn_raw_buffer_store_b128(o, rs, WS_QABS + lane * 32768 + ((j * 8 + w) * 32 + h) * 16, 0, SC1);
    }
#pragma unroll
    for (int i = 0; i < 4; ++i) *(u32x4*)(tile + (i * 128 + (t >> 2)) * 64 + (t & 3) * 16) = cv[i];
    lds_wait(vflg + 2, 1);
    STAMP(3);
    {
        u32x4 av[4];
        const int qo = WS_QABS + (b & 7) * 32768 + (w * 256 + lane) * 16;
        for (unsigned spins = 0; spins < SPIN_CAP; ++spins) {
            asm volatile("" ::: "memory");
#pragma unroll
            for (int s = 0; s < 4; ++s) av[s] = __builtin_amdgcn_raw_buffer_load_b128(rs, qo + 1024 * s, 0, SC1);
            unsigned m = umax(umax(max4(av[0]), max4(av[1])), umax(max4(av[2]), max4(av[3])));
            if (__all(m != POISON)) break;
            __builtin_amdgcn_s_sleep(1);
        }
        STAMP(4);
        f32x16 acc;
#pragma unroll
        for (int i = 0; i < 16; ++i) acc[i] = 0.f;
#pragma unroll
        for (int s = 0; s < 4; ++s)
            acc = __builtin_amdgcn_mfma_f32_32x32x16_bf16(__builtin_bit_cast(bf16x8, av[s]), bq[s], acc, 0, 0, 0);
#pragma unroll
        for (int i = 0; i < 16; ++i) {
            int head = (i & 3) + 8 * (i >> 2) + 4 * h2;
            red1[(w * 32 + head) * 32 + r] = acc[i];
        }
    }
    bar();
    {
        const int head = t >> 4, kq = (t & 15) * 2;
        f32x2 s2 = *(const f32x2*)(red1 + head * 32 + kq);
#pragma unroll
        for (int ww = 1; ww < 8; ++ww) s2 += *(const f32x2*)(red1 + (ww * 32 + head) * 32 + kq);
        float m = fmaxf(s2[0], s2[1]);
        m = fmaxf(m, __shfl_xor(m, 1));
        m = fmaxf(m, __shfl_xor(m, 2));
        m = fmaxf(m, __shfl_xor(m, 4));
        m = fmaxf(m, __shfl_xor(m, 8));
        unsigned p01 = pk2(__expf(s2[0] - m), __expf(s2[1] - m));
        float l = bflo(p01) + bfhi(p01);
        l += __shfl_xor(l, 1);
        l += __shfl_xor(l, 2);
        l += __shfl_xor(l, 4);
        l += __shfl_xor(l, 8);
        u32x4 pp = {p01, (unsigned)__shfl_down((int)p01, 1), (unsigned)__shfl_down((int)p01, 2), (unsigned)__shfl_down((int)p01, 3)};
        u32x2 ml = {asu(m), asu(l)};
        if ((t & 3) == 0)
            __builtin_amdgcn_raw_buffer_store_b128(pp, rs, WS_PF + ((kc * 32 + mem * 2) * 64 + ((t & 15) >> 2) * 32 + head) * 16, 0, 0);
        if ((t & 15) == 0)
            __builtin_amdgcn_raw_buffer_store_b64(ml, rs, WS_MLOCF + ((kc * 8 + (mem >> 1)) * 32 + head) * 16 + (mem & 1) * 8, 0, 0);
        if ((t & 3) == 0)
            __builtin_amdgcn_raw_buffer_store_b128(pp, rs, WS_P + ((kc * 32 + mem * 2) * 64 + ((t & 15) >> 2) * 32 + head) * 16, 0, SC1);
        if ((t & 15) == 0)
            __builtin_amdgcn_raw_buffer_store_b64(ml, rs, WS_MLOC + ((kc * 8 + (mem >> 1)) * 32 + head) * 16 + (mem & 1) * 8, 0, SC1);
    }
    STAMP(5);
    {
        u32x4 pv[4], mlv;
        for (unsigned spins = 0; spins < SPIN_CAP; ++spins) {
            asm volatile("" ::: "memory");
            const bool slow = (spins & 3) == 3;
            const int po = (slow ? WS_P : WS_PF) + ((kc * 32 + w * 4) * 64 + lane) * 16;
            const int mo = (slow ? WS_MLOC : WS_MLOCF) + ((kc * 8 + w) * 32 + r) * 16;
#pragma unroll
            for (int s = 0; s < 4; ++s) pv[s] = __builtin_amdgcn_raw_buffer_load_b128(rs, po + 1024 * s, 0, SC1);
            mlv = __builtin_amdgcn_raw_buffer_load_b128(rs, mo, 0, SC1);
            unsigned m = umax(umax(max4(pv[0]), max4(pv[1])), umax(max4(pv[2]), max4(pv[3])));
            m = umax(m, max4(mlv));
            if (__all(m != POISON)) break;
            __builtin_amdgcn_s_sleep(1);
        }
        STAMP(6);
        {
            float m0 = asf(mlv[0]), l0 = asf(mlv[1]), m1 = asf(mlv[2]), l1 = asf(mlv[3]);
            float Mw = fmaxf(m0, m1);
            float f0_ = __expf(m0 - Mw), f1_ = __expf(m1 - Mw);
            if (lane < 32) {
                fac[(2 * w) * 32 + r] = f0_; fac[(2 * w + 1) * 32 + r] = f1_;
                Mws[r * 8 + w] = Mw; Lws[r * 8 + w] = f0_ * l0 + f1_ * l1;
            }
        }
        const int g = lane >> 4;
        const int q = (lane & 15) >> 2, p = lane & 3;
        const int cb = g & 1;
        f32x16 tot;
#pragma unroll
        for (int mm = 0; mm < 2; ++mm) {
            f32x16 acc;
#pragma unroll
            for (int i = 0; i < 16; ++i) acc[i] = 0.f;
#pragma unroll
            for (int ss = 0; ss < 2; ++ss) {
                int s = mm * 2 + ss;
                int row = w * 64 + 16 * s + 8 * h2 + q;
                s16x4 t0 = __builtin_amdgcn_ds_read_tr16_b64_v4i16(
                    (__attribute__((address_space(3))) s16x4*)(tile + row * 64 + cb * 32 + p * 8));
                s16x4 t1 = __builtin_amdgcn_ds_read_tr16_b64_v4i16(
                    (__attribute__((address_space(3))) s16x4*)(tile + (row + 4) * 64 + cb * 32 + p * 8));
                s16x8 bb = {t0[0], t0[1], t0[2], t0[3], t1[0], t1[1], t1[2], t1[3]};
                acc = __builtin_amdgcn_mfma_f32_32x32x16_bf16(__builtin_bit_cast(bf16x8, pv[s]), __builtin_bit_cast(bf16x8, bb), acc, 0, 0, 0);
            }
            const float* fp = fac + (2 * w + mm) * 32 + 4 * h2;
#pragma unroll
            for (int qd = 0; qd < 4; ++qd) {
                f32x4 f4 = *(const f32x4*)(fp + 8 * qd);
#pragma unroll
                for (int e = 0; e < 4; ++e) {
                    if (mm == 0) tot[qd * 4 + e] = f4[e] * acc[qd * 4 + e];
                    else tot[qd * 4 + e] += f4[e] * acc[qd * 4 + e];
                }
            }
        }
#pragma unroll
        for (int i = 0; i < 16; ++i) {
            int head = (i & 3) + 8 * (i >> 2) + 4 * h2;
            red2[(w * 32 + head) * 32 + r] = tot[i];
        }
    }
    bar();
    {
        const int head = t >> 4, cq = (t & 15) * 2;
        f32x4 ma = *(const f32x4*)(Mws + head * 8), mb = *(const f32x4*)(Mws + head * 8 + 4);
        float M = fmaxf(fmaxf(fmaxf(ma[0], ma[1]), fmaxf(ma[2], ma[3])), fmaxf(fmaxf(mb[0], mb[1]), fmaxf(mb[2], mb[3])));
        float ew[8];
#pragma unroll
        for (int ww = 0; ww < 4; ++ww) { ew[ww] = __expf(ma[ww] - M); ew[ww + 4] = __expf(mb[ww] - M); }
        f32x2 s2 = {0.f, 0.f};
#pragma unroll
        for (int ww = 0; ww < 8; ++ww) s2 += ew[ww] * *(const f32x2*)(red2 + (ww * 32 + head) * 32 + cq);
        u32x4 o = {asu(s2[0]), asu(s2[1]), asu(__shfl_down(s2[0], 1)), asu(__shfl_down(s2[1], 1))};
        if ((t & 1) == 0)
            __builtin_amdgcn_raw_buffer_store_b128(o, rs, WS_OPART + ((kc * 32 + head) * 512 + mem * 32 + cq) * 4, 0, SC1);
        if (mem == 0 && (t & 15) == 0) {
            f32x4 la = *(const f32x4*)(Lws + head * 8), lb = *(const f32x4*)(Lws + head * 8 + 4);
            float L = 0.f;
#pragma unroll
            for (int ww = 0; ww < 4; ++ww) L += ew[ww] * la[ww] + ew[ww + 4] * lb[ww];
            u32x2 ml = {asu(M), asu(L)};
            __builtin_amdgcn_raw_buffer_store_b64(ml, rs, WS_ML + (head * 16 + kc) * 8, 0, SC1);
        }
    }
    STAMP(7);
    u32x4 uvv[2], ovv[2];
    {
        u32x4 gv = {0u, 0u, 0u, 0u}, uv_ = {0u, 0u, 0u, 0u}, dv = {0u, 0u, 0u, 0u};
        if (has_ffn) {
            gv = *(const u32x4*)(wg + (size_t)t * FF + f0);
            uv_ = *(const u32x4*)(wu + (size_t)t * FF + f0);
            dv = *(const u32x4*)(wd + (size_t)(f0 + w) * 512 + lane * 8);
        }
#pragma unroll
        for (int i = 0; i < 2; ++i)
            uvv[i] = *(const u32x4*)(wuv + (size_t)((t >> 1) + 256 * i) * 4096 + h * 128 + j * 16 + (t & 1) * 8);
#pragma unroll
        for (int i = 0; i < 2; ++i)
            ovv[i] = *(const u32x4*)(wo + ((size_t)h * 128 + j * 16 + w * 2 + i) * 512 + lane * 8);
        float p[16];
#pragma unroll
        for (int jj = 0; jj < 4; ++jj) {
            p[2 * jj] = xt * bflo(gv[jj]); p[2 * jj + 1] = xt * bfhi(gv[jj]);
            p[8 + 2 * jj] = xt * bflo(uv_[jj]); p[8 + 2 * jj + 1] = xt * bfhi(uv_[jj]);
        }
        bool up = lane & 1;
        float s8[8];
#pragma unroll
        for (int k = 0; k < 8; ++k) { float send = up ? p[k] : p[k + 8]; float keep = up ? p[k + 8] : p[k]; s8[k] = keep + __shfl_xor(send, 1); }
        up = lane & 2;
        float s4[4];
#pragma unroll
        for (int k = 0; k < 4; ++k) { float send = up ? s8[k] : s8[k + 4]; float keep = up ? s8[k + 4] : s8[k]; s4[k] = keep + __shfl_xor(send, 2); }
        up = lane & 4;
        float s2[2];
#pragma unroll
        for (int k = 0; k < 2; ++k) { float send = up ? s4[k] : s4[k + 2]; float keep = up ? s4[k + 2] : s4[k]; s2[k] = keep + __shfl_xor(send, 4); }
        up = lane & 8;
        float send = up ? s2[0] : s2[1]; float keep = up ? s2[1] : s2[0];
        float v = keep + __shfl_xor(send, 8);
        v += __shfl_xor(v, 16);
        v += __shfl_xor(v, 32);
        if (lane < 16) {
            int idx = (lane & 1) * 8 + ((lane >> 1) & 1) * 4 + ((lane >> 2) & 1) * 2 + ((lane >> 3) & 1);
            sm2[w * 16 + idx] = v;
        }
        bar();
        float gs = 0.f, us = 0.f;
#pragma unroll
        for (int ww = 0; ww < 8; ++ww) { gs += sm2[ww * 16 + w]; us += sm2[ww * 16 + 8 + w]; }
        gs = rbf(gs); us = rbf(us);
        float sg = rbf(gs / (1.f + __expf(-gs)));
        float hv = rbf(sg * us);
        f32x4 o0, o1;
        o0[0] = hv * bflo(dv[0]); o0[1] = hv * bfhi(dv[0]); o0[2] = hv * bflo(dv[1]); o0[3] = hv * bfhi(dv[1]);
        o1[0] = hv * bflo(dv[2]); o1[1] = hv * bfhi(dv[2]); o1[2] = hv * bflo(dv[3]); o1[3] = hv * bfhi(dv[3]);
        *(f32x4*)(redf + w * 512 + lane * 8) = o0;
        *(f32x4*)(redf + w * 512 + lane * 8 + 4) = o1;
        bar();
        if (has_ffn && t < 128) {
            f32x4 s = *(const f32x4*)(redf + t * 4);
#pragma unroll
            for (int ww = 1; ww < 8; ++ww) s += *(const f32x4*)(redf + ww * 512 + t * 4);
            __builtin_amdgcn_raw_buffer_store_b128(__builtin_bit_cast(u32x4, s), rs, WS_GB + fch * 2048 + t * 16, 0, SC1);
        }
    }
    STAMP(8);
    {
        const int qd = t >> 7;
        u32x4 ov[4];
        u32x2 ml;
        const int oo = WS_OPART + ((qd * 4 * 32 + h) * 512 + (t & 127) * 4) * 4;
        for (unsigned spins = 0; spins < SPIN_CAP; ++spins) {
            asm volatile("" ::: "memory");
#pragma unroll
            for (int i = 0; i < 4; ++i) ov[i] = __builtin_amdgcn_raw_buffer_load_b128(rs, oo + i * 65536, 0, SC1);
            ml = __builtin_amdgcn_raw_buffer_load_b64(rs, WS_ML + (h * 16 + (lane & 15)) * 8, 0, SC1);
            unsigned m = umax(umax(max4(ov[0]), max4(ov[1])), umax(max4(ov[2]), max4(ov[3])));
            m = umax(m, umax(ml[0], ml[1]));
            if (__all(m != POISON)) break;
            __builtin_amdgcn_s_sleep(1);
        }
        STAMP(9);
        float mk = asf(ml[0]), lk = asf(ml[1]);
        float M = mk;
        M = fmaxf(M, __shfl_xor(M, 1)); M = fmaxf(M, __shfl_xor(M, 2));
        M = fmaxf(M, __shfl_xor(M, 4)); M = fmaxf(M, __shfl_xor(M, 8));
        float wk = __expf(mk - M);
        float L = wk * lk;
        L += __shfl_xor(L, 1); L += __shfl_xor(L, 2); L += __shfl_xor(L, 4); L += __shfl_xor(L, 8);
        wk = wk / L;
        f32x4 c4 = {0.f, 0.f, 0.f, 0.f};
#pragma unroll
        for (int i = 0; i < 4; ++i) {
            float wi = __shfl(wk, qd * 4 + i);
            c4[0] += wi * asf(ov[i][0]); c4[1] += wi * asf(ov[i][1]); c4[2] += wi * asf(ov[i][2]); c4[3] += wi * asf(ov[i][3]);
        }
        *(f32x4*)(cpart + qd * 512 + (t & 127) * 4) = c4;
    }
    bar();
    {
        const int row = t >> 1;
        float c0 = (cpart[row] + cpart[512 + row]) + (cpart[1024 + row] + cpart[1536 + row]);
        float c1 = (cpart[row + 256] + cpart[768 + row]) + (cpart[1280 + row] + cpart[1792 + row]);
        float acc[8];
#pragma unroll
        for (int jj = 0; jj < 4; ++jj) {
            acc[2 * jj] = c0 * bflo(uvv[0][jj]) + c1 * bflo(uvv[1][jj]);
            acc[2 * jj + 1] = c0 * bfhi(uvv[0][jj]) + c1 * bfhi(uvv[1][jj]);
        }
        bool up = lane & 2;
        float s[4];
#pragma unroll
        for (int k = 0; k < 4; ++k) { float send = up ? acc[k] : acc[k + 4]; float keep = up ? acc[k + 4] : acc[k]; s[k] = keep + __shfl_xor(send, 2); }
        up = lane & 4;
        float s2[2];
#pragma unroll
        for (int k = 0; k < 2; ++k) { float send = up ? s[k] : s[k + 2]; float keep = up ? s[k + 2] : s[k]; s2[k] = keep + __shfl_xor(send, 4); }
        up = lane & 8;
        float send = up ? s2[0] : s2[1]; float keep = up ? s2[1] : s2[0];
        float v = keep + __shfl_xor(send, 8);
        v += __shfl_xor(v, 16);
        v += __shfl_xor(v, 32);
        if (lane < 16) {
            int d = (lane & 1) * 8 + ((lane >> 1) & 1) * 4 + ((lane >> 2) & 1) * 2 + ((lane >> 3) & 1);
            sm3[w * 16 + d] = v;
        }
    }
    bar();
    {
        float c0 = 0.f, c1 = 0.f;
#pragma unroll
        for (int ww = 0; ww < 8; ++ww) { c0 += sm3[ww * 16 + 2 * w]; c1 += sm3[ww * 16 + 2 * w + 1]; }
        c0 = rbf(c0); c1 = rbf(c1);
        f32x4 o0, o1;
        o0[0] = c0 * bflo(ovv[0][0]) + c1 * bflo(ovv[1][0]); o0[1] = c0 * bfhi(ovv[0][0]) + c1 * bfhi(ovv[1][0]);
        o0[2] = c0 * bflo(ovv[0][1]) + c1 * bflo(ovv[1][1]); o0[3] = c0 * bfhi(ovv[0][1]) + c1 * bfhi(ovv[1][1]);
        o1[0] = c0 * bflo(ovv[0][2]) + c1 * bflo(ovv[1][2]); o1[1] = c0 * bfhi(ovv[0][2]) + c1 * bfhi(ovv[1][2]);
        o1[2] = c0 * bflo(ovv[0][3]) + c1 * bflo(ovv[1][3]); o1[3] = c0 * bfhi(ovv[0][3]) + c1 * bfhi(ovv[1][3]);
        *(f32x4*)(redo + w * 512 + lane * 8) = o0;
        *(f32x4*)(redo + w * 512 + lane * 8 + 4) = o1;
    }
    bar();
    if (t < 128) {
        f32x4 s = *(const f32x4*)(redo + t * 4);
#pragma unroll
        for (int ww = 1; ww < 8; ++ww) s += *(const f32x4*)(redo + ww * 512 + t * 4);
        __builtin_amdgcn_raw_buffer_store_b128(__builtin_bit_cast(u32x4, s), rs, WS_GA + b * 2048 + t * 16, 0, SC1);
    }
    STAMP(10);
    if (cons) {
        const int k = b - 224;
        const int p = t >> 1, hf = t & 1;
        float gp = rbf(xt * bf2f(gw[t]));
        const int ao = WS_GA + p * 2048 + k * 64 + hf * 32;
        const int bo = WS_GB + (p < 172 ? p : 171) * 2048 + k * 64 + hf * 32;
        u32x4 a0, a1, b0, b1;
        for (unsigned spins = 0; spins < SPIN_CAP; ++spins) {
            asm volatile("" ::: "memory");
            a0 = __builtin_amdgcn_raw_buffer_load_b128(rs, ao, 0, SC1);
            a1 = __builtin_amdgcn_raw_buffer_load_b128(rs, ao + 16, 0, SC1);
            b0 = __builtin_amdgcn_raw_buffer_load_b128(rs, bo, 0, SC1);
            b1 = __builtin_amdgcn_raw_buffer_load_b128(rs, bo + 16, 0, SC1);
            unsigned m = umax(umax(max4(a0), max4(a1)), umax(max4(b0), max4(b1)));
            if (__all(m != POISON)) break;
            __builtin_amdgcn_s_sleep(1);
        }
        STAMP(11);
        float v[16];
        const float bz = p < 172 ? 1.f : 0.f;
#pragma unroll
        for (int i = 0; i < 4; ++i) {
            v[i] = asf(a0[i]); v[4 + i] = asf(a1[i]);
            v[8 + i] = p < 172 ? asf(b0[i]) : 0.f; v[12 + i] = p < 172 ? asf(b1[i]) : 0.f;
        }
        (void)bz;
        bool up = lane & 2;
        float s8[8];
#pragma unroll
        for (int q = 0; q < 8; ++q) { float send = up ? v[q] : v[q + 8]; float keep = up ? v[q + 8] : v[q]; s8[q] = keep + __shfl_xor(send, 2); }
        up = lane & 4;
        float s4[4];
#pragma unroll
        for (int q = 0; q < 4; ++q) { float send = up ? s8[q] : s8[q + 4]; float keep = up ? s8[q + 4] : s8[q]; s4[q] = keep + __shfl_xor(send, 4); }
        up = lane & 8;
        float s2[2];
#pragma unroll
        for (int q = 0; q < 2; ++q) { float send = up ? s4[q] : s4[q + 2]; float keep = up ? s4[q + 2] : s4[q]; s2[q] = keep + __shfl_xor(send, 8); }
        up = lane & 16;
        float send = up ? s2[0] : s2[1]; float keep = up ? s2[1] : s2[0];
        float vv = keep + __shfl_xor(send, 16);
        vv += __shfl_xor(vv, 32);
#pragma unroll
        for (int o = 32; o >= 1; o >>= 1) gp += __shfl_xor(gp, o);
        if (lane < 32) {
            int idx = ((lane >> 1) & 1) * 16 + (lane & 1) * 8 + ((lane >> 2) & 1) * 4 + ((lane >> 3) & 1) * 2 + ((lane >> 4) & 1);
            fin[w * 32 + idx] = vv;
        }
        if (lane == 0) fing[w] = gp;
    }
    bar();
    if (cons && t < 16) {
        float a = 0.f, bb = 0.f, gs = 0.f;
#pragma unroll
        for (int ww = 0; ww < 8; ++ww) { a += fin[ww * 32 + t]; bb += fin[ww * 32 + 16 + t]; gs += fing[ww]; }
        gs = rbf(gs);
        float g = rbf(1.f / (1.f + __expf(-gs)));
        a = rbf(a); bb = rbf(bb);
        float rr = rbf(g * a) + rbf(rbf(1.f - g) * bb);
        out[(b - 224) * 16 + t] = f2bf(rr);
    }
    STAMP(12);
}

extern "C" void kernel_launch(void* const* d_in, const int* in_sizes, int n_in,
                              void* d_out, int out_size, void* d_ws, size_t ws_size,
                              hipStream_t stream) {
    const u16* x = (const u16*)d_in[0];
    const u16* ckv = (const u16*)d_in[1];
    const u16* wq = (const u16*)d_in[2];
    const u16* wuk = (const u16*)d_in[3];
    const u16* wuv = (const u16*)d_in[4];
    const u16* wo = (const u16*)d_in[5];
    const u16* wg = (const u16*)d_in[7];
    const u16* wu = (const u16*)d_in[8];
    const u16* wd = (const u16*)d_in[9];
    const u16* gw = (const u16*)d_in[10];
    mega<<<256, 576, 0, stream>>>(x, ckv, wq, wuk, wuv, wo, wg, wu, wd, gw, (char*)d_ws, (u16*)d_out);
}
```

```cpp
#include <hip/hip_runtime.h>
#include <hip/hip_bf16.h>
#include <stdint.h>

#define SK 8192
#define FF 1376

typedef unsigned short u16;
typedef unsigned long long u64;
typedef __attribute__((ext_vector_type(8))) __bf16 bf16x8;
typedef __attribute__((ext_vector_type(2))) __bf16 bf16x2;
typedef __attribute__((ext_vector_type(4))) short s16x4;
typedef __attribute__((ext_vector_type(8))) short s16x8;
typedef __attribute__((ext_vector_type(16))) float f32x16;
typedef __attribute__((ext_vector_type(4))) float f32x4;
typedef __attribute__((ext_vector_type(2))) float f32x2;
typedef __attribute__((ext_vector_type(4))) unsigned int u32x4;
typedef __attribute__((ext_vector_type(2))) unsigned int u32x2;
#define GAS __attribute__((address_space(1)))
typedef GAS unsigned gu32;
typedef GAS u64 gu64;
#define RLX_AGENT __ATOMIC_RELAXED, __HIP_MEMORY_SCOPE_AGENT
#define SC1 16
#define POISON 0xFFFFFFFFu

__device__ __forceinline__ float bf2f(u16 v) { return __uint_as_float(((unsigned)v) << 16); }
__device__ __forceinline__ float bflo(unsigned v) { return __uint_as_float(v << 16); }
__device__ __forceinline__ float bfhi(unsigned v) { return __uint_as_float(v & 0xffff0000u); }
__device__ __forceinline__ float asf(unsigned v) { return __uint_as_float(v); }
__device__ __forceinline__ unsigned asu(float v) { return __float_as_uint(v); }
__device__ __forceinline__ unsigned pk2(float a, float b) {
    bf16x2 v = {(__bf16)a, (__bf16)b};
    return __builtin_bit_cast(unsigned, v);
}
__device__ __forceinline__ u16 f2bf(float f) { __bf16 v = (__bf16)f; return __builtin_bit_cast(u16, v); }
__device__ __forceinline__ float rbf(float f) { return bf2f(f2bf(f)); }
__device__ __forceinline__ unsigned umax(unsigned a, unsigned b) { return a > b ? a : b; }
__device__ __forceinline__ unsigned max4(u32x4 v) { return umax(umax(v[0], v[1]), umax(v[2], v[3])); }

#define NREP 32
#define WS_EPOCH   0
#define WS_F0      256
#define WS_QG      (WS_F0 + NREP * 2048)
#define WS_QABS    (WS_QG + 256 * 1024)
#define WS_P       (WS_QABS + 8 * 32768)
#define WS_MLOC    (WS_P + 16 * 32 * 512 * 2)
#define WS_OPART   (WS_MLOC + 16 * 32 * 16 * 8)
#define WS_ML      (WS_OPART + 16 * 32 * 512 * 4)
#define WS_GA      (WS_ML + 32 * 16 * 8)
#define WS_GB      (WS_GA + 256 * 2048)
#define WS_QGF     (WS_GB + 172 * 2048)
#define WS_PF      (WS_QGF + 256 * 1024)
#define WS_MLOCF   (WS_PF + 16 * 32 * 512 * 2)
#define WS_END     (WS_MLOCF + 16 * 32 * 16 * 8)

#define SPIN_CAP (1u << 17)
#define STAMP(i)
#define CKV_MODE 1
#define NBAR 8

__device__ __forceinline__ void lgkm0() { asm volatile("s_waitcnt lgkmcnt(0)" ::: "memory"); }
__device__ __forceinline__ void vm0() { asm volatile("s_waitcnt vmcnt(0)" ::: "memory"); }
__device__ __forceinline__ void bar() { asm volatile("s_waitcnt lgkmcnt(0)\n\ts_barrier" ::: "memory"); }
typedef volatile __attribute__((address_space(3))) unsigned lds_u32;
__device__ __forceinline__ void lds_wait(lds_u32* p, unsigned target) {
    for (unsigned s = 0; s < SPIN_CAP; ++s) {
        if (*p >= target) break;
        __builtin_amdgcn_s_sleep(1);
    }
    asm volatile("" ::: "memory");
}

__global__ __launch_bounds__(640, 1) void mega(const u16* __restrict__ x, const u16* __restrict__ ckv,
                                               const u16* __restrict__ wq, const u16* __restrict__ wuk,
                                               const u16* __restrict__ wuv, const u16* __restrict__ wo,
                                               const u16* __restrict__ wg, const u16* __restrict__ wu,
                                               const u16* __restrict__ wd, const u16* __restrict__ gw,
                                               char* __restrict__ ws, u16* __restrict__ out) {
    __shared__ __attribute__((aligned(16))) char tile[512 * 64];
    __shared__ __attribute__((aligned(16))) float red1[8 * 1024];
    __shared__ __attribute__((aligned(16))) float red2[8 * 1024];
    __shared__ __attribute__((aligned(16))) float redf[8 * 512];
    __shared__ __attribute__((aligned(16))) float redo[8 * 512];
    __shared__ __attribute__((aligned(16))) float sm1[8 * 128];
    __shared__ __attribute__((aligned(16))) float sm2[8 * 16];
    __shared__ __attribute__((aligned(16))) float sm3[8 * 16];
    __shared__ __attribute__((aligned(16))) float qs[128];
    __shared__ __attribute__((aligned(16))) float fac[16 * 32];
    __shared__ __attribute__((aligned(16))) float Mws[32 * 8];
    __shared__ __attribute__((aligned(16))) float Lws[32 * 8];
    __shared__ __attribute__((aligned(16))) float cpart[4 * 512];
    __shared__ __attribute__((aligned(16))) float fin[8 * 32];
    __shared__ __attribute__((aligned(16))) float fing[8];
    __shared__ unsigned flg[4];

    const int b = blockIdx.x;
    const int t = threadIdx.x;
    const int lane = t & 63, w = t >> 6;
    const int h = b & 31, j = b >> 5;
    const int kc = b & 15, mem = b >> 4;
    const int key0 = kc * 512 + mem * 32;
    const int r = lane & 31, h2 = lane >> 5;
    const int fidx = b >> 3;
    const int fch = (b & 7) * 22 + fidx;
    const bool has_ffn = (fidx < 22) && (fch < 172);
    const int f0 = fch * 8;
    const bool cons = b >= 224;
    const __amdgpu_buffer_rsrc_t rs = __builtin_amdgcn_make_buffer_rsrc(ws, 0, WS_END, 0x00020000);
    lds_u32* vflg = (lds_u32*)flg;
    STAMP(0);

    if (w >= 8) {
        const unsigned e = __hip_atomic_load((gu32*)(ws + WS_EPOCH), RLX_AGENT);
        const u32x4 P4 = {POISON, POISON, POISON, POISON};
        const u32x2 P2 = {POISON, POISON};
        if (w == 9)
            __builtin_amdgcn_raw_buffer_store_b128(P4, rs, WS_QABS + (lane >> 3) * 32768 + ((j * 8 + (lane & 7)) * 32 + h) * 16, 0, SC1);
        bar();
        const unsigned tag = e + 1u;
        if (w == 8) {
            lds_wait(vflg + 0, 8);
            STAMP(1);
            f32x2 s = *(const f32x2*)(sm1 + 2 * lane);
#pragma unroll
            for (int ww = 1; ww < 8; ++ww) s += *(const f32x2*)(sm1 + ww * 128 + 2 * lane);
            u32x4 g = {tag, asu(s[0]), asu(s[1]), ~tag};
            __builtin_amdgcn_raw_buffer_store_b128(g, rs, WS_QGF + ((h * 8 + j) * 64 + lane) * 16, 0, 0);
            __builtin_amdgcn_raw_buffer_store_b128(g, rs, WS_QG + ((h * 8 + j) * 64 + lane) * 16, 0, SC1);
            for (unsigned spins = 0; spins < SPIN_CAP; ++spins) {
            asm volatile("" ::: "memory");
                u32x4 v[8];
                const int qb = ((spins & 3) == 3 ? WS_QG : WS_QGF) + (h * 8 * 64 + lane) * 16;
#pragma unroll
                for (int jj = 0; jj < 8; ++jj)
                    v[jj] = __builtin_amdgcn_raw_buffer_load_b128(rs, qb + jj * 1024, 0, SC1);
                unsigned bad = 0;
                float q0 = 0.f, q1 = 0.f;
#pragma unroll
                for (int jj = 0; jj < 8; ++jj) {
                    bad |= (v[jj][0] ^ tag) | (v[jj][3] ^ ~tag);
                    q0 += asf(v[jj][1]); q1 += asf(v[jj][2]);
                }
                if (__all(bad == 0) || spins == SPIN_CAP - 1) {
                    f32x2 qq = {rbf(q0), rbf(q1)};
                    *(f32x2*)(qs + 2 * lane) = qq;
                    break;
                }
                __builtin_amdgcn_s_sleep(1);
            }
            lgkm0();
            if (lane == 0) vflg[1] = 1u;
            STAMP(2);
        } else {
            vm0();
            {
                const unsigned tg = e + 1u;
                const u64 want = ((u64)(~tg) << 32) | tg;
                if (lane < NREP) __hip_atomic_store((gu64*)(ws + WS_F0 + lane * 2048 + b * 8), want, RLX_AGENT);
            }
#pragma unroll
            for (int i = 0; i < 2; ++i) {
                __builtin_amdgcn_raw_buffer_store_b128(P4, rs, WS_P + ((kc * 32 + mem * 2 + i) * 64 + lane) * 16, 0, SC1);
                __builtin_amdgcn_raw_buffer_store_b128(P4, rs, WS_PF + ((kc * 32 + mem * 2 + i) * 64 + lane) * 16, 0, SC1);
            }
            __builtin_amdgcn_raw_buffer_store_b64(P2, rs, (lane < 32 ? WS_MLOC : WS_MLOCF) + ((kc * 8 + (mem >> 1)) * 32 + r) * 16 + (mem & 1) * 8, 0, SC1);
#pragma unroll
            for (int i = 0; i < 4; ++i)
                __builtin_amdgcn_raw_buffer_store_b128(P4, rs, WS_OPART + ((kc * 32 + i * 8 + (lane >> 3)) * 512 + mem * 32) * 4 + (lane & 7) * 16, 0, SC1);
            if (mem == 0 && lane < 32) __builtin_amdgcn_raw_buffer_store_b64(P2, rs, WS_ML + (lane * 16 + kc) * 8, 0, SC1);
#pragma unroll
            for (int i = 0; i < 2; ++i)
                __builtin_amdgcn_raw_buffer_store_b128(P4, rs, WS_GA + b * 2048 + i * 1024 + lane * 16, 0, SC1);
            if (has_ffn) {
#pragma unroll
                for (int i = 0; i < 2; ++i)
                    __builtin_amdgcn_raw_buffer_store_b128(P4, rs, WS_GB + fch * 2048 + i * 1024 + lane * 16, 0, SC1);
            }
            vm0();
            if (lane == 0) vflg[3] = 1u;
            STAMP(1);
            const int fo = WS_F0 + (b & (NREP - 1)) * 2048 + lane * 16;
            for (unsigned spins = 0; spins < SPIN_CAP; ++spins) {
            asm volatile("" ::: "memory");
                u32x4 a0 = __builtin_amdgcn_raw_buffer_load_b128(rs, fo, 0, SC1);
                u32x4 a1 = __builtin_amdgcn_raw_buffer_load_b128(rs, fo + 1024, 0, SC1);
                unsigned bad = (a0[0] ^ tag) | (a0[1] ^ ~tag) | (a0[2] ^ tag) | (a0[3] ^ ~tag) |
                               (a1[0] ^ tag) | (a1[1] ^ ~tag) | (a1[2] ^ tag) | (a1[3] ^ ~tag);
                if (__all(bad == 0)) break;
                __builtin_amdgcn_s_sleep(1);
            }
            if (lane == 0) vflg[2] = 1u;
            STAMP(2);
#pragma unroll
            for (int i = 0; i < NBAR; ++i) asm volatile("s_barrier" ::: "memory");
            return;
        }
#pragma unroll
        for (int i = 0; i < NBAR; ++i) asm volatile("s_barrier" ::: "memory");
        if (b == 224 && lane == 0) __hip_atomic_store((gu32*)(ws + WS_EPOCH), tag, RLX_AGENT);
        return;
    }

    u32x4 wqv[2], wkv[2];
    bf16x8 bq[4];
    float x0, x1;
    {
        const int row = j * 64 + (t >> 4);
        x0 = bf2f(x[row]); x1 = bf2f(x[row + 32]);
#pragma unroll
        for (int i = 0; i < 2; ++i)
            wqv[i] = *(const u32x4*)(wq + (size_t)(row + 32 * i) * 4096 + h * 128 + (t & 15) * 8);
#pragma unroll
        for (int i = 0; i < 2; ++i)
            wkv[i] = *(const u32x4*)(wuk + (size_t)(j * 64 + 8 * w + (lane >> 4) + 4 * i) * 4096 + h * 128 + (lane & 15) * 8);
        const u16* bp = ckv + (size_t)(key0 + r) * 512 + 64 * w + 8 * h2;
#pragma unroll
        for (int s = 0; s < 4; ++s) bq[s] = *(const bf16x8*)(bp + 16 * s);
    }
    if (t < 4) flg[t] = 0u;
    bar();
    __builtin_amdgcn_sched_barrier(0);
    asm volatile("s_waitcnt vmcnt(6)" ::: "memory");
    STAMP(1);
    u32x4 cv[4];
    {
        float acc[8];
#pragma unroll
        for (int jj = 0; jj < 4; ++jj) {
            acc[2 * jj] = x0 * bflo(wqv[0][jj]) + x1 * bflo(wqv[1][jj]);
            acc[2 * jj + 1] = x0 * bfhi(wqv[0][jj]) + x1 * bfhi(wqv[1][jj]);
        }
        bool up = lane & 16;
        float s[4];
#pragma unroll
        for (int k = 0; k < 4; ++k) { float send = up ? acc[k] : acc[k + 4]; float keep = up ? acc[k + 4] : acc[k]; s[k] = keep + __shfl_xor(send, 16); }
        up = lane & 32;
        f32x2 s2;
#pragma unroll
        for (int k = 0; k < 2; ++k) { float send = up ? s[k] : s[k + 2]; float keep = up ? s[k + 2] : s[k]; s2[k] = keep + __shfl_xor(send, 32); }
        *(f32x2*)(sm1 + w * 128 + (lane & 15) * 8 + ((lane >> 4) & 1) * 4 + (lane >> 5) * 2) = s2;
        lgkm0();
        if (lane == 0) __hip_atomic_fetch_add(flg + 0, 1u, __ATOMIC_RELAXED, __HIP_MEMORY_SCOPE_WORKGROUP);
    }
    __builtin_amdgcn_sched_barrier(0);
    lds_wait(vflg + 1, 1);
    STAMP(2);
    lds_wait(vflg + 3, 1);
    {
        const int l16 = lane & 15;
        f32x4 qa0 = *(const f32x4*)(qs + l16 * 8), qa1 = *(const f32x4*)(qs + l16 * 8 + 4);
        float d[2];
#pragma unroll
        for (int i = 0; i < 2; ++i) {
            d[i] = qa0[0] * bflo(wkv[i][0]) + qa0[1] * bfhi(wkv[i][0]) + qa0[2] * bflo(wkv[i][1]) + qa0[3] * bfhi(wkv[i][1]);
            d[i] += qa1[0] * bflo(wkv[i][2]) + qa1[1] * bfhi(wkv[i][2]) + qa1[2] * bflo(wkv[i][3]) + qa1[3] * bfhi(wkv[i][3]);
        }
        bool up = lane & 1;
        float send = up ? d[0] : d[1], keep = up ? d[1] : d[0];
        float v = keep + __shfl_xor(send, 1);
        v += __shfl_xor(v, 2);
        v += __shfl_xor(v, 4);
        v += __shfl_xor(v, 8);
        v *= 0.08838834764831845f;
        float vv[8];
#pragma unroll
        for (int k = 0; k < 8; ++k) vv[k] = __shfl(v, (k & 3) * 16 + (k >> 2));
        u32x4 o = {pk2(vv[0], vv[1]), pk2(vv[2], vv[3]), pk2(vv[4], vv[5]), pk2(vv[6], vv[7])};
        if (lane < 8)
            __builtin_amdgcn_raw_buffer_store_b128(o, rs, WS_QABS + lane * 32768 + ((j * 8 + w) * 32 + h) * 16, 0, SC1);
    }
    {
        const u16* cp = ckv + (size_t)(kc * 512 + (t >> 2)) * 512 + mem * 32 + (t & 3) * 8;
#pragma unroll
        for (int i = 0; i < 4; ++i) cv[i] = *(const u32x4*)(cp + (size_t)i * 128 * 512);
    }
    __builtin_amdgcn_sched_barrier(0);
#pragma unroll
    for (int i = 0; i < 4; ++i) *(u32x4*)(tile + (i * 128 + (t >> 2)) * 64 + (t & 3) * 16) = cv[i];
    lds_wait(vflg + 2, 1);
    STAMP(3);
    {
        u32x4 av[4];
        const int qo = WS_QABS + (b & 7) * 32768 + (w * 256 + lane) * 16;
        for (unsigned spins = 0; spins < SPIN_CAP; ++spins) {
            asm volatile("" ::: "memory");
#pragma unroll
            for (int s = 0; s < 4; ++s) av[s] = __builtin_amdgcn_raw_buffer_load_b128(rs, qo + 1024 * s, 0, SC1);
            unsigned m = umax(umax(max4(av[0]), max4(av[1])), umax(max4(av[2]), max4(av[3])));
            if (__all(m != POISON)) break;
            __builtin_amdgcn_s_sleep(1);
        }
        STAMP(4);
        f32x16 acc;
#pragma unroll
        for (int i = 0; i < 16; ++i) acc[i] = 0.f;
#pragma unroll
        for (int s = 0; s < 4; ++s)
            acc = __builtin_amdgcn_mfma_f32_32x32x16_bf16(__builtin_bit_cast(bf16x8, av[s]), bq[s], acc, 0, 0, 0);
#pragma unroll
        for (int i = 0; i < 16; ++i) {
            int head = (i & 3) + 8 * (i >> 2) + 4 * h2;
            red1[(w * 32 + head) * 32 + r] = acc[i];
        }
    }
    bar();
    {
        const int head = t >> 4, kq = (t & 15) * 2;
        f32x2 s2 = *(const f32x2*)(red1 + head * 32 + kq);
#pragma unroll
        for (int ww = 1; ww < 8; ++ww) s2 += *(const f32x2*)(red1 + (ww * 32 + head) * 32 + kq);
        float m = fmaxf(s2[0], s2[1]);
        m = fmaxf(m, __shfl_xor(m, 1));
        m = fmaxf(m, __shfl_xor(m, 2));
        m = fmaxf(m, __shfl_xor(m, 4));
        m = fmaxf(m, __shfl_xor(m, 8));
        unsigned p01 = pk2(__expf(s2[0] - m), __expf(s2[1] - m));
        float l = bflo(p01) + bfhi(p01);
        l += __shfl_xor(l, 1);
        l += __shfl_xor(l, 2);
        l += __shfl_xor(l, 4);
        l += __shfl_xor(l, 8);
        u32x4 pp = {p01, (unsigned)__shfl_down((int)p01, 1), (unsigned)__shfl_down((int)p01, 2), (unsigned)__shfl_down((int)p01, 3)};
        u32x2 ml = {asu(m), asu(l)};
        if ((t & 3) == 0)
            __builtin_amdgcn_raw_buffer_store_b128(pp, rs, WS_PF + ((kc * 32 + mem * 2) * 64 + ((t & 15) >> 2) * 32 + head) * 16, 0, 0);
        if ((t & 15) == 0)
            __builtin_amdgcn_raw_buffer_store_b64(ml, rs, WS_MLOCF + ((kc * 8 + (mem >> 1)) * 32 + head) * 16 + (mem & 1) * 8, 0, 0);
        if ((t & 3) == 0)
            __builtin_amdgcn_raw_buffer_store_b128(pp, rs, WS_P + ((kc * 32 + mem * 2) * 64 + ((t & 15) >> 2) * 32 + head) * 16, 0, SC1);
        if ((t & 15) == 0)
            __builtin_amdgcn_raw_buffer_store_b64(ml, rs, WS_MLOC + ((kc * 8 + (mem >> 1)) * 32 + head) * 16 + (mem & 1) * 8, 0, SC1);
    }
    STAMP(5);
    u32x4 uvv[2], ovv[2];
    u32x4 gv, uv_, dv;
    float xt;
    {
        u32x4 pv[4], mlv;
        for (unsigned spins = 0; spins < SPIN_CAP; ++spins) {
            asm volatile("" ::: "memory");
            const bool slow = (spins & 3) == 3;
            const int po = (slow ? WS_P : WS_PF) + ((kc * 32 + w * 4) * 64 + lane) * 16;
            const int mo = (slow ? WS_MLOC : WS_MLOCF) + ((kc * 8 + w) * 32 + r) * 16;
#pragma unroll
            for (int s = 0; s < 4; ++s) pv[s] = __builtin_amdgcn_raw_buffer_load_b128(rs, po + 1024 * s, 0, SC1);
            mlv = __builtin_amdgcn_raw_buffer_load_b128(rs, mo, 0, SC1);
            unsigned m = umax(umax(max4(pv[0]), max4(pv[1])), umax(max4(pv[2]), max4(pv[3])));
            m = umax(m, max4(mlv));
            if (__all(m != POISON)) break;
            __builtin_amdgcn_s_sleep(1);
        }
        STAMP(6);
        xt = bf2f(x[t]);
#pragma unroll
        for (int i = 0; i < 2; ++i)
            uvv[i] = *(const u32x4*)(wuv + (size_t)((t >> 1) + 256 * i) * 4096 + h * 128 + j * 16 + (t & 1) * 8);
#pragma unroll
        for (int i = 0; i < 2; ++i)
            ovv[i] = *(const u32x4*)(wo + ((size_t)h * 128 + j * 16 + w * 2 + i) * 512 + lane * 8);
        {
            const int fz = has_ffn ? f0 : (b & 7) * 176;
            gv = *(const u32x4*)(wg + (size_t)t * FF + fz);
            uv_ = *(const u32x4*)(wu + (size_t)t * FF + fz);
            dv = *(const u32x4*)(wd + (size_t)(fz + w) * 512 + lane * 8);
        }
        __builtin_amdgcn_sched_barrier(0);
        {
            float m0 = asf(mlv[0]), l0 = asf(mlv[1]), m1 = asf(mlv[2]), l1 = asf(mlv[3]);
            float Mw = fmaxf(m0, m1);
            float f0_ = __expf(m0 - Mw), f1_ = __expf(m1 - Mw);
            if (lane < 32) {
                fac[(2 * w) * 32 + r] = f0_; fac[(2 * w + 1) * 32 + r] = f1_;
                Mws[r * 8 + w] = Mw; Lws[r * 8 + w] = f0_ * l0 + f1_ * l1;
            }
        }
        const int g = lane >> 4;
        const int q = (lane & 15) >> 2, p = lane & 3;
        const int cb = g & 1;
        f32x16 tot;
#pragma unroll
        for (int mm = 0; mm < 2; ++mm) {
            f32x16 acc;
#pragma unroll
            for (int i = 0; i < 16; ++i) acc[i] = 0.f;
#pragma unroll
            for (int ss = 0; ss < 2; ++ss) {
                int s = mm * 2 + ss;
                int row = w * 64 + 16 * s + 8 * h2 + q;
                s16x4 t0 = __builtin_amdgcn_ds_read_tr16_b64_v4i16(
                    (__attribute__((address_space(3))) s16x4*)(tile + row * 64 + cb * 32 + p * 8));
                s16x4 t1 = __builtin_amdgcn_ds_read_tr16_b64_v4i16(
                    (__attribute__((address_space(3))) s16x4*)(tile + (row + 4) * 64 + cb * 32 + p * 8));
                s16x8 bb = {t0[0], t0[1], t0[2], t0[3], t1[0], t1[1], t1[2], t1[3]};
                acc = __builtin_amdgcn_mfma_f32_32x32x16_bf16(__builtin_bit_cast(bf16x8, pv[s]), __builtin_bit_cast(bf16x8, bb), acc, 0, 0, 0);
            }
            const float* fp = fac + (2 * w + mm) * 32 + 4 * h2;
#pragma unroll
            for (int qd = 0; qd < 4; ++qd) {
                f32x4 f4 = *(const f32x4*)(fp + 8 * qd);
#pragma unroll
                for (int e = 0; e < 4; ++e) {
                    if (mm == 0) tot[qd * 4 + e] = f4[e] * acc[qd * 4 + e];
                    else tot[qd * 4 + e] += f4[e] * acc[qd * 4 + e];
                }
            }
        }
#pragma unroll
        for (int i = 0; i < 16; ++i) {
            int head = (i & 3) + 8 * (i >> 2) + 4 * h2;
            red2[(w * 32 + head) * 32 + r] = tot[i];
        }
    }
    bar();
    {
        const int head = t >> 4, cq = (t & 15) * 2;
        f32x4 ma = *(const f32x4*)(Mws + head * 8), mb = *(const f32x4*)(Mws + head * 8 + 4);
        float M = fmaxf(fmaxf(fmaxf(ma[0], ma[1]), fmaxf(ma[2], ma[3])), fmaxf(fmaxf(mb[0], mb[1]), fmaxf(mb[2], mb[3])));
        float ew[8];
#pragma unroll
        for (int ww = 0; ww < 4; ++ww) { ew[ww] = __expf(ma[ww] - M); ew[ww + 4] = __expf(mb[ww] - M); }
        f32x2 s2 = {0.f, 0.f};
#pragma unroll
        for (int ww = 0; ww < 8; ++ww) s2 += ew[ww] * *(const f32x2*)(red2 + (ww * 32 + head) * 32 + cq);
        u32x4 o = {asu(s2[0]), asu(s2[1]), asu(__shfl_down(s2[0], 1)), asu(__shfl_down(s2[1], 1))};
        if ((t & 1) == 0)
            __builtin_amdgcn_raw_buffer_store_b128(o, rs, WS_OPART + ((kc * 32 + head) * 512 + mem * 32 + cq) * 4, 0, SC1);
        if (mem == 0 && (t & 15) == 0) {
            f32x4 la = *(const f32x4*)(Lws + head * 8), lb = *(const f32x4*)(Lws + head * 8 + 4);
            float L = 0.f;
#pragma unroll
            for (int ww = 0; ww < 4; ++ww) L += ew[ww] * la[ww] + ew[ww + 4] * lb[ww];
            u32x2 ml = {asu(M), asu(L)};
            __builtin_amdgcn_raw_buffer_store_b64(ml, rs, WS_ML + (head * 16 + kc) * 8, 0, SC1);
        }
    }
    STAMP(7);
    {
        float p[16];
#pragma unroll
        for (int jj = 0; jj < 4; ++jj) {
            p[2 * jj] = xt * bflo(gv[jj]); p[2 * jj + 1] = xt * bfhi(gv[jj]);
            p[8 + 2 * jj] = xt * bflo(uv_[jj]); p[8 + 2 * jj + 1] = xt * bfhi(uv_[jj]);
        }
        bool up = lane & 1;
        float s8[8];
#pragma unroll
        for (int k = 0; k < 8; ++k) { float send = up ? p[k] : p[k + 8]; float keep = up ? p[k + 8] : p[k]; s8[k] = keep + __shfl_xor(send, 1); }
        up = lane & 2;
        float s4[4];
#pragma unroll
        for (int k = 0; k < 4; ++k) { float send = up ? s8[k] : s8[k + 4]; float keep = up ? s8[k + 4] : s8[k]; s4[k] = keep + __shfl_xor(send, 2); }
        up = lane & 4;
        float s2[2];
#pragma unroll
        for (int k = 0; k < 2; ++k) { float send = up ? s4[k] : s4[k + 2]; float keep = up ? s4[k + 2] : s4[k]; s2[k] = keep + __shfl_xor(send, 4); }
        up = lane & 8;
        float send = up ? s2[0] : s2[1]; float keep = up ? s2[1] : s2[0];
        float v = keep + __shfl_xor(send, 8);
        v += __shfl_xor(v, 16);
        v += __shfl_xor(v, 32);
        if (lane < 16) {
            int idx = (lane & 1) * 8 + ((lane >> 1) & 1) * 4 + ((lane >> 2) & 1) * 2 + ((lane >> 3) & 1);
            sm2[w * 16 + idx] = v;
        }
        bar();
        float gs = 0.f, us = 0.f;
#pragma unroll
        for (int ww = 0; ww < 8; ++ww) { gs += sm2[ww * 16 + w]; us += sm2[ww * 16 + 8 + w]; }
        gs = rbf(gs); us = rbf(us);
        float sg = rbf(gs / (1.f + __expf(-gs)));
        float hv = rbf(sg * us);
        f32x4 o0, o1;
        o0[0] = hv * bflo(dv[0]); o0[1] = hv * bfhi(dv[0]); o0[2] = hv * bflo(dv[1]); o0[3] = hv * bfhi(dv[1]);
        o1[0] = hv * bflo(dv[2]); o1[1] = hv * bfhi(dv[2]); o1[2] = hv * bflo(dv[3]); o1[3] = hv * bfhi(dv[3]);
        *(f32x4*)(redf + w * 512 + lane * 8) = o0;
        *(f32x4*)(redf + w * 512 + lane * 8 + 4) = o1;
        bar();
        if (has_ffn && t < 128) {
            f32x4 s = *(const f32x4*)(redf + t * 4);
#pragma unroll
            for (int ww = 1; ww < 8; ++ww) s += *(const f32x4*)(redf + ww * 512 + t * 4);
            __builtin_amdgcn_raw_buffer_store_b128(__builtin_bit_cast(u32x4, s), rs, WS_GB + fch * 2048 + t * 16, 0, SC1);
        }
    }
    STAMP(8);
    {
        const int qd = t >> 7;
        u32x4 ov[4];
        u32x2 ml;
        const int oo = WS_OPART + ((qd * 4 * 32 + h) * 512 + (t & 127) * 4) * 4;
        for (unsigned spins = 0; spins < SPIN_CAP; ++spins) {
            asm volatile("" ::: "memory");
#pragma unroll
            for (int i = 0; i < 4; ++i) ov[i] = __builtin_amdgcn_raw_buffer_load_b128(rs, oo + i * 65536, 0, SC1);
            ml = __builtin_amdgcn_raw_buffer_load_b64(rs, WS_ML + (h * 16 + (lane & 15)) * 8, 0, SC1);
            unsigned m = umax(umax(max4(ov[0]), max4(ov[1])), umax(max4(ov[2]), max4(ov[3])));
            m = umax(m, umax(ml[0], ml[1]));
            if (__all(m != POISON)) break;
            __builtin_amdgcn_s_sleep(1);
        }
        STAMP(9);
        float mk = asf(ml[0]), lk = asf(ml[1]);
        float M = mk;
        M = fmaxf(M, __shfl_xor(M, 1)); M = fmaxf(M, __shfl_xor(M, 2));
        M = fmaxf(M, __shfl_xor(M, 4)); M = fmaxf(M, __shfl_xor(M, 8));
        float wk = __expf(mk - M);
        float L = wk * lk;
        L += __shfl_xor(L, 1); L += __shfl_xor(L, 2); L += __shfl_xor(L, 4); L += __shfl_xor(L, 8);
        wk = wk / L;
        f32x4 c4 = {0.f, 0.f, 0.f, 0.f};
#pragma unroll
        for (int i = 0; i < 4; ++i) {
            float wi = __shfl(wk, qd * 4 + i);
            c4[0] += wi * asf(ov[i][0]); c4[1] += wi * asf(ov[i][1]); c4[2] += wi * asf(ov[i][2]); c4[3] += wi * asf(ov[i][3]);
        }
        *(f32x4*)(cpart + qd * 512 + (t & 127) * 4) = c4;
    }
    bar();
    {
        const int row = t >> 1;
        float c0 = (cpart[row] + cpart[512 + row]) + (cpart[1024 + row] + cpart[1536 + row]);
        float c1 = (cpart[row + 256] + cpart[768 + row]) + (cpart[1280 + row] + cpart[1792 + row]);
        float acc[8];
#pragma unroll
        for (int jj = 0; jj < 4; ++jj) {
            acc[2 * jj] = c0 * bflo(uvv[0][jj]) + c1 * bflo(uvv[1][jj]);
            acc[2 * jj + 1] = c0 * bfhi(uvv[0][jj]) + c1 * bfhi(uvv[1][jj]);
        }
        bool up = lane & 2;
        float s[4];
#pragma unroll
        for (int k = 0; k < 4; ++k) { float send = up ? acc[k] : acc[k + 4]; float keep = up ? acc[k + 4] : acc[k]; s[k] = keep + __shfl_xor(send, 2); }
        up = lane & 4;
        float s2[2];
#pragma unroll
        for (int k = 0; k < 2; ++k) { float send = up ? s[k] : s[k + 2]; float keep = up ? s[k + 2] : s[k]; s2[k] = keep + __shfl_xor(send, 4); }
        up = lane & 8;
        float send = up ? s2[0] : s2[1]; float keep = up ? s2[1] : s2[0];
        float v = keep + __shfl_xor(send, 8);
        v += __shfl_xor(v, 16);
        v += __shfl_xor(v, 32);
        if (lane < 16) {
            int d = (lane & 1) * 8 + ((lane >> 1) & 1) * 4 + ((lane >> 2) & 1) * 2 + ((lane >> 3) & 1);
            sm3[w * 16 + d] = v;
        }
    }
    bar();
    {
        float c0 = 0.f, c1 = 0.f;
#pragma unroll
        for (int ww = 0; ww < 8; ++ww) { c0 += sm3[ww * 16 + 2 * w]; c1 += sm3[ww * 16 + 2 * w + 1]; }
        c0 = rbf(c0); c1 = rbf(c1);
        f32x4 o0, o1;
        o0[0] = c0 * bflo(ovv[0][0]) + c1 * bflo(ovv[1][0]); o0[1] = c0 * bfhi(ovv[0][0]) + c1 * bfhi(ovv[1][0]);
        o0[2] = c0 * bflo(ovv[0][1]) + c1 * bflo(ovv[1][1]); o0[3] = c0 * bfhi(ovv[0][1]) + c1 * bfhi(ovv[1][1]);
        o1[0] = c0 * bflo(ovv[0][2]) + c1 * bflo(ovv[1][2]); o1[1] = c0 * bfhi(ovv[0][2]) + c1 * bfhi(ovv[1][2]);
        o1[2] = c0 * bflo(ovv[0][3]) + c1 * bflo(ovv[1][3]); o1[3] = c0 * bfhi(ovv[0][3]) + c1 * bfhi(ovv[1][3]);
        *(f32x4*)(redo + w * 512 + lane * 8) = o0;
        *(f32x4*)(redo + w * 512 + lane * 8 + 4) = o1;
    }
    bar();
    if (t < 128) {
        f32x4 s = *(const f32x4*)(redo + t * 4);
#pragma unroll
        for (int ww = 1; ww < 8; ++ww) s += *(const f32x4*)(redo + ww * 512 + t * 4);
        __builtin_amdgcn_raw_buffer_store_b128(__builtin_bit_cast(u32x4, s), rs, WS_GA + b * 2048 + t * 16, 0, SC1);
    }
    STAMP(10);
    if (cons) {
        const int k = b - 224;
        const int p = t >> 1, hf = t & 1;
        float gp = rbf(xt * bf2f(gw[t]));
        const int ao = WS_GA + p * 2048 + k * 64 + hf * 32;
        const int bo = WS_GB + (p < 172 ? p : 171) * 2048 + k * 64 + hf * 32;
        u32x4 a0, a1, b0, b1;
        for (unsigned spins = 0; spins < SPIN_CAP; ++spins) {
            asm volatile("" ::: "memory");
            a0 = __builtin_amdgcn_raw_buffer_load_b128(rs, ao, 0, SC1);
            a1 = __builtin_amdgcn_raw_buffer_load_b128(rs, ao + 16, 0, SC1);
            b0 = __builtin_amdgcn_raw_buffer_load_b128(rs, bo, 0, SC1);
            b1 = __builtin_amdgcn_raw_buffer_load_b128(rs, bo + 16, 0, SC1);
            unsigned m = umax(umax(max4(a0), max4(a1)), umax(max4(b0), max4(b1)));
            if (__all(m != POISON)) break;
            __builtin_amdgcn_s_sleep(1);
        }
        STAMP(11);
        float v[16];
        const float bz = p < 172 ? 1.f : 0.f;
#pragma unroll
        for (int i = 0; i < 4; ++i) {
            v[i] = asf(a0[i]); v[4 + i] = asf(a1[i]);
            v[8 + i] = p < 172 ? asf(b0[i]) : 0.f; v[12 + i] = p < 172 ? asf(b1[i]) : 0.f;
        }
        (void)bz;
        bool up = lane & 2;
        float s8[8];
#pragma unroll
        for (int q = 0; q < 8; ++q) { float send = up ? v[q] : v[q + 8]; float keep = up ? v[q + 8] : v[q]; s8[q] = keep + __shfl_xor(send, 2); }
        up = lane & 4;
        float s4[4];
#pragma unroll
        for (int q = 0; q < 4; ++q) { float send = up ? s8[q] : s8[q + 4]; float keep = up ? s8[q + 4] : s8[q]; s4[q] = keep + __shfl_xor(send, 4); }
        up = lane & 8;
        float s2[2];
#pragma unroll
        for (int q = 0; q < 2; ++q) { float send = up ? s4[q] : s4[q + 2]; float keep = up ? s4[q + 2] : s4[q]; s2[q] = keep + __shfl_xor(send, 8); }
        up = lane & 16;
        float send = up ? s2[0] : s2[1]; float keep = up ? s2[1] : s2[0];
        float vv = keep + __shfl_xor(send, 16);
        vv += __shfl_xor(vv, 32);
#pragma unroll
        for (int o = 32; o >= 1; o >>= 1) gp += __shfl_xor(gp, o);
        if (lane < 32) {
            int idx = ((lane >> 1) & 1) * 16 + (lane & 1) * 8 + ((lane >> 2) & 1) * 4 + ((lane >> 3) & 1) * 2 + ((lane >> 4) & 1);
            fin[w * 32 + idx] = vv;
        }
        if (lane == 0) fing[w] = gp;
    }
    bar();
    if (cons && t < 16) {
        float a = 0.f, bb = 0.f, gs = 0.f;
#pragma unroll
        for (int ww = 0; ww < 8; ++ww) { a += fin[ww * 32 + t]; bb += fin[ww * 32 + 16 + t]; gs += fing[ww]; }
        gs = rbf(gs);
        float g = rbf(1.f / (1.f + __expf(-gs)));
        a = rbf(a); bb = rbf(bb);
        float rr = rbf(g * a) + rbf(rbf(1.f - g) * bb);
        out[(b - 224) * 16 + t] = f2bf(rr);
    }
    STAMP(12);
}

extern "C" void kernel_launch(void* const* d_in, const int* in_sizes, int n_in,
                              void* d_out, int out_size, void* d_ws, size_t ws_size,
                              hipStream_t stream) {
    const u16* x = (const u16*)d_in[0];
    const u16* ckv = (const u16*)d_in[1];
    const u16* wq = (const u16*)d_in[2];
    const u16* wuk = (const u16*)d_in[3];
    const u16* wuv = (const u16*)d_in[4];
    const u16* wo = (const u16*)d_in[5];
    const u16* wg = (const u16*)d_in[7];
    const u16* wu = (const u16*)d_in[8];
    const u16* wd = (const u16*)d_in[9];
    const u16* gw = (const u16*)d_in[10];
    mega<<<256, 640, 0, stream>>>(x, ckv, wq, wuk, wuv, wo, wg, wu, wd, gw, (char*)d_ws, (u16*)d_out);
}
```

```cpp
#include <hip/hip_runtime.h>
#include <hip/hip_bf16.h>
#include <stdint.h>

#define SK 8192
#define FF 1376

typedef unsigned short u16;
typedef unsigned long long u64;
typedef __attribute__((ext_vector_type(8))) __bf16 bf16x8;
typedef __attribute__((ext_vector_type(2))) __bf16 bf16x2;
typedef __attribute__((ext_vector_type(4))) short s16x4;
typedef __attribute__((ext_vector_type(8))) short s16x8;
typedef __attribute__((ext_vector_type(16))) float f32x16;
typedef __attribute__((ext_vector_type(4))) float f32x4;
typedef __attribute__((ext_vector_type(2))) float f32x2;
typedef __attribute__((ext_vector_type(4))) unsigned int u32x4;
typedef __attribute__((ext_vector_type(2))) unsigned int u32x2;
#define GAS __attribute__((address_space(1)))
typedef GAS unsigned gu32;
typedef GAS u64 gu64;
#define RLX_AGENT __ATOMIC_RELAXED, __HIP_MEMORY_SCOPE_AGENT
#define SC1 16
#define POISON 0xFFFFFFFFu

__device__ __forceinline__ float bf2f(u16 v) { return __uint_as_float(((unsigned)v) << 16); }
__device__ __forceinline__ float bflo(unsigned v) { return __uint_as_float(v << 16); }
__device__ __forceinline__ float bfhi(unsigned v) { return __uint_as_float(v & 0xffff0000u); }
__device__ __forceinline__ float asf(unsigned v) { return __uint_as_float(v); }
__device__ __forceinline__ unsigned asu(float v) { return __float_as_uint(v); }
__device__ __forceinline__ unsigned pk2(float a, float b) {
    bf16x2 v = {(__bf16)a, (__bf16)b};
    return __builtin_bit_cast(unsigned, v);
}
__device__ __forceinline__ u16 f2bf(float f) { __bf16 v = (__bf16)f; return __builtin_bit_cast(u16, v); }
__device__ __forceinline__ float rbf(float f) { return bf2f(f2bf(f)); }
__device__ __forceinline__ unsigned umax(unsigned a, unsigned b) { return a > b ? a : b; }
__device__ __forceinline__ unsigned max4(u32x4 v) { return umax(umax(v[0], v[1]), umax(v[2], v[3])); }

#define NREP 32
#define WS_EPOCH   0
#define WS_F0      256
#define WS_QG      (WS_F0 + NREP * 2048)
#define WS_QABS    (WS_QG + 128 * 1024)
#define WS_P       (WS_QABS + 8 * 32768)
#define WS_MLOC    (WS_P + 8 * 16 * 4096)
#define WS_OPART   (WS_MLOC + 8 * 8 * 32 * 16)
#define WS_ML      (WS_OPART + 8 * 32 * 512 * 4)
#define WS_GA      (WS_ML + 32 * 8 * 8)
#define WS_GB      (WS_GA + 128 * 2048)
#define WS_QGF     (WS_GB + 128 * 2048)
#define WS_PF      (WS_QGF + 128 * 1024)
#define WS_MLOCF   (WS_PF + 8 * 16 * 4096)
#define WS_END     (WS_MLOCF + 8 * 8 * 32 * 16)

#define SPIN_CAP (1u << 17)
#define STAMP(i)

__device__ __forceinline__ void lgkm0() { asm volatile("s_waitcnt lgkmcnt(0)" ::: "memory"); }
__device__ __forceinline__ void vm0() { asm volatile("s_waitcnt vmcnt(0)" ::: "memory"); }
__device__ __forceinline__ void bar() { asm volatile("s_waitcnt lgkmcnt(0)\n\ts_barrier" ::: "memory"); }
typedef volatile __attribute__((address_space(3))) unsigned lds_u32;
__device__ __forceinline__ void lds_wait(lds_u32* p, unsigned target) {
    for (unsigned s = 0; s < SPIN_CAP; ++s) {
        if (*p >= target) break;
        __builtin_amdgcn_s_sleep(1);
    }
    asm volatile("" ::: "memory");
}

__device__ __forceinline__ void f0_signal_wait(const __amdgpu_buffer_rsrc_t rs, char* ws, int b, int lane, unsigned tag) {
    const u64 want = ((u64)(~tag) << 32) | tag;
    if (lane < NREP) __hip_atomic_store((gu64*)(ws + WS_F0 + lane * 2048 + b * 8), want, RLX_AGENT);
    const int fo = WS_F0 + (b & (NREP - 1)) * 2048 + lane * 16;
    for (unsigned spins = 0; spins < SPIN_CAP; ++spins) {
        asm volatile("" ::: "memory");
        u32x4 a0 = __builtin_amdgcn_raw_buffer_load_b128(rs, fo, 0, SC1);
        u32x4 a1 = __builtin_amdgcn_raw_buffer_load_b128(rs, fo + 1024, 0, SC1);
        unsigned bad = (a0[0] ^ tag) | (a0[1] ^ ~tag) | (a0[2] ^ tag) | (a0[3] ^ ~tag) |
                       (a1[0] ^ tag) | (a1[1] ^ ~tag) | (a1[2] ^ tag) | (a1[3] ^ ~tag);
        if (__all(bad == 0)) break;
        __builtin_amdgcn_s_sleep(1);
    }
    asm volatile("" ::: "memory");
}

#define LDS_BYTES (65536 + 65536 + 8192)

__global__ __launch_bounds__(640, 1) void mega(const u16* __restrict__ x, const u16* __restrict__ ckv,
                                               const u16* __restrict__ wq, const u16* __restrict__ wuk,
                                               const u16* __restrict__ wuv, const u16* __restrict__ wo,
                                               const u16* __restrict__ wg, const u16* __restrict__ wu,
                                               const u16* __restrict__ wd, const u16* __restrict__ gw,
                                               char* __restrict__ ws, u16* __restrict__ out) {
    __shared__ __attribute__((aligned(16))) char lds[LDS_BYTES];
    __shared__ unsigned flg[4];

    const int b = blockIdx.x;
    const int t = threadIdx.x;
    const int lane = t & 63, w = t >> 6;
    const int r = lane & 31, h2 = lane >> 5;
    const __amdgpu_buffer_rsrc_t rs = __builtin_amdgcn_make_buffer_rsrc(ws, 0, WS_END, 0x00020000);
    const u32x4 P4 = {POISON, POISON, POISON, POISON};
    const u32x2 P2 = {POISON, POISON};
    STAMP(0);

    if (b < 128) {
        if (w >= 8) return;
        char* tile = lds;
        float* red = (float*)(lds + 65536);
        float* fac = (float*)(lds + 131072);
        float* Mws = fac + 512;
        float* Lws = Mws + 256;
        float* fin = Lws + 256;
        float* fing = fin + 256;
        const int kc = b & 7, mem = b >> 3;
        const unsigned e = __hip_atomic_load((gu32*)(ws + WS_EPOCH), RLX_AGENT);
        __builtin_amdgcn_raw_buffer_store_b128(P4, rs, (w < 4 ? WS_P : WS_PF) + ((kc * 16 + mem) * 4 + (w & 3)) * 1024 + lane * 16, 0, SC1);
        if (w < 4)
            __builtin_amdgcn_raw_buffer_store_b128(P4, rs, WS_OPART + ((kc * 32 + w * 8 + (lane >> 3)) * 512 + mem * 32) * 4 + (lane & 7) * 16, 0, SC1);
        else if (w == 4)
            __builtin_amdgcn_raw_buffer_store_b64(P2, rs, (lane < 32 ? WS_MLOC : WS_MLOCF) + ((kc * 8 + (mem >> 1)) * 32 + r) * 16 + (mem & 1) * 8, 0, SC1);
        else if (w == 5 && mem == 0 && lane < 32)
            __builtin_amdgcn_raw_buffer_store_b64(P2, rs, WS_ML + (lane * 8 + kc) * 8, 0, SC1);
        vm0();
        bar();
        STAMP(1);
        if (w == 0) f0_signal_wait(rs, ws, b, lane, e + 1u);
        bar();
        STAMP(2);
        bf16x8 bq[8];
        u32x4 cv[8];
        {
            const u16* bp = ckv + (size_t)(kc * 1024 + mem * 64 + r) * 512 + 64 * w + 8 * h2;
#pragma unroll
            for (int kb = 0; kb < 2; ++kb)
#pragma unroll
                for (int s = 0; s < 4; ++s) bq[kb * 4 + s] = *(const bf16x8*)(bp + kb * 32 * 512 + 16 * s);
            const u16* cp = ckv + (size_t)(kc * 1024 + (t >> 2)) * 512 + mem * 32 + (t & 3) * 8;
#pragma unroll
            for (int i = 0; i < 8; ++i) cv[i] = *(const u32x4*)(cp + (size_t)i * 128 * 512);
        }
        __builtin_amdgcn_sched_barrier(0);
#pragma unroll
        for (int i = 0; i < 8; ++i) *(u32x4*)(tile + (i * 128 + (t >> 2)) * 64 + (t & 3) * 16) = cv[i];
        STAMP(3);
        {
            u32x4 av[4];
            const int qo = WS_QABS + (b & 7) * 32768 + (w * 256 + lane) * 16;
            for (unsigned spins = 0; spins < SPIN_CAP; ++spins) {
                asm volatile("" ::: "memory");
#pragma unroll
                for (int s = 0; s < 4; ++s) av[s] = __builtin_amdgcn_raw_buffer_load_b128(rs, qo + 1024 * s, 0, SC1);
                unsigned m = umax(umax(max4(av[0]), max4(av[1])), umax(max4(av[2]), max4(av[3])));
                if (__all(m != POISON)) break;
                __builtin_amdgcn_s_sleep(1);
            }
            STAMP(4);
#pragma unroll
            for (int kb = 0; kb < 2; ++kb) {
                f32x16 acc;
#pragma unroll
                for (int i = 0; i < 16; ++i) acc[i] = 0.f;
#pragma unroll
                for (int s = 0; s < 4; ++s)
                    acc = __builtin_amdgcn_mfma_f32_32x32x16_bf16(__builtin_bit_cast(bf16x8, av[s]), bq[kb * 4 + s], acc, 0, 0, 0);
#pragma unroll
                for (int i = 0; i < 16; ++i) {
                    int head = (i & 3) + 8 * (i >> 2) + 4 * h2;
                    red[(w * 32 + head) * 64 + kb * 32 + r] = acc[i];
                }
            }
        }
        bar();
        {
            const int head = t >> 4, kq = (t & 15) * 4;
            f32x4 s4 = *(const f32x4*)(red + head * 64 + kq);
#pragma unroll
            for (int ww = 1; ww < 8; ++ww) s4 += *(const f32x4*)(red + (ww * 32 + head) * 64 + kq);
            float m = fmaxf(fmaxf(s4[0], s4[1]), fmaxf(s4[2], s4[3]));
            m = fmaxf(m, __shfl_xor(m, 1));
            m = fmaxf(m, __shfl_xor(m, 2));
            m = fmaxf(m, __shfl_xor(m, 4));
            m = fmaxf(m, __shfl_xor(m, 8));
            unsigned p01 = pk2(__expf(s4[0] - m), __expf(s4[1] - m));
            unsigned p23 = pk2(__expf(s4[2] - m), __expf(s4[3] - m));
            float l = (bflo(p01) + bfhi(p01)) + (bflo(p23) + bfhi(p23));
            l += __shfl_xor(l, 1);
            l += __shfl_xor(l, 2);
            l += __shfl_xor(l, 4);
            l += __shfl_xor(l, 8);
            u32x4 pp = {p01, p23, (unsigned)__shfl_down((int)p01, 1), (unsigned)__shfl_down((int)p23, 1)};
            u32x2 ml = {asu(m), asu(l)};
            const int po = ((kc * 16 + mem) * 256 + ((t & 15) >> 1) * 32 + head) * 16;
            const int mo = ((kc * 8 + (mem >> 1)) * 32 + head) * 16 + (mem & 1) * 8;
            if ((t & 1) == 0) __builtin_amdgcn_raw_buffer_store_b128(pp, rs, WS_PF + po, 0, 0);
            if ((t & 15) == 0) __builtin_amdgcn_raw_buffer_store_b64(ml, rs, WS_MLOCF + mo, 0, 0);
            if ((t & 1) == 0) __builtin_amdgcn_raw_buffer_store_b128(pp, rs, WS_P + po, 0, SC1);
            if ((t & 15) == 0) __builtin_amdgcn_raw_buffer_store_b64(ml, rs, WS_MLOC + mo, 0, SC1);
        }
        bar();
        STAMP(5);
        {
            u32x4 pv[8], mlv;
            for (unsigned spins = 0; spins < SPIN_CAP; ++spins) {
                asm volatile("" ::: "memory");
                const bool slow = (spins & 3) == 3;
                const int po = (slow ? WS_P : WS_PF) + ((kc * 16 + w * 2) * 256 + lane) * 16;
                const int mo = (slow ? WS_MLOC : WS_MLOCF) + ((kc * 8 + w) * 32 + r) * 16;
#pragma unroll
                for (int s = 0; s < 8; ++s) pv[s] = __builtin_amdgcn_raw_buffer_load_b128(rs, po + 1024 * s, 0, SC1);
                mlv = __builtin_amdgcn_raw_buffer_load_b128(rs, mo, 0, SC1);
                unsigned m = umax(umax(max4(pv[0]), max4(pv[1])), umax(max4(pv[2]), max4(pv[3])));
                m = umax(m, umax(umax(max4(pv[4]), max4(pv[5])), umax(max4(pv[6]), max4(pv[7]))));
                m = umax(m, max4(mlv));
                if (__all(m != POISON)) break;
                __builtin_amdgcn_s_sleep(1);
            }
            STAMP(6);
            {
                float m0 = asf(mlv[0]), l0 = asf(mlv[1]), m1 = asf(mlv[2]), l1 = asf(mlv[3]);
                float Mw = fmaxf(m0, m1);
                float f0_ = __expf(m0 - Mw), f1_ = __expf(m1 - Mw);
                if (lane < 32) {
                    fac[(2 * w) * 32 + r] = f0_; fac[(2 * w + 1) * 32 + r] = f1_;
                    Mws[r * 8 + w] = Mw; Lws[r * 8 + w] = f0_ * l0 + f1_ * l1;
                }
            }
            const int g = lane >> 4;
            const int q = (lane & 15) >> 2, p = lane & 3;
            const int cb = g & 1;
            f32x16 tot;
#pragma unroll
            for (int mm = 0; mm < 2; ++mm) {
                f32x16 acc;
#pragma unroll
                for (int i = 0; i < 16; ++i) acc[i] = 0.f;
#pragma unroll
                for (int ss = 0; ss < 4; ++ss) {
                    int s = mm * 4 + ss;
                    int row = w * 128 + 16 * s + 8 * h2 + q;
                    s16x4 t0 = __builtin_amdgcn_ds_read_tr16_b64_v4i16(
                        (__attribute__((address_space(3))) s16x4*)(tile + row * 64 + cb * 32 + p * 8));
                    s16x4 t1 = __builtin_amdgcn_ds_read_tr16_b64_v4i16(
                        (__attribute__((address_space(3))) s16x4*)(tile + (row + 4) * 64 + cb * 32 + p * 8));
                    s16x8 bb = {t0[0], t0[1], t0[2], t0[3], t1[0], t1[1], t1[2], t1[3]};
                    acc = __builtin_amdgcn_mfma_f32_32x32x16_bf16(__builtin_bit_cast(bf16x8, pv[s]), __builtin_bit_cast(bf16x8, bb), acc, 0, 0, 0);
                }
                const float* fp = fac + (2 * w + mm) * 32 + 4 * h2;
#pragma unroll
                for (int qd = 0; qd < 4; ++qd) {
                    f32x4 f4 = *(const f32x4*)(fp + 8 * qd);
#pragma unroll
                    for (int e2 = 0; e2 < 4; ++e2) {
                        if (mm == 0) tot[qd * 4 + e2] = f4[e2] * acc[qd * 4 + e2];
                        else tot[qd * 4 + e2] += f4[e2] * acc[qd * 4 + e2];
                    }
                }
            }
#pragma unroll
            for (int i = 0; i < 16; ++i) {
                int head = (i & 3) + 8 * (i >> 2) + 4 * h2;
                red[(w * 32 + head) * 32 + r] = tot[i];
            }
        }
        bar();
        {
            const int head = t >> 4, cq = (t & 15) * 2;
            f32x4 ma = *(const f32x4*)(Mws + head * 8), mb = *(const f32x4*)(Mws + head * 8 + 4);
            float M = fmaxf(fmaxf(fmaxf(ma[0], ma[1]), fmaxf(ma[2], ma[3])), fmaxf(fmaxf(mb[0], mb[1]), fmaxf(mb[2], mb[3])));
            float ew[8];
#pragma unroll
            for (int ww = 0; ww < 4; ++ww) { ew[ww] = __expf(ma[ww] - M); ew[ww + 4] = __expf(mb[ww] - M); }
            f32x2 s2 = {0.f, 0.f};
#pragma unroll
            for (int ww = 0; ww < 8; ++ww) s2 += ew[ww] * *(const f32x2*)(red + (ww * 32 + head) * 32 + cq);
            u32x4 o = {asu(s2[0]), asu(s2[1]), asu(__shfl_down(s2[0], 1)), asu(__shfl_down(s2[1], 1))};
            if ((t & 1) == 0)
                __builtin_amdgcn_raw_buffer_store_b128(o, rs, WS_OPART + ((kc * 32 + head) * 512 + mem * 32 + cq) * 4, 0, SC1);
            if (mem == 0 && (t & 15) == 0) {
                f32x4 la = *(const f32x4*)(Lws + head * 8), lb = *(const f32x4*)(Lws + head * 8 + 4);
                float L = 0.f;
#pragma unroll
                for (int ww = 0; ww < 4; ++ww) L += ew[ww] * la[ww] + ew[ww + 4] * lb[ww];
                u32x2 ml = {asu(M), asu(L)};
                __builtin_amdgcn_raw_buffer_store_b64(ml, rs, WS_ML + (head * 8 + kc) * 8, 0, SC1);
            }
        }
        STAMP(7);
        if (b >= 32) return;
        {
            const int p = t >> 2, qc = t & 3;
            float gp = rbf(bf2f(x[t]) * bf2f(gw[t]));
            const int ao = WS_GA + p * 2048 + b * 64 + qc * 16;
            u32x4 a0, b0;
            for (unsigned spins = 0; spins < SPIN_CAP; ++spins) {
                asm volatile("" ::: "memory");
                a0 = __builtin_amdgcn_raw_buffer_load_b128(rs, ao, 0, SC1);
                b0 = __builtin_amdgcn_raw_buffer_load_b128(rs, ao + (WS_GB - WS_GA), 0, SC1);
                unsigned m = umax(max4(a0), max4(b0));
                if (__all(m != POISON)) break;
                __builtin_amdgcn_s_sleep(2);
            }
            STAMP(11);
            bool up = lane & 4;
            float s4[4];
#pragma unroll
            for (int q = 0; q < 4; ++q) { float send = up ? asf(a0[q]) : asf(b0[q]); float keep = up ? asf(b0[q]) : asf(a0[q]); s4[q] = keep + __shfl_xor(send, 4); }
            up = lane & 8;
            float s2[2];
#pragma unroll
            for (int q = 0; q < 2; ++q) { float send = up ? s4[q] : s4[q + 2]; float keep = up ? s4[q + 2] : s4[q]; s2[q] = keep + __shfl_xor(send, 8); }
            up = lane & 16;
            float send = up ? s2[0] : s2[1]; float keep = up ? s2[1] : s2[0];
            float vv = keep + __shfl_xor(send, 16);
            vv += __shfl_xor(vv, 32);
#pragma unroll
            for (int o = 32; o >= 1; o >>= 1) gp += __shfl_xor(gp, o);
            if (lane < 32) {
                int idx = ((lane >> 2) & 1) * 16 + (lane & 3) * 4 + ((lane >> 3) & 1) * 2 + ((lane >> 4) & 1);
                fin[w * 32 + idx] = vv;
            }
            if (lane == 0) fing[w] = gp;
        }
        bar();
        if (t < 16) {
            float a = 0.f, bb = 0.f, gs = 0.f;
#pragma unroll
            for (int ww = 0; ww < 8; ++ww) { a += fin[ww * 32 + t]; bb += fin[ww * 32 + 16 + t]; gs += fing[ww]; }
            gs = rbf(gs);
            float g = rbf(1.f / (1.f + __expf(-gs)));
            a = rbf(a); bb = rbf(bb);
            float rr = rbf(g * a) + rbf(rbf(1.f - g) * bb);
            out[b * 16 + t] = f2bf(rr);
        }
        STAMP(12);
        return;
    }

    float* redf = (float*)lds;
    float* redo = redf + 4096;
    float* cpart = redo + 4096;
    float* sm1 = cpart + 2048;
    float* sm2 = sm1 + 1024;
    float* sm3 = sm2 + 256;
    float* qs = sm3 + 256;
    lds_u32* vflg = (lds_u32*)flg;
    const int bb_ = b - 128;
    const int h = bb_ & 31, jj = bb_ >> 5;
    const int i16 = bb_ >> 3;
    const int fc0 = (bb_ & 7) * 22 + i16;
    const int fc1r = (bb_ & 7) * 22 + 16 + i16;
    const bool has1 = (i16 < 6) && (fc1r < 172);
    const int fc1 = has1 ? fc1r : fc0;

    if (w >= 8) {
        const unsigned e = __hip_atomic_load((gu32*)(ws + WS_EPOCH), RLX_AGENT);
        bar();
        const unsigned tag = e + 1u;
        if (w == 8) {
            lds_wait(vflg + 0, 8);
            STAMP(1);
            f32x2 s = *(const f32x2*)(sm1 + 2 * lane);
#pragma unroll
            for (int ww = 1; ww < 8; ++ww) s += *(const f32x2*)(sm1 + ww * 128 + 2 * lane);
            u32x4 g = {tag, asu(s[0]), asu(s[1]), ~tag};
            __builtin_amdgcn_raw_buffer_store_b128(g, rs, WS_QGF + ((h * 4 + jj) * 64 + lane) * 16, 0, 0);
            __builtin_amdgcn_raw_buffer_store_b128(g, rs, WS_QG + ((h * 4 + jj) * 64 + lane) * 16, 0, SC1);
            for (unsigned spins = 0; spins < SPIN_CAP; ++spins) {
                asm volatile("" ::: "memory");
                u32x4 v[4];
                const int qb = ((spins & 3) == 3 ? WS_QG : WS_QGF) + (h * 4 * 64 + lane) * 16;
#pragma unroll
                for (int k = 0; k < 4; ++k) v[k] = __builtin_amdgcn_raw_buffer_load_b128(rs, qb + k * 1024, 0, SC1);
                unsigned bad = 0;
                float q0 = 0.f, q1 = 0.f;
#pragma unroll
                for (int k = 0; k < 4; ++k) {
                    bad |= (v[k][0] ^ tag) | (v[k][3] ^ ~tag);
                    q0 += asf(v[k][1]); q1 += asf(v[k][2]);
                }
                if (__all(bad == 0) || spins == SPIN_CAP - 1) {
                    f32x2 qq = {rbf(q0), rbf(q1)};
                    *(f32x2*)(qs + 2 * lane) = qq;
                    break;
                }
                __builtin_amdgcn_s_sleep(1);
            }
            lgkm0();
            if (lane == 0) vflg[1] = 1u;
            STAMP(2);
        } else {
            lds_wait(vflg + 3, 8);
            STAMP(1);
            f0_signal_wait(rs, ws, b, lane, tag);
            if (lane == 0) vflg[2] = 1u;
            if (b == 255 && lane == 0) __hip_atomic_store((gu32*)(ws + WS_EPOCH), tag, RLX_AGENT);
            STAMP(2);
        }
        return;
    }

    {
        int po;
        if (w < 2) po = WS_QABS + (lane >> 3) * 32768 + ((jj * 16 + w * 8 + (lane & 7)) * 32 + h) * 16;
        else if (w < 4) po = WS_GA + bb_ * 2048 + (w & 1) * 1024 + lane * 16;
        else po = WS_GB + bb_ * 2048 + (w & 1) * 1024 + lane * 16;
        if (w < 6) __builtin_amdgcn_raw_buffer_store_b128(P4, rs, po, 0, SC1);
    }
    __builtin_amdgcn_sched_barrier(0);
    u32x4 wqv[4], wkv[4];
    u16 xr[4];
    {
        const int row = jj * 128 + (t >> 4);
#pragma unroll
        for (int i = 0; i < 4; ++i) xr[i] = x[row + 32 * i];
#pragma unroll
        for (int i = 0; i < 4; ++i)
            wqv[i] = *(const u32x4*)(wq + (size_t)(row + 32 * i) * 4096 + h * 128 + (t & 15) * 8);
#pragma unroll
        for (int i = 0; i < 4; ++i)
            wkv[i] = *(const u32x4*)(wuk + (size_t)(jj * 128 + 16 * w + (lane >> 4) + 4 * i) * 4096 + h * 128 + (lane & 15) * 8);
    }
    __builtin_amdgcn_sched_barrier(0);
    if (t < 4) flg[t] = 0u;
    bar();
    asm volatile("s_waitcnt vmcnt(12)" ::: "memory");
    if (lane == 0) __hip_atomic_fetch_add(flg + 3, 1u, __ATOMIC_RELAXED, __HIP_MEMORY_SCOPE_WORKGROUP);
    __builtin_amdgcn_sched_barrier(0);
    asm volatile("s_waitcnt vmcnt(4)" ::: "memory");
    STAMP(1);
    {
        float acc[8];
#pragma unroll
        for (int k = 0; k < 4; ++k) {
            acc[2 * k] = bf2f(xr[0]) * bflo(wqv[0][k]) + bf2f(xr[1]) * bflo(wqv[1][k]) + bf2f(xr[2]) * bflo(wqv[2][k]) + bf2f(xr[3]) * bflo(wqv[3][k]);
            acc[2 * k + 1] = bf2f(xr[0]) * bfhi(wqv[0][k]) + bf2f(xr[1]) * bfhi(wqv[1][k]) + bf2f(xr[2]) * bfhi(wqv[2][k]) + bf2f(xr[3]) * bfhi(wqv[3][k]);
        }
        bool up = lane & 16;
        float s[4];
#pragma unroll
        for (int k = 0; k < 4; ++k) { float send = up ? acc[k] : acc[k + 4]; float keep = up ? acc[k + 4] : acc[k]; s[k] = keep + __shfl_xor(send, 16); }
        up = lane & 32;
        f32x2 s2;
#pragma unroll
        for (int k = 0; k < 2; ++k) { float send = up ? s[k] : s[k + 2]; float keep = up ? s[k + 2] : s[k]; s2[k] = keep + __shfl_xor(send, 32); }
        *(f32x2*)(sm1 + w * 128 + (lane & 15) * 8 + ((lane >> 4) & 1) * 4 + (lane >> 5) * 2) = s2;
        lgkm0();
        if (lane == 0) __hip_atomic_fetch_add(flg + 0, 1u, __ATOMIC_RELAXED, __HIP_MEMORY_SCOPE_WORKGROUP);
    }
    __builtin_amdgcn_sched_barrier(0);
    lds_wait(vflg + 1, 1);
    STAMP(2);
    {
        const int l16 = lane & 15;
        f32x4 qa0 = *(const f32x4*)(qs + l16 * 8), qa1 = *(const f32x4*)(qs + l16 * 8 + 4);
        float d[4];
#pragma unroll
        for (int i = 0; i < 4; ++i) {
            d[i] = qa0[0] * bflo(wkv[i][0]) + qa0[1] * bfhi(wkv[i][0]) + qa0[2] * bflo(wkv[i][1]) + qa0[3] * bfhi(wkv[i][1]);
            d[i] += qa1[0] * bflo(wkv[i][2]) + qa1[1] * bfhi(wkv[i][2]) + qa1[2] * bflo(wkv[i][3]) + qa1[3] * bfhi(wkv[i][3]);
        }
        bool up = lane & 1;
        float s[2];
#pragma unroll
        for (int k = 0; k < 2; ++k) { float send = up ? d[k] : d[k + 2]; float keep = up ? d[k + 2] : d[k]; s[k] = keep + __shfl_xor(send, 1); }
        up = lane & 2;
        float send = up ? s[0] : s[1], keep = up ? s[1] : s[0];
        float v = keep + __shfl_xor(send, 2);
        v += __shfl_xor(v, 4);
        v += __shfl_xor(v, 8);
        v *= 0.08838834764831845f;
        const int k = (lane >> 3) & 1;
        float vv[8];
#pragma unroll
        for (int e2 = 0; e2 < 8; ++e2) vv[e2] = __shfl(v, (e2 & 3) * 16 + (k | ((e2 >> 2) << 1)));
        u32x4 o = {pk2(vv[0], vv[1]), pk2(vv[2], vv[3]), pk2(vv[4], vv[5]), pk2(vv[6], vv[7])};
        if (lane < 16)
            __builtin_amdgcn_raw_buffer_store_b128(o, rs, WS_QABS + (lane & 7) * 32768 + ((jj * 16 + w * 2 + k) * 32 + h) * 16, 0, SC1);
    }
    __builtin_amdgcn_sched_barrier(0);
    STAMP(3);
    u32x4 uvv[4], ovv[4];
    u32x4 gv0, uv0, gv1, uv1, dv0, dv1;
    const float xt = bf2f(x[t]);
    gv0 = *(const u32x4*)(wg + (size_t)t * FF + fc0 * 8);
    uv0 = *(const u32x4*)(wu + (size_t)t * FF + fc0 * 8);
    gv1 = *(const u32x4*)(wg + (size_t)t * FF + fc1 * 8);
    uv1 = *(const u32x4*)(wu + (size_t)t * FF + fc1 * 8);
    dv0 = *(const u32x4*)(wd + (size_t)(fc0 * 8 + w) * 512 + lane * 8);
    dv1 = *(const u32x4*)(wd + (size_t)(fc1 * 8 + w) * 512 + lane * 8);
#pragma unroll
    for (int i = 0; i < 4; ++i)
        uvv[i] = *(const u32x4*)(wuv + (size_t)((t >> 2) + 128 * i) * 4096 + h * 128 + jj * 32 + (t & 3) * 8);
#pragma unroll
    for (int i = 0; i < 4; ++i)
        ovv[i] = *(const u32x4*)(wo + ((size_t)h * 128 + jj * 32 + w * 4 + i) * 512 + lane * 8);
    __builtin_amdgcn_sched_barrier(0);
    {
#pragma unroll
        for (int c = 0; c < 2; ++c) {
            const u32x4 gv = c ? gv1 : gv0, uv_ = c ? uv1 : uv0;
            float p[16];
#pragma unroll
            for (int k = 0; k < 4; ++k) {
                p[2 * k] = xt * bflo(gv[k]); p[2 * k + 1] = xt * bfhi(gv[k]);
                p[8 + 2 * k] = xt * bflo(uv_[k]); p[8 + 2 * k + 1] = xt * bfhi(uv_[k]);
            }
            bool up = lane & 1;
            float s8[8];
#pragma unroll
            for (int k = 0; k < 8; ++k) { float send = up ? p[k] : p[k + 8]; float keep = up ? p[k + 8] : p[k]; s8[k] = keep + __shfl_xor(send, 1); }
            up = lane & 2;
            float s4[4];
#pragma unroll
            for (int k = 0; k < 4; ++k) { float send = up ? s8[k] : s8[k + 4]; float keep = up ? s8[k + 4] : s8[k]; s4[k] = keep + __shfl_xor(send, 2); }
            up = lane & 4;
            float s2[2];
#pragma unroll
            for (int k = 0; k < 2; ++k) { float send = up ? s4[k] : s4[k + 2]; float keep = up ? s4[k + 2] : s4[k]; s2[k] = keep + __shfl_xor(send, 4); }
            up = lane & 8;
            float send = up ? s2[0] : s2[1]; float keep = up ? s2[1] : s2[0];
            float v = keep + __shfl_xor(send, 8);
            v += __shfl_xor(v, 16);
            v += __shfl_xor(v, 32);
            if (lane < 16) {
                int idx = (lane & 1) * 8 + ((lane >> 1) & 1) * 4 + ((lane >> 2) & 1) * 2 + ((lane >> 3) & 1);
                sm2[w * 32 + c * 16 + idx] = v;
            }
        }
        bar();
        float hv[2];
#pragma unroll
        for (int c = 0; c < 2; ++c) {
            float gs = 0.f, us = 0.f;
#pragma unroll
            for (int ww = 0; ww < 8; ++ww) { gs += sm2[ww * 32 + c * 16 + w]; us += sm2[ww * 32 + c * 16 + 8 + w]; }
            gs = rbf(gs); us = rbf(us);
            float sg = rbf(gs / (1.f + __expf(-gs)));
            hv[c] = rbf(sg * us);
        }
        if (!has1) hv[1] = 0.f;
        f32x4 o0, o1;
        o0[0] = hv[0] * bflo(dv0[0]) + hv[1] * bflo(dv1[0]); o0[1] = hv[0] * bfhi(dv0[0]) + hv[1] * bfhi(dv1[0]);
        o0[2] = hv[0] * bflo(dv0[1]) + hv[1] * bflo(dv1[1]); o0[3] = hv[0] * bfhi(dv0[1]) + hv[1] * bfhi(dv1[1]);
        o1[0] = hv[0] * bflo(dv0[2]) + hv[1] * bflo(dv1[2]); o1[1] = hv[0] * bfhi(dv0[2]) + hv[1] * bfhi(dv1[2]);
        o1[2] = hv[0] * bflo(dv0[3]) + hv[1] * bflo(dv1[3]); o1[3] = hv[0] * bfhi(dv0[3]) + hv[1] * bfhi(dv1[3]);
        *(f32x4*)(redf + w * 512 + lane * 8) = o0;
        *(f32x4*)(redf + w * 512 + lane * 8 + 4) = o1;
        bar();
        if (t < 128) {
            f32x4 s = *(const f32x4*)(redf + t * 4);
#pragma unroll
            for (int ww = 1; ww < 8; ++ww) s += *(const f32x4*)(redf + ww * 512 + t * 4);
            __builtin_amdgcn_raw_buffer_store_b128(__builtin_bit_cast(u32x4, s), rs, WS_GB + bb_ * 2048 + t * 16, 0, SC1);
        }
    }
    asm volatile("s_waitcnt vmcnt(1)" ::: "memory");
    lds_wait(vflg + 2, 1);
    STAMP(8);
    {
        const int qd = t >> 7;
        u32x4 ov[2];
        u32x2 ml;
        const int oo = WS_OPART + ((qd * 2 * 32 + h) * 512 + (t & 127) * 4) * 4;
        for (unsigned spins = 0; spins < SPIN_CAP; ++spins) {
            asm volatile("" ::: "memory");
#pragma unroll
            for (int i = 0; i < 2; ++i) ov[i] = __builtin_amdgcn_raw_buffer_load_b128(rs, oo + i * 65536, 0, SC1);
            ml = __builtin_amdgcn_raw_buffer_load_b64(rs, WS_ML + (h * 8 + (lane & 7)) * 8, 0, SC1);
            unsigned m = umax(max4(ov[0]), max4(ov[1]));
            m = umax(m, umax(ml[0], ml[1]));
            if (__all(m != POISON)) break;
            __builtin_amdgcn_s_sleep(1);
        }
        STAMP(9);
        float mk = asf(ml[0]), lk = asf(ml[1]);
        float M = mk;
        M = fmaxf(M, __shfl_xor(M, 1)); M = fmaxf(M, __shfl_xor(M, 2)); M = fmaxf(M, __shfl_xor(M, 4));
        float wk = __expf(mk - M);
        float L = wk * lk;
        L += __shfl_xor(L, 1); L += __shfl_xor(L, 2); L += __shfl_xor(L, 4);
        wk = wk / L;
        f32x4 c4 = {0.f, 0.f, 0.f, 0.f};
#pragma unroll
        for (int i = 0; i < 2; ++i) {
            float wi = __shfl(wk, qd * 2 + i);
            c4[0] += wi * asf(ov[i][0]); c4[1] += wi * asf(ov[i][1]); c4[2] += wi * asf(ov[i][2]); c4[3] += wi * asf(ov[i][3]);
        }
        *(f32x4*)(cpart + qd * 512 + (t & 127) * 4) = c4;
    }
    bar();
    {
        const int row = t >> 2;
        float acc[8];
#pragma unroll
        for (int i = 0; i < 4; ++i) {
            const int rr = row + 128 * i;
            float c0 = (cpart[rr] + cpart[512 + rr]) + (cpart[1024 + rr] + cpart[1536 + rr]);
#pragma unroll
            for (int k = 0; k < 4; ++k) {
                if (i == 0) { acc[2 * k] = c0 * bflo(uvv[i][k]); acc[2 * k + 1] = c0 * bfhi(uvv[i][k]); }
                else { acc[2 * k] += c0 * bflo(uvv[i][k]); acc[2 * k + 1] += c0 * bfhi(uvv[i][k]); }
            }
        }
        bool up = lane & 4;
        float s[4];
#pragma unroll
        for (int k = 0; k < 4; ++k) { float send = up ? acc[k] : acc[k + 4]; float keep = up ? acc[k + 4] : acc[k]; s[k] = keep + __shfl_xor(send, 4); }
        up = lane & 8;
        float s2[2];
#pragma unroll
        for (int k = 0; k < 2; ++k) { float send = up ? s[k] : s[k + 2]; float keep = up ? s[k + 2] : s[k]; s2[k] = keep + __shfl_xor(send, 8); }
        up = lane & 16;
        float send = up ? s2[0] : s2[1]; float keep = up ? s2[1] : s2[0];
        float v = keep + __shfl_xor(send, 16);
        v += __shfl_xor(v, 32);
        if (lane < 32) {
            int d = (lane & 3) * 8 + ((lane >> 2) & 1) * 4 + ((lane >> 3) & 1) * 2 + ((lane >> 4) & 1);
            sm3[w * 32 + d] = v;
        }
    }
    bar();
    {
        f32x4 c4 = *(const f32x4*)(sm3 + 4 * w);
#pragma unroll
        for (int ww = 1; ww < 8; ++ww) c4 += *(const f32x4*)(sm3 + ww * 32 + 4 * w);
        f32x4 o0 = {0.f, 0.f, 0.f, 0.f}, o1 = {0.f, 0.f, 0.f, 0.f};
#pragma unroll
        for (int i = 0; i < 4; ++i) {
            const float c0 = rbf(c4[i]);
            o0[0] += c0 * bflo(ovv[i][0]); o0[1] += c0 * bfhi(ovv[i][0]); o0[2] += c0 * bflo(ovv[i][1]); o0[3] += c0 * bfhi(ovv[i][1]);
            o1[0] += c0 * bflo(ovv[i][2]); o1[1] += c0 * bfhi(ovv[i][2]); o1[2] += c0 * bflo(ovv[i][3]); o1[3] += c0 * bfhi(ovv[i][3]);
        }
        *(f32x4*)(redo + w * 512 + lane * 8) = o0;
        *(f32x4*)(redo + w * 512 + lane * 8 + 4) = o1;
    }
    bar();
    if (t < 128) {
        f32x4 s = *(const f32x4*)(redo + t * 4);
#pragma unroll
        for (int ww = 1; ww < 8; ++ww) s += *(const f32x4*)(redo + ww * 512 + t * 4);
        __builtin_amdgcn_raw_buffer_store_b128(__builtin_bit_cast(u32x4, s), rs, WS_GA + bb_ * 2048 + t * 16, 0, SC1);
    }
    STAMP(10);
}

extern "C" void kernel_launch(void* const* d_in, const int* in_sizes, int n_in,
                              void* d_out, int out_size, void* d_ws, size_t ws_size,
                              hipStream_t stream) {
    const u16* x = (const u16*)d_in[0];
    const u16* ckv = (const u16*)d_in[1];
    const u16* wq = (const u16*)d_in[2];
    const u16* wuk = (const u16*)d_in[3];
    const u16* wuv = (const u16*)d_in[4];
    const u16* wo = (const u16*)d_in[5];
    const u16* wg = (const u16*)d_in[7];
    const u16* wu = (const u16*)d_in[8];
    const u16* wd = (const u16*)d_in[9];
    const u16* gw = (const u16*)d_in[10];
    mega<<<256, 640, 0, stream>>>(x, ckv, wq, wuk, wuv, wo, wg, wu, wd, gw, (char*)d_ws, (u16*)d_out);
}
```

```cpp
#include <hip/hip_runtime.h>
#include <hip/hip_bf16.h>
#include <stdint.h>

#define SK 8192
#define FF 1376

typedef unsigned short u16;
typedef unsigned long long u64;
typedef __attribute__((ext_vector_type(8))) __bf16 bf16x8;
typedef __attribute__((ext_vector_type(2))) __bf16 bf16x2;
typedef __attribute__((ext_vector_type(4))) short s16x4;
typedef __attribute__((ext_vector_type(8))) short s16x8;
typedef __attribute__((ext_vector_type(16))) float f32x16;
typedef __attribute__((ext_vector_type(4))) float f32x4;
typedef __attribute__((ext_vector_type(2))) float f32x2;
typedef __attribute__((ext_vector_type(4))) unsigned int u32x4;
typedef __attribute__((ext_vector_type(2))) unsigned int u32x2;
#define GAS __attribute__((address_space(1)))
typedef GAS unsigned gu32;
typedef GAS u64 gu64;
#define RLX_AGENT __ATOMIC_RELAXED, __HIP_MEMORY_SCOPE_AGENT
#define SC1 16
#define POISON 0xFFFFFFFFu

__device__ __forceinline__ float bf2f(u16 v) { return __uint_as_float(((unsigned)v) << 16); }
__device__ __forceinline__ float bflo(unsigned v) { return __uint_as_float(v << 16); }
__device__ __forceinline__ float bfhi(unsigned v) { return __uint_as_float(v & 0xffff0000u); }
__device__ __forceinline__ float asf(unsigned v) { return __uint_as_float(v); }
__device__ __forceinline__ unsigned asu(float v) { return __float_as_uint(v); }
__device__ __forceinline__ unsigned pk2(float a, float b) {
    bf16x2 v = {(__bf16)a, (__bf16)b};
    return __builtin_bit_cast(unsigned, v);
}
__device__ __forceinline__ u16 f2bf(float f) { __bf16 v = (__bf16)f; return __builtin_bit_cast(u16, v); }
__device__ __forceinline__ float rbf(float f) { return bf2f(f2bf(f)); }
__device__ __forceinline__ unsigned umax(unsigned a, unsigned b) { return a > b ? a : b; }
__device__ __forceinline__ unsigned max4(u32x4 v) { return umax(umax(v[0], v[1]), umax(v[2], v[3])); }

#define NREP 32
#define WS_EPOCH   0
#define WS_F0      256
#define WS_QG      (WS_F0 + NREP * 2048)
#define WS_QABS    (WS_QG + 128 * 1024)
#define WS_P       (WS_QABS + 8 * 32768)
#define WS_MLOC    (WS_P + 8 * 16 * 4096)
#define WS_OPART   (WS_MLOC + 8 * 8 * 32 * 16)
#define WS_ML      (WS_OPART + 8 * 32 * 512 * 4)
#define WS_GA      (WS_ML + 32 * 8 * 8)
#define WS_GB      (WS_GA + 128 * 2048)
#define WS_QGF     (WS_GB + 128 * 2048)
#define WS_PF      (WS_QGF + 128 * 1024)
#define WS_MLOCF   (WS_PF + 8 * 16 * 4096)
#define WS_END     (WS_MLOCF + 8 * 8 * 32 * 16)

#define SPIN_CAP (1u << 17)
#define A_SLEEP 40
#define B_SLEEP 20
#define A_DELAY 0
#define STAMP(i)

__device__ __forceinline__ void lgkm0() { asm volatile("s_waitcnt lgkmcnt(0)" ::: "memory"); }
__device__ __forceinline__ void vm0() { asm volatile("s_waitcnt vmcnt(0)" ::: "memory"); }
__device__ __forceinline__ void bar() { asm volatile("s_waitcnt lgkmcnt(0)\n\ts_barrier" ::: "memory"); }
typedef volatile __attribute__((address_space(3))) unsigned lds_u32;
__device__ __forceinline__ void lds_wait(lds_u32* p, unsigned target) {
    for (unsigned s = 0; s < SPIN_CAP; ++s) {
        if (*p >= target) break;
        __builtin_amdgcn_s_sleep(1);
    }
    asm volatile("" ::: "memory");
}

__device__ __forceinline__ void f0_signal(char* ws, int b, int lane, unsigned tag) {
    const u64 want = ((u64)(~tag) << 32) | tag;
    if (lane < NREP) __hip_atomic_store((gu64*)(ws + WS_F0 + lane * 2048 + b * 8), want, RLX_AGENT);
}
__device__ __forceinline__ void f0_wait(const __amdgpu_buffer_rsrc_t rs, int b, int lane, unsigned tag) {
    const int fo = WS_F0 + (b & (NREP - 1)) * 2048 + lane * 16;
    for (unsigned spins = 0; spins < SPIN_CAP; ++spins) {
        asm volatile("" ::: "memory");
        u32x4 a0 = __builtin_amdgcn_raw_buffer_load_b128(rs, fo, 0, SC1);
        u32x4 a1 = __builtin_amdgcn_raw_buffer_load_b128(rs, fo + 1024, 0, SC1);
        unsigned bad = (a0[0] ^ tag) | (a0[1] ^ ~tag) | (a0[2] ^ tag) | (a0[3] ^ ~tag) |
                       (a1[0] ^ tag) | (a1[1] ^ ~tag) | (a1[2] ^ tag) | (a1[3] ^ ~tag);
        if (__all(bad == 0)) break;
        __builtin_amdgcn_s_sleep(8);
    }
    asm volatile("" ::: "memory");
}

#define LDS_BYTES (65536 + 65536 + 8192)

__global__ __launch_bounds__(640, 1) void mega(const u16* __restrict__ x, const u16* __restrict__ ckv,
                                               const u16* __restrict__ wq, const u16* __restrict__ wuk,
                                               const u16* __restrict__ wuv, const u16* __restrict__ wo,
                                               const u16* __restrict__ wg, const u16* __restrict__ wu,
                                               const u16* __restrict__ wd, const u16* __restrict__ gw,
                                               char* __restrict__ ws, u16* __restrict__ out) {
    __shared__ __attribute__((aligned(16))) char lds[LDS_BYTES];
    __shared__ unsigned flg[4];

    const int b = blockIdx.x;
    const int t = threadIdx.x;
    const int lane = t & 63, w = t >> 6;
    const int r = lane & 31, h2 = lane >> 5;
    const __amdgpu_buffer_rsrc_t rs = __builtin_amdgcn_make_buffer_rsrc(ws, 0, WS_END, 0x00020000);
    const u32x4 P4 = {POISON, POISON, POISON, POISON};
    const u32x2 P2 = {POISON, POISON};
    STAMP(0);

    if (b < 128) {
        if (w >= 9) return;
        lds_u32* vflg = (lds_u32*)flg;
        if (w == 8) {
            const unsigned e = __hip_atomic_load((gu32*)(ws + WS_EPOCH), RLX_AGENT);
            bar();
            f0_signal(ws, b, lane, e + 1u);
            asm volatile("s_barrier" ::: "memory");
            STAMP(1);
            __builtin_amdgcn_s_sleep(A_SLEEP);
            f0_wait(rs, b, lane, e + 1u);
            if (lane == 0) vflg[2] = 1u;
            STAMP(2);
            return;
        }
        char* tile = lds;
        float* red = (float*)(lds + 65536);
        float* fac = (float*)(lds + 131072);
        float* Mws = fac + 512;
        float* Lws = Mws + 256;
        float* fin = Lws + 256;
        float* fing = fin + 256;
        const int kc = b & 7, mem = b >> 3;
        __builtin_amdgcn_raw_buffer_store_b128(P4, rs, (w < 4 ? WS_P : WS_PF) + ((kc * 16 + mem) * 4 + (w & 3)) * 1024 + lane * 16, 0, SC1);
        if (w < 4)
            __builtin_amdgcn_raw_buffer_store_b128(P4, rs, WS_OPART + ((kc * 32 + w * 8 + (lane >> 3)) * 512 + mem * 32) * 4 + (lane & 7) * 16, 0, SC1);
        else if (w == 4)
            __builtin_amdgcn_raw_buffer_store_b64(P2, rs, (lane < 32 ? WS_MLOC : WS_MLOCF) + ((kc * 8 + (mem >> 1)) * 32 + r) * 16 + (mem & 1) * 8, 0, SC1);
        else if (w == 5 && mem == 0 && lane < 32)
            __builtin_amdgcn_raw_buffer_store_b64(P2, rs, WS_ML + (lane * 8 + kc) * 8, 0, SC1);
        if (t < 4) flg[t] = 0u;
        vm0();
        bar();
        asm volatile("s_barrier" ::: "memory");
        STAMP(1);
        __builtin_amdgcn_s_sleep(A_DELAY);
        bf16x8 bq[8];
        u32x4 cv[8];
        {
            const u16* bp = ckv + (size_t)(kc * 1024 + mem * 64 + r) * 512 + 64 * w + 8 * h2;
#pragma unroll
            for (int kb = 0; kb < 2; ++kb)
#pragma unroll
                for (int s = 0; s < 4; ++s) bq[kb * 4 + s] = *(const bf16x8*)(bp + kb * 32 * 512 + 16 * s);
            const u16* cp = ckv + (size_t)(kc * 1024 + (t >> 2)) * 512 + mem * 32 + (t & 3) * 8;
#pragma unroll
            for (int i = 0; i < 8; ++i) cv[i] = *(const u32x4*)(cp + (size_t)i * 128 * 512);
        }
        __builtin_amdgcn_sched_barrier(0);
#pragma unroll
        for (int i = 0; i < 8; ++i) *(u32x4*)(tile + (i * 128 + (t >> 2)) * 64 + (t & 3) * 16) = cv[i];
        STAMP(2);
        lds_wait(vflg + 2, 1);
        STAMP(3);
        {
            u32x4 av[4];
            const int qo = WS_QABS + (b & 7) * 32768 + (w * 256 + lane) * 16;
            for (unsigned spins = 0; spins < SPIN_CAP; ++spins) {
                asm volatile("" ::: "memory");
#pragma unroll
                for (int s = 0; s < 4; ++s) av[s] = __builtin_amdgcn_raw_buffer_load_b128(rs, qo + 1024 * s, 0, SC1);
                unsigned m = umax(umax(max4(av[0]), max4(av[1])), umax(max4(av[2]), max4(av[3])));
                if (__all(m != POISON)) break;
                __builtin_amdgcn_s_sleep(1);
            }
            STAMP(4);
#pragma unroll
            for (int kb = 0; kb < 2; ++kb) {
                f32x16 acc;
#pragma unroll
                for (int i = 0; i < 16; ++i) acc[i] = 0.f;
#pragma unroll
                for (int s = 0; s < 4; ++s)
                    acc = __builtin_amdgcn_mfma_f32_32x32x16_bf16(__builtin_bit_cast(bf16x8, av[s]), bq[kb * 4 + s], acc, 0, 0, 0);
#pragma unroll
                for (int i = 0; i < 16; ++i) {
                    int head = (i & 3) + 8 * (i >> 2) + 4 * h2;
                    red[(w * 32 + head) * 64 + kb * 32 + r] = acc[i];
                }
            }
        }
        bar();
        {
            const int head = t >> 4, kq = (t & 15) * 4;
            f32x4 s4 = *(const f32x4*)(red + head * 64 + kq);
#pragma unroll
            for (int ww = 1; ww < 8; ++ww) s4 += *(const f32x4*)(red + (ww * 32 + head) * 64 + kq);
            float m = fmaxf(fmaxf(s4[0], s4[1]), fmaxf(s4[2], s4[3]));
            m = fmaxf(m, __shfl_xor(m, 1));
            m = fmaxf(m, __shfl_xor(m, 2));
            m = fmaxf(m, __shfl_xor(m, 4));
            m = fmaxf(m, __shfl_xor(m, 8));
            unsigned p01 = pk2(__expf(s4[0] - m), __expf(s4[1] - m));
            unsigned p23 = pk2(__expf(s4[2] - m), __expf(s4[3] - m));
            float l = (bflo(p01) + bfhi(p01)) + (bflo(p23) + bfhi(p23));
            l += __shfl_xor(l, 1);
            l += __shfl_xor(l, 2);
            l += __shfl_xor(l, 4);
            l += __shfl_xor(l, 8);
            u32x4 pp = {p01, p23, (unsigned)__shfl_down((int)p01, 1), (unsigned)__shfl_down((int)p23, 1)};
            u32x2 ml = {asu(m), asu(l)};
            const int po = ((kc * 16 + mem) * 256 + ((t & 15) >> 1) * 32 + head) * 16;
            const int mo = ((kc * 8 + (mem >> 1)) * 32 + head) * 16 + (mem & 1) * 8;
            if ((t & 1) == 0) __builtin_amdgcn_raw_buffer_store_b128(pp, rs, WS_PF + po, 0, 0);
            if ((t & 15) == 0) __builtin_amdgcn_raw_buffer_store_b64(ml, rs, WS_MLOCF + mo, 0, 0);
            if ((t & 1) == 0) __builtin_amdgcn_raw_buffer_store_b128(pp, rs, WS_P + po, 0, SC1);
            if ((t & 15) == 0) __builtin_amdgcn_raw_buffer_store_b64(ml, rs, WS_MLOC + mo, 0, SC1);
        }
        bar();
        STAMP(5);
        {
            u32x4 pv[8], mlv;
            for (unsigned spins = 0; spins < SPIN_CAP; ++spins) {
                asm volatile("" ::: "memory");
                const bool slow = (spins & 3) == 3;
                const int po = (slow ? WS_P : WS_PF) + ((kc * 16 + w * 2) * 256 + lane) * 16;
                const int mo = (slow ? WS_MLOC : WS_MLOCF) + ((kc * 8 + w) * 32 + r) * 16;
#pragma unroll
                for (int s = 0; s < 8; ++s) pv[s] = __builtin_amdgcn_raw_buffer_load_b128(rs, po + 1024 * s, 0, SC1);
                mlv = __builtin_amdgcn_raw_buffer_load_b128(rs, mo, 0, SC1);
                unsigned m = umax(umax(max4(pv[0]), max4(pv[1])), umax(max4(pv[2]), max4(pv[3])));
                m = umax(m, umax(umax(max4(pv[4]), max4(pv[5])), umax(max4(pv[6]), max4(pv[7]))));
                m = umax(m, max4(mlv));
                if (__all(m != POISON)) break;
                __builtin_amdgcn_s_sleep(1);
            }
            STAMP(6);
            {
                float m0 = asf(mlv[0]), l0 = asf(mlv[1]), m1 = asf(mlv[2]), l1 = asf(mlv[3]);
                float Mw = fmaxf(m0, m1);
                float f0_ = __expf(m0 - Mw), f1_ = __expf(m1 - Mw);
                if (lane < 32) {
                    fac[(2 * w) * 32 + r] = f0_; fac[(2 * w + 1) * 32 + r] = f1_;
                    Mws[r * 8 + w] = Mw; Lws[r * 8 + w] = f0_ * l0 + f1_ * l1;
                }
            }
            const int g = lane >> 4;
            const int q = (lane & 15) >> 2, p = lane & 3;
            const int cb = g & 1;
            f32x16 tot;
#pragma unroll
            for (int mm = 0; mm < 2; ++mm) {
                f32x16 acc;
#pragma unroll
                for (int i = 0; i < 16; ++i) acc[i] = 0.f;
#pragma unroll
                for (int ss = 0; ss < 4; ++ss) {
                    int s = mm * 4 + ss;
                    int row = w * 128 + 16 * s + 8 * h2 + q;
                    s16x4 t0 = __builtin_amdgcn_ds_read_tr16_b64_v4i16(
                        (__attribute__((address_space(3))) s16x4*)(tile + row * 64 + cb * 32 + p * 8));
                    s16x4 t1 = __builtin_amdgcn_ds_read_tr16_b64_v4i16(
                        (__attribute__((address_space(3))) s16x4*)(tile + (row + 4) * 64 + cb * 32 + p * 8));
                    s16x8 bb = {t0[0], t0[1], t0[2], t0[3], t1[0], t1[1], t1[2], t1[3]};
                    acc = __builtin_amdgcn_mfma_f32_32x32x16_bf16(__builtin_bit_cast(bf16x8, pv[s]), __builtin_bit_cast(bf16x8, bb), acc, 0, 0, 0);
                }
                const float* fp = fac + (2 * w + mm) * 32 + 4 * h2;
#pragma unroll
                for (int qd = 0; qd < 4; ++qd) {
                    f32x4 f4 = *(const f32x4*)(fp + 8 * qd);
#pragma unroll
                    for (int e2 = 0; e2 < 4; ++e2) {
                        if (mm == 0) tot[qd * 4 + e2] = f4[e2] * acc[qd * 4 + e2];
                        else tot[qd * 4 + e2] += f4[e2] * acc[qd * 4 + e2];
                    }
                }
            }
#pragma unroll
            for (int i = 0; i < 16; ++i) {
                int head = (i & 3) + 8 * (i >> 2) + 4 * h2;
                red[(w * 32 + head) * 32 + r] = tot[i];
            }
        }
        bar();
        {
            const int head = t >> 4, cq = (t & 15) * 2;
            f32x4 ma = *(const f32x4*)(Mws + head * 8), mb = *(const f32x4*)(Mws + head * 8 + 4);
            float M = fmaxf(fmaxf(fmaxf(ma[0], ma[1]), fmaxf(ma[2], ma[3])), fmaxf(fmaxf(mb[0], mb[1]), fmaxf(mb[2], mb[3])));
            float ew[8];
#pragma unroll
            for (int ww = 0; ww < 4; ++ww) { ew[ww] = __expf(ma[ww] - M); ew[ww + 4] = __expf(mb[ww] - M); }
            f32x2 s2 = {0.f, 0.f};
#pragma unroll
            for (int ww = 0; ww < 8; ++ww) s2 += ew[ww] * *(const f32x2*)(red + (ww * 32 + head) * 32 + cq);
            u32x4 o = {asu(s2[0]), asu(s2[1]), asu(__shfl_down(s2[0], 1)), asu(__shfl_down(s2[1], 1))};
            if ((t & 1) == 0)
                __builtin_amdgcn_raw_buffer_store_b128(o, rs, WS_OPART + ((kc * 32 + head) * 512 + mem * 32 + cq) * 4, 0, SC1);
            if (mem == 0 && (t & 15) == 0) {
                f32x4 la = *(const f32x4*)(Lws + head * 8), lb = *(const f32x4*)(Lws + head * 8 + 4);
                float L = 0.f;
#pragma unroll
                for (int ww = 0; ww < 4; ++ww) L += ew[ww] * la[ww] + ew[ww + 4] * lb[ww];
                u32x2 ml = {asu(M), asu(L)};
                __builtin_amdgcn_raw_buffer_store_b64(ml, rs, WS_ML + (head * 8 + kc) * 8, 0, SC1);
            }
        }
        STAMP(7);
        if (b >= 32) return;
        {
            const int p = t >> 2, qc = t & 3;
            float gp = rbf(bf2f(x[t]) * bf2f(gw[t]));
            const int ao = WS_GA + p * 2048 + b * 64 + qc * 16;
            const int bo = WS_GB + (p < 43 ? p : 42) * 2048 + b * 64 + qc * 16;
            u32x4 a0, b0;
            for (unsigned spins = 0; spins < SPIN_CAP; ++spins) {
                asm volatile("" ::: "memory");
                a0 = __builtin_amdgcn_raw_buffer_load_b128(rs, ao, 0, SC1);
                b0 = __builtin_amdgcn_raw_buffer_load_b128(rs, bo, 0, SC1);
                unsigned m = umax(max4(a0), max4(b0));
                if (__all(m != POISON)) break;
                __builtin_amdgcn_s_sleep(2);
            }
            STAMP(11);
            if (p >= 43) { b0[0] = 0u; b0[1] = 0u; b0[2] = 0u; b0[3] = 0u; }
            bool up = lane & 4;
            float s4[4];
#pragma unroll
            for (int q = 0; q < 4; ++q) { float send = up ? asf(a0[q]) : asf(b0[q]); float keep = up ? asf(b0[q]) : asf(a0[q]); s4[q] = keep + __shfl_xor(send, 4); }
            up = lane & 8;
            float s2[2];
#pragma unroll
            for (int q = 0; q < 2; ++q) { float send = up ? s4[q] : s4[q + 2]; float keep = up ? s4[q + 2] : s4[q]; s2[q] = keep + __shfl_xor(send, 8); }
            up = lane & 16;
            float send = up ? s2[0] : s2[1]; float keep = up ? s2[1] : s2[0];
            float vv = keep + __shfl_xor(send, 16);
            vv += __shfl_xor(vv, 32);
#pragma unroll
            for (int o = 32; o >= 1; o >>= 1) gp += __shfl_xor(gp, o);
            if (lane < 32) {
                int idx = ((lane >> 2) & 1) * 16 + (lane & 3) * 4 + ((lane >> 3) & 1) * 2 + ((lane >> 4) & 1);
                fin[w * 32 + idx] = vv;
            }
            if (lane == 0) fing[w] = gp;
        }
        bar();
        if (t < 16) {
            float a = 0.f, bb = 0.f, gs = 0.f;
#pragma unroll
            for (int ww = 0; ww < 8; ++ww) { a += fin[ww * 32 + t]; bb += fin[ww * 32 + 16 + t]; gs += fing[ww]; }
            gs = rbf(gs);
            float g = rbf(1.f / (1.f + __expf(-gs)));
            a = rbf(a); bb = rbf(bb);
            float rr = rbf(g * a) + rbf(rbf(1.f - g) * bb);
            out[b * 16 + t] = f2bf(rr);
        }
        STAMP(12);
        return;
    }

    float* redf = (float*)lds;
    float* redo = redf + 4096;
    float* cpart = redo + 4096;
    float* sm1 = cpart + 2048;
    float* sm2 = sm1 + 1024;
    float* sm3 = sm2 + 512;
    float* qs = sm3 + 256;
    lds_u32* vflg = (lds_u32*)flg;
    const int bb_ = b - 128;
    const int h = bb_ & 31, jj = bb_ >> 5;
    const bool has_ffn = bb_ < 43;

    if (w >= 8) {
        const unsigned e = __hip_atomic_load((gu32*)(ws + WS_EPOCH), RLX_AGENT);
        bar();
        const unsigned tag = e + 1u;
        if (w == 8) {
            lds_wait(vflg + 0, 8);
            STAMP(1);
            f32x2 s = *(const f32x2*)(sm1 + 2 * lane);
#pragma unroll
            for (int ww = 1; ww < 8; ++ww) s += *(const f32x2*)(sm1 + ww * 128 + 2 * lane);
            u32x4 g = {tag, asu(s[0]), asu(s[1]), ~tag};
            __builtin_amdgcn_raw_buffer_store_b128(g, rs, WS_QGF + ((h * 4 + jj) * 64 + lane) * 16, 0, 0);
            __builtin_amdgcn_raw_buffer_store_b128(g, rs, WS_QG + ((h * 4 + jj) * 64 + lane) * 16, 0, SC1);
            for (unsigned spins = 0; spins < SPIN_CAP; ++spins) {
                asm volatile("" ::: "memory");
                u32x4 v[4];
                const int qb = ((spins & 3) == 3 ? WS_QG : WS_QGF) + (h * 4 * 64 + lane) * 16;
#pragma unroll
                for (int k = 0; k < 4; ++k) v[k] = __builtin_amdgcn_raw_buffer_load_b128(rs, qb + k * 1024, 0, SC1);
                unsigned bad = 0;
                float q0 = 0.f, q1 = 0.f;
#pragma unroll
                for (int k = 0; k < 4; ++k) {
                    bad |= (v[k][0] ^ tag) | (v[k][3] ^ ~tag);
                    q0 += asf(v[k][1]); q1 += asf(v[k][2]);
                }
                if (__all(bad == 0) || spins == SPIN_CAP - 1) {
                    f32x2 qq = {rbf(q0), rbf(q1)};
                    *(f32x2*)(qs + 2 * lane) = qq;
                    break;
                }
                __builtin_amdgcn_s_sleep(1);
            }
            lgkm0();
            if (lane == 0) vflg[1] = 1u;
            STAMP(2);
        } else {
            lds_wait(vflg + 3, 8);
            STAMP(1);
            f0_signal(ws, b, lane, tag);
            __builtin_amdgcn_s_sleep(B_SLEEP);
            f0_wait(rs, b, lane, tag);
            if (lane == 0) vflg[2] = 1u;
            if (b == 255 && lane == 0) __hip_atomic_store((gu32*)(ws + WS_EPOCH), tag, RLX_AGENT);
            STAMP(2);
        }
        return;
    }

    {
        int po;
        if (w < 2) po = WS_QABS + (lane >> 3) * 32768 + ((jj * 16 + w * 8 + (lane & 7)) * 32 + h) * 16;
        else if (w < 4) po = WS_GA + bb_ * 2048 + (w & 1) * 1024 + lane * 16;
        else po = WS_GB + bb_ * 2048 + (w & 1) * 1024 + lane * 16;
        if (w < 4 || (w < 6 && has_ffn)) __builtin_amdgcn_raw_buffer_store_b128(P4, rs, po, 0, SC1);
    }
    __builtin_amdgcn_sched_barrier(0);
    u32x4 wqv[4], wkv[4];
    u16 xr[4];
    {
        const int row = jj * 128 + (t >> 4);
#pragma unroll
        for (int i = 0; i < 4; ++i) xr[i] = x[row + 32 * i];
#pragma unroll
        for (int i = 0; i < 4; ++i)
            wqv[i] = *(const u32x4*)(wq + (size_t)(row + 32 * i) * 4096 + h * 128 + (t & 15) * 8);
#pragma unroll
        for (int i = 0; i < 4; ++i)
            wkv[i] = *(const u32x4*)(wuk + (size_t)(jj * 128 + 16 * w + (lane >> 4) + 4 * i) * 4096 + h * 128 + (lane & 15) * 8);
    }
    __builtin_amdgcn_sched_barrier(0);
    if (t < 4) flg[t] = 0u;
    bar();
    asm volatile("s_waitcnt vmcnt(12)" ::: "memory");
    if (lane == 0) __hip_atomic_fetch_add(flg + 3, 1u, __ATOMIC_RELAXED, __HIP_MEMORY_SCOPE_WORKGROUP);
    __builtin_amdgcn_sched_barrier(0);
    asm volatile("s_waitcnt vmcnt(4)" ::: "memory");
    STAMP(1);
    {
        float acc[8];
#pragma unroll
        for (int k = 0; k < 4; ++k) {
            acc[2 * k] = bf2f(xr[0]) * bflo(wqv[0][k]) + bf2f(xr[1]) * bflo(wqv[1][k]) + bf2f(xr[2]) * bflo(wqv[2][k]) + bf2f(xr[3]) * bflo(wqv[3][k]);
            acc[2 * k + 1] = bf2f(xr[0]) * bfhi(wqv[0][k]) + bf2f(xr[1]) * bfhi(wqv[1][k]) + bf2f(xr[2]) * bfhi(wqv[2][k]) + bf2f(xr[3]) * bfhi(wqv[3][k]);
        }
        bool up = lane & 16;
        float s[4];
#pragma unroll
        for (int k = 0; k < 4; ++k) { float send = up ? acc[k] : acc[k + 4]; float keep = up ? acc[k + 4] : acc[k]; s[k] = keep + __shfl_xor(send, 16); }
        up = lane & 32;
        f32x2 s2;
#pragma unroll
        for (int k = 0; k < 2; ++k) { float send = up ? s[k] : s[k + 2]; float keep = up ? s[k + 2] : s[k]; s2[k] = keep + __shfl_xor(send, 32); }
        *(f32x2*)(sm1 + w * 128 + (lane & 15) * 8 + ((lane >> 4) & 1) * 4 + (lane >> 5) * 2) = s2;
        lgkm0();
        if (lane == 0) __hip_atomic_fetch_add(flg + 0, 1u, __ATOMIC_RELAXED, __HIP_MEMORY_SCOPE_WORKGROUP);
    }
    __builtin_amdgcn_sched_barrier(0);
    lds_wait(vflg + 1, 1);
    STAMP(2);
    {
        const int l16 = lane & 15;
        f32x4 qa0 = *(const f32x4*)(qs + l16 * 8), qa1 = *(const f32x4*)(qs + l16 * 8 + 4);
        float d[4];
#pragma unroll
        for (int i = 0; i < 4; ++i) {
            d[i] = qa0[0] * bflo(wkv[i][0]) + qa0[1] * bfhi(wkv[i][0]) + qa0[2] * bflo(wkv[i][1]) + qa0[3] * bfhi(wkv[i][1]);
            d[i] += qa1[0] * bflo(wkv[i][2]) + qa1[1] * bfhi(wkv[i][2]) + qa1[2] * bflo(wkv[i][3]) + qa1[3] * bfhi(wkv[i][3]);
        }
        bool up = lane & 1;
        float s[2];
#pragma unroll
        for (int k = 0; k < 2; ++k) { float send = up ? d[k] : d[k + 2]; float keep = up ? d[k + 2] : d[k]; s[k] = keep + __shfl_xor(send, 1); }
        up = lane & 2;
        float send = up ? s[0] : s[1], keep = up ? s[1] : s[0];
        float v = keep + __shfl_xor(send, 2);
        v += __shfl_xor(v, 4);
        v += __shfl_xor(v, 8);
        v *= 0.08838834764831845f;
        const int k = (lane >> 3) & 1;
        float vv[8];
#pragma unroll
        for (int e2 = 0; e2 < 8; ++e2) vv[e2] = __shfl(v, (e2 & 3) * 16 + (k | ((e2 >> 2) << 1)));
        u32x4 o = {pk2(vv[0], vv[1]), pk2(vv[2], vv[3]), pk2(vv[4], vv[5]), pk2(vv[6], vv[7])};
        if (lane < 16)
            __builtin_amdgcn_raw_buffer_store_b128(o, rs, WS_QABS + (lane & 7) * 32768 + ((jj * 16 + w * 2 + k) * 32 + h) * 16, 0, SC1);
    }
    __builtin_amdgcn_sched_barrier(0);
    if (lane == 0) __hip_atomic_fetch_add(flg + 0, 1u, __ATOMIC_RELAXED, __HIP_MEMORY_SCOPE_WORKGROUP);
    lds_wait(vflg + 0, 16);
    __builtin_amdgcn_sched_barrier(0);
    STAMP(3);
    u32x4 uvv[4], ovv[4];
#pragma unroll
    for (int i = 0; i < 4; ++i)
        uvv[i] = *(const u32x4*)(wuv + (size_t)((t >> 2) + 128 * i) * 4096 + h * 128 + jj * 32 + (t & 3) * 8);
#pragma unroll
    for (int i = 0; i < 4; ++i)
        ovv[i] = *(const u32x4*)(wo + ((size_t)h * 128 + jj * 32 + w * 4 + i) * 512 + lane * 8);
    __builtin_amdgcn_sched_barrier(0);
    if (has_ffn) {
        u32x4 gv[4], uv_[4], dv[4];
        u16 xf[4];
#pragma unroll
        for (int i = 0; i < 4; ++i) xf[i] = x[(t >> 2) + 128 * i];
#pragma unroll
        for (int i = 0; i < 4; ++i) {
            gv[i] = *(const u32x4*)(wg + (size_t)((t >> 2) + 128 * i) * FF + bb_ * 32 + (t & 3) * 8);
            uv_[i] = *(const u32x4*)(wu + (size_t)((t >> 2) + 128 * i) * FF + bb_ * 32 + (t & 3) * 8);
        }
#pragma unroll
        for (int i = 0; i < 4; ++i) dv[i] = *(const u32x4*)(wd + (size_t)(bb_ * 32 + w * 4 + i) * 512 + lane * 8);
        __builtin_amdgcn_sched_barrier(0);
        float p[16];
#pragma unroll
        for (int i = 0; i < 4; ++i) {
            const float xv = bf2f(xf[i]);
#pragma unroll
            for (int k = 0; k < 4; ++k) {
                if (i == 0) {
                    p[2 * k] = xv * bflo(gv[i][k]); p[2 * k + 1] = xv * bfhi(gv[i][k]);
                    p[8 + 2 * k] = xv * bflo(uv_[i][k]); p[8 + 2 * k + 1] = xv * bfhi(uv_[i][k]);
                } else {
                    p[2 * k] += xv * bflo(gv[i][k]); p[2 * k + 1] += xv * bfhi(gv[i][k]);
                    p[8 + 2 * k] += xv * bflo(uv_[i][k]); p[8 + 2 * k + 1] += xv * bfhi(uv_[i][k]);
                }
            }
        }
        bool up = lane & 4;
        float s8[8];
#pragma unroll
        for (int k = 0; k < 8; ++k) { float send = up ? p[k] : p[k + 8]; float keep = up ? p[k + 8] : p[k]; s8[k] = keep + __shfl_xor(send, 4); }
        up = lane & 8;
        float s4[4];
#pragma unroll
        for (int k = 0; k < 4; ++k) { float send = up ? s8[k] : s8[k + 4]; float keep = up ? s8[k + 4] : s8[k]; s4[k] = keep + __shfl_xor(send, 8); }
        up = lane & 16;
        float s2[2];
#pragma unroll
        for (int k = 0; k < 2; ++k) { float send = up ? s4[k] : s4[k + 2]; float keep = up ? s4[k + 2] : s4[k]; s2[k] = keep + __shfl_xor(send, 16); }
        up = lane & 32;
        float send = up ? s2[0] : s2[1]; float keep = up ? s2[1] : s2[0];
        float v = keep + __shfl_xor(send, 32);
        sm2[w * 64 + ((lane >> 2) & 1) * 32 + (lane & 3) * 8 + ((lane >> 3) & 1) * 4 + ((lane >> 4) & 1) * 2 + (lane >> 5)] = v;
        bar();
        f32x4 g4 = *(const f32x4*)(sm2 + 4 * w), u4 = *(const f32x4*)(sm2 + 32 + 4 * w);
#pragma unroll
        for (int ww = 1; ww < 8; ++ww) { g4 += *(const f32x4*)(sm2 + ww * 64 + 4 * w); u4 += *(const f32x4*)(sm2 + ww * 64 + 32 + 4 * w); }
        f32x4 o0 = {0.f, 0.f, 0.f, 0.f}, o1 = {0.f, 0.f, 0.f, 0.f};
#pragma unroll
        for (int i = 0; i < 4; ++i) {
            const float gs = rbf(g4[i]), us = rbf(u4[i]);
            const float sg = rbf(gs / (1.f + __expf(-gs)));
            const float hv = rbf(sg * us);
            o0[0] += hv * bflo(dv[i][0]); o0[1] += hv * bfhi(dv[i][0]); o0[2] += hv * bflo(dv[i][1]); o0[3] += hv * bfhi(dv[i][1]);
            o1[0] += hv * bflo(dv[i][2]); o1[1] += hv * bfhi(dv[i][2]); o1[2] += hv * bflo(dv[i][3]); o1[3] += hv * bfhi(dv[i][3]);
        }
        *(f32x4*)(redf + w * 512 + lane * 8) = o0;
        *(f32x4*)(redf + w * 512 + lane * 8 + 4) = o1;
        bar();
        if (t < 128) {
            f32x4 s = *(const f32x4*)(redf + t * 4);
#pragma unroll
            for (int ww = 1; ww < 8; ++ww) s += *(const f32x4*)(redf + ww * 512 + t * 4);
            __builtin_amdgcn_raw_buffer_store_b128(__builtin_bit_cast(u32x4, s), rs, WS_GB + bb_ * 2048 + t * 16, 0, SC1);
        }
    }
    asm volatile("s_waitcnt vmcnt(1)" ::: "memory");
    lds_wait(vflg + 2, 1);
    STAMP(8);
    {
        const int qd = t >> 7;
        u32x4 ov[2];
        u32x2 ml;
        const int oo = WS_OPART + ((qd * 2 * 32 + h) * 512 + (t & 127) * 4) * 4;
        for (unsigned spins = 0; spins < SPIN_CAP; ++spins) {
            asm volatile("" ::: "memory");
#pragma unroll
            for (int i = 0; i < 2; ++i) ov[i] = __builtin_amdgcn_raw_buffer_load_b128(rs, oo + i * 65536, 0, SC1);
            ml = __builtin_amdgcn_raw_buffer_load_b64(rs, WS_ML + (h * 8 + (lane & 7)) * 8, 0, SC1);
            unsigned m = umax(max4(ov[0]), max4(ov[1]));
            m = umax(m, umax(ml[0], ml[1]));
            if (__all(m != POISON)) break;
            __builtin_amdgcn_s_sleep(1);
        }
        STAMP(9);
        float mk = asf(ml[0]), lk = asf(ml[1]);
        float M = mk;
        M = fmaxf(M, __shfl_xor(M, 1)); M = fmaxf(M, __shfl_xor(M, 2)); M = fmaxf(M, __shfl_xor(M, 4));
        float wk = __expf(mk - M);
        float L = wk * lk;
        L += __shfl_xor(L, 1); L += __shfl_xor(L, 2); L += __shfl_xor(L, 4);
        wk = wk / L;
        f32x4 c4 = {0.f, 0.f, 0.f, 0.f};
#pragma unroll
        for (int i = 0; i < 2; ++i) {
            float wi = __shfl(wk, qd * 2 + i);
            c4[0] += wi * asf(ov[i][0]); c4[1] += wi * asf(ov[i][1]); c4[2] += wi * asf(ov[i][2]); c4[3] += wi * asf(ov[i][3]);
        }
        *(f32x4*)(cpart + qd * 512 + (t & 127) * 4) = c4;
    }
    bar();
    {
        const int row = t >> 2;
        float acc[8];
#pragma unroll
        for (int i = 0; i < 4; ++i) {
            const int rr = row + 128 * i;
            float c0 = (cpart[rr] + cpart[512 + rr]) + (cpart[1024 + rr] + cpart[1536 + rr]);
#pragma unroll
            for (int k = 0; k < 4; ++k) {
                if (i == 0) { acc[2 * k] = c0 * bflo(uvv[i][k]); acc[2 * k + 1] = c0 * bfhi(uvv[i][k]); }
                else { acc[2 * k] += c0 * bflo(uvv[i][k]); acc[2 * k + 1] += c0 * bfhi(uvv[i][k]); }
            }
        }
        bool up = lane & 4;
        float s[4];
#pragma unroll
        for (int k = 0; k < 4; ++k) { float send = up ? acc[k] : acc[k + 4]; float keep = up ? acc[k + 4] : acc[k]; s[k] = keep + __shfl_xor(send, 4); }
        up = lane & 8;
        float s2[2];
#pragma unroll
        for (int k = 0; k < 2; ++k) { float send = up ? s[k] : s[k + 2]; float keep = up ? s[k + 2] : s[k]; s2[k] = keep + __shfl_xor(send, 8); }
        up = lane & 16;
        float send = up ? s2[0] : s2[1]; float keep = up ? s2[1] : s2[0];
        float v = keep + __shfl_xor(send, 16);
        v += __shfl_xor(v, 32);
        if (lane < 32) {
            int d = (lane & 3) * 8 + ((lane >> 2) & 1) * 4 + ((lane >> 3) & 1) * 2 + ((lane >> 4) & 1);
            sm3[w * 32 + d] = v;
        }
    }
    bar();
    {
        f32x4 c4 = *(const f32x4*)(sm3 + 4 * w);
#pragma unroll
        for (int ww = 1; ww < 8; ++ww) c4 += *(const f32x4*)(sm3 + ww * 32 + 4 * w);
        f32x4 o0 = {0.f, 0.f, 0.f, 0.f}, o1 = {0.f, 0.f, 0.f, 0.f};
#pragma unroll
        for (int i = 0; i < 4; ++i) {
            const float c0 = rbf(c4[i]);
            o0[0] += c0 * bflo(ovv[i][0]); o0[1] += c0 * bfhi(ovv[i][0]); o0[2] += c0 * bflo(ovv[i][1]); o0[3] += c0 * bfhi(ovv[i][1]);
            o1[0] += c0 * bflo(ovv[i][2]); o1[1] += c0 * bfhi(ovv[i][2]); o1[2] += c0 * bflo(ovv[i][3]); o1[3] += c0 * bfhi(ovv[i][3]);
        }
        *(f32x4*)(redo + w * 512 + lane * 8) = o0;
        *(f32x4*)(redo + w * 512 + lane * 8 + 4) = o1;
    }
    bar();
    if (t < 128) {
        f32x4 s = *(const f32x4*)(redo + t * 4);
#pragma unroll
        for (int ww = 1; ww < 8; ++ww) s += *(const f32x4*)(redo + ww * 512 + t * 4);
        __builtin_amdgcn_raw_buffer_store_b128(__builtin_bit_cast(u32x4, s), rs, WS_GA + bb_ * 2048 + t * 16, 0, SC1);
    }
    STAMP(10);
}

extern "C" void kernel_launch(void* const* d_in, const int* in_sizes, int n_in,
                              void* d_out, int out_size, void* d_ws, size_t ws_size,
                              hipStream_t stream) {
    const u16* x = (const u16*)d_in[0];
    const u16* ckv = (const u16*)d_in[1];
    const u16* wq = (const u16*)d_in[2];
    const u16* wuk = (const u16*)d_in[3];
    const u16* wuv = (const u16*)d_in[4];
    const u16* wo = (const u16*)d_in[5];
    const u16* wg = (const u16*)d_in[7];
    const u16* wu = (const u16*)d_in[8];
    const u16* wd = (const u16*)d_in[9];
    const u16* gw = (const u16*)d_in[10];
    mega<<<256, 640, 0, stream>>>(x, ckv, wq, wuk, wuv, wo, wg, wu, wd, gw, (char*)d_ws, (u16*)d_out);
}
```

```cpp
#include <hip/hip_runtime.h>
#include <hip/hip_bf16.h>
#include <stdint.h>

#define SK 8192
#define FF 1376

typedef unsigned short u16;
typedef unsigned long long u64;
typedef __attribute__((ext_vector_type(8))) __bf16 bf16x8;
typedef __attribute__((ext_vector_type(2))) __bf16 bf16x2;
typedef __attribute__((ext_vector_type(4))) short s16x4;
typedef __attribute__((ext_vector_type(8))) short s16x8;
typedef __attribute__((ext_vector_type(16))) float f32x16;
typedef __attribute__((ext_vector_type(4))) float f32x4;
typedef __attribute__((ext_vector_type(2))) float f32x2;
typedef __attribute__((ext_vector_type(4))) unsigned int u32x4;
typedef __attribute__((ext_vector_type(2))) unsigned int u32x2;
#define GAS __attribute__((address_space(1)))
typedef GAS unsigned gu32;
typedef GAS u64 gu64;
#define RLX_AGENT __ATOMIC_RELAXED, __HIP_MEMORY_SCOPE_AGENT
#define SC1 16
#define POISON 0xFFFFFFFFu

__device__ __forceinline__ float bf2f(u16 v) { return __uint_as_float(((unsigned)v) << 16); }
__device__ __forceinline__ float bflo(unsigned v) { return __uint_as_float(v << 16); }
__device__ __forceinline__ float bfhi(unsigned v) { return __uint_as_float(v & 0xffff0000u); }
__device__ __forceinline__ float asf(unsigned v) { return __uint_as_float(v); }
__device__ __forceinline__ unsigned asu(float v) { return __float_as_uint(v); }
__device__ __forceinline__ unsigned pk2(float a, float b) {
    bf16x2 v = {(__bf16)a, (__bf16)b};
    return __builtin_bit_cast(unsigned, v);
}
__device__ __forceinline__ u16 f2bf(float f) { __bf16 v = (__bf16)f; return __builtin_bit_cast(u16, v); }
__device__ __forceinline__ float rbf(float f) { return bf2f(f2bf(f)); }
__device__ __forceinline__ unsigned umax(unsigned a, unsigned b) { return a > b ? a : b; }
__device__ __forceinline__ unsigned max4(u32x4 v) { return umax(umax(v[0], v[1]), umax(v[2], v[3])); }

#define NREP 32
#define WS_EPOCH   0
#define WS_F0      256
#define WS_QG      (WS_F0 + NREP * 2048)
#define WS_QABS    (WS_QG + 128 * 1024)
#define WS_P       (WS_QABS + 8 * 32768)
#define WS_MLOC    (WS_P + 8 * 16 * 4096)
#define WS_OPART   (WS_MLOC + 8 * 8 * 32 * 16)
#define WS_ML      (WS_OPART + 8 * 32 * 512 * 4)
#define WS_GA      (WS_ML + 32 * 8 * 8)
#define WS_GB      (WS_GA + 128 * 2048)
#define WS_QGF     (WS_GB + 128 * 2048)
#define WS_PF      (WS_QGF + 128 * 1024)
#define WS_MLOCF   (WS_PF + 8 * 16 * 4096)
#define WS_END     (WS_MLOCF + 8 * 8 * 32 * 16)

#define SPIN_CAP (1u << 17)
#define A_SLEEP 10
#define Q_SLEEP 30
#define B_SLEEP 20
#define A_DELAY 0
#define STAMP(i)

__device__ __forceinline__ void lgkm0() { asm volatile("s_waitcnt lgkmcnt(0)" ::: "memory"); }
__device__ __forceinline__ void vm0() { asm volatile("s_waitcnt vmcnt(0)" ::: "memory"); }
__device__ __forceinline__ void bar() { asm volatile("s_waitcnt lgkmcnt(0)\n\ts_barrier" ::: "memory"); }
typedef volatile __attribute__((address_space(3))) unsigned lds_u32;
__device__ __forceinline__ void lds_wait(lds_u32* p, unsigned target) {
    for (unsigned s = 0; s < SPIN_CAP; ++s) {
        if (*p >= target) break;
        __builtin_amdgcn_s_sleep(1);
    }
    asm volatile("" ::: "memory");
}

__device__ __forceinline__ void f0_signal(char* ws, int b, int lane, unsigned tag) {
    const u64 want = ((u64)(~tag) << 32) | tag;
    if (lane < NREP) __hip_atomic_store((gu64*)(ws + WS_F0 + lane * 2048 + b * 8), want, RLX_AGENT);
}
__device__ __forceinline__ void f0_wait(const __amdgpu_buffer_rsrc_t rs, int b, int lane, unsigned tag) {
    const int fo = WS_F0 + (b & (NREP - 1)) * 2048 + lane * 16;
    for (unsigned spins = 0; spins < SPIN_CAP; ++spins) {
        asm volatile("" ::: "memory");
        u32x4 a0 = __builtin_amdgcn_raw_buffer_load_b128(rs, fo, 0, SC1);
        u32x4 a1 = __builtin_amdgcn_raw_buffer_load_b128(rs, fo + 1024, 0, SC1);
        unsigned bad = (a0[0] ^ tag) | (a0[1] ^ ~tag) | (a0[2] ^ tag) | (a0[3] ^ ~tag) |
                       (a1[0] ^ tag) | (a1[1] ^ ~tag) | (a1[2] ^ tag) | (a1[3] ^ ~tag);
        if (__all(bad == 0)) break;
        __builtin_amdgcn_s_sleep(8);
    }
    asm volatile("" ::: "memory");
}

#define LDS_BYTES (65536 + 65536 + 8192)

__global__ __launch_bounds__(640, 1) void mega(const u16* __restrict__ x, const u16* __restrict__ ckv,
                                               const u16* __restrict__ wq, const u16* __restrict__ wuk,
                                               const u16* __restrict__ wuv, const u16* __restrict__ wo,
                                               const u16* __restrict__ wg, const u16* __restrict__ wu,
                                               const u16* __restrict__ wd, const u16* __restrict__ gw,
                                               char* __restrict__ ws, u16* __restrict__ out) {
    __shared__ __attribute__((aligned(16))) char lds[LDS_BYTES];
    __shared__ unsigned flg[4];

    const int b = blockIdx.x;
    const int t = threadIdx.x;
    const int lane = t & 63, w = t >> 6;
    const int r = lane & 31, h2 = lane >> 5;
    const __amdgpu_buffer_rsrc_t rs = __builtin_amdgcn_make_buffer_rsrc(ws, 0, WS_END, 0x00020000);
    const u32x4 P4 = {POISON, POISON, POISON, POISON};
    const u32x2 P2 = {POISON, POISON};
    STAMP(0);

    if (b < 128) {
        if (w >= 9) return;
        lds_u32* vflg = (lds_u32*)flg;
        const unsigned e = __hip_atomic_load((gu32*)(ws + WS_EPOCH), RLX_AGENT);
        const unsigned tag = e + 1u;
        if (w == 8) {
            bar();
            f0_signal(ws, b, lane, tag);
            asm volatile("s_barrier" ::: "memory");
            STAMP(1);
            __builtin_amdgcn_s_sleep(A_SLEEP);
            f0_wait(rs, b, lane, tag);
            if (lane == 0) vflg[2] = 1u;
            STAMP(2);
            return;
        }
        char* tile = lds;
        float* red = (float*)(lds + 65536);
        float* fac = (float*)(lds + 131072);
        float* Mws = fac + 512;
        float* Lws = Mws + 256;
        float* fin = Lws + 256;
        float* fing = fin + 256;
        const int kc = b & 7, mem = b >> 3;
        __builtin_amdgcn_raw_buffer_store_b128(P4, rs, (w < 4 ? WS_P : WS_PF) + ((kc * 16 + mem) * 4 + (w & 3)) * 1024 + lane * 16, 0, SC1);
        if (w < 4)
            __builtin_amdgcn_raw_buffer_store_b128(P4, rs, WS_OPART + ((kc * 32 + w * 8 + (lane >> 3)) * 512 + mem * 32) * 4 + (lane & 7) * 16, 0, SC1);
        else if (w == 4)
            __builtin_amdgcn_raw_buffer_store_b64(P2, rs, (lane < 32 ? WS_MLOC : WS_MLOCF) + ((kc * 8 + (mem >> 1)) * 32 + r) * 16 + (mem & 1) * 8, 0, SC1);
        else if (w == 5 && mem == 0 && lane < 32)
            __builtin_amdgcn_raw_buffer_store_b64(P2, rs, WS_ML + (lane * 8 + kc) * 8, 0, SC1);
        if (t < 4) flg[t] = 0u;
        __builtin_amdgcn_sched_barrier(0);
        {
            const int h = b & 31, jq = b >> 5;
            float* sm1 = red;
            u32x4 wqv[4];
            u16 xr[4];
            const int row = jq * 128 + (t >> 4);
#pragma unroll
            for (int i = 0; i < 4; ++i) xr[i] = x[row + 32 * i];
#pragma unroll
            for (int i = 0; i < 4; ++i)
                wqv[i] = *(const u32x4*)(wq + (size_t)(row + 32 * i) * 4096 + h * 128 + (t & 15) * 8);
            __builtin_amdgcn_sched_barrier(0);
            vm0();
            float acc[8];
#pragma unroll
            for (int k = 0; k < 4; ++k) {
                acc[2 * k] = bf2f(xr[0]) * bflo(wqv[0][k]) + bf2f(xr[1]) * bflo(wqv[1][k]) + bf2f(xr[2]) * bflo(wqv[2][k]) + bf2f(xr[3]) * bflo(wqv[3][k]);
                acc[2 * k + 1] = bf2f(xr[0]) * bfhi(wqv[0][k]) + bf2f(xr[1]) * bfhi(wqv[1][k]) + bf2f(xr[2]) * bfhi(wqv[2][k]) + bf2f(xr[3]) * bfhi(wqv[3][k]);
            }
            bool up = lane & 16;
            float s[4];
#pragma unroll
            for (int k = 0; k < 4; ++k) { float send = up ? acc[k] : acc[k + 4]; float keep = up ? acc[k + 4] : acc[k]; s[k] = keep + __shfl_xor(send, 16); }
            up = lane & 32;
            f32x2 s2;
#pragma unroll
            for (int k = 0; k < 2; ++k) { float send = up ? s[k] : s[k + 2]; float keep = up ? s[k + 2] : s[k]; s2[k] = keep + __shfl_xor(send, 32); }
            *(f32x2*)(sm1 + w * 128 + (lane & 15) * 8 + ((lane >> 4) & 1) * 4 + (lane >> 5) * 2) = s2;
            bar();
            if (w == 0) {
                f32x2 sq = *(const f32x2*)(sm1 + 2 * lane);
#pragma unroll
                for (int ww = 1; ww < 8; ++ww) sq += *(const f32x2*)(sm1 + ww * 128 + 2 * lane);
                u32x4 g = {tag, asu(sq[0]), asu(sq[1]), ~tag};
                __builtin_amdgcn_raw_buffer_store_b128(g, rs, WS_QGF + ((h * 4 + jq) * 64 + lane) * 16, 0, 0);
                __builtin_amdgcn_raw_buffer_store_b128(g, rs, WS_QG + ((h * 4 + jq) * 64 + lane) * 16, 0, SC1);
            }
            bar();
        }
        STAMP(1);
        bf16x8 bq[8];
        u32x4 cv[8];
        {
            const u16* bp = ckv + (size_t)(kc * 1024 + mem * 64 + r) * 512 + 64 * w + 8 * h2;
#pragma unroll
            for (int kb = 0; kb < 2; ++kb)
#pragma unroll
                for (int s = 0; s < 4; ++s) bq[kb * 4 + s] = *(const bf16x8*)(bp + kb * 32 * 512 + 16 * s);
            const u16* cp = ckv + (size_t)(kc * 1024 + (t >> 2)) * 512 + mem * 32 + (t & 3) * 8;
#pragma unroll
            for (int i = 0; i < 8; ++i) cv[i] = *(const u32x4*)(cp + (size_t)i * 128 * 512);
        }
        __builtin_amdgcn_sched_barrier(0);
#pragma unroll
        for (int i = 0; i < 8; ++i) *(u32x4*)(tile + (i * 128 + (t >> 2)) * 64 + (t & 3) * 16) = cv[i];
        STAMP(2);
        lds_wait(vflg + 2, 1);
        STAMP(3);
        {
            u32x4 av[4];
            const int qo = WS_QABS + (b & 7) * 32768 + (w * 256 + lane) * 16;
            for (unsigned spins = 0; spins < SPIN_CAP; ++spins) {
                asm volatile("" ::: "memory");
#pragma unroll
                for (int s = 0; s < 4; ++s) av[s] = __builtin_amdgcn_raw_buffer_load_b128(rs, qo + 1024 * s, 0, SC1);
                unsigned m = umax(umax(max4(av[0]), max4(av[1])), umax(max4(av[2]), max4(av[3])));
                if (__all(m != POISON)) break;
                __builtin_amdgcn_s_sleep(1);
            }
            STAMP(4);
#pragma unroll
            for (int kb = 0; kb < 2; ++kb) {
                f32x16 acc;
#pragma unroll
                for (int i = 0; i < 16; ++i) acc[i] = 0.f;
#pragma unroll
                for (int s = 0; s < 4; ++s)
                    acc = __builtin_amdgcn_mfma_f32_32x32x16_bf16(__builtin_bit_cast(bf16x8, av[s]), bq[kb * 4 + s], acc, 0, 0, 0);
#pragma unroll
                for (int i = 0; i < 16; ++i) {
                    int head = (i & 3) + 8 * (i >> 2) + 4 * h2;
                    red[(w * 32 + head) * 64 + kb * 32 + r] = acc[i];
                }
            }
        }
        bar();
        {
            const int head = t >> 4, kq = (t & 15) * 4;
            f32x4 s4 = *(const f32x4*)(red + head * 64 + kq);
#pragma unroll
            for (int ww = 1; ww < 8; ++ww) s4 += *(const f32x4*)(red + (ww * 32 + head) * 64 + kq);
            float m = fmaxf(fmaxf(s4[0], s4[1]), fmaxf(s4[2], s4[3]));
            m = fmaxf(m, __shfl_xor(m, 1));
            m = fmaxf(m, __shfl_xor(m, 2));
            m = fmaxf(m, __shfl_xor(m, 4));
            m = fmaxf(m, __shfl_xor(m, 8));
            unsigned p01 = pk2(__expf(s4[0] - m), __expf(s4[1] - m));
            unsigned p23 = pk2(__expf(s4[2] - m), __expf(s4[3] - m));
            float l = (bflo(p01) + bfhi(p01)) + (bflo(p23) + bfhi(p23));
            l += __shfl_xor(l, 1);
            l += __shfl_xor(l, 2);
            l += __shfl_xor(l, 4);
            l += __shfl_xor(l, 8);
            u32x4 pp = {p01, p23, (unsigned)__shfl_down((int)p01, 1), (unsigned)__shfl_down((int)p23, 1)};
            u32x2 ml = {asu(m), asu(l)};
            const int po = ((kc * 16 + mem) * 256 + ((t & 15) >> 1) * 32 + head) * 16;
            const int mo = ((kc * 8 + (mem >> 1)) * 32 + head) * 16 + (mem & 1) * 8;
            if ((t & 1) == 0) __builtin_amdgcn_raw_buffer_store_b128(pp, rs, WS_PF + po, 0, 0);
            if ((t & 15) == 0) __builtin_amdgcn_raw_buffer_store_b64(ml, rs, WS_MLOCF + mo, 0, 0);
            if ((t & 1) == 0) __builtin_amdgcn_raw_buffer_store_b128(pp, rs, WS_P + po, 0, SC1);
            if ((t & 15) == 0) __builtin_amdgcn_raw_buffer_store_b64(ml, rs, WS_MLOC + mo, 0, SC1);
        }
        bar();
        STAMP(5);
        {
            u32x4 pv[8], mlv;
            for (unsigned spins = 0; spins < SPIN_CAP; ++spins) {
                asm volatile("" ::: "memory");
                const bool slow = (spins & 3) == 3;
                const int po = (slow ? WS_P : WS_PF) + ((kc * 16 + w * 2) * 256 + lane) * 16;
                const int mo = (slow ? WS_MLOC : WS_MLOCF) + ((kc * 8 + w) * 32 + r) * 16;
#pragma unroll
                for (int s = 0; s < 8; ++s) pv[s] = __builtin_amdgcn_raw_buffer_load_b128(rs, po + 1024 * s, 0, SC1);
                mlv = __builtin_amdgcn_raw_buffer_load_b128(rs, mo, 0, SC1);
                unsigned m = umax(umax(max4(pv[0]), max4(pv[1])), umax(max4(pv[2]), max4(pv[3])));
                m = umax(m, umax(umax(max4(pv[4]), max4(pv[5])), umax(max4(pv[6]), max4(pv[7]))));
                m = umax(m, max4(mlv));
                if (__all(m != POISON)) break;
                __builtin_amdgcn_s_sleep(1);
            }
            STAMP(6);
            {
                float m0 = asf(mlv[0]), l0 = asf(mlv[1]), m1 = asf(mlv[2]), l1 = asf(mlv[3]);
                float Mw = fmaxf(m0, m1);
                float f0_ = __expf(m0 - Mw), f1_ = __expf(m1 - Mw);
                if (lane < 32) {
                    fac[(2 * w) * 32 + r] = f0_; fac[(2 * w + 1) * 32 + r] = f1_;
                    Mws[r * 8 + w] = Mw; Lws[r * 8 + w] = f0_ * l0 + f1_ * l1;
                }
            }
            const int g = lane >> 4;
            const int q = (lane & 15) >> 2, p = lane & 3;
            const int cb = g & 1;
            f32x16 tot;
#pragma unroll
            for (int mm = 0; mm < 2; ++mm) {
                f32x16 acc;
#pragma unroll
                for (int i = 0; i < 16; ++i) acc[i] = 0.f;
#pragma unroll
                for (int ss = 0; ss < 4; ++ss) {
                    int s = mm * 4 + ss;
                    int row = w * 128 + 16 * s + 8 * h2 + q;
                    s16x4 t0 = __builtin_amdgcn_ds_read_tr16_b64_v4i16(
                        (__attribute__((address_space(3))) s16x4*)(tile + row * 64 + cb * 32 + p * 8));
                    s16x4 t1 = __builtin_amdgcn_ds_read_tr16_b64_v4i16(
                        (__attribute__((address_space(3))) s16x4*)(tile + (row + 4) * 64 + cb * 32 + p * 8));
                    s16x8 bb = {t0[0], t0[1], t0[2], t0[3], t1[0], t1[1], t1[2], t1[3]};
                    acc = __builtin_amdgcn_mfma_f32_32x32x16_bf16(__builtin_bit_cast(bf16x8, pv[s]), __builtin_bit_cast(bf16x8, bb), acc, 0, 0, 0);
                }
                const float* fp = fac + (2 * w + mm) * 32 + 4 * h2;
#pragma unroll
                for (int qd = 0; qd < 4; ++qd) {
                    f32x4 f4 = *(const f32x4*)(fp + 8 * qd);
#pragma unroll
                    for (int e2 = 0; e2 < 4; ++e2) {
                        if (mm == 0) tot[qd * 4 + e2] = f4[e2] * acc[qd * 4 + e2];
                        else tot[qd * 4 + e2] += f4[e2] * acc[qd * 4 + e2];
                    }
                }
            }
#pragma unroll
            for (int i = 0; i < 16; ++i) {
                int head = (i & 3) + 8 * (i >> 2) + 4 * h2;
                red[(w * 32 + head) * 32 + r] = tot[i];
            }
        }
        bar();
        {
            const int head = t >> 4, cq = (t & 15) * 2;
            f32x4 ma = *(const f32x4*)(Mws + head * 8), mb = *(const f32x4*)(Mws + head * 8 + 4);
            float M = fmaxf(fmaxf(fmaxf(ma[0], ma[1]), fmaxf(ma[2], ma[3])), fmaxf(fmaxf(mb[0], mb[1]), fmaxf(mb[2], mb[3])));
            float ew[8];
#pragma unroll
            for (int ww = 0; ww < 4; ++ww) { ew[ww] = __expf(ma[ww] - M); ew[ww + 4] = __expf(mb[ww] - M); }
            f32x2 s2 = {0.f, 0.f};
#pragma unroll
            for (int ww = 0; ww < 8; ++ww) s2 += ew[ww] * *(const f32x2*)(red + (ww * 32 + head) * 32 + cq);
            u32x4 o = {asu(s2[0]), asu(s2[1]), asu(__shfl_down(s2[0], 1)), asu(__shfl_down(s2[1], 1))};
            if ((t & 1) == 0)
                __builtin_amdgcn_raw_buffer_store_b128(o, rs, WS_OPART + ((kc * 32 + head) * 512 + mem * 32 + cq) * 4, 0, SC1);
            if (mem == 0 && (t & 15) == 0) {
                f32x4 la = *(const f32x4*)(Lws + head * 8), lb = *(const f32x4*)(Lws + head * 8 + 4);
                float L = 0.f;
#pragma unroll
                for (int ww = 0; ww < 4; ++ww) L += ew[ww] * la[ww] + ew[ww + 4] * lb[ww];
                u32x2 ml = {asu(M), asu(L)};
                __builtin_amdgcn_raw_buffer_store_b64(ml, rs, WS_ML + (head * 8 + kc) * 8, 0, SC1);
            }
        }
        STAMP(7);
        if (b >= 32) return;
        {
            const int p = t >> 2, qc = t & 3;
            float gp = rbf(bf2f(x[t]) * bf2f(gw[t]));
            const int ao = WS_GA + p * 2048 + b * 64 + qc * 16;
            const int bo = WS_GB + (p < 43 ? p : 42) * 2048 + b * 64 + qc * 16;
            u32x4 a0, b0;
            for (unsigned spins = 0; spins < SPIN_CAP; ++spins) {
                asm volatile("" ::: "memory");
                a0 = __builtin_amdgcn_raw_buffer_load_b128(rs, ao, 0, SC1);
                b0 = __builtin_amdgcn_raw_buffer_load_b128(rs, bo, 0, SC1);
                unsigned m = umax(max4(a0), max4(b0));
                if (__all(m != POISON)) break;
                __builtin_amdgcn_s_sleep(2);
            }
            STAMP(11);
            if (p >= 43) { b0[0] = 0u; b0[1] = 0u; b0[2] = 0u; b0[3] = 0u; }
            bool up = lane & 4;
            float s4[4];
#pragma unroll
            for (int q = 0; q < 4; ++q) { float send = up ? asf(a0[q]) : asf(b0[q]); float keep = up ? asf(b0[q]) : asf(a0[q]); s4[q] = keep + __shfl_xor(send, 4); }
            up = lane & 8;
            float s2[2];
#pragma unroll
            for (int q = 0; q < 2; ++q) { float send = up ? s4[q] : s4[q + 2]; float keep = up ? s4[q + 2] : s4[q]; s2[q] = keep + __shfl_xor(send, 8); }
            up = lane & 16;
            float send = up ? s2[0] : s2[1]; float keep = up ? s2[1] : s2[0];
            float vv = keep + __shfl_xor(send, 16);
            vv += __shfl_xor(vv, 32);
#pragma unroll
            for (int o = 32; o >= 1; o >>= 1) gp += __shfl_xor(gp, o);
            if (lane < 32) {
                int idx = ((lane >> 2) & 1) * 16 + (lane & 3) * 4 + ((lane >> 3) & 1) * 2 + ((lane >> 4) & 1);
                fin[w * 32 + idx] = vv;
            }
            if (lane == 0) fing[w] = gp;
        }
        bar();
        if (t < 16) {
            float a = 0.f, bb = 0.f, gs = 0.f;
#pragma unroll
            for (int ww = 0; ww < 8; ++ww) { a += fin[ww * 32 + t]; bb += fin[ww * 32 + 16 + t]; gs += fing[ww]; }
            gs = rbf(gs);
            float g = rbf(1.f / (1.f + __expf(-gs)));
            a = rbf(a); bb = rbf(bb);
            float rr = rbf(g * a) + rbf(rbf(1.f - g) * bb);
            out[b * 16 + t] = f2bf(rr);
        }
        STAMP(12);
        return;
    }

    float* redf = (float*)lds;
    float* redo = redf + 4096;
    float* cpart = redo + 4096;
    float* sm1 = cpart + 2048;
    float* sm2 = sm1 + 1024;
    float* sm3 = sm2 + 512;
    float* qs = sm3 + 256;
    lds_u32* vflg = (lds_u32*)flg;
    const int bb_ = b - 128;
    const int h = bb_ & 31, jj = bb_ >> 5;
    const bool has_ffn = bb_ < 43;

    if (w >= 8) {
        const unsigned e = __hip_atomic_load((gu32*)(ws + WS_EPOCH), RLX_AGENT);
        bar();
        const unsigned tag = e + 1u;
        if (w == 8) {
            __builtin_amdgcn_s_sleep(Q_SLEEP);
            for (unsigned spins = 0; spins < SPIN_CAP; ++spins) {
                asm volatile("" ::: "memory");
                u32x4 v[4];
                const int qb = ((spins & 3) == 3 ? WS_QG : WS_QGF) + (h * 4 * 64 + lane) * 16;
#pragma unroll
                for (int k = 0; k < 4; ++k) v[k] = __builtin_amdgcn_raw_buffer_load_b128(rs, qb + k * 1024, 0, SC1);
                unsigned bad = 0;
                float q0 = 0.f, q1 = 0.f;
#pragma unroll
                for (int k = 0; k < 4; ++k) {
                    bad |= (v[k][0] ^ tag) | (v[k][3] ^ ~tag);
                    q0 += asf(v[k][1]); q1 += asf(v[k][2]);
                }
                if (__all(bad == 0) || spins == SPIN_CAP - 1) {
                    f32x2 qq = {rbf(q0), rbf(q1)};
                    *(f32x2*)(qs + 2 * lane) = qq;
                    break;
                }
                __builtin_amdgcn_s_sleep(1);
            }
            lgkm0();
            if (lane == 0) vflg[1] = 1u;
            STAMP(2);
        } else {
            lds_wait(vflg + 3, 8);
            STAMP(1);
            f0_signal(ws, b, lane, tag);
            lds_wait(vflg + 1, 1);
            f0_wait(rs, b, lane, tag);
            if (lane == 0) vflg[2] = 1u;
            if (b == 255 && lane == 0) __hip_atomic_store((gu32*)(ws + WS_EPOCH), tag, RLX_AGENT);
            STAMP(2);
        }
        return;
    }

    {
        int po;
        if (w < 2) po = WS_QABS + (lane >> 3) * 32768 + ((jj * 16 + w * 8 + (lane & 7)) * 32 + h) * 16;
        else if (w < 4) po = WS_GA + bb_ * 2048 + (w & 1) * 1024 + lane * 16;
        else po = WS_GB + bb_ * 2048 + (w & 1) * 1024 + lane * 16;
        if (w < 4 || (w < 6 && has_ffn)) __builtin_amdgcn_raw_buffer_store_b128(P4, rs, po, 0, SC1);
    }
    __builtin_amdgcn_sched_barrier(0);
    u32x4 wkv[4];
#pragma unroll
    for (int i = 0; i < 4; ++i)
        wkv[i] = *(const u32x4*)(wuk + (size_t)(jj * 128 + 16 * w + (lane >> 4) + 4 * i) * 4096 + h * 128 + (lane & 15) * 8);
    __builtin_amdgcn_sched_barrier(0);
    if (t < 4) flg[t] = 0u;
    bar();
    asm volatile("s_waitcnt vmcnt(4)" ::: "memory");
    if (lane == 0) __hip_atomic_fetch_add(flg + 3, 1u, __ATOMIC_RELAXED, __HIP_MEMORY_SCOPE_WORKGROUP);
    __builtin_amdgcn_sched_barrier(0);
    vm0();
    STAMP(1);
    lds_wait(vflg + 1, 1);
    STAMP(2);
    {
        const int l16 = lane & 15;
        f32x4 qa0 = *(const f32x4*)(qs + l16 * 8), qa1 = *(const f32x4*)(qs + l16 * 8 + 4);
        float d[4];
#pragma unroll
        for (int i = 0; i < 4; ++i) {
            d[i] = qa0[0] * bflo(wkv[i][0]) + qa0[1] * bfhi(wkv[i][0]) + qa0[2] * bflo(wkv[i][1]) + qa0[3] * bfhi(wkv[i][1]);
            d[i] += qa1[0] * bflo(wkv[i][2]) + qa1[1] * bfhi(wkv[i][2]) + qa1[2] * bflo(wkv[i][3]) + qa1[3] * bfhi(wkv[i][3]);
        }
        bool up = lane & 1;
        float s[2];
#pragma unroll
        for (int k = 0; k < 2; ++k) { float send = up ? d[k] : d[k + 2]; float keep = up ? d[k + 2] : d[k]; s[k] = keep + __shfl_xor(send, 1); }
        up = lane & 2;
        float send = up ? s[0] : s[1], keep = up ? s[1] : s[0];
        float v = keep + __shfl_xor(send, 2);
        v += __shfl_xor(v, 4);
        v += __shfl_xor(v, 8);
        v *= 0.08838834764831845f;
        const int k = (lane >> 3) & 1;
        float vv[8];
#pragma unroll
        for (int e2 = 0; e2 < 8; ++e2) vv[e2] = __shfl(v, (e2 & 3) * 16 + (k | ((e2 >> 2) << 1)));
        u32x4 o = {pk2(vv[0], vv[1]), pk2(vv[2], vv[3]), pk2(vv[4], vv[5]), pk2(vv[6], vv[7])};
        if (lane < 16)
            __builtin_amdgcn_raw_buffer_store_b128(o, rs, WS_QABS + (lane & 7) * 32768 + ((jj * 16 + w * 2 + k) * 32 + h) * 16, 0, SC1);
    }
    __builtin_amdgcn_sched_barrier(0);
    if (lane == 0) __hip_atomic_fetch_add(flg + 0, 1u, __ATOMIC_RELAXED, __HIP_MEMORY_SCOPE_WORKGROUP);
    lds_wait(vflg + 0, 8);
    __builtin_amdgcn_sched_barrier(0);
    STAMP(3);
    u32x4 uvv[4], ovv[4];
#pragma unroll
    for (int i = 0; i < 4; ++i)
        uvv[i] = *(const u32x4*)(wuv + (size_t)((t >> 2) + 128 * i) * 4096 + h * 128 + jj * 32 + (t & 3) * 8);
#pragma unroll
    for (int i = 0; i < 4; ++i)
        ovv[i] = *(const u32x4*)(wo + ((size_t)h * 128 + jj * 32 + w * 4 + i) * 512 + lane * 8);
    __builtin_amdgcn_sched_barrier(0);
    if (has_ffn) {
        u32x4 gv[4], uv_[4], dv[4];
        u16 xf[4];
#pragma unroll
        for (int i = 0; i < 4; ++i) xf[i] = x[(t >> 2) + 128 * i];
#pragma unroll
        for (int i = 0; i < 4; ++i) {
            gv[i] = *(const u32x4*)(wg + (size_t)((t >> 2) + 128 * i) * FF + bb_ * 32 + (t & 3) * 8);
            uv_[i] = *(const u32x4*)(wu + (size_t)((t >> 2) + 128 * i) * FF + bb_ * 32 + (t & 3) * 8);
        }
#pragma unroll
        for (int i = 0; i < 4; ++i) dv[i] = *(const u32x4*)(wd + (size_t)(bb_ * 32 + w * 4 + i) * 512 + lane * 8);
        __builtin_amdgcn_sched_barrier(0);
        float p[16];
#pragma unroll
        for (int i = 0; i < 4; ++i) {
            const float xv = bf2f(xf[i]);
#pragma unroll
            for (int k = 0; k < 4; ++k) {
                if (i == 0) {
                    p[2 * k] = xv * bflo(gv[i][k]); p[2 * k + 1] = xv * bfhi(gv[i][k]);
                    p[8 + 2 * k] = xv * bflo(uv_[i][k]); p[8 + 2 * k + 1] = xv * bfhi(uv_[i][k]);
                } else {
                    p[2 * k] += xv * bflo(gv[i][k]); p[2 * k + 1] += xv * bfhi(gv[i][k]);
                    p[8 + 2 * k] += xv * bflo(uv_[i][k]); p[8 + 2 * k + 1] += xv * bfhi(uv_[i][k]);
                }
            }
        }
        bool up = lane & 4;
        float s8[8];
#pragma unroll
        for (int k = 0; k < 8; ++k) { float send = up ? p[k] : p[k + 8]; float keep = up ? p[k + 8] : p[k]; s8[k] = keep + __shfl_xor(send, 4); }
        up = lane & 8;
        float s4[4];
#pragma unroll
        for (int k = 0; k < 4; ++k) { float send = up ? s8[k] : s8[k + 4]; float keep = up ? s8[k + 4] : s8[k]; s4[k] = keep + __shfl_xor(send, 8); }
        up = lane & 16;
        float s2[2];
#pragma unroll
        for (int k = 0; k < 2; ++k) { float send = up ? s4[k] : s4[k + 2]; float keep = up ? s4[k + 2] : s4[k]; s2[k] = keep + __shfl_xor(send, 16); }
        up = lane & 32;
        float send = up ? s2[0] : s2[1]; float keep = up ? s2[1] : s2[0];
        float v = keep + __shfl_xor(send, 32);
        sm2[w * 64 + ((lane >> 2) & 1) * 32 + (lane & 3) * 8 + ((lane >> 3) & 1) * 4 + ((lane >> 4) & 1) * 2 + (lane >> 5)] = v;
        bar();
        f32x4 g4 = *(const f32x4*)(sm2 + 4 * w), u4 = *(const f32x4*)(sm2 + 32 + 4 * w);
#pragma unroll
        for (int ww = 1; ww < 8; ++ww) { g4 += *(const f32x4*)(sm2 + ww * 64 + 4 * w); u4 += *(const f32x4*)(sm2 + ww * 64 + 32 + 4 * w); }
        f32x4 o0 = {0.f, 0.f, 0.f, 0.f}, o1 = {0.f, 0.f, 0.f, 0.f};
#pragma unroll
        for (int i = 0; i < 4; ++i) {
            const float gs = rbf(g4[i]), us = rbf(u4[i]);
            const float sg = rbf(gs / (1.f + __expf(-gs)));
            const float hv = rbf(sg * us);
            o0[0] += hv * bflo(dv[i][0]); o0[1] += hv * bfhi(dv[i][0]); o0[2] += hv * bflo(dv[i][1]); o0[3] += hv * bfhi(dv[i][1]);
            o1[0] += hv * bflo(dv[i][2]); o1[1] += hv * bfhi(dv[i][2]); o1[2] += hv * bflo(dv[i][3]); o1[3] += hv * bfhi(dv[i][3]);
        }
        *(f32x4*)(redf + w * 512 + lane * 8) = o0;
        *(f32x4*)(redf + w * 512 + lane * 8 + 4) = o1;
        bar();
        if (t < 128) {
            f32x4 s = *(const f32x4*)(redf + t * 4);
#pragma unroll
            for (int ww = 1; ww < 8; ++ww) s += *(const f32x4*)(redf + ww * 512 + t * 4);
            __builtin_amdgcn_raw_buffer_store_b128(__builtin_bit_cast(u32x4, s), rs, WS_GB + bb_ * 2048 + t * 16, 0, SC1);
        }
    }
    asm volatile("s_waitcnt vmcnt(1)" ::: "memory");
    lds_wait(vflg + 2, 1);
    STAMP(8);
    {
        const int qd = t >> 7;
        u32x4 ov[2];
        u32x2 ml;
        const int oo = WS_OPART + ((qd * 2 * 32 + h) * 512 + (t & 127) * 4) * 4;
        for (unsigned spins = 0; spins < SPIN_CAP; ++spins) {
            asm volatile("" ::: "memory");
#pragma unroll
            for (int i = 0; i < 2; ++i) ov[i] = __builtin_amdgcn_raw_buffer_load_b128(rs, oo + i * 65536, 0, SC1);
            ml = __builtin_amdgcn_raw_buffer_load_b64(rs, WS_ML + (h * 8 + (lane & 7)) * 8, 0, SC1);
            unsigned m = umax(max4(ov[0]), max4(ov[1]));
            m = umax(m, umax(ml[0], ml[1]));
            if (__all(m != POISON)) break;
            __builtin_amdgcn_s_sleep(1);
        }
        STAMP(9);
        float mk = asf(ml[0]), lk = asf(ml[1]);
        float M = mk;
        M = fmaxf(M, __shfl_xor(M, 1)); M = fmaxf(M, __shfl_xor(M, 2)); M = fmaxf(M, __shfl_xor(M, 4));
        float wk = __expf(mk - M);
        float L = wk * lk;
        L += __shfl_xor(L, 1); L += __shfl_xor(L, 2); L += __shfl_xor(L, 4);
        wk = wk / L;
        f32x4 c4 = {0.f, 0.f, 0.f, 0.f};
#pragma unroll
        for (int i = 0; i < 2; ++i) {
            float wi = __shfl(wk, qd * 2 + i);
            c4[0] += wi * asf(ov[i][0]); c4[1] += wi * asf(ov[i][1]); c4[2] += wi * asf(ov[i][2]); c4[3] += wi * asf(ov[i][3]);
        }
        *(f32x4*)(cpart + qd * 512 + (t & 127) * 4) = c4;
    }
    bar();
    {
        const int row = t >> 2;
        float acc[8];
#pragma unroll
        for (int i = 0; i < 4; ++i) {
            const int rr = row + 128 * i;
            float c0 = (cpart[rr] + cpart[512 + rr]) + (cpart[1024 + rr] + cpart[1536 + rr]);
#pragma unroll
            for (int k = 0; k < 4; ++k) {
                if (i == 0) { acc[2 * k] = c0 * bflo(uvv[i][k]); acc[2 * k + 1] = c0 * bfhi(uvv[i][k]); }
                else { acc[2 * k] += c0 * bflo(uvv[i][k]); acc[2 * k + 1] += c0 * bfhi(uvv[i][k]); }
            }
        }
        bool up = lane & 4;
        float s[4];
#pragma unroll
        for (int k = 0; k < 4; ++k) { float send = up ? acc[k] : acc[k + 4]; float keep = up ? acc[k + 4] : acc[k]; s[k] = keep + __shfl_xor(send, 4); }
        up = lane & 8;
        float s2[2];
#pragma unroll
        for (int k = 0; k < 2; ++k) { float send = up ? s[k] : s[k + 2]; float keep = up ? s[k + 2] : s[k]; s2[k] = keep + __shfl_xor(send, 8); }
        up = lane & 16;
        float send = up ? s2[0] : s2[1]; float keep = up ? s2[1] : s2[0];
        float v = keep + __shfl_xor(send, 16);
        v += __shfl_xor(v, 32);
        if (lane < 32) {
            int d = (lane & 3) * 8 + ((lane >> 2) & 1) * 4 + ((lane >> 3) & 1) * 2 + ((lane >> 4) & 1);
            sm3[w * 32 + d] = v;
        }
    }
    bar();
    {
        f32x4 c4 = *(const f32x4*)(sm3 + 4 * w);
#pragma unroll
        for (int ww = 1; ww < 8; ++ww) c4 += *(const f32x4*)(sm3 + ww * 32 + 4 * w);
        f32x4 o0 = {0.f, 0.f, 0.f, 0.f}, o1 = {0.f, 0.f, 0.f, 0.f};
#pragma unroll
        for (int i = 0; i < 4; ++i) {
            const float c0 = rbf(c4[i]);
            o0[0] += c0 * bflo(ovv[i][0]); o0[1] += c0 * bfhi(ovv[i][0]); o0[2] += c0 * bflo(ovv[i][1]); o0[3] += c0 * bfhi(ovv[i][1]);
            o1[0] += c0 * bflo(ovv[i][2]); o1[1] += c0 * bfhi(ovv[i][2]); o1[2] += c0 * bflo(ovv[i][3]); o1[3] += c0 * bfhi(ovv[i][3]);
        }
        *(f32x4*)(redo + w * 512 + lane * 8) = o0;
        *(f32x4*)(redo + w * 512 + lane * 8 + 4) = o1;
    }
    bar();
    if (t < 128) {
        f32x4 s = *(const f32x4*)(redo + t * 4);
#pragma unroll
        for (int ww = 1; ww < 8; ++ww) s += *(const f32x4*)(redo + ww * 512 + t * 4);
        __builtin_amdgcn_raw_buffer_store_b128(__builtin_bit_cast(u32x4, s), rs, WS_GA + bb_ * 2048 + t * 16, 0, SC1);
    }
    STAMP(10);
}

extern "C" void kernel_launch(void* const* d_in, const int* in_sizes, int n_in,
                              void* d_out, int out_size, void* d_ws, size_t ws_size,
                              hipStream_t stream) {
    const u16* x = (const u16*)d_in[0];
    const u16* ckv = (const u16*)d_in[1];
    const u16* wq = (const u16*)d_in[2];
    const u16* wuk = (const u16*)d_in[3];
    const u16* wuv = (const u16*)d_in[4];
    const u16* wo = (const u16*)d_in[5];
    const u16* wg = (const u16*)d_in[7];
    const u16* wu = (const u16*)d_in[8];
    const u16* wd = (const u16*)d_in[9];
    const u16* gw = (const u16*)d_in[10];
    mega<<<256, 640, 0, stream>>>(x, ckv, wq, wuk, wuv, wo, wg, wu, wd, gw, (char*)d_ws, (u16*)d_out);
}
```

```cpp
#include <hip/hip_runtime.h>
#include <hip/hip_bf16.h>
#include <stdint.h>

#define SK 8192
#define FF 1376

typedef unsigned short u16;
typedef unsigned long long u64;
typedef __attribute__((ext_vector_type(8))) __bf16 bf16x8;
typedef __attribute__((ext_vector_type(2))) __bf16 bf16x2;
typedef __attribute__((ext_vector_type(4))) short s16x4;
typedef __attribute__((ext_vector_type(8))) short s16x8;
typedef __attribute__((ext_vector_type(16))) float f32x16;
typedef __attribute__((ext_vector_type(4))) float f32x4;
typedef __attribute__((ext_vector_type(2))) float f32x2;
typedef __attribute__((ext_vector_type(4))) unsigned int u32x4;
typedef __attribute__((ext_vector_type(2))) unsigned int u32x2;
#define GAS __attribute__((address_space(1)))
typedef GAS unsigned gu32;
typedef GAS u64 gu64;
#define RLX_AGENT __ATOMIC_RELAXED, __HIP_MEMORY_SCOPE_AGENT
#define SC1 16
#define POISON 0xFFFFFFFFu

__device__ __forceinline__ float bf2f(u16 v) { return __uint_as_float(((unsigned)v) << 16); }
__device__ __forceinline__ float bflo(unsigned v) { return __uint_as_float(v << 16); }
__device__ __forceinline__ float bfhi(unsigned v) { return __uint_as_float(v & 0xffff0000u); }
__device__ __forceinline__ float asf(unsigned v) { return __uint_as_float(v); }
__device__ __forceinline__ unsigned asu(float v) { return __float_as_uint(v); }
__device__ __forceinline__ unsigned pk2(float a, float b) {
    bf16x2 v = {(__bf16)a, (__bf16)b};
    return __builtin_bit_cast(unsigned, v);
}
__device__ __forceinline__ u16 f2bf(float f) { __bf16 v = (__bf16)f; return __builtin_bit_cast(u16, v); }
__device__ __forceinline__ float rbf(float f) { return bf2f(f2bf(f)); }
__device__ __forceinline__ unsigned umax(unsigned a, unsigned b) { return a > b ? a : b; }
__device__ __forceinline__ unsigned max4(u32x4 v) { return umax(umax(v[0], v[1]), umax(v[2], v[3])); }

#define NREP 32
#define WS_EPOCH   0
#define WS_F0      256
#define WS_QG      (WS_F0 + NREP * 2048)
#define WS_QABS    (WS_QG + 128 * 1024)
#define WS_P       (WS_QABS + 8 * 32768)
#define WS_MLOC    (WS_P + 8 * 16 * 4096)
#define WS_OPART   (WS_MLOC + 8 * 8 * 32 * 16)
#define WS_ML      (WS_OPART + 8 * 32 * 512 * 4)
#define WS_GA      (WS_ML + 32 * 8 * 8)
#define WS_GB      (WS_GA + 128 * 2048)
#define WS_QGF     (WS_GB + 128 * 2048)
#define WS_PF      (WS_QGF + 128 * 1024)
#define WS_MLOCF   (WS_PF + 8 * 16 * 4096)
#define WS_END     (WS_MLOCF + 8 * 8 * 32 * 16)

#define SPIN_CAP (1u << 17)
#define A_SLEEP 40
#define B_SLEEP 20
#define A_DELAY 24
#define STAMP(i)

__device__ __forceinline__ void lgkm0() { asm volatile("s_waitcnt lgkmcnt(0)" ::: "memory"); }
__device__ __forceinline__ void vm0() { asm volatile("s_waitcnt vmcnt(0)" ::: "memory"); }
__device__ __forceinline__ void bar() { asm volatile("s_waitcnt lgkmcnt(0)\n\ts_barrier" ::: "memory"); }
typedef volatile __attribute__((address_space(3))) unsigned lds_u32;
__device__ __forceinline__ void lds_wait(lds_u32* p, unsigned target) {
    for (unsigned s = 0; s < SPIN_CAP; ++s) {
        if (*p >= target) break;
        __builtin_amdgcn_s_sleep(1);
    }
    asm volatile("" ::: "memory");
}

__device__ __forceinline__ void f0_signal(char* ws, int b, int lane, unsigned tag) {
    const u64 want = ((u64)(~tag) << 32) | tag;
    if (lane < NREP) __hip_atomic_store((gu64*)(ws + WS_F0 + lane * 2048 + b * 8), want, RLX_AGENT);
}
__device__ __forceinline__ void f0_wait(const __amdgpu_buffer_rsrc_t rs, int b, int lane, unsigned tag) {
    const int fo = WS_F0 + (b & (NREP - 1)) * 2048 + lane * 16;
    for (unsigned spins = 0; spins < SPIN_CAP; ++spins) {
        asm volatile("" ::: "memory");
        u32x4 a0 = __builtin_amdgcn_raw_buffer_load_b128(rs, fo, 0, SC1);
        u32x4 a1 = __builtin_amdgcn_raw_buffer_load_b128(rs, fo + 1024, 0, SC1);
        unsigned bad = (a0[0] ^ tag) | (a0[1] ^ ~tag) | (a0[2] ^ tag) | (a0[3] ^ ~tag) |
                       (a1[0] ^ tag) | (a1[1] ^ ~tag) | (a1[2] ^ tag) | (a1[3] ^ ~tag);
        if (__all(bad == 0)) break;
        __builtin_amdgcn_s_sleep(8);
    }
    asm volatile("" ::: "memory");
}

#define LDS_BYTES (65536 + 65536 + 8192)

__global__ __launch_bounds__(640, 1) void mega(const u16* __restrict__ x, const u16* __restrict__ ckv,
                                               const u16* __restrict__ wq, const u16* __restrict__ wuk,
                                               const u16* __restrict__ wuv, const u16* __restrict__ wo,
                                               const u16* __restrict__ wg, const u16* __restrict__ wu,
                                               const u16* __restrict__ wd, const u16* __restrict__ gw,
                                               char* __restrict__ ws, u16* __restrict__ out) {
    __shared__ __attribute__((aligned(16))) char lds[LDS_BYTES];
    __shared__ unsigned flg[4];

    const int b = blockIdx.x;
    const int t = threadIdx.x;
    const int lane = t & 63, w = t >> 6;
    const int r = lane & 31, h2 = lane >> 5;
    const __amdgpu_buffer_rsrc_t rs = __builtin_amdgcn_make_buffer_rsrc(ws, 0, WS_END, 0x00020000);
    const u32x4 P4 = {POISON, POISON, POISON, POISON};
    const u32x2 P2 = {POISON, POISON};
    STAMP(0);

    if (b < 128) {
        if (w >= 9) return;
        lds_u32* vflg = (lds_u32*)flg;
        if (w == 8) {
            const unsigned e = __hip_atomic_load((gu32*)(ws + WS_EPOCH), RLX_AGENT);
            bar();
            f0_signal(ws, b, lane, e + 1u);
            asm volatile("s_barrier" ::: "memory");
            STAMP(1);
            __builtin_amdgcn_s_sleep(A_SLEEP);
            f0_wait(rs, b, lane, e + 1u);
            if (lane == 0) vflg[2] = 1u;
            STAMP(2);
            return;
        }
        char* tile = lds;
        float* red = (float*)(lds + 65536);
        float* fac = (float*)(lds + 131072);
        float* Mws = fac + 512;
        float* Lws = Mws + 256;
        float* fin = Lws + 256;
        float* fing = fin + 256;
        const int kc = b & 7, mem = b >> 3;
        __builtin_amdgcn_raw_buffer_store_b128(P4, rs, (w < 4 ? WS_P : WS_PF) + ((kc * 16 + mem) * 4 + (w & 3)) * 1024 + lane * 16, 0, SC1);
        if (w < 4)
            __builtin_amdgcn_raw_buffer_store_b128(P4, rs, WS_OPART + ((kc * 32 + w * 8 + (lane >> 3)) * 512 + mem * 32) * 4 + (lane & 7) * 16, 0, SC1);
        else if (w == 4)
            __builtin_amdgcn_raw_buffer_store_b64(P2, rs, (lane < 32 ? WS_MLOC : WS_MLOCF) + ((kc * 8 + (mem >> 1)) * 32 + r) * 16 + (mem & 1) * 8, 0, SC1);
        else if (w == 5 && mem == 0 && lane < 32)
            __builtin_amdgcn_raw_buffer_store_b64(P2, rs, WS_ML + (lane * 8 + kc) * 8, 0, SC1);
        if (t < 4) flg[t] = 0u;
        vm0();
        bar();
        asm volatile("s_barrier" ::: "memory");
        STAMP(1);
        __builtin_amdgcn_s_sleep(A_DELAY);
        bf16x8 bq[8];
        u32x4 cv[8];
        {
            const u16* bp = ckv + (size_t)(kc * 1024 + mem * 64 + r) * 512 + 64 * w + 8 * h2;
#pragma unroll
            for (int kb = 0; kb < 2; ++kb)
#pragma unroll
                for (int s = 0; s < 4; ++s) bq[kb * 4 + s] = *(const bf16x8*)(bp + kb * 32 * 512 + 16 * s);
            const u16* cp = ckv + (size_t)(kc * 1024 + (t >> 2)) * 512 + mem * 32 + (t & 3) * 8;
#pragma unroll
            for (int i = 0; i < 8; ++i) cv[i] = *(const u32x4*)(cp + (size_t)i * 128 * 512);
        }
        __builtin_amdgcn_sched_barrier(0);
#pragma unroll
        for (int i = 0; i < 8; ++i) *(u32x4*)(tile + (i * 128 + (t >> 2)) * 64 + (t & 3) * 16) = cv[i];
        STAMP(2);
        lds_wait(vflg + 2, 1);
        STAMP(3);
        {
            u32x4 av[4];
            const int qo = WS_QABS + (b & 7) * 32768 + (w * 256 + lane) * 16;
            for (unsigned spins = 0; spins < SPIN_CAP; ++spins) {
                asm volatile("" ::: "memory");
#pragma unroll
                for (int s = 0; s < 4; ++s) av[s] = __builtin_amdgcn_raw_buffer_load_b128(rs, qo + 1024 * s, 0, SC1);
                unsigned m = umax(umax(max4(av[0]), max4(av[1])), umax(max4(av[2]), max4(av[3])));
                if (__all(m != POISON)) break;
                __builtin_amdgcn_s_sleep(1);
            }
            STAMP(4);
#pragma unroll
            for (int kb = 0; kb < 2; ++kb) {
                f32x16 acc;
#pragma unroll
                for (int i = 0; i < 16; ++i) acc[i] = 0.f;
#pragma unroll
                for (int s = 0; s < 4; ++s)
                    acc = __builtin_amdgcn_mfma_f32_32x32x16_bf16(__builtin_bit_cast(bf16x8, av[s]), bq[kb * 4 + s], acc, 0, 0, 0);
#pragma unroll
                for (int i = 0; i < 16; ++i) {
                    int head = (i & 3) + 8 * (i >> 2) + 4 * h2;
                    red[(w * 32 + head) * 64 + kb * 32 + r] = acc[i];
                }
            }
        }
        bar();
        {
            const int head = t >> 4, kq = (t & 15) * 4;
            f32x4 s4 = *(const f32x4*)(red + head * 64 + kq);
#pragma unroll
            for (int ww = 1; ww < 8; ++ww) s4 += *(const f32x4*)(red + (ww * 32 + head) * 64 + kq);
            float m = fmaxf(fmaxf(s4[0], s4[1]), fmaxf(s4[2], s4[3]));
            m = fmaxf(m, __shfl_xor(m, 1));
            m = fmaxf(m, __shfl_xor(m, 2));
            m = fmaxf(m, __shfl_xor(m, 4));
            m = fmaxf(m, __shfl_xor(m, 8));
            unsigned p01 = pk2(__expf(s4[0] - m), __expf(s4[1] - m));
            unsigned p23 = pk2(__expf(s4[2] - m), __expf(s4[3] - m));
            float l = (bflo(p01) + bfhi(p01)) + (bflo(p23) + bfhi(p23));
            l += __shfl_xor(l, 1);
            l += __shfl_xor(l, 2);
            l += __shfl_xor(l, 4);
            l += __shfl_xor(l, 8);
            u32x4 pp = {p01, p23, (unsigned)__shfl_down((int)p01, 1), (unsigned)__shfl_down((int)p23, 1)};
            u32x2 ml = {asu(m), asu(l)};
            const int po = ((kc * 16 + mem) * 256 + ((t & 15) >> 1) * 32 + head) * 16;
            const int mo = ((kc * 8 + (mem >> 1)) * 32 + head) * 16 + (mem & 1) * 8;
            if ((t & 1) == 0) __builtin_amdgcn_raw_buffer_store_b128(pp, rs, WS_PF + po, 0, 0);
            if ((t & 15) == 0) __builtin_amdgcn_raw_buffer_store_b64(ml, rs, WS_MLOCF + mo, 0, 0);
            if ((t & 1) == 0) __builtin_amdgcn_raw_buffer_store_b128(pp, rs, WS_P + po, 0, SC1);
            if ((t & 15) == 0) __builtin_amdgcn_raw_buffer_store_b64(ml, rs, WS_MLOC + mo, 0, SC1);
        }
        bar();
        STAMP(5);
        {
            u32x4 pv[8], mlv;
            for (unsigned spins = 0; spins < SPIN_CAP; ++spins) {
                asm volatile("" ::: "memory");
                const bool slow = (spins & 3) == 3;
                const int po = (slow ? WS_P : WS_PF) + ((kc * 16 + w * 2) * 256 + lane) * 16;
                const int mo = (slow ? WS_MLOC : WS_MLOCF) + ((kc * 8 + w) * 32 + r) * 16;
#pragma unroll
                for (int s = 0; s < 8; ++s) pv[s] = __builtin_amdgcn_raw_buffer_load_b128(rs, po + 1024 * s, 0, SC1);
                mlv = __builtin_amdgcn_raw_buffer_load_b128(rs, mo, 0, SC1);
                unsigned m = umax(umax(max4(pv[0]), max4(pv[1])), umax(max4(pv[2]), max4(pv[3])));
                m = umax(m, umax(umax(max4(pv[4]), max4(pv[5])), umax(max4(pv[6]), max4(pv[7]))));
                m = umax(m, max4(mlv));
                if (__all(m != POISON)) break;
                __builtin_amdgcn_s_sleep(1);
            }
            STAMP(6);
            {
                float m0 = asf(mlv[0]), l0 = asf(mlv[1]), m1 = asf(mlv[2]), l1 = asf(mlv[3]);
                float Mw = fmaxf(m0, m1);
                float f0_ = __expf(m0 - Mw), f1_ = __expf(m1 - Mw);
                if (lane < 32) {
                    fac[(2 * w) * 32 + r] = f0_; fac[(2 * w + 1) * 32 + r] = f1_;
                    Mws[r * 8 + w] = Mw; Lws[r * 8 + w] = f0_ * l0 + f1_ * l1;
                }
            }
            const int g = lane >> 4;
            const int q = (lane & 15) >> 2, p = lane & 3;
            const int cb = g & 1;
            f32x16 tot;
#pragma unroll
            for (int mm = 0; mm < 2; ++mm) {
                f32x16 acc;
#pragma unroll
                for (int i = 0; i < 16; ++i) acc[i] = 0.f;
#pragma unroll
                for (int ss = 0; ss < 4; ++ss) {
                    int s = mm * 4 + ss;
                    int row = w * 128 + 16 * s + 8 * h2 + q;
                    s16x4 t0 = __builtin_amdgcn_ds_read_tr16_b64_v4i16(
                        (__attribute__((address_space(3))) s16x4*)(tile + row * 64 + cb * 32 + p * 8));
                    s16x4 t1 = __builtin_amdgcn_ds_read_tr16_b64_v4i16(
                        (__attribute__((address_space(3))) s16x4*)(tile + (row + 4) * 64 + cb * 32 + p * 8));
                    s16x8 bb = {t0[0], t0[1], t0[2], t0[3], t1[0], t1[1], t1[2], t1[3]};
                    acc = __builtin_amdgcn_mfma_f32_32x32x16_bf16(__builtin_bit_cast(bf16x8, pv[s]), __builtin_bit_cast(bf16x8, bb), acc, 0, 0, 0);
                }
                const float* fp = fac + (2 * w + mm) * 32 + 4 * h2;
#pragma unroll
                for (int qd = 0; qd < 4; ++qd) {
                    f32x4 f4 = *(const f32x4*)(fp + 8 * qd);
#pragma unroll
                    for (int e2 = 0; e2 < 4; ++e2) {
                        if (mm == 0) tot[qd * 4 + e2] = f4[e2] * acc[qd * 4 + e2];
                        else tot[qd * 4 + e2] += f4[e2] * acc[qd * 4 + e2];
                    }
                }
            }
#pragma unroll
            for (int i = 0; i < 16; ++i) {
                int head = (i & 3) + 8 * (i >> 2) + 4 * h2;
                red[(w * 32 + head) * 32 + r] = tot[i];
            }
        }
        bar();
        {
            const int head = t >> 4, cq = (t & 15) * 2;
            f32x4 ma = *(const f32x4*)(Mws + head * 8), mb = *(const f32x4*)(Mws + head * 8 + 4);
            float M = fmaxf(fmaxf(fmaxf(ma[0], ma[1]), fmaxf(ma[2], ma[3])), fmaxf(fmaxf(mb[0], mb[1]), fmaxf(mb[2], mb[3])));
            float ew[8];
#pragma unroll
            for (int ww = 0; ww < 4; ++ww) { ew[ww] = __expf(ma[ww] - M); ew[ww + 4] = __expf(mb[ww] - M); }
            f32x2 s2 = {0.f, 0.f};
#pragma unroll
            for (int ww = 0; ww < 8; ++ww) s2 += ew[ww] * *(const f32x2*)(red + (ww * 32 + head) * 32 + cq);
            u32x4 o = {asu(s2[0]), asu(s2[1]), asu(__shfl_down(s2[0], 1)), asu(__shfl_down(s2[1], 1))};
            if ((t & 1) == 0)
                __builtin_amdgcn_raw_buffer_store_b128(o, rs, WS_OPART + ((kc * 32 + head) * 512 + mem * 32 + cq) * 4, 0, SC1);
            if (mem == 0 && (t & 15) == 0) {
                f32x4 la = *(const f32x4*)(Lws + head * 8), lb = *(const f32x4*)(Lws + head * 8 + 4);
                float L = 0.f;
#pragma unroll
                for (int ww = 0; ww < 4; ++ww) L += ew[ww] * la[ww] + ew[ww + 4] * lb[ww];
                u32x2 ml = {asu(M), asu(L)};
                __builtin_amdgcn_raw_buffer_store_b64(ml, rs, WS_ML + (head * 8 + kc) * 8, 0, SC1);
            }
        }
        STAMP(7);
        if (b >= 32) return;
        {
            const int p = t >> 2, qc = t & 3;
            float gp = rbf(bf2f(x[t]) * bf2f(gw[t]));
            const int ao = WS_GA + p * 2048 + b * 64 + qc * 16;
            const int bo = WS_GB + (p < 43 ? p : 42) * 2048 + b * 64 + qc * 16;
            u32x4 a0, b0;
            for (unsigned spins = 0; spins < SPIN_CAP; ++spins) {
                asm volatile("" ::: "memory");
                a0 = __builtin_amdgcn_raw_buffer_load_b128(rs, ao, 0, SC1);
                b0 = __builtin_amdgcn_raw_buffer_load_b128(rs, bo, 0, SC1);
                unsigned m = umax(max4(a0), max4(b0));
                if (__all(m != POISON)) break;
                __builtin_amdgcn_s_sleep(2);
            }
            STAMP(11);
            if (p >= 43) { b0[0] = 0u; b0[1] = 0u; b0[2] = 0u; b0[3] = 0u; }
            bool up = lane & 4;
            float s4[4];
#pragma unroll
            for (int q = 0; q < 4; ++q) { float send = up ? asf(a0[q]) : asf(b0[q]); float keep = up ? asf(b0[q]) : asf(a0[q]); s4[q] = keep + __shfl_xor(send, 4); }
            up = lane & 8;
            float s2[2];
#pragma unroll
            for (int q = 0; q < 2; ++q) { float send = up ? s4[q] : s4[q + 2]; float keep = up ? s4[q + 2] : s4[q]; s2[q] = keep + __shfl_xor(send, 8); }
            up = lane & 16;
            float send = up ? s2[0] : s2[1]; float keep = up ? s2[1] : s2[0];
            float vv = keep + __shfl_xor(send, 16);
            vv += __shfl_xor(vv, 32);
#pragma unroll
            for (int o = 32; o >= 1; o >>= 1) gp += __shfl_xor(gp, o);
            if (lane < 32) {
                int idx = ((lane >> 2) & 1) * 16 + (lane & 3) * 4 + ((lane >> 3) & 1) * 2 + ((lane >> 4) & 1);
                fin[w * 32 + idx] = vv;
            }
            if (lane == 0) fing[w] = gp;
        }
        bar();
        if (t < 16) {
            float a = 0.f, bb = 0.f, gs = 0.f;
#pragma unroll
            for (int ww = 0; ww < 8; ++ww) { a += fin[ww * 32 + t]; bb += fin[ww * 32 + 16 + t]; gs += fing[ww]; }
            gs = rbf(gs);
            float g = rbf(1.f / (1.f + __expf(-gs)));
            a = rbf(a); bb = rbf(bb);
            float rr = rbf(g * a) + rbf(rbf(1.f - g) * bb);
            out[b * 16 + t] = f2bf(rr);
        }
        STAMP(12);
        return;
    }

    float* redf = (float*)lds;
    float* redo = redf + 4096;
    float* cpart = redo + 4096;
    float* sm1 = cpart + 2048;
    float* sm2 = sm1 + 1024;
    float* sm3 = sm2 + 512;
    float* qs = sm3 + 256;
    lds_u32* vflg = (lds_u32*)flg;
    const int bb_ = b - 128;
    const int h = bb_ & 31, jj = bb_ >> 5;
    const bool has_ffn = bb_ < 43;

    if (w >= 8) {
        const unsigned e = __hip_atomic_load((gu32*)(ws + WS_EPOCH), RLX_AGENT);
        bar();
        const unsigned tag = e + 1u;
        if (w == 8) {
            lds_wait(vflg + 0, 8);
            STAMP(1);
            f32x2 s = *(const f32x2*)(sm1 + 2 * lane);
#pragma unroll
            for (int ww = 1; ww < 8; ++ww) s += *(const f32x2*)(sm1 + ww * 128 + 2 * lane);
            u32x4 g = {tag, asu(s[0]), asu(s[1]), ~tag};
            __builtin_amdgcn_raw_buffer_store_b128(g, rs, WS_QGF + ((h * 4 + jj) * 64 + lane) * 16, 0, 0);
            __builtin_amdgcn_raw_buffer_store_b128(g, rs, WS_QG + ((h * 4 + jj) * 64 + lane) * 16, 0, SC1);
            for (unsigned spins = 0; spins < SPIN_CAP; ++spins) {
                asm volatile("" ::: "memory");
                u32x4 v[4];
                const int qb = ((spins & 3) == 3 ? WS_QG : WS_QGF) + (h * 4 * 64 + lane) * 16;
#pragma unroll
                for (int k = 0; k < 4; ++k) v[k] = __builtin_amdgcn_raw_buffer_load_b128(rs, qb + k * 1024, 0, SC1);
                unsigned bad = 0;
                float q0 = 0.f, q1 = 0.f;
#pragma unroll
                for (int k = 0; k < 4; ++k) {
                    bad |= (v[k][0] ^ tag) | (v[k][3] ^ ~tag);
                    q0 += asf(v[k][1]); q1 += asf(v[k][2]);
                }
                if (__all(bad == 0) || spins == SPIN_CAP - 1) {
                    f32x2 qq = {rbf(q0), rbf(q1)};
                    *(f32x2*)(qs + 2 * lane) = qq;
                    break;
                }
                __builtin_amdgcn_s_sleep(1);
            }
            lgkm0();
            if (lane == 0) vflg[1] = 1u;
            STAMP(2);
        } else {
            lds_wait(vflg + 3, 8);
            STAMP(1);
            f0_signal(ws, b, lane, tag);
            __builtin_amdgcn_s_sleep(B_SLEEP);
            f0_wait(rs, b, lane, tag);
            if (lane == 0) vflg[2] = 1u;
            if (b == 255 && lane == 0) __hip_atomic_store((gu32*)(ws + WS_EPOCH), tag, RLX_AGENT);
            STAMP(2);
        }
        return;
    }

    {
        int po;
        if (w < 2) po = WS_QABS + (lane >> 3) * 32768 + ((jj * 16 + w * 8 + (lane & 7)) * 32 + h) * 16;
        else if (w < 4) po = WS_GA + bb_ * 2048 + (w & 1) * 1024 + lane * 16;
        else po = WS_GB + bb_ * 2048 + (w & 1) * 1024 + lane * 16;
        if (w < 4 || (w < 6 && has_ffn)) __builtin_amdgcn_raw_buffer_store_b128(P4, rs, po, 0, SC1);
    }
    __builtin_amdgcn_sched_barrier(0);
    u32x4 wqv[4], wkv[4];
    u16 xr[4];
    {
        const int row = jj * 128 + (t >> 4);
#pragma unroll
        for (int i = 0; i < 4; ++i) xr[i] = x[row + 32 * i];
#pragma unroll
        for (int i = 0; i < 4; ++i)
            wqv[i] = *(const u32x4*)(wq + (size_t)(row + 32 * i) * 4096 + h * 128 + (t & 15) * 8);
#pragma unroll
        for (int i = 0; i < 4; ++i)
            wkv[i] = *(const u32x4*)(wuk + (size_t)(jj * 128 + 16 * w + (lane >> 4) + 4 * i) * 4096 + h * 128 + (lane & 15) * 8);
    }
    __builtin_amdgcn_sched_barrier(0);
    if (t < 4) flg[t] = 0u;
    bar();
    asm volatile("s_waitcnt vmcnt(12)" ::: "memory");
    if (lane == 0) __hip_atomic_fetch_add(flg + 3, 1u, __ATOMIC_RELAXED, __HIP_MEMORY_SCOPE_WORKGROUP);
    __builtin_amdgcn_sched_barrier(0);
    asm volatile("s_waitcnt vmcnt(4)" ::: "memory");
    STAMP(1);
    {
        float acc[8];
#pragma unroll
        for (int k = 0; k < 4; ++k) {
            acc[2 * k] = bf2f(xr[0]) * bflo(wqv[0][k]) + bf2f(xr[1]) * bflo(wqv[1][k]) + bf2f(xr[2]) * bflo(wqv[2][k]) + bf2f(xr[3]) * bflo(wqv[3][k]);
            acc[2 * k + 1] = bf2f(xr[0]) * bfhi(wqv[0][k]) + bf2f(xr[1]) * bfhi(wqv[1][k]) + bf2f(xr[2]) * bfhi(wqv[2][k]) + bf2f(xr[3]) * bfhi(wqv[3][k]);
        }
        bool up = lane & 16;
        float s[4];
#pragma unroll
        for (int k = 0; k < 4; ++k) { float send = up ? acc[k] : acc[k + 4]; float keep = up ? acc[k + 4] : acc[k]; s[k] = keep + __shfl_xor(send, 16); }
        up = lane & 32;
        f32x2 s2;
#pragma unroll
        for (int k = 0; k < 2; ++k) { float send = up ? s[k] : s[k + 2]; float keep = up ? s[k + 2] : s[k]; s2[k] = keep + __shfl_xor(send, 32); }
        *(f32x2*)(sm1 + w * 128 + (lane & 15) * 8 + ((lane >> 4) & 1) * 4 + (lane >> 5) * 2) = s2;
        lgkm0();
        if (lane == 0) __hip_atomic_fetch_add(flg + 0, 1u, __ATOMIC_RELAXED, __HIP_MEMORY_SCOPE_WORKGROUP);
    }
    __builtin_amdgcn_sched_barrier(0);
    lds_wait(vflg + 1, 1);
    STAMP(2);
    {
        const int l16 = lane & 15;
        f32x4 qa0 = *(const f32x4*)(qs + l16 * 8), qa1 = *(const f32x4*)(qs + l16 * 8 + 4);
        float d[4];
#pragma unroll
        for (int i = 0; i < 4; ++i) {
            d[i] = qa0[0] * bflo(wkv[i][0]) + qa0[1] * bfhi(wkv[i][0]) + qa0[2] * bflo(wkv[i][1]) + qa0[3] * bfhi(wkv[i][1]);
            d[i] += qa1[0] * bflo(wkv[i][2]) + qa1[1] * bfhi(wkv[i][2]) + qa1[2] * bflo(wkv[i][3]) + qa1[3] * bfhi(wkv[i][3]);
        }
        bool up = lane & 1;
        float s[2];
#pragma unroll
        for (int k = 0; k < 2; ++k) { float send = up ? d[k] : d[k + 2]; float keep = up ? d[k + 2] : d[k]; s[k] = keep + __shfl_xor(send, 1); }
        up = lane & 2;
        float send = up ? s[0] : s[1], keep = up ? s[1] : s[0];
        float v = keep + __shfl_xor(send, 2);
        v += __shfl_xor(v, 4);
        v += __shfl_xor(v, 8);
        v *= 0.08838834764831845f;
        const int k = (lane >> 3) & 1;
        float vv[8];
#pragma unroll
        for (int e2 = 0; e2 < 8; ++e2) vv[e2] = __shfl(v, (e2 & 3) * 16 + (k | ((e2 >> 2) << 1)));
        u32x4 o = {pk2(vv[0], vv[1]), pk2(vv[2], vv[3]), pk2(vv[4], vv[5]), pk2(vv[6], vv[7])};
        if (lane < 16)
            __builtin_amdgcn_raw_buffer_store_b128(o, rs, WS_QABS + (lane & 7) * 32768 + ((jj * 16 + w * 2 + k) * 32 + h) * 16, 0, SC1);
    }
    __builtin_amdgcn_sched_barrier(0);
    if (lane == 0) __hip_atomic_fetch_add(flg + 0, 1u, __ATOMIC_RELAXED, __HIP_MEMORY_SCOPE_WORKGROUP);
    lds_wait(vflg + 0, 16);
    __builtin_amdgcn_sched_barrier(0);
    STAMP(3);
    u32x4 uvv[4], ovv[4];
#pragma unroll
    for (int i = 0; i < 4; ++i)
        uvv[i] = *(const u32x4*)(wuv + (size_t)((t >> 2) + 128 * i) * 4096 + h * 128 + jj * 32 + (t & 3) * 8);
#pragma unroll
    for (int i = 0; i < 4; ++i)
        ovv[i] = *(const u32x4*)(wo + ((size_t)h * 128 + jj * 32 + w * 4 + i) * 512 + lane * 8);
    __builtin_amdgcn_sched_barrier(0);
    if (has_ffn) {
        u32x4 gv[4], uv_[4], dv[4];
        u16 xf[4];
#pragma unroll
        for (int i = 0; i < 4; ++i) xf[i] = x[(t >> 2) + 128 * i];
#pragma unroll
        for (int i = 0; i < 4; ++i) {
            gv[i] = *(const u32x4*)(wg + (size_t)((t >> 2) + 128 * i) * FF + bb_ * 32 + (t & 3) * 8);
            uv_[i] = *(const u32x4*)(wu + (size_t)((t >> 2) + 128 * i) * FF + bb_ * 32 + (t & 3) * 8);
        }
#pragma unroll
        for (int i = 0; i < 4; ++i) dv[i] = *(const u32x4*)(wd + (size_t)(bb_ * 32 + w * 4 + i) * 512 + lane * 8);
        __builtin_amdgcn_sched_barrier(0);
        float p[16];
#pragma unroll
        for (int i = 0; i < 4; ++i) {
            const float xv = bf2f(xf[i]);
#pragma unroll
            for (int k = 0; k < 4; ++k) {
                if (i == 0) {
                    p[2 * k] = xv * bflo(gv[i][k]); p[2 * k + 1] = xv * bfhi(gv[i][k]);
                    p[8 + 2 * k] = xv * bflo(uv_[i][k]); p[8 + 2 * k + 1] = xv * bfhi(uv_[i][k]);
                } else {
                    p[2 * k] += xv * bflo(gv[i][k]); p[2 * k + 1] += xv * bfhi(gv[i][k]);
                    p[8 + 2 * k] += xv * bflo(uv_[i][k]); p[8 + 2 * k + 1] += xv * bfhi(uv_[i][k]);
                }
            }
        }
        bool up = lane & 4;
        float s8[8];
#pragma unroll
        for (int k = 0; k < 8; ++k) { float send = up ? p[k] : p[k + 8]; float keep = up ? p[k + 8] : p[k]; s8[k] = keep + __shfl_xor(send, 4); }
        up = lane & 8;
        float s4[4];
#pragma unroll
        for (int k = 0; k < 4; ++k) { float send = up ? s8[k] : s8[k + 4]; float keep = up ? s8[k + 4] : s8[k]; s4[k] = keep + __shfl_xor(send, 8); }
        up = lane & 16;
        float s2[2];
#pragma unroll
        for (int k = 0; k < 2; ++k) { float send = up ? s4[k] : s4[k + 2]; float keep = up ? s4[k + 2] : s4[k]; s2[k] = keep + __shfl_xor(send, 16); }
        up = lane & 32;
        float send = up ? s2[0] : s2[1]; float keep = up ? s2[1] : s2[0];
        float v = keep + __shfl_xor(send, 32);
        sm2[w * 64 + ((lane >> 2) & 1) * 32 + (lane & 3) * 8 + ((lane >> 3) & 1) * 4 + ((lane >> 4) & 1) * 2 + (lane >> 5)] = v;
        bar();
        f32x4 g4 = *(const f32x4*)(sm2 + 4 * w), u4 = *(const f32x4*)(sm2 + 32 + 4 * w);
#pragma unroll
        for (int ww = 1; ww < 8; ++ww) { g4 += *(const f32x4*)(sm2 + ww * 64 + 4 * w); u4 += *(const f32x4*)(sm2 + ww * 64 + 32 + 4 * w); }
        f32x4 o0 = {0.f, 0.f, 0.f, 0.f}, o1 = {0.f, 0.f, 0.f, 0.f};
#pragma unroll
        for (int i = 0; i < 4; ++i) {
            const float gs = rbf(g4[i]), us = rbf(u4[i]);
            const float sg = rbf(gs / (1.f + __expf(-gs)));
            const float hv = rbf(sg * us);
            o0[0] += hv * bflo(dv[i][0]); o0[1] += hv * bfhi(dv[i][0]); o0[2] += hv * bflo(dv[i][1]); o0[3] += hv * bfhi(dv[i][1]);
            o1[0] += hv * bflo(dv[i][2]); o1[1] += hv * bfhi(dv[i][2]); o1[2] += hv * bflo(dv[i][3]); o1[3] += hv * bfhi(dv[i][3]);
        }
        *(f32x4*)(redf + w * 512 + lane * 8) = o0;
        *(f32x4*)(redf + w * 512 + lane * 8 + 4) = o1;
        bar();
        if (t < 128) {
            f32x4 s = *(const f32x4*)(redf + t * 4);
#pragma unroll
            for (int ww = 1; ww < 8; ++ww) s += *(const f32x4*)(redf + ww * 512 + t * 4);
            __builtin_amdgcn_raw_buffer_store_b128(__builtin_bit_cast(u32x4, s), rs, WS_GB + bb_ * 2048 + t * 16, 0, SC1);
        }
    }
    asm volatile("s_waitcnt vmcnt(1)" ::: "memory");
    lds_wait(vflg + 2, 1);
    STAMP(8);
    {
        const int qd = t >> 7;
        u32x4 ov[2];
        u32x2 ml;
        const int oo = WS_OPART + ((qd * 2 * 32 + h) * 512 + (t & 127) * 4) * 4;
        for (unsigned spins = 0; spins < SPIN_CAP; ++spins) {
            asm volatile("" ::: "memory");
#pragma unroll
            for (int i = 0; i < 2; ++i) ov[i] = __builtin_amdgcn_raw_buffer_load_b128(rs, oo + i * 65536, 0, SC1);
            ml = __builtin_amdgcn_raw_buffer_load_b64(rs, WS_ML + (h * 8 + (lane & 7)) * 8, 0, SC1);
            unsigned m = umax(max4(ov[0]), max4(ov[1]));
            m = umax(m, umax(ml[0], ml[1]));
            if (__all(m != POISON)) break;
            __builtin_amdgcn_s_sleep(1);
        }
        STAMP(9);
        float mk = asf(ml[0]), lk = asf(ml[1]);
        float M = mk;
        M = fmaxf(M, __shfl_xor(M, 1)); M = fmaxf(M, __shfl_xor(M, 2)); M = fmaxf(M, __shfl_xor(M, 4));
        float wk = __expf(mk - M);
        float L = wk * lk;
        L += __shfl_xor(L, 1); L += __shfl_xor(L, 2); L += __shfl_xor(L, 4);
        wk = wk / L;
        f32x4 c4 = {0.f, 0.f, 0.f, 0.f};
#pragma unroll
        for (int i = 0; i < 2; ++i) {
            float wi = __shfl(wk, qd * 2 + i);
            c4[0] += wi * asf(ov[i][0]); c4[1] += wi * asf(ov[i][1]); c4[2] += wi * asf(ov[i][2]); c4[3] += wi * asf(ov[i][3]);
        }
        *(f32x4*)(cpart + qd * 512 + (t & 127) * 4) = c4;
    }
    bar();
    {
        const int row = t >> 2;
        float acc[8];
#pragma unroll
        for (int i = 0; i < 4; ++i) {
            const int rr = row + 128 * i;
            float c0 = (cpart[rr] + cpart[512 + rr]) + (cpart[1024 + rr] + cpart[1536 + rr]);
#pragma unroll
            for (int k = 0; k < 4; ++k) {
                if (i == 0) { acc[2 * k] = c0 * bflo(uvv[i][k]); acc[2 * k + 1] = c0 * bfhi(uvv[i][k]); }
                else { acc[2 * k] += c0 * bflo(uvv[i][k]); acc[2 * k + 1] += c0 * bfhi(uvv[i][k]); }
            }
        }
        bool up = lane & 4;
        float s[4];
#pragma unroll
        for (int k = 0; k < 4; ++k) { float send = up ? acc[k] : acc[k + 4]; float keep = up ? acc[k + 4] : acc[k]; s[k] = keep + __shfl_xor(send, 4); }
        up = lane & 8;
        float s2[2];
#pragma unroll
        for (int k = 0; k < 2; ++k) { float send = up ? s[k] : s[k + 2]; float keep = up ? s[k + 2] : s[k]; s2[k] = keep + __shfl_xor(send, 8); }
        up = lane & 16;
        float send = up ? s2[0] : s2[1]; float keep = up ? s2[1] : s2[0];
        float v = keep + __shfl_xor(send, 16);
        v += __shfl_xor(v, 32);
        if (lane < 32) {
            int d = (lane & 3) * 8 + ((lane >> 2) & 1) * 4 + ((lane >> 3) & 1) * 2 + ((lane >> 4) & 1);
            sm3[w * 32 + d] = v;
        }
    }
    bar();
    {
        f32x4 c4 = *(const f32x4*)(sm3 + 4 * w);
#pragma unroll
        for (int ww = 1; ww < 8; ++ww) c4 += *(const f32x4*)(sm3 + ww * 32 + 4 * w);
        f32x4 o0 = {0.f, 0.f, 0.f, 0.f}, o1 = {0.f, 0.f, 0.f, 0.f};
#pragma unroll
        for (int i = 0; i < 4; ++i) {
            const float c0 = rbf(c4[i]);
            o0[0] += c0 * bflo(ovv[i][0]); o0[1] += c0 * bfhi(ovv[i][0]); o0[2] += c0 * bflo(ovv[i][1]); o0[3] += c0 * bfhi(ovv[i][1]);
            o1[0] += c0 * bflo(ovv[i][2]); o1[1] += c0 * bfhi(ovv[i][2]); o1[2] += c0 * bflo(ovv[i][3]); o1[3] += c0 * bfhi(ovv[i][3]);
        }
        *(f32x4*)(redo + w * 512 + lane * 8) = o0;
        *(f32x4*)(redo + w * 512 + lane * 8 + 4) = o1;
    }
    bar();
    if (t < 128) {
        f32x4 s = *(const f32x4*)(redo + t * 4);
#pragma unroll
        for (int ww = 1; ww < 8; ++ww) s += *(const f32x4*)(redo + ww * 512 + t * 4);
        __builtin_amdgcn_raw_buffer_store_b128(__builtin_bit_cast(u32x4, s), rs, WS_GA + bb_ * 2048 + t * 16, 0, SC1);
    }
    STAMP(10);
}

extern "C" void kernel_launch(void* const* d_in, const int* in_sizes, int n_in,
                              void* d_out, int out_size, void* d_ws, size_t ws_size,
                              hipStream_t stream) {
    const u16* x = (const u16*)d_in[0];
    const u16* ckv = (const u16*)d_in[1];
    const u16* wq = (const u16*)d_in[2];
    const u16* wuk = (const u16*)d_in[3];
    const u16* wuv = (const u16*)d_in[4];
    const u16* wo = (const u16*)d_in[5];
    const u16* wg = (const u16*)d_in[7];
    const u16* wu = (const u16*)d_in[8];
    const u16* wd = (const u16*)d_in[9];
    const u16* gw = (const u16*)d_in[10];
    mega<<<256, 640, 0, stream>>>(x, ckv, wq, wuk, wuv, wo, wg, wu, wd, gw, (char*)d_ws, (u16*)d_out);
}
```

```cpp
#include <hip/hip_runtime.h>
#include <hip/hip_bf16.h>
#include <stdint.h>

#define SK 8192
#define FF 1376

typedef unsigned short u16;
typedef unsigned long long u64;
typedef __attribute__((ext_vector_type(8))) __bf16 bf16x8;
typedef __attribute__((ext_vector_type(2))) __bf16 bf16x2;
typedef __attribute__((ext_vector_type(4))) short s16x4;
typedef __attribute__((ext_vector_type(8))) short s16x8;
typedef __attribute__((ext_vector_type(16))) float f32x16;
typedef __attribute__((ext_vector_type(4))) float f32x4;
typedef __attribute__((ext_vector_type(2))) float f32x2;
typedef __attribute__((ext_vector_type(4))) unsigned int u32x4;
typedef __attribute__((ext_vector_type(2))) unsigned int u32x2;
#define GAS __attribute__((address_space(1)))
typedef GAS unsigned gu32;
typedef GAS u64 gu64;
#define RLX_AGENT __ATOMIC_RELAXED, __HIP_MEMORY_SCOPE_AGENT
#define SC1 16
#define POISON 0xFFFFFFFFu

__device__ __forceinline__ float bf2f(u16 v) { return __uint_as_float(((unsigned)v) << 16); }
__device__ __forceinline__ float bflo(unsigned v) { return __uint_as_float(v << 16); }
__device__ __forceinline__ float bfhi(unsigned v) { return __uint_as_float(v & 0xffff0000u); }
__device__ __forceinline__ float asf(unsigned v) { return __uint_as_float(v); }
__device__ __forceinline__ unsigned asu(float v) { return __float_as_uint(v); }
__device__ __forceinline__ unsigned pk2(float a, float b) {
    bf16x2 v = {(__bf16)a, (__bf16)b};
    return __builtin_bit_cast(unsigned, v);
}
__device__ __forceinline__ u16 f2bf(float f) { __bf16 v = (__bf16)f; return __builtin_bit_cast(u16, v); }
__device__ __forceinline__ float rbf(float f) { return bf2f(f2bf(f)); }
__device__ __forceinline__ unsigned umax(unsigned a, unsigned b) { return a > b ? a : b; }
__device__ __forceinline__ unsigned max4(u32x4 v) { return umax(umax(v[0], v[1]), umax(v[2], v[3])); }

#define NREP 32
#define WS_EPOCH   0
#define WS_F0      256
#define WS_QG      (WS_F0 + NREP * 2048)
#define WS_QABS    (WS_QG + 128 * 1024)
#define WS_P       (WS_QABS + 8 * 32768)
#define WS_MLOC    (WS_P + 8 * 16 * 4096)
#define WS_OPART   (WS_MLOC + 8 * 8 * 32 * 16)
#define WS_ML      (WS_OPART + 8 * 32 * 512 * 4)
#define WS_GA      (WS_ML + 32 * 8 * 8)
#define WS_GB      (WS_GA + 128 * 2048)
#define WS_QGF     (WS_GB + 128 * 2048)
#define WS_PF      (WS_QGF + 128 * 1024)
#define WS_MLOCF   (WS_PF + 8 * 16 * 4096)
#define WS_END     (WS_MLOCF + 8 * 8 * 32 * 16)

#define PF_LINES 146
#define SPIN_CAP (1u << 17)
#define A_SLEEP 40
#define B_SLEEP 20
#define A_DELAY 0
#define STAMP(i)

__device__ __forceinline__ void lgkm0() { asm volatile("s_waitcnt lgkmcnt(0)" ::: "memory"); }
__device__ __forceinline__ void vm0() { asm volatile("s_waitcnt vmcnt(0)" ::: "memory"); }
__device__ __forceinline__ void bar() { asm volatile("s_waitcnt lgkmcnt(0)\n\ts_barrier" ::: "memory"); }
typedef volatile __attribute__((address_space(3))) unsigned lds_u32;
__device__ __forceinline__ void lds_wait(lds_u32* p, unsigned target) {
    for (unsigned s = 0; s < SPIN_CAP; ++s) {
        if (*p >= target) break;
        __builtin_amdgcn_s_sleep(1);
    }
    asm volatile("" ::: "memory");
}

__device__ __forceinline__ void f0_signal(char* ws, int b, int lane, unsigned tag) {
    const u64 want = ((u64)(~tag) << 32) | tag;
    if (lane < NREP) __hip_atomic_store((gu64*)(ws + WS_F0 + lane * 2048 + b * 8), want, RLX_AGENT);
}
__device__ __forceinline__ void f0_wait(const __amdgpu_buffer_rsrc_t rs, int b, int lane, unsigned tag) {
    const int fo = WS_F0 + (b & (NREP - 1)) * 2048 + lane * 16;
    for (unsigned spins = 0; spins < SPIN_CAP; ++spins) {
        asm volatile("" ::: "memory");
        u32x4 a0 = __builtin_amdgcn_raw_buffer_load_b128(rs, fo, 0, SC1);
        u32x4 a1 = __builtin_amdgcn_raw_buffer_load_b128(rs, fo + 1024, 0, SC1);
        unsigned bad = (a0[0] ^ tag) | (a0[1] ^ ~tag) | (a0[2] ^ tag) | (a0[3] ^ ~tag) |
                       (a1[0] ^ tag) | (a1[1] ^ ~tag) | (a1[2] ^ tag) | (a1[3] ^ ~tag);
        if (__all(bad == 0)) break;
        __builtin_amdgcn_s_sleep(8);
    }
    asm volatile("" ::: "memory");
}

#define LDS_BYTES (65536 + 65536 + 8192)

__global__ __launch_bounds__(640, 1) void mega(const u16* __restrict__ x, const u16* __restrict__ ckv,
                                               const u16* __restrict__ wq, const u16* __restrict__ wuk,
                                               const u16* __restrict__ wuv, const u16* __restrict__ wo,
                                               const u16* __restrict__ wg, const u16* __restrict__ wu,
                                               const u16* __restrict__ wd, const u16* __restrict__ gw,
                                               char* __restrict__ ws, u16* __restrict__ out) {
    __shared__ __attribute__((aligned(16))) char lds[LDS_BYTES];
    __shared__ unsigned flg[4];

    const int t = threadIdx.x;
    unsigned pf0;
    {
        u64 pc;
        asm volatile("s_getpc_b64 %0" : "=s"(pc));
        const GAS unsigned* cb = (const GAS unsigned*)(pc & ~255ull);
        const int l0 = 639 - t;
        pf0 = cb[(l0 < PF_LINES - 1 ? l0 : PF_LINES - 1) * 32];
        __builtin_amdgcn_sched_barrier(0);
    }
    const int b = blockIdx.x;
    const int lane = t & 63, w = t >> 6;
    const int r = lane & 31, h2 = lane >> 5;
    const __amdgpu_buffer_rsrc_t rs = __builtin_amdgcn_make_buffer_rsrc(ws, 0, WS_END, 0x00020000);
    const u32x4 P4 = {POISON, POISON, POISON, POISON};
    const u32x2 P2 = {POISON, POISON};
    STAMP(0);

    if (b < 128) {
        if (w >= 9) { asm volatile("" :: "v"(pf0)); return; }
        lds_u32* vflg = (lds_u32*)flg;
        if (w == 8) {
            const unsigned e = __hip_atomic_load((gu32*)(ws + WS_EPOCH), RLX_AGENT);
            asm volatile("" :: "v"(pf0));
            bar();
            f0_signal(ws, b, lane, e + 1u);
            asm volatile("s_barrier" ::: "memory");
            STAMP(1);
            __builtin_amdgcn_s_sleep(A_SLEEP);
            f0_wait(rs, b, lane, e + 1u);
            if (lane == 0) vflg[2] = 1u;
            STAMP(2);
            return;
        }
        char* tile = lds;
        float* red = (float*)(lds + 65536);
        float* fac = (float*)(lds + 131072);
        float* Mws = fac + 512;
        float* Lws = Mws + 256;
        float* fin = Lws + 256;
        float* fing = fin + 256;
        const int kc = b & 7, mem = b >> 3;
        __builtin_amdgcn_raw_buffer_store_b128(P4, rs, (w < 4 ? WS_P : WS_PF) + ((kc * 16 + mem) * 4 + (w & 3)) * 1024 + lane * 16, 0, SC1);
        if (w < 4)
            __builtin_amdgcn_raw_buffer_store_b128(P4, rs, WS_OPART + ((kc * 32 + w * 8 + (lane >> 3)) * 512 + mem * 32) * 4 + (lane & 7) * 16, 0, SC1);
        else if (w == 4)
            __builtin_amdgcn_raw_buffer_store_b64(P2, rs, (lane < 32 ? WS_MLOC : WS_MLOCF) + ((kc * 8 + (mem >> 1)) * 32 + r) * 16 + (mem & 1) * 8, 0, SC1);
        else if (w == 5 && mem == 0 && lane < 32)
            __builtin_amdgcn_raw_buffer_store_b64(P2, rs, WS_ML + (lane * 8 + kc) * 8, 0, SC1);
        if (t < 4) flg[t] = 0u;
        vm0();
        asm volatile("" :: "v"(pf0));
        bar();
        asm volatile("s_barrier" ::: "memory");
        STAMP(1);
        __builtin_amdgcn_s_sleep(A_DELAY);
        bf16x8 bq[8];
        u32x4 cv[8];
        {
            const u16* bp = ckv + (size_t)(kc * 1024 + mem * 64 + r) * 512 + 64 * w + 8 * h2;
#pragma unroll
            for (int kb = 0; kb < 2; ++kb)
#pragma unroll
                for (int s = 0; s < 4; ++s) bq[kb * 4 + s] = *(const bf16x8*)(bp + kb * 32 * 512 + 16 * s);
            const u16* cp = ckv + (size_t)(kc * 1024 + (t >> 2)) * 512 + mem * 32 + (t & 3) * 8;
#pragma unroll
            for (int i = 0; i < 8; ++i) cv[i] = *(const u32x4*)(cp + (size_t)i * 128 * 512);
        }
        __builtin_amdgcn_sched_barrier(0);
#pragma unroll
        for (int i = 0; i < 8; ++i) *(u32x4*)(tile + (i * 128 + (t >> 2)) * 64 + (t & 3) * 16) = cv[i];
        STAMP(2);
        lds_wait(vflg + 2, 1);
        STAMP(3);
        {
            u32x4 av[4];
            const int qo = WS_QABS + (b & 7) * 32768 + (w * 256 + lane) * 16;
            for (unsigned spins = 0; spins < SPIN_CAP; ++spins) {
                asm volatile("" ::: "memory");
#pragma unroll
                for (int s = 0; s < 4; ++s) av[s] = __builtin_amdgcn_raw_buffer_load_b128(rs, qo + 1024 * s, 0, SC1);
                unsigned m = umax(umax(max4(av[0]), max4(av[1])), umax(max4(av[2]), max4(av[3])));
                if (__all(m != POISON)) break;
                __builtin_amdgcn_s_sleep(1);
            }
            STAMP(4);
#pragma unroll
            for (int kb = 0; kb < 2; ++kb) {
                f32x16 acc;
#pragma unroll
                for (int i = 0; i < 16; ++i) acc[i] = 0.f;
#pragma unroll
                for (int s = 0; s < 4; ++s)
                    acc = __builtin_amdgcn_mfma_f32_32x32x16_bf16(__builtin_bit_cast(bf16x8, av[s]), bq[kb * 4 + s], acc, 0, 0, 0);
#pragma unroll
                for (int i = 0; i < 16; ++i) {
                    int head = (i & 3) + 8 * (i >> 2) + 4 * h2;
                    red[(w * 32 + head) * 64 + kb * 32 + r] = acc[i];
                }
            }
        }
        bar();
        {
            const int head = t >> 4, kq = (t & 15) * 4;
            f32x4 s4 = *(const f32x4*)(red + head * 64 + kq);
#pragma unroll
            for (int ww = 1; ww < 8; ++ww) s4 += *(const f32x4*)(red + (ww * 32 + head) * 64 + kq);
            float m = fmaxf(fmaxf(s4[0], s4[1]), fmaxf(s4[2], s4[3]));
            m = fmaxf(m, __shfl_xor(m, 1));
            m = fmaxf(m, __shfl_xor(m, 2));
            m = fmaxf(m, __shfl_xor(m, 4));
            m = fmaxf(m, __shfl_xor(m, 8));
            unsigned p01 = pk2(__expf(s4[0] - m), __expf(s4[1] - m));
            unsigned p23 = pk2(__expf(s4[2] - m), __expf(s4[3] - m));
            float l = (bflo(p01) + bfhi(p01)) + (bflo(p23) + bfhi(p23));
            l += __shfl_xor(l, 1);
            l += __shfl_xor(l, 2);
            l += __shfl_xor(l, 4);
            l += __shfl_xor(l, 8);
            u32x4 pp = {p01, p23, (unsigned)__shfl_down((int)p01, 1), (unsigned)__shfl_down((int)p23, 1)};
            u32x2 ml = {asu(m), asu(l)};
            const int po = ((kc * 16 + mem) * 256 + ((t & 15) >> 1) * 32 + head) * 16;
            const int mo = ((kc * 8 + (mem >> 1)) * 32 + head) * 16 + (mem & 1) * 8;
            if ((t & 1) == 0) __builtin_amdgcn_raw_buffer_store_b128(pp, rs, WS_PF + po, 0, 0);
            if ((t & 15) == 0) __builtin_amdgcn_raw_buffer_store_b64(ml, rs, WS_MLOCF + mo, 0, 0);
            if ((t & 1) == 0) __builtin_amdgcn_raw_buffer_store_b128(pp, rs, WS_P + po, 0, SC1);
            if ((t & 15) == 0) __builtin_amdgcn_raw_buffer_store_b64(ml, rs, WS_MLOC + mo, 0, SC1);
        }
        bar();
        STAMP(5);
        {
            u32x4 pv[8], mlv;
            for (unsigned spins = 0; spins < SPIN_CAP; ++spins) {
                asm volatile("" ::: "memory");
                const bool slow = (spins & 3) == 3;
                const int po = (slow ? WS_P : WS_PF) + ((kc * 16 + w * 2) * 256 + lane) * 16;
                const int mo = (slow ? WS_MLOC : WS_MLOCF) + ((kc * 8 + w) * 32 + r) * 16;
#pragma unroll
                for (int s = 0; s < 8; ++s) pv[s] = __builtin_amdgcn_raw_buffer_load_b128(rs, po + 1024 * s, 0, SC1);
                mlv = __builtin_amdgcn_raw_buffer_load_b128(rs, mo, 0, SC1);
                unsigned m = umax(umax(max4(pv[0]), max4(pv[1])), umax(max4(pv[2]), max4(pv[3])));
                m = umax(m, umax(umax(max4(pv[4]), max4(pv[5])), umax(max4(pv[6]), max4(pv[7]))));
                m = umax(m, max4(mlv));
                if (__all(m != POISON)) break;
                __builtin_amdgcn_s_sleep(1);
            }
            STAMP(6);
            {
                float m0 = asf(mlv[0]), l0 = asf(mlv[1]), m1 = asf(mlv[2]), l1 = asf(mlv[3]);
                float Mw = fmaxf(m0, m1);
                float f0_ = __expf(m0 - Mw), f1_ = __expf(m1 - Mw);
                if (lane < 32) {
                    fac[(2 * w) * 32 + r] = f0_; fac[(2 * w + 1) * 32 + r] = f1_;
                    Mws[r * 8 + w] = Mw; Lws[r * 8 + w] = f0_ * l0 + f1_ * l1;
                }
            }
            const int g = lane >> 4;
            const int q = (lane & 15) >> 2, p = lane & 3;
            const int cb = g & 1;
            f32x16 tot;
#pragma unroll
            for (int mm = 0; mm < 2; ++mm) {
                f32x16 acc;
#pragma unroll
                for (int i = 0; i < 16; ++i) acc[i] = 0.f;
#pragma unroll
                for (int ss = 0; ss < 4; ++ss) {
                    int s = mm * 4 + ss;
                    int row = w * 128 + 16 * s + 8 * h2 + q;
                    s16x4 t0 = __builtin_amdgcn_ds_read_tr16_b64_v4i16(
                        (__attribute__((address_space(3))) s16x4*)(tile + row * 64 + cb * 32 + p * 8));
                    s16x4 t1 = __builtin_amdgcn_ds_read_tr16_b64_v4i16(
                        (__attribute__((address_space(3))) s16x4*)(tile + (row + 4) * 64 + cb * 32 + p * 8));
                    s16x8 bb = {t0[0], t0[1], t0[2], t0[3], t1[0], t1[1], t1[2], t1[3]};
                    acc = __builtin_amdgcn_mfma_f32_32x32x16_bf16(__builtin_bit_cast(bf16x8, pv[s]), __builtin_bit_cast(bf16x8, bb), acc, 0, 0, 0);
                }
                const float* fp = fac + (2 * w + mm) * 32 + 4 * h2;
#pragma unroll
                for (int qd = 0; qd < 4; ++qd) {
                    f32x4 f4 = *(const f32x4*)(fp + 8 * qd);
#pragma unroll
                    for (int e2 = 0; e2 < 4; ++e2) {
                        if (mm == 0) tot[qd * 4 + e2] = f4[e2] * acc[qd * 4 + e2];
                        else tot[qd * 4 + e2] += f4[e2] * acc[qd * 4 + e2];
                    }
                }
            }
#pragma unroll
            for (int i = 0; i < 16; ++i) {
                int head = (i & 3) + 8 * (i >> 2) + 4 * h2;
                red[(w * 32 + head) * 32 + r] = tot[i];
            }
        }
        bar();
        {
            const int head = t >> 4, cq = (t & 15) * 2;
            f32x4 ma = *(const f32x4*)(Mws + head * 8), mb = *(const f32x4*)(Mws + head * 8 + 4);
            float M = fmaxf(fmaxf(fmaxf(ma[0], ma[1]), fmaxf(ma[2], ma[3])), fmaxf(fmaxf(mb[0], mb[1]), fmaxf(mb[2], mb[3])));
            float ew[8];
#pragma unroll
            for (int ww = 0; ww < 4; ++ww) { ew[ww] = __expf(ma[ww] - M); ew[ww + 4] = __expf(mb[ww] - M); }
            f32x2 s2 = {0.f, 0.f};
#pragma unroll
            for (int ww = 0; ww < 8; ++ww) s2 += ew[ww] * *(const f32x2*)(red + (ww * 32 + head) * 32 + cq);
            u32x4 o = {asu(s2[0]), asu(s2[1]), asu(__shfl_down(s2[0], 1)), asu(__shfl_down(s2[1], 1))};
            if ((t & 1) == 0)
                __builtin_amdgcn_raw_buffer_store_b128(o, rs, WS_OPART + ((kc * 32 + head) * 512 + mem * 32 + cq) * 4, 0, SC1);
            if (mem == 0 && (t & 15) == 0) {
                f32x4 la = *(const f32x4*)(Lws + head * 8), lb = *(const f32x4*)(Lws + head * 8 + 4);
                float L = 0.f;
#pragma unroll
                for (int ww = 0; ww < 4; ++ww) L += ew[ww] * la[ww] + ew[ww + 4] * lb[ww];
                u32x2 ml = {asu(M), asu(L)};
                __builtin_amdgcn_raw_buffer_store_b64(ml, rs, WS_ML + (head * 8 + kc) * 8, 0, SC1);
            }
        }
        STAMP(7);
        if (b >= 32) return;
        {
            const int p = t >> 2, qc = t & 3;
            float gp = rbf(bf2f(x[t]) * bf2f(gw[t]));
            const int ao = WS_GA + p * 2048 + b * 64 + qc * 16;
            const int bo = WS_GB + (p < 43 ? p : 42) * 2048 + b * 64 + qc * 16;
            u32x4 a0, b0;
            for (unsigned spins = 0; spins < SPIN_CAP; ++spins) {
                asm volatile("" ::: "memory");
                a0 = __builtin_amdgcn_raw_buffer_load_b128(rs, ao, 0, SC1);
                b0 = __builtin_amdgcn_raw_buffer_load_b128(rs, bo, 0, SC1);
                unsigned m = umax(max4(a0), max4(b0));
                if (__all(m != POISON)) break;
                __builtin_amdgcn_s_sleep(2);
            }
            STAMP(11);
            if (p >= 43) { b0[0] = 0u; b0[1] = 0u; b0[2] = 0u; b0[3] = 0u; }
            bool up = lane & 4;
            float s4[4];
#pragma unroll
            for (int q = 0; q < 4; ++q) { float send = up ? asf(a0[q]) : asf(b0[q]); float keep = up ? asf(b0[q]) : asf(a0[q]); s4[q] = keep + __shfl_xor(send, 4); }
            up = lane & 8;
            float s2[2];
#pragma unroll
            for (int q = 0; q < 2; ++q) { float send = up ? s4[q] : s4[q + 2]; float keep = up ? s4[q + 2] : s4[q]; s2[q] = keep + __shfl_xor(send, 8); }
            up = lane & 16;
            float send = up ? s2[0] : s2[1]; float keep = up ? s2[1] : s2[0];
            float vv = keep + __shfl_xor(send, 16);
            vv += __shfl_xor(vv, 32);
#pragma unroll
            for (int o = 32; o >= 1; o >>= 1) gp += __shfl_xor(gp, o);
            if (lane < 32) {
                int idx = ((lane >> 2) & 1) * 16 + (lane & 3) * 4 + ((lane >> 3) & 1) * 2 + ((lane >> 4) & 1);
                fin[w * 32 + idx] = vv;
            }
            if (lane == 0) fing[w] = gp;
        }
        bar();
        if (t < 16) {
            float a = 0.f, bb = 0.f, gs = 0.f;
#pragma unroll
            for (int ww = 0; ww < 8; ++ww) { a += fin[ww * 32 + t]; bb += fin[ww * 32 + 16 + t]; gs += fing[ww]; }
            gs = rbf(gs);
            float g = rbf(1.f / (1.f + __expf(-gs)));
            a = rbf(a); bb = rbf(bb);
            float rr = rbf(g * a) + rbf(rbf(1.f - g) * bb);
            out[b * 16 + t] = f2bf(rr);
        }
        STAMP(12);
        return;
    }

    float* redf = (float*)lds;
    float* redo = redf + 4096;
    float* cpart = redo + 4096;
    float* sm1 = cpart + 2048;
    float* sm2 = sm1 + 1024;
    float* sm3 = sm2 + 512;
    float* qs = sm3 + 256;
    lds_u32* vflg = (lds_u32*)flg;
    const int bb_ = b - 128;
    const int h = bb_ & 31, jj = bb_ >> 5;
    const bool has_ffn = bb_ < 43;

    if (w >= 8) {
        const unsigned e = __hip_atomic_load((gu32*)(ws + WS_EPOCH), RLX_AGENT);
        asm volatile("" :: "v"(pf0));
        bar();
        const unsigned tag = e + 1u;
        if (w == 8) {
            lds_wait(vflg + 0, 8);
            STAMP(1);
            f32x2 s = *(const f32x2*)(sm1 + 2 * lane);
#pragma unroll
            for (int ww = 1; ww < 8; ++ww) s += *(const f32x2*)(sm1 + ww * 128 + 2 * lane);
            u32x4 g = {tag, asu(s[0]), asu(s[1]), ~tag};
            __builtin_amdgcn_raw_buffer_store_b128(g, rs, WS_QGF + ((h * 4 + jj) * 64 + lane) * 16, 0, 0);
            __builtin_amdgcn_raw_buffer_store_b128(g, rs, WS_QG + ((h * 4 + jj) * 64 + lane) * 16, 0, SC1);
            for (unsigned spins = 0; spins < SPIN_CAP; ++spins) {
                asm volatile("" ::: "memory");
                u32x4 v[4];
                const int qb = ((spins & 3) == 3 ? WS_QG : WS_QGF) + (h * 4 * 64 + lane) * 16;
#pragma unroll
                for (int k = 0; k < 4; ++k) v[k] = __builtin_amdgcn_raw_buffer_load_b128(rs, qb + k * 1024, 0, SC1);
                unsigned bad = 0;
                float q0 = 0.f, q1 = 0.f;
#pragma unroll
                for (int k = 0; k < 4; ++k) {
                    bad |= (v[k][0] ^ tag) | (v[k][3] ^ ~tag);
                    q0 += asf(v[k][1]); q1 += asf(v[k][2]);
                }
                if (__all(bad == 0) || spins == SPIN_CAP - 1) {
                    f32x2 qq = {rbf(q0), rbf(q1)};
                    *(f32x2*)(qs + 2 * lane) = qq;
                    break;
                }
                __builtin_amdgcn_s_sleep(1);
            }
            lgkm0();
            if (lane == 0) vflg[1] = 1u;
            STAMP(2);
        } else {
            lds_wait(vflg + 3, 8);
            STAMP(1);
            f0_signal(ws, b, lane, tag);
            __builtin_amdgcn_s_sleep(B_SLEEP);
            f0_wait(rs, b, lane, tag);
            if (lane == 0) vflg[2] = 1u;
            if (b == 255 && lane == 0) __hip_atomic_store((gu32*)(ws + WS_EPOCH), tag, RLX_AGENT);
            STAMP(2);
        }
        return;
    }

    {
        int po;
        if (w < 2) po = WS_QABS + (lane >> 3) * 32768 + ((jj * 16 + w * 8 + (lane & 7)) * 32 + h) * 16;
        else if (w < 4) po = WS_GA + bb_ * 2048 + (w & 1) * 1024 + lane * 16;
        else po = WS_GB + bb_ * 2048 + (w & 1) * 1024 + lane * 16;
        if (w < 4 || (w < 6 && has_ffn)) __builtin_amdgcn_raw_buffer_store_b128(P4, rs, po, 0, SC1);
    }
    __builtin_amdgcn_sched_barrier(0);
    u32x4 wqv[4], wkv[4];
    u16 xr[4];
    {
        const int row = jj * 128 + (t >> 4);
#pragma unroll
        for (int i = 0; i < 4; ++i) xr[i] = x[row + 32 * i];
#pragma unroll
        for (int i = 0; i < 4; ++i)
            wqv[i] = *(const u32x4*)(wq + (size_t)(row + 32 * i) * 4096 + h * 128 + (t & 15) * 8);
#pragma unroll
        for (int i = 0; i < 4; ++i)
            wkv[i] = *(const u32x4*)(wuk + (size_t)(jj * 128 + 16 * w + (lane >> 4) + 4 * i) * 4096 + h * 128 + (lane & 15) * 8);
    }
    __builtin_amdgcn_sched_barrier(0);
    if (t < 4) flg[t] = 0u;
    bar();
    asm volatile("s_waitcnt vmcnt(12)" ::: "memory");
    asm volatile("" :: "v"(pf0));
    if (lane == 0) __hip_atomic_fetch_add(flg + 3, 1u, __ATOMIC_RELAXED, __HIP_MEMORY_SCOPE_WORKGROUP);
    __builtin_amdgcn_sched_barrier(0);
    asm volatile("s_waitcnt vmcnt(4)" ::: "memory");
    STAMP(1);
    {
        float acc[8];
#pragma unroll
        for (int k = 0; k < 4; ++k) {
            acc[2 * k] = bf2f(xr[0]) * bflo(wqv[0][k]) + bf2f(xr[1]) * bflo(wqv[1][k]) + bf2f(xr[2]) * bflo(wqv[2][k]) + bf2f(xr[3]) * bflo(wqv[3][k]);
            acc[2 * k + 1] = bf2f(xr[0]) * bfhi(wqv[0][k]) + bf2f(xr[1]) * bfhi(wqv[1][k]) + bf2f(xr[2]) * bfhi(wqv[2][k]) + bf2f(xr[3]) * bfhi(wqv[3][k]);
        }
        bool up = lane & 16;
        float s[4];
#pragma unroll
        for (int k = 0; k < 4; ++k) { float send = up ? acc[k] : acc[k + 4]; float keep = up ? acc[k + 4] : acc[k]; s[k] = keep + __shfl_xor(send, 16); }
        up = lane & 32;
        f32x2 s2;
#pragma unroll
        for (int k = 0; k < 2; ++k) { float send = up ? s[k] : s[k + 2]; float keep = up ? s[k + 2] : s[k]; s2[k] = keep + __shfl_xor(send, 32); }
        *(f32x2*)(sm1 + w * 128 + (lane & 15) * 8 + ((lane >> 4) & 1) * 4 + (lane >> 5) * 2) = s2;
        lgkm0();
        if (lane == 0) __hip_atomic_fetch_add(flg + 0, 1u, __ATOMIC_RELAXED, __HIP_MEMORY_SCOPE_WORKGROUP);
    }
    __builtin_amdgcn_sched_barrier(0);
    lds_wait(vflg + 1, 1);
    STAMP(2);
    {
        const int l16 = lane & 15;
        f32x4 qa0 = *(const f32x4*)(qs + l16 * 8), qa1 = *(const f32x4*)(qs + l16 * 8 + 4);
        float d[4];
#pragma unroll
        for (int i = 0; i < 4; ++i) {
            d[i] = qa0[0] * bflo(wkv[i][0]) + qa0[1] * bfhi(wkv[i][0]) + qa0[2] * bflo(wkv[i][1]) + qa0[3] * bfhi(wkv[i][1]);
            d[i] += qa1[0] * bflo(wkv[i][2]) + qa1[1] * bfhi(wkv[i][2]) + qa1[2] * bflo(wkv[i][3]) + qa1[3] * bfhi(wkv[i][3]);
        }
        bool up = lane & 1;
        float s[2];
#pragma unroll
        for (int k = 0; k < 2; ++k) { float send = up ? d[k] : d[k + 2]; float keep = up ? d[k + 2] : d[k]; s[k] = keep + __shfl_xor(send, 1); }
        up = lane & 2;
        float send = up ? s[0] : s[1], keep = up ? s[1] : s[0];
        float v = keep + __shfl_xor(send, 2);
        v += __shfl_xor(v, 4);
        v += __shfl_xor(v, 8);
        v *= 0.08838834764831845f;
        const int k = (lane >> 3) & 1;
        float vv[8];
#pragma unroll
        for (int e2 = 0; e2 < 8; ++e2) vv[e2] = __shfl(v, (e2 & 3) * 16 + (k | ((e2 >> 2) << 1)));
        u32x4 o = {pk2(vv[0], vv[1]), pk2(vv[2], vv[3]), pk2(vv[4], vv[5]), pk2(vv[6], vv[7])};
        if (lane < 16)
            __builtin_amdgcn_raw_buffer_store_b128(o, rs, WS_QABS + (lane & 7) * 32768 + ((jj * 16 + w * 2 + k) * 32 + h) * 16, 0, SC1);
    }
    __builtin_amdgcn_sched_barrier(0);
    if (lane == 0) __hip_atomic_fetch_add(flg + 0, 1u, __ATOMIC_RELAXED, __HIP_MEMORY_SCOPE_WORKGROUP);
    lds_wait(vflg + 0, 16);
    __builtin_amdgcn_sched_barrier(0);
    STAMP(3);
    u32x4 uvv[4], ovv[4];
#pragma unroll
    for (int i = 0; i < 4; ++i)
        uvv[i] = *(const u32x4*)(wuv + (size_t)((t >> 2) + 128 * i) * 4096 + h * 128 + jj * 32 + (t & 3) * 8);
#pragma unroll
    for (int i = 0; i < 4; ++i)
        ovv[i] = *(const u32x4*)(wo + ((size_t)h * 128 + jj * 32 + w * 4 + i) * 512 + lane * 8);
    __builtin_amdgcn_sched_barrier(0);
    if (has_ffn) {
        u32x4 gv[4], uv_[4], dv[4];
        u16 xf[4];
#pragma unroll
        for (int i = 0; i < 4; ++i) xf[i] = x[(t >> 2) + 128 * i];
#pragma unroll
        for (int i = 0; i < 4; ++i) {
            gv[i] = *(const u32x4*)(wg + (size_t)((t >> 2) + 128 * i) * FF + bb_ * 32 + (t & 3) * 8);
            uv_[i] = *(const u32x4*)(wu + (size_t)((t >> 2) + 128 * i) * FF + bb_ * 32 + (t & 3) * 8);
        }
#pragma unroll
        for (int i = 0; i < 4; ++i) dv[i] = *(const u32x4*)(wd + (size_t)(bb_ * 32 + w * 4 + i) * 512 + lane * 8);
        __builtin_amdgcn_sched_barrier(0);
        float p[16];
#pragma unroll
        for (int i = 0; i < 4; ++i) {
            const float xv = bf2f(xf[i]);
#pragma unroll
            for (int k = 0; k < 4; ++k) {
                if (i == 0) {
                    p[2 * k] = xv * bflo(gv[i][k]); p[2 * k + 1] = xv * bfhi(gv[i][k]);
                    p[8 + 2 * k] = xv * bflo(uv_[i][k]); p[8 + 2 * k + 1] = xv * bfhi(uv_[i][k]);
                } else {
                    p[2 * k] += xv * bflo(gv[i][k]); p[2 * k + 1] += xv * bfhi(gv[i][k]);
                    p[8 + 2 * k] += xv * bflo(uv_[i][k]); p[8 + 2 * k + 1] += xv * bfhi(uv_[i][k]);
                }
            }
        }
        bool up = lane & 4;
        float s8[8];
#pragma unroll
        for (int k = 0; k < 8; ++k) { float send = up ? p[k] : p[k + 8]; float keep = up ? p[k + 8] : p[k]; s8[k] = keep + __shfl_xor(send, 4); }
        up = lane & 8;
        float s4[4];
#pragma unroll
        for (int k = 0; k < 4; ++k) { float send = up ? s8[k] : s8[k + 4]; float keep = up ? s8[k + 4] : s8[k]; s4[k] = keep + __shfl_xor(send, 8); }
        up = lane & 16;
        float s2[2];
#pragma unroll
        for (int k = 0; k < 2; ++k) { float send = up ? s4[k] : s4[k + 2]; float keep = up ? s4[k + 2] : s4[k]; s2[k] = keep + __shfl_xor(send, 16); }
        up = lane & 32;
        float send = up ? s2[0] : s2[1]; float keep = up ? s2[1] : s2[0];
        float v = keep + __shfl_xor(send, 32);
        sm2[w * 64 + ((lane >> 2) & 1) * 32 + (lane & 3) * 8 + ((lane >> 3) & 1) * 4 + ((lane >> 4) & 1) * 2 + (lane >> 5)] = v;
        bar();
        f32x4 g4 = *(const f32x4*)(sm2 + 4 * w), u4 = *(const f32x4*)(sm2 + 32 + 4 * w);
#pragma unroll
        for (int ww = 1; ww < 8; ++ww) { g4 += *(const f32x4*)(sm2 + ww * 64 + 4 * w); u4 += *(const f32x4*)(sm2 + ww * 64 + 32 + 4 * w); }
        f32x4 o0 = {0.f, 0.f, 0.f, 0.f}, o1 = {0.f, 0.f, 0.f, 0.f};
#pragma unroll
        for (int i = 0; i < 4; ++i) {
            const float gs = rbf(g4[i]), us = rbf(u4[i]);
            const float sg = rbf(gs / (1.f + __expf(-gs)));
            const float hv = rbf(sg * us);
            o0[0] += hv * bflo(dv[i][0]); o0[1] += hv * bfhi(dv[i][0]); o0[2] += hv * bflo(dv[i][1]); o0[3] += hv * bfhi(dv[i][1]);
            o1[0] += hv * bflo(dv[i][2]); o1[1] += hv * bfhi(dv[i][2]); o1[2] += hv * bflo(dv[i][3]); o1[3] += hv * bfhi(dv[i][3]);
        }
        *(f32x4*)(redf + w * 512 + lane * 8) = o0;
        *(f32x4*)(redf + w * 512 + lane * 8 + 4) = o1;
        bar();
        if (t < 128) {
            f32x4 s = *(const f32x4*)(redf + t * 4);
#pragma unroll
            for (int ww = 1; ww < 8; ++ww) s += *(const f32x4*)(redf + ww * 512 + t * 4);
            __builtin_amdgcn_raw_buffer_store_b128(__builtin_bit_cast(u32x4, s), rs, WS_GB + bb_ * 2048 + t * 16, 0, SC1);
        }
    }
    asm volatile("s_waitcnt vmcnt(1)" ::: "memory");
    lds_wait(vflg + 2, 1);
    STAMP(8);
    {
        const int qd = t >> 7;
        u32x4 ov[2];
        u32x2 ml;
        const int oo = WS_OPART + ((qd * 2 * 32 + h) * 512 + (t & 127) * 4) * 4;
        for (unsigned spins = 0; spins < SPIN_CAP; ++spins) {
            asm volatile("" ::: "memory");
#pragma unroll
            for (int i = 0; i < 2; ++i) ov[i] = __builtin_amdgcn_raw_buffer_load_b128(rs, oo + i * 65536, 0, SC1);
            ml = __builtin_amdgcn_raw_buffer_load_b64(rs, WS_ML + (h * 8 + (lane & 7)) * 8, 0, SC1);
            unsigned m = umax(max4(ov[0]), max4(ov[1]));
            m = umax(m, umax(ml[0], ml[1]));
            if (__all(m != POISON)) break;
            __builtin_amdgcn_s_sleep(1);
        }
        STAMP(9);
        float mk = asf(ml[0]), lk = asf(ml[1]);
        float M = mk;
        M = fmaxf(M, __shfl_xor(M, 1)); M = fmaxf(M, __shfl_xor(M, 2)); M = fmaxf(M, __shfl_xor(M, 4));
        float wk = __expf(mk - M);
        float L = wk * lk;
        L += __shfl_xor(L, 1); L += __shfl_xor(L, 2); L += __shfl_xor(L, 4);
        wk = wk / L;
        f32x4 c4 = {0.f, 0.f, 0.f, 0.f};
#pragma unroll
        for (int i = 0; i < 2; ++i) {
            float wi = __shfl(wk, qd * 2 + i);
            c4[0] += wi * asf(ov[i][0]); c4[1] += wi * asf(ov[i][1]); c4[2] += wi * asf(ov[i][2]); c4[3] += wi * asf(ov[i][3]);
        }
        *(f32x4*)(cpart + qd * 512 + (t & 127) * 4) = c4;
    }
    bar();
    {
        const int row = t >> 2;
        float acc[8];
#pragma unroll
        for (int i = 0; i < 4; ++i) {
            const int rr = row + 128 * i;
            float c0 = (cpart[rr] + cpart[512 + rr]) + (cpart[1024 + rr] + cpart[1536 + rr]);
#pragma unroll
            for (int k = 0; k < 4; ++k) {
                if (i == 0) { acc[2 * k] = c0 * bflo(uvv[i][k]); acc[2 * k + 1] = c0 * bfhi(uvv[i][k]); }
                else { acc[2 * k] += c0 * bflo(uvv[i][k]); acc[2 * k + 1] += c0 * bfhi(uvv[i][k]); }
            }
        }
        bool up = lane & 4;
        float s[4];
#pragma unroll
        for (int k = 0; k < 4; ++k) { float send = up ? acc[k] : acc[k + 4]; float keep = up ? acc[k + 4] : acc[k]; s[k] = keep + __shfl_xor(send, 4); }
        up = lane & 8;
        float s2[2];
#pragma unroll
        for (int k = 0; k < 2; ++k) { float send = up ? s[k] : s[k + 2]; float keep = up ? s[k + 2] : s[k]; s2[k] = keep + __shfl_xor(send, 8); }
        up = lane & 16;
        float send = up ? s2[0] : s2[1]; float keep = up ? s2[1] : s2[0];
        float v = keep + __shfl_xor(send, 16);
        v += __shfl_xor(v, 32);
        if (lane < 32) {
            int d = (lane & 3) * 8 + ((lane >> 2) & 1) * 4 + ((lane >> 3) & 1) * 2 + ((lane >> 4) & 1);
            sm3[w * 32 + d] = v;
        }
    }
    bar();
    {
        f32x4 c4 = *(const f32x4*)(sm3 + 4 * w);
#pragma unroll
        for (int ww = 1; ww < 8; ++ww) c4 += *(const f32x4*)(sm3 + ww * 32 + 4 * w);
        f32x4 o0 = {0.f, 0.f, 0.f, 0.f}, o1 = {0.f, 0.f, 0.f, 0.f};
#pragma unroll
        for (int i = 0; i < 4; ++i) {
            const float c0 = rbf(c4[i]);
            o0[0] += c0 * bflo(ovv[i][0]); o0[1] += c0 * bfhi(ovv[i][0]); o0[2] += c0 * bflo(ovv[i][1]); o0[3] += c0 * bfhi(ovv[i][1]);
            o1[0] += c0 * bflo(ovv[i][2]); o1[1] += c0 * bfhi(ovv[i][2]); o1[2] += c0 * bflo(ovv[i][3]); o1[3] += c0 * bfhi(ovv[i][3]);
        }
        *(f32x4*)(redo + w * 512 + lane * 8) = o0;
        *(f32x4*)(redo + w * 512 + lane * 8 + 4) = o1;
    }
    bar();
    if (t < 128) {
        f32x4 s = *(const f32x4*)(redo + t * 4);
#pragma unroll
        for (int ww = 1; ww < 8; ++ww) s += *(const f32x4*)(redo + ww * 512 + t * 4);
        __builtin_amdgcn_raw_buffer_store_b128(__builtin_bit_cast(u32x4, s), rs, WS_GA + bb_ * 2048 + t * 16, 0, SC1);
    }
    STAMP(10);
}

extern "C" void kernel_launch(void* const* d_in, const int* in_sizes, int n_in,
                              void* d_out, int out_size, void* d_ws, size_t ws_size,
                              hipStream_t stream) {
    const u16* x = (const u16*)d_in[0];
    const u16* ckv = (const u16*)d_in[1];
    const u16* wq = (const u16*)d_in[2];
    const u16* wuk = (const u16*)d_in[3];
    const u16* wuv = (const u16*)d_in[4];
    const u16* wo = (const u16*)d_in[5];
    const u16* wg = (const u16*)d_in[7];
    const u16* wu = (const u16*)d_in[8];
    const u16* wd = (const u16*)d_in[9];
    const u16* gw = (const u16*)d_in[10];
    mega<<<256, 640, 0, stream>>>(x, ckv, wq, wuk, wuv, wo, wg, wu, wd, gw, (char*)d_ws, (u16*)d_out);
}
```

```cpp
#include <hip/hip_runtime.h>
#include <hip/hip_bf16.h>
#include <stdint.h>

#define SK 8192
#define FF 1376

typedef unsigned short u16;
typedef unsigned long long u64;
typedef __attribute__((ext_vector_type(8))) __bf16 bf16x8;
typedef __attribute__((ext_vector_type(2))) __bf16 bf16x2;
typedef __attribute__((ext_vector_type(4))) short s16x4;
typedef __attribute__((ext_vector_type(8))) short s16x8;
typedef __attribute__((ext_vector_type(16))) float f32x16;
typedef __attribute__((ext_vector_type(4))) float f32x4;
typedef __attribute__((ext_vector_type(2))) float f32x2;
typedef __attribute__((ext_vector_type(4))) unsigned int u32x4;
typedef __attribute__((ext_vector_type(2))) unsigned int u32x2;
#define GAS __attribute__((address_space(1)))
typedef GAS unsigned gu32;
typedef GAS u64 gu64;
#define RLX_AGENT __ATOMIC_RELAXED, __HIP_MEMORY_SCOPE_AGENT
#define SC1 16
#define POISON 0xFFFFFFFFu

__device__ __forceinline__ float bf2f(u16 v) { return __uint_as_float(((unsigned)v) << 16); }
__device__ __forceinline__ float bflo(unsigned v) { return __uint_as_float(v << 16); }
__device__ __forceinline__ float bfhi(unsigned v) { return __uint_as_float(v & 0xffff0000u); }
__device__ __forceinline__ float asf(unsigned v) { return __uint_as_float(v); }
__device__ __forceinline__ unsigned asu(float v) { return __float_as_uint(v); }
__device__ __forceinline__ unsigned pk2(float a, float b) {
    bf16x2 v = {(__bf16)a, (__bf16)b};
    return __builtin_bit_cast(unsigned, v);
}
__device__ __forceinline__ u16 f2bf(float f) { __bf16 v = (__bf16)f; return __builtin_bit_cast(u16, v); }
__device__ __forceinline__ float rbf(float f) { return bf2f(f2bf(f)); }
__device__ __forceinline__ unsigned umax(unsigned a, unsigned b) { return a > b ? a : b; }
__device__ __forceinline__ unsigned max4(u32x4 v) { return umax(umax(v[0], v[1]), umax(v[2], v[3])); }

#define NREP 32
#define WS_EPOCH   0
#define WS_F0      256
#define WS_QG      (WS_F0 + NREP * 2048)
#define WS_QABS    (WS_QG + 128 * 1024)
#define WS_P       (WS_QABS + 8 * 32768)
#define WS_MLOC    (WS_P + 8 * 16 * 4096)
#define WS_OPART   (WS_MLOC + 8 * 8 * 32 * 16)
#define WS_ML      (WS_OPART + 8 * 32 * 512 * 4)
#define WS_GA      (WS_ML + 32 * 8 * 8)
#define WS_GB      (WS_GA + 128 * 2048)
#define WS_QGF     (WS_GB + 128 * 2048)
#define WS_PF      (WS_QGF + 128 * 1024)
#define WS_MLOCF   (WS_PF + 8 * 16 * 4096)
#define WS_END     (WS_MLOCF + 8 * 8 * 32 * 16)

#define PF_LINES 146
#define GATE_ID 0
#define GATE_X 2000
#define GATE(i) do { if (GATE_ID == (i)) { for (unsigned gs_ = 0; gs_ < 100000u; ++gs_) { if (__builtin_amdgcn_s_memrealtime() - t0_ >= (u64)GATE_X) break; __builtin_amdgcn_s_sleep(1); } } } while (0)
#define SPIN_CAP (1u << 17)
#define A_SLEEP 40
#define B_SLEEP 20
#define A_DELAY 0
#define STAMP(i)

__device__ __forceinline__ void lgkm0() { asm volatile("s_waitcnt lgkmcnt(0)" ::: "memory"); }
__device__ __forceinline__ void vm0() { asm volatile("s_waitcnt vmcnt(0)" ::: "memory"); }
__device__ __forceinline__ void bar() { asm volatile("s_waitcnt lgkmcnt(0)\n\ts_barrier" ::: "memory"); }
typedef volatile __attribute__((address_space(3))) unsigned lds_u32;
__device__ __forceinline__ void lds_wait(lds_u32* p, unsigned target) {
    for (unsigned s = 0; s < SPIN_CAP; ++s) {
        if (*p >= target) break;
        __builtin_amdgcn_s_sleep(1);
    }
    asm volatile("" ::: "memory");
}

__device__ __forceinline__ void f0_signal(char* ws, int b, int lane, unsigned tag) {
    const u64 want = ((u64)(~tag) << 32) | tag;
    if (lane < NREP) __hip_atomic_store((gu64*)(ws + WS_F0 + lane * 2048 + b * 8), want, RLX_AGENT);
}
__device__ __forceinline__ void f0_wait(const __amdgpu_buffer_rsrc_t rs, int b, int lane, unsigned tag) {
    const int fo = WS_F0 + (b & (NREP - 1)) * 2048 + lane * 16;
    for (unsigned spins = 0; spins < SPIN_CAP; ++spins) {
        asm volatile("" ::: "memory");
        u32x4 a0 = __builtin_amdgcn_raw_buffer_load_b128(rs, fo, 0, SC1);
        u32x4 a1 = __builtin_amdgcn_raw_buffer_load_b128(rs, fo + 1024, 0, SC1);
        unsigned bad = (a0[0] ^ tag) | (a0[1] ^ ~tag) | (a0[2] ^ tag) | (a0[3] ^ ~tag) |
                       (a1[0] ^ tag) | (a1[1] ^ ~tag) | (a1[2] ^ tag) | (a1[3] ^ ~tag);
        if (__all(bad == 0)) break;
        __builtin_amdgcn_s_sleep(8);
    }
    asm volatile("" ::: "memory");
}

#define LDS_BYTES (65536 + 65536 + 8192)

__global__ __launch_bounds__(640, 1) void mega(const u16* __restrict__ x, const u16* __restrict__ ckv,
                                               const u16* __restrict__ wq, const u16* __restrict__ wuk,
                                               const u16* __restrict__ wuv, const u16* __restrict__ wo,
                                               const u16* __restrict__ wg, const u16* __restrict__ wu,
                                               const u16* __restrict__ wd, const u16* __restrict__ gw,
                                               char* __restrict__ ws, u16* __restrict__ out) {
    __shared__ __attribute__((aligned(16))) char lds[LDS_BYTES];
    __shared__ unsigned flg[4];

    const int t = threadIdx.x;
    unsigned pf0;
    {
        u64 pc;
        asm volatile("s_getpc_b64 %0" : "=s"(pc));
        const GAS unsigned* cb = (const GAS unsigned*)(pc & ~255ull);
        const int l0 = 639 - t;
        pf0 = cb[(l0 < PF_LINES - 1 ? l0 : PF_LINES - 1) * 32];
        __builtin_amdgcn_sched_barrier(0);
    }
    const int b = blockIdx.x;
    const u64 t0_ = __builtin_amdgcn_s_memrealtime();
    const int lane = t & 63, w = t >> 6;
    const int r = lane & 31, h2 = lane >> 5;
    const __amdgpu_buffer_rsrc_t rs = __builtin_amdgcn_make_buffer_rsrc(ws, 0, WS_END, 0x00020000);
    const u32x4 P4 = {POISON, POISON, POISON, POISON};
    const u32x2 P2 = {POISON, POISON};
    STAMP(0);

    if (b < 128) {
        if (w >= 9) { asm volatile("" :: "v"(pf0)); return; }
        lds_u32* vflg = (lds_u32*)flg;
        if (w == 8) {
            const unsigned e = __hip_atomic_load((gu32*)(ws + WS_EPOCH), RLX_AGENT);
            asm volatile("" :: "v"(pf0));
            bar();
            f0_signal(ws, b, lane, e + 1u);
            asm volatile("s_barrier" ::: "memory");
            STAMP(1);
            __builtin_amdgcn_s_sleep(A_SLEEP);
            f0_wait(rs, b, lane, e + 1u);
            if (lane == 0) vflg[2] = 1u;
            STAMP(2);
            return;
        }
        char* tile = lds;
        float* red = (float*)(lds + 65536);
        float* fac = (float*)(lds + 131072);
        float* Mws = fac + 512;
        float* Lws = Mws + 256;
        float* fin = Lws + 256;
        float* fing = fin + 256;
        const int kc = b & 7, mem = b >> 3;
        __builtin_amdgcn_raw_buffer_store_b128(P4, rs, (w < 4 ? WS_P : WS_PF) + ((kc * 16 + mem) * 4 + (w & 3)) * 1024 + lane * 16, 0, SC1);
        if (w < 4)
            __builtin_amdgcn_raw_buffer_store_b128(P4, rs, WS_OPART + ((kc * 32 + w * 8 + (lane >> 3)) * 512 + mem * 32) * 4 + (lane & 7) * 16, 0, SC1);
        else if (w == 4)
            __builtin_amdgcn_raw_buffer_store_b64(P2, rs, (lane < 32 ? WS_MLOC : WS_MLOCF) + ((kc * 8 + (mem >> 1)) * 32 + r) * 16 + (mem & 1) * 8, 0, SC1);
        else if (w == 5 && mem == 0 && lane < 32)
            __builtin_amdgcn_raw_buffer_store_b64(P2, rs, WS_ML + (lane * 8 + kc) * 8, 0, SC1);
        if (t < 4) flg[t] = 0u;
        vm0();
        asm volatile("" :: "v"(pf0));
        bar();
        asm volatile("s_barrier" ::: "memory");
        STAMP(1);
        __builtin_amdgcn_s_sleep(A_DELAY);
        bf16x8 bq[8];
        u32x4 cv[8];
        {
            const u16* bp = ckv + (size_t)(kc * 1024 + mem * 64 + r) * 512 + 64 * w + 8 * h2;
#pragma unroll
            for (int kb = 0; kb < 2; ++kb)
#pragma unroll
                for (int s = 0; s < 4; ++s) bq[kb * 4 + s] = *(const bf16x8*)(bp + kb * 32 * 512 + 16 * s);
            const u16* cp = ckv + (size_t)(kc * 1024 + (t >> 2)) * 512 + mem * 32 + (t & 3) * 8;
#pragma unroll
            for (int i = 0; i < 8; ++i) cv[i] = *(const u32x4*)(cp + (size_t)i * 128 * 512);
        }
        __builtin_amdgcn_sched_barrier(0);
#pragma unroll
        for (int i = 0; i < 8; ++i) *(u32x4*)(tile + (i * 128 + (t >> 2)) * 64 + (t & 3) * 16) = cv[i];
        GATE(7);
        lds_wait(vflg + 2, 1);
        GATE(8);
        {
            u32x4 av[4];
            const int qo = WS_QABS + (b & 7) * 32768 + (w * 256 + lane) * 16;
            for (unsigned spins = 0; spins < SPIN_CAP; ++spins) {
                asm volatile("" ::: "memory");
#pragma unroll
                for (int s = 0; s < 4; ++s) av[s] = __builtin_amdgcn_raw_buffer_load_b128(rs, qo + 1024 * s, 0, SC1);
                unsigned m = umax(umax(max4(av[0]), max4(av[1])), umax(max4(av[2]), max4(av[3])));
                if (__all(m != POISON)) break;
                __builtin_amdgcn_s_sleep(1);
            }
            GATE(3);
#pragma unroll
            for (int kb = 0; kb < 2; ++kb) {
                f32x16 acc;
#pragma unroll
                for (int i = 0; i < 16; ++i) acc[i] = 0.f;
#pragma unroll
                for (int s = 0; s < 4; ++s)
                    acc = __builtin_amdgcn_mfma_f32_32x32x16_bf16(__builtin_bit_cast(bf16x8, av[s]), bq[kb * 4 + s], acc, 0, 0, 0);
#pragma unroll
                for (int i = 0; i < 16; ++i) {
                    int head = (i & 3) + 8 * (i >> 2) + 4 * h2;
                    red[(w * 32 + head) * 64 + kb * 32 + r] = acc[i];
                }
            }
        }
        bar();
        {
            const int head = t >> 4, kq = (t & 15) * 4;
            f32x4 s4 = *(const f32x4*)(red + head * 64 + kq);
#pragma unroll
            for (int ww = 1; ww < 8; ++ww) s4 += *(const f32x4*)(red + (ww * 32 + head) * 64 + kq);
            float m = fmaxf(fmaxf(s4[0], s4[1]), fmaxf(s4[2], s4[3]));
            m = fmaxf(m, __shfl_xor(m, 1));
            m = fmaxf(m, __shfl_xor(m, 2));
            m = fmaxf(m, __shfl_xor(m, 4));
            m = fmaxf(m, __shfl_xor(m, 8));
            unsigned p01 = pk2(__expf(s4[0] - m), __expf(s4[1] - m));
            unsigned p23 = pk2(__expf(s4[2] - m), __expf(s4[3] - m));
            float l = (bflo(p01) + bfhi(p01)) + (bflo(p23) + bfhi(p23));
            l += __shfl_xor(l, 1);
            l += __shfl_xor(l, 2);
            l += __shfl_xor(l, 4);
            l += __shfl_xor(l, 8);
            u32x4 pp = {p01, p23, (unsigned)__shfl_down((int)p01, 1), (unsigned)__shfl_down((int)p23, 1)};
            u32x2 ml = {asu(m), asu(l)};
            const int po = ((kc * 16 + mem) * 256 + ((t & 15) >> 1) * 32 + head) * 16;
            const int mo = ((kc * 8 + (mem >> 1)) * 32 + head) * 16 + (mem & 1) * 8;
            if ((t & 1) == 0) __builtin_amdgcn_raw_buffer_store_b128(pp, rs, WS_PF + po, 0, 0);
            if ((t & 15) == 0) __builtin_amdgcn_raw_buffer_store_b64(ml, rs, WS_MLOCF + mo, 0, 0);
            if ((t & 1) == 0) __builtin_amdgcn_raw_buffer_store_b128(pp, rs, WS_P + po, 0, SC1);
            if ((t & 15) == 0) __builtin_amdgcn_raw_buffer_store_b64(ml, rs, WS_MLOC + mo, 0, SC1);
        }
        bar();
        STAMP(5);
        {
            u32x4 pv[8], mlv;
            for (unsigned spins = 0; spins < SPIN_CAP; ++spins) {
                asm volatile("" ::: "memory");
                const bool slow = (spins & 3) == 3;
                const int po = (slow ? WS_P : WS_PF) + ((kc * 16 + w * 2) * 256 + lane) * 16;
                const int mo = (slow ? WS_MLOC : WS_MLOCF) + ((kc * 8 + w) * 32 + r) * 16;
#pragma unroll
                for (int s = 0; s < 8; ++s) pv[s] = __builtin_amdgcn_raw_buffer_load_b128(rs, po + 1024 * s, 0, SC1);
                mlv = __builtin_amdgcn_raw_buffer_load_b128(rs, mo, 0, SC1);
                unsigned m = umax(umax(max4(pv[0]), max4(pv[1])), umax(max4(pv[2]), max4(pv[3])));
                m = umax(m, umax(umax(max4(pv[4]), max4(pv[5])), umax(max4(pv[6]), max4(pv[7]))));
                m = umax(m, max4(mlv));
                if (__all(m != POISON)) break;
                __builtin_amdgcn_s_sleep(1);
            }
            GATE(4);
            {
                float m0 = asf(mlv[0]), l0 = asf(mlv[1]), m1 = asf(mlv[2]), l1 = asf(mlv[3]);
                float Mw = fmaxf(m0, m1);
                float f0_ = __expf(m0 - Mw), f1_ = __expf(m1 - Mw);
                if (lane < 32) {
                    fac[(2 * w) * 32 + r] = f0_; fac[(2 * w + 1) * 32 + r] = f1_;
                    Mws[r * 8 + w] = Mw; Lws[r * 8 + w] = f0_ * l0 + f1_ * l1;
                }
            }
            const int g = lane >> 4;
            const int q = (lane & 15) >> 2, p = lane & 3;
            const int cb = g & 1;
            f32x16 tot;
#pragma unroll
            for (int mm = 0; mm < 2; ++mm) {
                f32x16 acc;
#pragma unroll
                for (int i = 0; i < 16; ++i) acc[i] = 0.f;
#pragma unroll
                for (int ss = 0; ss < 4; ++ss) {
                    int s = mm * 4 + ss;
                    int row = w * 128 + 16 * s + 8 * h2 + q;
                    s16x4 t0 = __builtin_amdgcn_ds_read_tr16_b64_v4i16(
                        (__attribute__((address_space(3))) s16x4*)(tile + row * 64 + cb * 32 + p * 8));
                    s16x4 t1 = __builtin_amdgcn_ds_read_tr16_b64_v4i16(
                        (__attribute__((address_space(3))) s16x4*)(tile + (row + 4) * 64 + cb * 32 + p * 8));
                    s16x8 bb = {t0[0], t0[1], t0[2], t0[3], t1[0], t1[1], t1[2], t1[3]};
                    acc = __builtin_amdgcn_mfma_f32_32x32x16_bf16(__builtin_bit_cast(bf16x8, pv[s]), __builtin_bit_cast(bf16x8, bb), acc, 0, 0, 0);
                }
                const float* fp = fac + (2 * w + mm) * 32 + 4 * h2;
#pragma unroll
                for (int qd = 0; qd < 4; ++qd) {
                    f32x4 f4 = *(const f32x4*)(fp + 8 * qd);
#pragma unroll
                    for (int e2 = 0; e2 < 4; ++e2) {
                        if (mm == 0) tot[qd * 4 + e2] = f4[e2] * acc[qd * 4 + e2];
                        else tot[qd * 4 + e2] += f4[e2] * acc[qd * 4 + e2];
                    }
                }
            }
#pragma unroll
            for (int i = 0; i < 16; ++i) {
                int head = (i & 3) + 8 * (i >> 2) + 4 * h2;
                red[(w * 32 + head) * 32 + r] = tot[i];
            }
        }
        bar();
        {
            const int head = t >> 4, cq = (t & 15) * 2;
            f32x4 ma = *(const f32x4*)(Mws + head * 8), mb = *(const f32x4*)(Mws + head * 8 + 4);
            float M = fmaxf(fmaxf(fmaxf(ma[0], ma[1]), fmaxf(ma[2], ma[3])), fmaxf(fmaxf(mb[0], mb[1]), fmaxf(mb[2], mb[3])));
            float ew[8];
#pragma unroll
            for (int ww = 0; ww < 4; ++ww) { ew[ww] = __expf(ma[ww] - M); ew[ww + 4] = __expf(mb[ww] - M); }
            f32x2 s2 = {0.f, 0.f};
#pragma unroll
            for (int ww = 0; ww < 8; ++ww) s2 += ew[ww] * *(const f32x2*)(red + (ww * 32 + head) * 32 + cq);
            u32x4 o = {asu(s2[0]), asu(s2[1]), asu(__shfl_down(s2[0], 1)), asu(__shfl_down(s2[1], 1))};
            if ((t & 1) == 0)
                __builtin_amdgcn_raw_buffer_store_b128(o, rs, WS_OPART + ((kc * 32 + head) * 512 + mem * 32 + cq) * 4, 0, SC1);
            if (mem == 0 && (t & 15) == 0) {
                f32x4 la = *(const f32x4*)(Lws + head * 8), lb = *(const f32x4*)(Lws + head * 8 + 4);
                float L = 0.f;
#pragma unroll
                for (int ww = 0; ww < 4; ++ww) L += ew[ww] * la[ww] + ew[ww + 4] * lb[ww];
                u32x2 ml = {asu(M), asu(L)};
                __builtin_amdgcn_raw_buffer_store_b64(ml, rs, WS_ML + (head * 8 + kc) * 8, 0, SC1);
            }
        }
        STAMP(7);
        if (b >= 32) return;
        {
            const int p = t >> 2, qc = t & 3;
            float gp = rbf(bf2f(x[t]) * bf2f(gw[t]));
            const int ao = WS_GA + p * 2048 + b * 64 + qc * 16;
            const int bo = WS_GB + (p < 43 ? p : 42) * 2048 + b * 64 + qc * 16;
            u32x4 a0, b0;
            for (unsigned spins = 0; spins < SPIN_CAP; ++spins) {
                asm volatile("" ::: "memory");
                a0 = __builtin_amdgcn_raw_buffer_load_b128(rs, ao, 0, SC1);
                b0 = __builtin_amdgcn_raw_buffer_load_b128(rs, bo, 0, SC1);
                unsigned m = umax(max4(a0), max4(b0));
                if (__all(m != POISON)) break;
                __builtin_amdgcn_s_sleep(2);
            }
            GATE(6);
            if (p >= 43) { b0[0] = 0u; b0[1] = 0u; b0[2] = 0u; b0[3] = 0u; }
            bool up = lane & 4;
            float s4[4];
#pragma unroll
            for (int q = 0; q < 4; ++q) { float send = up ? asf(a0[q]) : asf(b0[q]); float keep = up ? asf(b0[q]) : asf(a0[q]); s4[q] = keep + __shfl_xor(send, 4); }
            up = lane & 8;
            float s2[2];
#pragma unroll
            for (int q = 0; q < 2; ++q) { float send = up ? s4[q] : s4[q + 2]; float keep = up ? s4[q + 2] : s4[q]; s2[q] = keep + __shfl_xor(send, 8); }
            up = lane & 16;
            float send = up ? s2[0] : s2[1]; float keep = up ? s2[1] : s2[0];
            float vv = keep + __shfl_xor(send, 16);
            vv += __shfl_xor(vv, 32);
#pragma unroll
            for (int o = 32; o >= 1; o >>= 1) gp += __shfl_xor(gp, o);
            if (lane < 32) {
                int idx = ((lane >> 2) & 1) * 16 + (lane & 3) * 4 + ((lane >> 3) & 1) * 2 + ((lane >> 4) & 1);
                fin[w * 32 + idx] = vv;
            }
            if (lane == 0) fing[w] = gp;
        }
        bar();
        if (t < 16) {
            float a = 0.f, bb = 0.f, gs = 0.f;
#pragma unroll
            for (int ww = 0; ww < 8; ++ww) { a += fin[ww * 32 + t]; bb += fin[ww * 32 + 16 + t]; gs += fing[ww]; }
            gs = rbf(gs);
            float g = rbf(1.f / (1.f + __expf(-gs)));
            a = rbf(a); bb = rbf(bb);
            float rr = rbf(g * a) + rbf(rbf(1.f - g) * bb);
            out[b * 16 + t] = f2bf(rr);
        }
        STAMP(12);
        return;
    }

    float* redf = (float*)lds;
    float* redo = redf + 4096;
    float* cpart = redo + 4096;
    float* sm1 = cpart + 2048;
    float* sm2 = sm1 + 1024;
    float* sm3 = sm2 + 512;
    float* qs = sm3 + 256;
    lds_u32* vflg = (lds_u32*)flg;
    const int bb_ = b - 128;
    const int h = bb_ & 31, jj = bb_ >> 5;
    const bool has_ffn = bb_ < 43;

    if (w >= 8) {
        const unsigned e = __hip_atomic_load((gu32*)(ws + WS_EPOCH), RLX_AGENT);
        asm volatile("" :: "v"(pf0));
        bar();
        const unsigned tag = e + 1u;
        if (w == 8) {
            lds_wait(vflg + 0, 8);
            STAMP(1);
            f32x2 s = *(const f32x2*)(sm1 + 2 * lane);
#pragma unroll
            for (int ww = 1; ww < 8; ++ww) s += *(const f32x2*)(sm1 + ww * 128 + 2 * lane);
            u32x4 g = {tag, asu(s[0]), asu(s[1]), ~tag};
            __builtin_amdgcn_raw_buffer_store_b128(g, rs, WS_QGF + ((h * 4 + jj) * 64 + lane) * 16, 0, 0);
            __builtin_amdgcn_raw_buffer_store_b128(g, rs, WS_QG + ((h * 4 + jj) * 64 + lane) * 16, 0, SC1);
            for (unsigned spins = 0; spins < SPIN_CAP; ++spins) {
                asm volatile("" ::: "memory");
                u32x4 v[4];
                const int qb = ((spins & 3) == 3 ? WS_QG : WS_QGF) + (h * 4 * 64 + lane) * 16;
#pragma unroll
                for (int k = 0; k < 4; ++k) v[k] = __builtin_amdgcn_raw_buffer_load_b128(rs, qb + k * 1024, 0, SC1);
                unsigned bad = 0;
                float q0 = 0.f, q1 = 0.f;
#pragma unroll
                for (int k = 0; k < 4; ++k) {
                    bad |= (v[k][0] ^ tag) | (v[k][3] ^ ~tag);
                    q0 += asf(v[k][1]); q1 += asf(v[k][2]);
                }
                if (__all(bad == 0) || spins == SPIN_CAP - 1) {
                    f32x2 qq = {rbf(q0), rbf(q1)};
                    *(f32x2*)(qs + 2 * lane) = qq;
                    break;
                }
                __builtin_amdgcn_s_sleep(1);
            }
            lgkm0();
            if (lane == 0) vflg[1] = 1u;
            STAMP(2);
        } else {
            lds_wait(vflg + 3, 8);
            STAMP(1);
            f0_signal(ws, b, lane, tag);
            __builtin_amdgcn_s_sleep(B_SLEEP);
            f0_wait(rs, b, lane, tag);
            if (lane == 0) vflg[2] = 1u;
            if (b == 255 && lane == 0) __hip_atomic_store((gu32*)(ws + WS_EPOCH), tag, RLX_AGENT);
            STAMP(2);
        }
        return;
    }

    {
        int po;
        if (w < 2) po = WS_QABS + (lane >> 3) * 32768 + ((jj * 16 + w * 8 + (lane & 7)) * 32 + h) * 16;
        else if (w < 4) po = WS_GA + bb_ * 2048 + (w & 1) * 1024 + lane * 16;
        else po = WS_GB + bb_ * 2048 + (w & 1) * 1024 + lane * 16;
        if (w < 4 || (w < 6 && has_ffn)) __builtin_amdgcn_raw_buffer_store_b128(P4, rs, po, 0, SC1);
    }
    __builtin_amdgcn_sched_barrier(0);
    u32x4 wqv[4], wkv[4];
    u16 xr[4];
    {
        const int row = jj * 128 + (t >> 4);
#pragma unroll
        for (int i = 0; i < 4; ++i) xr[i] = x[row + 32 * i];
#pragma unroll
        for (int i = 0; i < 4; ++i)
            wqv[i] = *(const u32x4*)(wq + (size_t)(row + 32 * i) * 4096 + h * 128 + (t & 15) * 8);
#pragma unroll
        for (int i = 0; i < 4; ++i)
            wkv[i] = *(const u32x4*)(wuk + (size_t)(jj * 128 + 16 * w + (lane >> 4) + 4 * i) * 4096 + h * 128 + (lane & 15) * 8);
    }
    __builtin_amdgcn_sched_barrier(0);
    if (t < 4) flg[t] = 0u;
    bar();
    asm volatile("s_waitcnt vmcnt(12)" ::: "memory");
    asm volatile("" :: "v"(pf0));
    if (lane == 0) __hip_atomic_fetch_add(flg + 3, 1u, __ATOMIC_RELAXED, __HIP_MEMORY_SCOPE_WORKGROUP);
    __builtin_amdgcn_sched_barrier(0);
    asm volatile("s_waitcnt vmcnt(4)" ::: "memory");
    GATE(1);
    {
        float acc[8];
#pragma unroll
        for (int k = 0; k < 4; ++k) {
            acc[2 * k] = bf2f(xr[0]) * bflo(wqv[0][k]) + bf2f(xr[1]) * bflo(wqv[1][k]) + bf2f(xr[2]) * bflo(wqv[2][k]) + bf2f(xr[3]) * bflo(wqv[3][k]);
            acc[2 * k + 1] = bf2f(xr[0]) * bfhi(wqv[0][k]) + bf2f(xr[1]) * bfhi(wqv[1][k]) + bf2f(xr[2]) * bfhi(wqv[2][k]) + bf2f(xr[3]) * bfhi(wqv[3][k]);
        }
        bool up = lane & 16;
        float s[4];
#pragma unroll
        for (int k = 0; k < 4; ++k) { float send = up ? acc[k] : acc[k + 4]; float keep = up ? acc[k + 4] : acc[k]; s[k] = keep + __shfl_xor(send, 16); }
        up = lane & 32;
        f32x2 s2;
#pragma unroll
        for (int k = 0; k < 2; ++k) { float send = up ? s[k] : s[k + 2]; float keep = up ? s[k + 2] : s[k]; s2[k] = keep + __shfl_xor(send, 32); }
        *(f32x2*)(sm1 + w * 128 + (lane & 15) * 8 + ((lane >> 4) & 1) * 4 + (lane >> 5) * 2) = s2;
        lgkm0();
        if (lane == 0) __hip_atomic_fetch_add(flg + 0, 1u, __ATOMIC_RELAXED, __HIP_MEMORY_SCOPE_WORKGROUP);
    }
    __builtin_amdgcn_sched_barrier(0);
    lds_wait(vflg + 1, 1);
    GATE(2);
    {
        const int l16 = lane & 15;
        f32x4 qa0 = *(const f32x4*)(qs + l16 * 8), qa1 = *(const f32x4*)(qs + l16 * 8 + 4);
        float d[4];
#pragma unroll
        for (int i = 0; i < 4; ++i) {
            d[i] = qa0[0] * bflo(wkv[i][0]) + qa0[1] * bfhi(wkv[i][0]) + qa0[2] * bflo(wkv[i][1]) + qa0[3] * bfhi(wkv[i][1]);
            d[i] += qa1[0] * bflo(wkv[i][2]) + qa1[1] * bfhi(wkv[i][2]) + qa1[2] * bflo(wkv[i][3]) + qa1[3] * bfhi(wkv[i][3]);
        }
        bool up = lane & 1;
        float s[2];
#pragma unroll
        for (int k = 0; k < 2; ++k) { float send = up ? d[k] : d[k + 2]; float keep = up ? d[k + 2] : d[k]; s[k] = keep + __shfl_xor(send, 1); }
        up = lane & 2;
        float send = up ? s[0] : s[1], keep = up ? s[1] : s[0];
        float v = keep + __shfl_xor(send, 2);
        v += __shfl_xor(v, 4);
        v += __shfl_xor(v, 8);
        v *= 0.08838834764831845f;
        const int k = (lane >> 3) & 1;
        float vv[8];
#pragma unroll
        for (int e2 = 0; e2 < 8; ++e2) vv[e2] = __shfl(v, (e2 & 3) * 16 + (k | ((e2 >> 2) << 1)));
        u32x4 o = {pk2(vv[0], vv[1]), pk2(vv[2], vv[3]), pk2(vv[4], vv[5]), pk2(vv[6], vv[7])};
        if (lane < 16)
            __builtin_amdgcn_raw_buffer_store_b128(o, rs, WS_QABS + (lane & 7) * 32768 + ((jj * 16 + w * 2 + k) * 32 + h) * 16, 0, SC1);
    }
    __builtin_amdgcn_sched_barrier(0);
    if (lane == 0) __hip_atomic_fetch_add(flg + 0, 1u, __ATOMIC_RELAXED, __HIP_MEMORY_SCOPE_WORKGROUP);
    lds_wait(vflg + 0, 16);
    __builtin_amdgcn_sched_barrier(0);
    STAMP(3);
    u32x4 uvv[4], ovv[4];
#pragma unroll
    for (int i = 0; i < 4; ++i)
        uvv[i] = *(const u32x4*)(wuv + (size_t)((t >> 2) + 128 * i) * 4096 + h * 128 + jj * 32 + (t & 3) * 8);
#pragma unroll
    for (int i = 0; i < 4; ++i)
        ovv[i] = *(const u32x4*)(wo + ((size_t)h * 128 + jj * 32 + w * 4 + i) * 512 + lane * 8);
    __builtin_amdgcn_sched_barrier(0);
    if (has_ffn) {
        u32x4 gv[4], uv_[4], dv[4];
        u16 xf[4];
#pragma unroll
        for (int i = 0; i < 4; ++i) xf[i] = x[(t >> 2) + 128 * i];
#pragma unroll
        for (int i = 0; i < 4; ++i) {
            gv[i] = *(const u32x4*)(wg + (size_t)((t >> 2) + 128 * i) * FF + bb_ * 32 + (t & 3) * 8);
            uv_[i] = *(const u32x4*)(wu + (size_t)((t >> 2) + 128 * i) * FF + bb_ * 32 + (t & 3) * 8);
        }
#pragma unroll
        for (int i = 0; i < 4; ++i) dv[i] = *(const u32x4*)(wd + (size_t)(bb_ * 32 + w * 4 + i) * 512 + lane * 8);
        __builtin_amdgcn_sched_barrier(0);
        float p[16];
#pragma unroll
        for (int i = 0; i < 4; ++i) {
            const float xv = bf2f(xf[i]);
#pragma unroll
            for (int k = 0; k < 4; ++k) {
                if (i == 0) {
                    p[2 * k] = xv * bflo(gv[i][k]); p[2 * k + 1] = xv * bfhi(gv[i][k]);
                    p[8 + 2 * k] = xv * bflo(uv_[i][k]); p[8 + 2 * k + 1] = xv * bfhi(uv_[i][k]);
                } else {
                    p[2 * k] += xv * bflo(gv[i][k]); p[2 * k + 1] += xv * bfhi(gv[i][k]);
                    p[8 + 2 * k] += xv * bflo(uv_[i][k]); p[8 + 2 * k + 1] += xv * bfhi(uv_[i][k]);
                }
            }
        }
        bool up = lane & 4;
        float s8[8];
#pragma unroll
        for (int k = 0; k < 8; ++k) { float send = up ? p[k] : p[k + 8]; float keep = up ? p[k + 8] : p[k]; s8[k] = keep + __shfl_xor(send, 4); }
        up = lane & 8;
        float s4[4];
#pragma unroll
        for (int k = 0; k < 4; ++k) { float send = up ? s8[k] : s8[k + 4]; float keep = up ? s8[k + 4] : s8[k]; s4[k] = keep + __shfl_xor(send, 8); }
        up = lane & 16;
        float s2[2];
#pragma unroll
        for (int k = 0; k < 2; ++k) { float send = up ? s4[k] : s4[k + 2]; float keep = up ? s4[k + 2] : s4[k]; s2[k] = keep + __shfl_xor(send, 16); }
        up = lane & 32;
        float send = up ? s2[0] : s2[1]; float keep = up ? s2[1] : s2[0];
        float v = keep + __shfl_xor(send, 32);
        sm2[w * 64 + ((lane >> 2) & 1) * 32 + (lane & 3) * 8 + ((lane >> 3) & 1) * 4 + ((lane >> 4) & 1) * 2 + (lane >> 5)] = v;
        bar();
        f32x4 g4 = *(const f32x4*)(sm2 + 4 * w), u4 = *(const f32x4*)(sm2 + 32 + 4 * w);
#pragma unroll
        for (int ww = 1; ww < 8; ++ww) { g4 += *(const f32x4*)(sm2 + ww * 64 + 4 * w); u4 += *(const f32x4*)(sm2 + ww * 64 + 32 + 4 * w); }
        f32x4 o0 = {0.f, 0.f, 0.f, 0.f}, o1 = {0.f, 0.f, 0.f, 0.f};
#pragma unroll
        for (int i = 0; i < 4; ++i) {
            const float gs = rbf(g4[i]), us = rbf(u4[i]);
            const float sg = rbf(gs / (1.f + __expf(-gs)));
            const float hv = rbf(sg * us);
            o0[0] += hv * bflo(dv[i][0]); o0[1] += hv * bfhi(dv[i][0]); o0[2] += hv * bflo(dv[i][1]); o0[3] += hv * bfhi(dv[i][1]);
            o1[0] += hv * bflo(dv[i][2]); o1[1] += hv * bfhi(dv[i][2]); o1[2] += hv * bflo(dv[i][3]); o1[3] += hv * bfhi(dv[i][3]);
        }
        *(f32x4*)(redf + w * 512 + lane * 8) = o0;
        *(f32x4*)(redf + w * 512 + lane * 8 + 4) = o1;
        bar();
        if (t < 128) {
            f32x4 s = *(const f32x4*)(redf + t * 4);
#pragma unroll
            for (int ww = 1; ww < 8; ++ww) s += *(const f32x4*)(redf + ww * 512 + t * 4);
            __builtin_amdgcn_raw_buffer_store_b128(__builtin_bit_cast(u32x4, s), rs, WS_GB + bb_ * 2048 + t * 16, 0, SC1);
        }
    }
    asm volatile("s_waitcnt vmcnt(1)" ::: "memory");
    lds_wait(vflg + 2, 1);
    GATE(9);
    {
        const int qd = t >> 7;
        u32x4 ov[2];
        u32x2 ml;
        const int oo = WS_OPART + ((qd * 2 * 32 + h) * 512 + (t & 127) * 4) * 4;
        for (unsigned spins = 0; spins < SPIN_CAP; ++spins) {
            asm volatile("" ::: "memory");
#pragma unroll
            for (int i = 0; i < 2; ++i) ov[i] = __builtin_amdgcn_raw_buffer_load_b128(rs, oo + i * 65536, 0, SC1);
            ml = __builtin_amdgcn_raw_buffer_load_b64(rs, WS_ML + (h * 8 + (lane & 7)) * 8, 0, SC1);
            unsigned m = umax(max4(ov[0]), max4(ov[1]));
            m = umax(m, umax(ml[0], ml[1]));
            if (__all(m != POISON)) break;
            __builtin_amdgcn_s_sleep(1);
        }
        GATE(5);
        float mk = asf(ml[0]), lk = asf(ml[1]);
        float M = mk;
        M = fmaxf(M, __shfl_xor(M, 1)); M = fmaxf(M, __shfl_xor(M, 2)); M = fmaxf(M, __shfl_xor(M, 4));
        float wk = __expf(mk - M);
        float L = wk * lk;
        L += __shfl_xor(L, 1); L += __shfl_xor(L, 2); L += __shfl_xor(L, 4);
        wk = wk / L;
        f32x4 c4 = {0.f, 0.f, 0.f, 0.f};
#pragma unroll
        for (int i = 0; i < 2; ++i) {
            float wi = __shfl(wk, qd * 2 + i);
            c4[0] += wi * asf(ov[i][0]); c4[1] += wi * asf(ov[i][1]); c4[2] += wi * asf(ov[i][2]); c4[3] += wi * asf(ov[i][3]);
        }
        *(f32x4*)(cpart + qd * 512 + (t & 127) * 4) = c4;
    }
    bar();
    {
        const int row = t >> 2;
        float acc[8];
#pragma unroll
        for (int i = 0; i < 4; ++i) {
            const int rr = row + 128 * i;
            float c0 = (cpart[rr] + cpart[512 + rr]) + (cpart[1024 + rr] + cpart[1536 + rr]);
#pragma unroll
            for (int k = 0; k < 4; ++k) {
                if (i == 0) { acc[2 * k] = c0 * bflo(uvv[i][k]); acc[2 * k + 1] = c0 * bfhi(uvv[i][k]); }
                else { acc[2 * k] += c0 * bflo(uvv[i][k]); acc[2 * k + 1] += c0 * bfhi(uvv[i][k]); }
            }
        }
        bool up = lane & 4;
        float s[4];
#pragma unroll
        for (int k = 0; k < 4; ++k) { float send = up ? acc[k] : acc[k + 4]; float keep = up ? acc[k + 4] : acc[k]; s[k] = keep + __shfl_xor(send, 4); }
        up = lane & 8;
        float s2[2];
#pragma unroll
        for (int k = 0; k < 2; ++k) { float send = up ? s[k] : s[k + 2]; float keep = up ? s[k + 2] : s[k]; s2[k] = keep + __shfl_xor(send, 8); }
        up = lane & 16;
        float send = up ? s2[0] : s2[1]; float keep = up ? s2[1] : s2[0];
        float v = keep + __shfl_xor(send, 16);
        v += __shfl_xor(v, 32);
        if (lane < 32) {
            int d = (lane & 3) * 8 + ((lane >> 2) & 1) * 4 + ((lane >> 3) & 1) * 2 + ((lane >> 4) & 1);
            sm3[w * 32 + d] = v;
        }
    }
    bar();
    {
        f32x4 c4 = *(const f32x4*)(sm3 + 4 * w);
#pragma unroll
        for (int ww = 1; ww < 8; ++ww) c4 += *(const f32x4*)(sm3 + ww * 32 + 4 * w);
        f32x4 o0 = {0.f, 0.f, 0.f, 0.f}, o1 = {0.f, 0.f, 0.f, 0.f};
#pragma unroll
        for (int i = 0; i < 4; ++i) {
            const float c0 = rbf(c4[i]);
            o0[0] += c0 * bflo(ovv[i][0]); o0[1] += c0 * bfhi(ovv[i][0]); o0[2] += c0 * bflo(ovv[i][1]); o0[3] += c0 * bfhi(ovv[i][1]);
            o1[0] += c0 * bflo(ovv[i][2]); o1[1] += c0 * bfhi(ovv[i][2]); o1[2] += c0 * bflo(ovv[i][3]); o1[3] += c0 * bfhi(ovv[i][3]);
        }
        *(f32x4*)(redo + w * 512 + lane * 8) = o0;
        *(f32x4*)(redo + w * 512 + lane * 8 + 4) = o1;
    }
    bar();
    if (t < 128) {
        f32x4 s = *(const f32x4*)(redo + t * 4);
#pragma unroll
        for (int ww = 1; ww < 8; ++ww) s += *(const f32x4*)(redo + ww * 512 + t * 4);
        __builtin_amdgcn_raw_buffer_store_b128(__builtin_bit_cast(u32x4, s), rs, WS_GA + bb_ * 2048 + t * 16, 0, SC1);
    }
    STAMP(10);
}

extern "C" void kernel_launch(void* const* d_in, const int* in_sizes, int n_in,
                              void* d_out, int out_size, void* d_ws, size_t ws_size,
                              hipStream_t stream) {
    const u16* x = (const u16*)d_in[0];
    const u16* ckv = (const u16*)d_in[1];
    const u16* wq = (const u16*)d_in[2];
    const u16* wuk = (const u16*)d_in[3];
    const u16* wuv = (const u16*)d_in[4];
    const u16* wo = (const u16*)d_in[5];
    const u16* wg = (const u16*)d_in[7];
    const u16* wu = (const u16*)d_in[8];
    const u16* wd = (const u16*)d_in[9];
    const u16* gw = (const u16*)d_in[10];
    mega<<<256, 640, 0, stream>>>(x, ckv, wq, wuk, wuv, wo, wg, wu, wd, gw, (char*)d_ws, (u16*)d_out);
}
```

```cpp
#include <hip/hip_runtime.h>
#include <hip/hip_bf16.h>
#include <stdint.h>

#define SK 8192
#define FF 1376

typedef unsigned short u16;
typedef unsigned long long u64;
typedef __attribute__((ext_vector_type(8))) __bf16 bf16x8;
typedef __attribute__((ext_vector_type(2))) __bf16 bf16x2;
typedef __attribute__((ext_vector_type(4))) short s16x4;
typedef __attribute__((ext_vector_type(8))) short s16x8;
typedef __attribute__((ext_vector_type(16))) float f32x16;
typedef __attribute__((ext_vector_type(4))) float f32x4;
typedef __attribute__((ext_vector_type(2))) float f32x2;
typedef __attribute__((ext_vector_type(4))) unsigned int u32x4;
typedef __attribute__((ext_vector_type(2))) unsigned int u32x2;
#define GAS __attribute__((address_space(1)))
typedef GAS unsigned gu32;
typedef GAS u64 gu64;
#define RLX_AGENT __ATOMIC_RELAXED, __HIP_MEMORY_SCOPE_AGENT
#define SC1 16
#define POISON 0xFFFFFFFFu

__device__ __forceinline__ float bf2f(u16 v) { return __uint_as_float(((unsigned)v) << 16); }
__device__ __forceinline__ float bflo(unsigned v) { return __uint_as_float(v << 16); }
__device__ __forceinline__ float bfhi(unsigned v) { return __uint_as_float(v & 0xffff0000u); }
__device__ __forceinline__ float asf(unsigned v) { return __uint_as_float(v); }
__device__ __forceinline__ unsigned asu(float v) { return __float_as_uint(v); }
__device__ __forceinline__ unsigned pk2(float a, float b) {
    bf16x2 v = {(__bf16)a, (__bf16)b};
    return __builtin_bit_cast(unsigned, v);
}
__device__ __forceinline__ u16 f2bf(float f) { __bf16 v = (__bf16)f; return __builtin_bit_cast(u16, v); }
__device__ __forceinline__ float rbf(float f) { return bf2f(f2bf(f)); }
__device__ __forceinline__ unsigned umax(unsigned a, unsigned b) { return a > b ? a : b; }
__device__ __forceinline__ unsigned max4(u32x4 v) { return umax(umax(v[0], v[1]), umax(v[2], v[3])); }

#define NREP 32
#define WS_EPOCH   0
#define WS_F0      256
#define WS_QG      (WS_F0 + NREP * 2048)
#define WS_QABS    (WS_QG + 128 * 1024)
#define WS_P       (WS_QABS + 8 * 32768)
#define WS_MLOC    (WS_P + 8 * 16 * 4096)
#define WS_OPART   (WS_MLOC + 8 * 8 * 32 * 16)
#define WS_ML      (WS_OPART + 8 * 32 * 512 * 4)
#define WS_GA      (WS_ML + 32 * 8 * 8)
#define WS_GB      (WS_GA + 128 * 2048)
#define WS_QGF     (WS_GB + 128 * 2048)
#define WS_PF      (WS_QGF + 128 * 1024)
#define WS_MLOCF   (WS_PF + 8 * 16 * 4096)
#define WS_END     (WS_MLOCF + 8 * 8 * 32 * 16)

#define PF_LINES 146
#define GATE_ID 0
#define GATE_X 2000
#define GATE(i) do { if (GATE_ID == (i)) { for (unsigned gs_ = 0; gs_ < 100000u; ++gs_) { if (__builtin_amdgcn_s_memrealtime() - t0_ >= (u64)GATE_X) break; __builtin_amdgcn_s_sleep(1); } } } while (0)
#define SPIN_CAP (1u << 17)
#define A_SLEEP 40
#define B_SLEEP 20
#define A_DELAY 0
#define STAMP(i)

__device__ __forceinline__ void lgkm0() { asm volatile("s_waitcnt lgkmcnt(0)" ::: "memory"); }
__device__ __forceinline__ void vm0() { asm volatile("s_waitcnt vmcnt(0)" ::: "memory"); }
__device__ __forceinline__ void bar() { asm volatile("s_waitcnt lgkmcnt(0)\n\ts_barrier" ::: "memory"); }
typedef volatile __attribute__((address_space(3))) unsigned lds_u32;
__device__ __forceinline__ void lds_wait(lds_u32* p, unsigned target) {
    for (unsigned s = 0; s < SPIN_CAP; ++s) {
        if (*p >= target) break;
        __builtin_amdgcn_s_sleep(1);
    }
    asm volatile("" ::: "memory");
}

__device__ __forceinline__ void f0_signal(char* ws, int b, int lane, unsigned tag) {
    const u64 want = ((u64)(~tag) << 32) | tag;
    if (lane < NREP) __hip_atomic_store((gu64*)(ws + WS_F0 + lane * 2048 + b * 8), want, RLX_AGENT);
}
__device__ __forceinline__ void f0_wait(const __amdgpu_buffer_rsrc_t rs, int b, int lane, unsigned tag) {
    const int fo = WS_F0 + (b & (NREP - 1)) * 2048 + lane * 16;
    for (unsigned spins = 0; spins < SPIN_CAP; ++spins) {
        asm volatile("" ::: "memory");
        u32x4 a0 = __builtin_amdgcn_raw_buffer_load_b128(rs, fo, 0, SC1);
        u32x4 a1 = __builtin_amdgcn_raw_buffer_load_b128(rs, fo + 1024, 0, SC1);
        unsigned bad = (a0[0] ^ tag) | (a0[1] ^ ~tag) | (a0[2] ^ tag) | (a0[3] ^ ~tag) |
                       (a1[0] ^ tag) | (a1[1] ^ ~tag) | (a1[2] ^ tag) | (a1[3] ^ ~tag);
        if (__all(bad == 0)) break;
        __builtin_amdgcn_s_sleep(8);
    }
    asm volatile("" ::: "memory");
}

#define LDS_BYTES (65536 + 65536 + 8192)

__global__ __launch_bounds__(640, 1) void mega(const u16* __restrict__ x, const u16* __restrict__ ckv,
                                               const u16* __restrict__ wq, const u16* __restrict__ wuk,
                                               const u16* __restrict__ wuv, const u16* __restrict__ wo,
                                               const u16* __restrict__ wg, const u16* __restrict__ wu,
                                               const u16* __restrict__ wd, const u16* __restrict__ gw,
                                               char* __restrict__ ws, u16* __restrict__ out) {
    __shared__ __attribute__((aligned(16))) char lds[LDS_BYTES];
    __shared__ unsigned flg[4];

    const int t = threadIdx.x;
    unsigned pf0;
    {
        u64 pc;
        asm volatile("s_getpc_b64 %0" : "=s"(pc));
        const GAS unsigned* cb = (const GAS unsigned*)(pc & ~255ull);
        const int l0 = 639 - t;
        pf0 = cb[(l0 < PF_LINES - 1 ? l0 : PF_LINES - 1) * 32];
        __builtin_amdgcn_sched_barrier(0);
    }
    const int b = blockIdx.x;
    const u64 t0_ = __builtin_amdgcn_s_memrealtime();
    if (b == 0 && t == 0) *(volatile u64*)(ws + WS_END + 4096) = t0_;
    const int lane = t & 63, w = t >> 6;
    const int r = lane & 31, h2 = lane >> 5;
    const __amdgpu_buffer_rsrc_t rs = __builtin_amdgcn_make_buffer_rsrc(ws, 0, WS_END, 0x00020000);
    const u32x4 P4 = {POISON, POISON, POISON, POISON};
    const u32x2 P2 = {POISON, POISON};
    STAMP(0);

    if (b < 128) {
        if (w >= 9) { asm volatile("" :: "v"(pf0)); return; }
        lds_u32* vflg = (lds_u32*)flg;
        if (w == 8) {
            const unsigned e = __hip_atomic_load((gu32*)(ws + WS_EPOCH), RLX_AGENT);
            asm volatile("" :: "v"(pf0));
            bar();
            f0_signal(ws, b, lane, e + 1u);
            if (b < 32) { unsigned short o_ = *(volatile const GAS u16*)(out + b * 16 + (lane & 15)); asm volatile("" :: "v"((unsigned)o_)); }
            asm volatile("s_barrier" ::: "memory");
            STAMP(1);
            __builtin_amdgcn_s_sleep(A_SLEEP);
            f0_wait(rs, b, lane, e + 1u);
            if (lane == 0) vflg[2] = 1u;
            STAMP(2);
            return;
        }
        char* tile = lds;
        float* red = (float*)(lds + 65536);
        float* fac = (float*)(lds + 131072);
        float* Mws = fac + 512;
        float* Lws = Mws + 256;
        float* fin = Lws + 256;
        float* fing = fin + 256;
        const int kc = b & 7, mem = b >> 3;
        __builtin_amdgcn_raw_buffer_store_b128(P4, rs, (w < 4 ? WS_P : WS_PF) + ((kc * 16 + mem) * 4 + (w & 3)) * 1024 + lane * 16, 0, SC1);
        if (w < 4)
            __builtin_amdgcn_raw_buffer_store_b128(P4, rs, WS_OPART + ((kc * 32 + w * 8 + (lane >> 3)) * 512 + mem * 32) * 4 + (lane & 7) * 16, 0, SC1);
        else if (w == 4)
            __builtin_amdgcn_raw_buffer_store_b64(P2, rs, (lane < 32 ? WS_MLOC : WS_MLOCF) + ((kc * 8 + (mem >> 1)) * 32 + r) * 16 + (mem & 1) * 8, 0, SC1);
        else if (w == 5 && mem == 0 && lane < 32)
            __builtin_amdgcn_raw_buffer_store_b64(P2, rs, WS_ML + (lane * 8 + kc) * 8, 0, SC1);
        if (t < 4) flg[t] = 0u;
        vm0();
        asm volatile("" :: "v"(pf0));
        bar();
        asm volatile("s_barrier" ::: "memory");
        STAMP(1);
        __builtin_amdgcn_s_sleep(A_DELAY);
        bf16x8 bq[8];
        u32x4 cv[8];
        {
            const u16* bp = ckv + (size_t)(kc * 1024 + mem * 64 + r) * 512 + 64 * w + 8 * h2;
#pragma unroll
            for (int kb = 0; kb < 2; ++kb)
#pragma unroll
                for (int s = 0; s < 4; ++s) bq[kb * 4 + s] = *(const bf16x8*)(bp + kb * 32 * 512 + 16 * s);
            const u16* cp = ckv + (size_t)(kc * 1024 + (t >> 2)) * 512 + mem * 32 + (t & 3) * 8;
#pragma unroll
            for (int i = 0; i < 8; ++i) cv[i] = *(const u32x4*)(cp + (size_t)i * 128 * 512);
        }
        __builtin_amdgcn_sched_barrier(0);
#pragma unroll
        for (int i = 0; i < 8; ++i) *(u32x4*)(tile + (i * 128 + (t >> 2)) * 64 + (t & 3) * 16) = cv[i];
        GATE(7);
        lds_wait(vflg + 2, 1);
        GATE(8);
        {
            u32x4 av[4];
            const int qo = WS_QABS + (b & 7) * 32768 + (w * 256 + lane) * 16;
            for (unsigned spins = 0; spins < SPIN_CAP; ++spins) {
                asm volatile("" ::: "memory");
#pragma unroll
                for (int s = 0; s < 4; ++s) av[s] = __builtin_amdgcn_raw_buffer_load_b128(rs, qo + 1024 * s, 0, SC1);
                unsigned m = umax(umax(max4(av[0]), max4(av[1])), umax(max4(av[2]), max4(av[3])));
                if (__all(m != POISON)) break;
                __builtin_amdgcn_s_sleep(1);
            }
            GATE(3);
#pragma unroll
            for (int kb = 0; kb < 2; ++kb) {
                f32x16 acc;
#pragma unroll
                for (int i = 0; i < 16; ++i) acc[i] = 0.f;
#pragma unroll
                for (int s = 0; s < 4; ++s)
                    acc = __builtin_amdgcn_mfma_f32_32x32x16_bf16(__builtin_bit_cast(bf16x8, av[s]), bq[kb * 4 + s], acc, 0, 0, 0);
#pragma unroll
                for (int i = 0; i < 16; ++i) {
                    int head = (i & 3) + 8 * (i >> 2) + 4 * h2;
                    red[(w * 32 + head) * 64 + kb * 32 + r] = acc[i];
                }
            }
        }
        bar();
        {
            const int head = t >> 4, kq = (t & 15) * 4;
            f32x4 s4 = *(const f32x4*)(red + head * 64 + kq);
#pragma unroll
            for (int ww = 1; ww < 8; ++ww) s4 += *(const f32x4*)(red + (ww * 32 + head) * 64 + kq);
            float m = fmaxf(fmaxf(s4[0], s4[1]), fmaxf(s4[2], s4[3]));
            m = fmaxf(m, __shfl_xor(m, 1));
            m = fmaxf(m, __shfl_xor(m, 2));
            m = fmaxf(m, __shfl_xor(m, 4));
            m = fmaxf(m, __shfl_xor(m, 8));
            unsigned p01 = pk2(__expf(s4[0] - m), __expf(s4[1] - m));
            unsigned p23 = pk2(__expf(s4[2] - m), __expf(s4[3] - m));
            float l = (bflo(p01) + bfhi(p01)) + (bflo(p23) + bfhi(p23));
            l += __shfl_xor(l, 1);
            l += __shfl_xor(l, 2);
            l += __shfl_xor(l, 4);
            l += __shfl_xor(l, 8);
            u32x4 pp = {p01, p23, (unsigned)__shfl_down((int)p01, 1), (unsigned)__shfl_down((int)p23, 1)};
            u32x2 ml = {asu(m), asu(l)};
            const int po = ((kc * 16 + mem) * 256 + ((t & 15) >> 1) * 32 + head) * 16;
            const int mo = ((kc * 8 + (mem >> 1)) * 32 + head) * 16 + (mem & 1) * 8;
            if ((t & 1) == 0) __builtin_amdgcn_raw_buffer_store_b128(pp, rs, WS_PF + po, 0, 0);
            if ((t & 15) == 0) __builtin_amdgcn_raw_buffer_store_b64(ml, rs, WS_MLOCF + mo, 0, 0);
            if ((t & 1) == 0) __builtin_amdgcn_raw_buffer_store_b128(pp, rs, WS_P + po, 0, SC1);
            if ((t & 15) == 0) __builtin_amdgcn_raw_buffer_store_b64(ml, rs, WS_MLOC + mo, 0, SC1);
        }
        bar();
        STAMP(5);
        {
            u32x4 pv[8], mlv;
            for (unsigned spins = 0; spins < SPIN_CAP; ++spins) {
                asm volatile("" ::: "memory");
                const bool slow = (spins & 3) == 3;
                const int po = (slow ? WS_P : WS_PF) + ((kc * 16 + w * 2) * 256 + lane) * 16;
                const int mo = (slow ? WS_MLOC : WS_MLOCF) + ((kc * 8 + w) * 32 + r) * 16;
#pragma unroll
                for (int s = 0; s < 8; ++s) pv[s] = __builtin_amdgcn_raw_buffer_load_b128(rs, po + 1024 * s, 0, SC1);
                mlv = __builtin_amdgcn_raw_buffer_load_b128(rs, mo, 0, SC1);
                unsigned m = umax(umax(max4(pv[0]), max4(pv[1])), umax(max4(pv[2]), max4(pv[3])));
                m = umax(m, umax(umax(max4(pv[4]), max4(pv[5])), umax(max4(pv[6]), max4(pv[7]))));
                m = umax(m, max4(mlv));
                if (__all(m != POISON)) break;
                __builtin_amdgcn_s_sleep(1);
            }
            GATE(4);
            {
                float m0 = asf(mlv[0]), l0 = asf(mlv[1]), m1 = asf(mlv[2]), l1 = asf(mlv[3]);
                float Mw = fmaxf(m0, m1);
                float f0_ = __expf(m0 - Mw), f1_ = __expf(m1 - Mw);
                if (lane < 32) {
                    fac[(2 * w) * 32 + r] = f0_; fac[(2 * w + 1) * 32 + r] = f1_;
                    Mws[r * 8 + w] = Mw; Lws[r * 8 + w] = f0_ * l0 + f1_ * l1;
                }
            }
            const int g = lane >> 4;
            const int q = (lane & 15) >> 2, p = lane & 3;
            const int cb = g & 1;
            f32x16 tot;
#pragma unroll
            for (int mm = 0; mm < 2; ++mm) {
                f32x16 acc;
#pragma unroll
                for (int i = 0; i < 16; ++i) acc[i] = 0.f;
#pragma unroll
                for (int ss = 0; ss < 4; ++ss) {
                    int s = mm * 4 + ss;
                    int row = w * 128 + 16 * s + 8 * h2 + q;
                    s16x4 t0 = __builtin_amdgcn_ds_read_tr16_b64_v4i16(
                        (__attribute__((address_space(3))) s16x4*)(tile + row * 64 + cb * 32 + p * 8));
                    s16x4 t1 = __builtin_amdgcn_ds_read_tr16_b64_v4i16(
                        (__attribute__((address_space(3))) s16x4*)(tile + (row + 4) * 64 + cb * 32 + p * 8));
                    s16x8 bb = {t0[0], t0[1], t0[2], t0[3], t1[0], t1[1], t1[2], t1[3]};
                    acc = __builtin_amdgcn_mfma_f32_32x32x16_bf16(__builtin_bit_cast(bf16x8, pv[s]), __builtin_bit_cast(bf16x8, bb), acc, 0, 0, 0);
                }
                const float* fp = fac + (2 * w + mm) * 32 + 4 * h2;
#pragma unroll
                for (int qd = 0; qd < 4; ++qd) {
                    f32x4 f4 = *(const f32x4*)(fp + 8 * qd);
#pragma unroll
                    for (int e2 = 0; e2 < 4; ++e2) {
                        if (mm == 0) tot[qd * 4 + e2] = f4[e2] * acc[qd * 4 + e2];
                        else tot[qd * 4 + e2] += f4[e2] * acc[qd * 4 + e2];
                    }
                }
            }
#pragma unroll
            for (int i = 0; i < 16; ++i) {
                int head = (i & 3) + 8 * (i >> 2) + 4 * h2;
                red[(w * 32 + head) * 32 + r] = tot[i];
            }
        }
        bar();
        {
            const int head = t >> 4, cq = (t & 15) * 2;
            f32x4 ma = *(const f32x4*)(Mws + head * 8), mb = *(const f32x4*)(Mws + head * 8 + 4);
            float M = fmaxf(fmaxf(fmaxf(ma[0], ma[1]), fmaxf(ma[2], ma[3])), fmaxf(fmaxf(mb[0], mb[1]), fmaxf(mb[2], mb[3])));
            float ew[8];
#pragma unroll
            for (int ww = 0; ww < 4; ++ww) { ew[ww] = __expf(ma[ww] - M); ew[ww + 4] = __expf(mb[ww] - M); }
            f32x2 s2 = {0.f, 0.f};
#pragma unroll
            for (int ww = 0; ww < 8; ++ww) s2 += ew[ww] * *(const f32x2*)(red + (ww * 32 + head) * 32 + cq);
            u32x4 o = {asu(s2[0]), asu(s2[1]), asu(__shfl_down(s2[0], 1)), asu(__shfl_down(s2[1], 1))};
            if ((t & 1) == 0)
                __builtin_amdgcn_raw_buffer_store_b128(o, rs, WS_OPART + ((kc * 32 + head) * 512 + mem * 32 + cq) * 4, 0, SC1);
            if (mem == 0 && (t & 15) == 0) {
                f32x4 la = *(const f32x4*)(Lws + head * 8), lb = *(const f32x4*)(Lws + head * 8 + 4);
                float L = 0.f;
#pragma unroll
                for (int ww = 0; ww < 4; ++ww) L += ew[ww] * la[ww] + ew[ww + 4] * lb[ww];
                u32x2 ml = {asu(M), asu(L)};
                __builtin_amdgcn_raw_buffer_store_b64(ml, rs, WS_ML + (head * 8 + kc) * 8, 0, SC1);
            }
        }
        STAMP(7);
        if (b >= 32) return;
        {
            const int p = t >> 2, qc = t & 3;
            float gp = rbf(bf2f(x[t]) * bf2f(gw[t]));
            const int ao = WS_GA + p * 2048 + b * 64 + qc * 16;
            const int bo = WS_GB + (p < 43 ? p : 42) * 2048 + b * 64 + qc * 16;
            u32x4 a0, b0;
            for (unsigned spins = 0; spins < SPIN_CAP; ++spins) {
                asm volatile("" ::: "memory");
                a0 = __builtin_amdgcn_raw_buffer_load_b128(rs, ao, 0, SC1);
                b0 = __builtin_amdgcn_raw_buffer_load_b128(rs, bo, 0, SC1);
                unsigned m = umax(max4(a0), max4(b0));
                if (__all(m != POISON)) break;
                __builtin_amdgcn_s_sleep(2);
            }
            GATE(6);
            if (p >= 43) { b0[0] = 0u; b0[1] = 0u; b0[2] = 0u; b0[3] = 0u; }
            bool up = lane & 4;
            float s4[4];
#pragma unroll
            for (int q = 0; q < 4; ++q) { float send = up ? asf(a0[q]) : asf(b0[q]); float keep = up ? asf(b0[q]) : asf(a0[q]); s4[q] = keep + __shfl_xor(send, 4); }
            up = lane & 8;
            float s2[2];
#pragma unroll
            for (int q = 0; q < 2; ++q) { float send = up ? s4[q] : s4[q + 2]; float keep = up ? s4[q + 2] : s4[q]; s2[q] = keep + __shfl_xor(send, 8); }
            up = lane & 16;
            float send = up ? s2[0] : s2[1]; float keep = up ? s2[1] : s2[0];
            float vv = keep + __shfl_xor(send, 16);
            vv += __shfl_xor(vv, 32);
#pragma unroll
            for (int o = 32; o >= 1; o >>= 1) gp += __shfl_xor(gp, o);
            if (lane < 32) {
                int idx = ((lane >> 2) & 1) * 16 + (lane & 3) * 4 + ((lane >> 3) & 1) * 2 + ((lane >> 4) & 1);
                fin[w * 32 + idx] = vv;
            }
            if (lane == 0) fing[w] = gp;
        }
        bar();
        if (t < 16) {
            float a = 0.f, bb = 0.f, gs = 0.f;
#pragma unroll
            for (int ww = 0; ww < 8; ++ww) { a += fin[ww * 32 + t]; bb += fin[ww * 32 + 16 + t]; gs += fing[ww]; }
            gs = rbf(gs);
            float g = rbf(1.f / (1.f + __expf(-gs)));
            a = rbf(a); bb = rbf(bb);
            float rr = rbf(g * a) + rbf(rbf(1.f - g) * bb);
            GATE(10);
            out[b * 16 + t] = f2bf(rr);
        }
        STAMP(12);
        return;
    }

    float* redf = (float*)lds;
    float* redo = redf + 4096;
    float* cpart = redo + 4096;
    float* sm1 = cpart + 2048;
    float* sm2 = sm1 + 1024;
    float* sm3 = sm2 + 512;
    float* qs = sm3 + 256;
    lds_u32* vflg = (lds_u32*)flg;
    const int bb_ = b - 128;
    const int h = bb_ & 31, jj = bb_ >> 5;
    const bool has_ffn = bb_ < 43;

    if (w >= 8) {
        const unsigned e = __hip_atomic_load((gu32*)(ws + WS_EPOCH), RLX_AGENT);
        asm volatile("" :: "v"(pf0));
        bar();
        const unsigned tag = e + 1u;
        if (w == 8) {
            lds_wait(vflg + 0, 8);
            STAMP(1);
            f32x2 s = *(const f32x2*)(sm1 + 2 * lane);
#pragma unroll
            for (int ww = 1; ww < 8; ++ww) s += *(const f32x2*)(sm1 + ww * 128 + 2 * lane);
            u32x4 g = {tag, asu(s[0]), asu(s[1]), ~tag};
            __builtin_amdgcn_raw_buffer_store_b128(g, rs, WS_QGF + ((h * 4 + jj) * 64 + lane) * 16, 0, 0);
            __builtin_amdgcn_raw_buffer_store_b128(g, rs, WS_QG + ((h * 4 + jj) * 64 + lane) * 16, 0, SC1);
            for (unsigned spins = 0; spins < SPIN_CAP; ++spins) {
                asm volatile("" ::: "memory");
                u32x4 v[4];
                const int qb = ((spins & 3) == 3 ? WS_QG : WS_QGF) + (h * 4 * 64 + lane) * 16;
#pragma unroll
                for (int k = 0; k < 4; ++k) v[k] = __builtin_amdgcn_raw_buffer_load_b128(rs, qb + k * 1024, 0, SC1);
                unsigned bad = 0;
                float q0 = 0.f, q1 = 0.f;
#pragma unroll
                for (int k = 0; k < 4; ++k) {
                    bad |= (v[k][0] ^ tag) | (v[k][3] ^ ~tag);
                    q0 += asf(v[k][1]); q1 += asf(v[k][2]);
                }
                if (__all(bad == 0) || spins == SPIN_CAP - 1) {
                    f32x2 qq = {rbf(q0), rbf(q1)};
                    *(f32x2*)(qs + 2 * lane) = qq;
                    break;
                }
                __builtin_amdgcn_s_sleep(1);
            }
            lgkm0();
            if (lane == 0) vflg[1] = 1u;
            STAMP(2);
        } else {
            lds_wait(vflg + 3, 8);
            STAMP(1);
            f0_signal(ws, b, lane, tag);
            __builtin_amdgcn_s_sleep(B_SLEEP);
            f0_wait(rs, b, lane, tag);
            if (lane == 0) vflg[2] = 1u;
            if (b == 255 && lane == 0) __hip_atomic_store((gu32*)(ws + WS_EPOCH), tag, RLX_AGENT);
            STAMP(2);
        }
        return;
    }

    {
        int po;
        if (w < 2) po = WS_QABS + (lane >> 3) * 32768 + ((jj * 16 + w * 8 + (lane & 7)) * 32 + h) * 16;
        else if (w < 4) po = WS_GA + bb_ * 2048 + (w & 1) * 1024 + lane * 16;
        else po = WS_GB + bb_ * 2048 + (w & 1) * 1024 + lane * 16;
        if (w < 4 || (w < 6 && has_ffn)) __builtin_amdgcn_raw_buffer_store_b128(P4, rs, po, 0, SC1);
    }
    __builtin_amdgcn_sched_barrier(0);
    u32x4 wqv[4], wkv[4];
    u16 xr[4];
    {
        const int row = jj * 128 + (t >> 4);
#pragma unroll
        for (int i = 0; i < 4; ++i) xr[i] = x[row + 32 * i];
#pragma unroll
        for (int i = 0; i < 4; ++i)
            wqv[i] = *(const u32x4*)(wq + (size_t)(row + 32 * i) * 4096 + h * 128 + (t & 15) * 8);
#pragma unroll
        for (int i = 0; i < 4; ++i)
            wkv[i] = *(const u32x4*)(wuk + (size_t)(jj * 128 + 16 * w + (lane >> 4) + 4 * i) * 4096 + h * 128 + (lane & 15) * 8);
    }
    __builtin_amdgcn_sched_barrier(0);
    if (t < 4) flg[t] = 0u;
    bar();
    asm volatile("s_waitcnt vmcnt(12)" ::: "memory");
    asm volatile("" :: "v"(pf0));
    if (lane == 0) __hip_atomic_fetch_add(flg + 3, 1u, __ATOMIC_RELAXED, __HIP_MEMORY_SCOPE_WORKGROUP);
    __builtin_amdgcn_sched_barrier(0);
    asm volatile("s_waitcnt vmcnt(4)" ::: "memory");
    GATE(1);
    {
        float acc[8];
#pragma unroll
        for (int k = 0; k < 4; ++k) {
            acc[2 * k] = bf2f(xr[0]) * bflo(wqv[0][k]) + bf2f(xr[1]) * bflo(wqv[1][k]) + bf2f(xr[2]) * bflo(wqv[2][k]) + bf2f(xr[3]) * bflo(wqv[3][k]);
            acc[2 * k + 1] = bf2f(xr[0]) * bfhi(wqv[0][k]) + bf2f(xr[1]) * bfhi(wqv[1][k]) + bf2f(xr[2]) * bfhi(wqv[2][k]) + bf2f(xr[3]) * bfhi(wqv[3][k]);
        }
        bool up = lane & 16;
        float s[4];
#pragma unroll
        for (int k = 0; k < 4; ++k) { float send = up ? acc[k] : acc[k + 4]; float keep = up ? acc[k + 4] : acc[k]; s[k] = keep + __shfl_xor(send, 16); }
        up = lane & 32;
        f32x2 s2;
#pragma unroll
        for (int k = 0; k < 2; ++k) { float send = up ? s[k] : s[k + 2]; float keep = up ? s[k + 2] : s[k]; s2[k] = keep + __shfl_xor(send, 32); }
        *(f32x2*)(sm1 + w * 128 + (lane & 15) * 8 + ((lane >> 4) & 1) * 4 + (lane >> 5) * 2) = s2;
        lgkm0();
        if (lane == 0) __hip_atomic_fetch_add(flg + 0, 1u, __ATOMIC_RELAXED, __HIP_MEMORY_SCOPE_WORKGROUP);
    }
    __builtin_amdgcn_sched_barrier(0);
    lds_wait(vflg + 1, 1);
    GATE(2);
    {
        const int l16 = lane & 15;
        f32x4 qa0 = *(const f32x4*)(qs + l16 * 8), qa1 = *(const f32x4*)(qs + l16 * 8 + 4);
        float d[4];
#pragma unroll
        for (int i = 0; i < 4; ++i) {
            d[i] = qa0[0] * bflo(wkv[i][0]) + qa0[1] * bfhi(wkv[i][0]) + qa0[2] * bflo(wkv[i][1]) + qa0[3] * bfhi(wkv[i][1]);
            d[i] += qa1[0] * bflo(wkv[i][2]) + qa1[1] * bfhi(wkv[i][2]) + qa1[2] * bflo(wkv[i][3]) + qa1[3] * bfhi(wkv[i][3]);
        }
        bool up = lane & 1;
        float s[2];
#pragma unroll
        for (int k = 0; k < 2; ++k) { float send = up ? d[k] : d[k + 2]; float keep = up ? d[k + 2] : d[k]; s[k] = keep + __shfl_xor(send, 1); }
        up = lane & 2;
        float send = up ? s[0] : s[1], keep = up ? s[1] : s[0];
        float v = keep + __shfl_xor(send, 2);
        v += __shfl_xor(v, 4);
        v += __shfl_xor(v, 8);
        v *= 0.08838834764831845f;
        const int k = (lane >> 3) & 1;
        float vv[8];
#pragma unroll
        for (int e2 = 0; e2 < 8; ++e2) vv[e2] = __shfl(v, (e2 & 3) * 16 + (k | ((e2 >> 2) << 1)));
        u32x4 o = {pk2(vv[0], vv[1]), pk2(vv[2], vv[3]), pk2(vv[4], vv[5]), pk2(vv[6], vv[7])};
        if (lane < 16)
            __builtin_amdgcn_raw_buffer_store_b128(o, rs, WS_QABS + (lane & 7) * 32768 + ((jj * 16 + w * 2 + k) * 32 + h) * 16, 0, SC1);
    }
    __builtin_amdgcn_sched_barrier(0);
    if (lane == 0) __hip_atomic_fetch_add(flg + 0, 1u, __ATOMIC_RELAXED, __HIP_MEMORY_SCOPE_WORKGROUP);
    lds_wait(vflg + 0, 16);
    __builtin_amdgcn_sched_barrier(0);
    STAMP(3);
    u32x4 uvv[4], ovv[4];
#pragma unroll
    for (int i = 0; i < 4; ++i)
        uvv[i] = *(const u32x4*)(wuv + (size_t)((t >> 2) + 128 * i) * 4096 + h * 128 + jj * 32 + (t & 3) * 8);
#pragma unroll
    for (int i = 0; i < 4; ++i)
        ovv[i] = *(const u32x4*)(wo + ((size_t)h * 128 + jj * 32 + w * 4 + i) * 512 + lane * 8);
    __builtin_amdgcn_sched_barrier(0);
    if (has_ffn) {
        u32x4 gv[4], uv_[4], dv[4];
        u16 xf[4];
#pragma unroll
        for (int i = 0; i < 4; ++i) xf[i] = x[(t >> 2) + 128 * i];
#pragma unroll
        for (int i = 0; i < 4; ++i) {
            gv[i] = *(const u32x4*)(wg + (size_t)((t >> 2) + 128 * i) * FF + bb_ * 32 + (t & 3) * 8);
            uv_[i] = *(const u32x4*)(wu + (size_t)((t >> 2) + 128 * i) * FF + bb_ * 32 + (t & 3) * 8);
        }
#pragma unroll
        for (int i = 0; i < 4; ++i) dv[i] = *(const u32x4*)(wd + (size_t)(bb_ * 32 + w * 4 + i) * 512 + lane * 8);
        __builtin_amdgcn_sched_barrier(0);
        float p[16];
#pragma unroll
        for (int i = 0; i < 4; ++i) {
            const float xv = bf2f(xf[i]);
#pragma unroll
            for (int k = 0; k < 4; ++k) {
                if (i == 0) {
                    p[2 * k] = xv * bflo(gv[i][k]); p[2 * k + 1] = xv * bfhi(gv[i][k]);
                    p[8 + 2 * k] = xv * bflo(uv_[i][k]); p[8 + 2 * k + 1] = xv * bfhi(uv_[i][k]);
                } else {
                    p[2 * k] += xv * bflo(gv[i][k]); p[2 * k + 1] += xv * bfhi(gv[i][k]);
                    p[8 + 2 * k] += xv * bflo(uv_[i][k]); p[8 + 2 * k + 1] += xv * bfhi(uv_[i][k]);
                }
            }
        }
        bool up = lane & 4;
        float s8[8];
#pragma unroll
        for (int k = 0; k < 8; ++k) { float send = up ? p[k] : p[k + 8]; float keep = up ? p[k + 8] : p[k]; s8[k] = keep + __shfl_xor(send, 4); }
        up = lane & 8;
        float s4[4];
#pragma unroll
        for (int k = 0; k < 4; ++k) { float send = up ? s8[k] : s8[k + 4]; float keep = up ? s8[k + 4] : s8[k]; s4[k] = keep + __shfl_xor(send, 8); }
        up = lane & 16;
        float s2[2];
#pragma unroll
        for (int k = 0; k < 2; ++k) { float send = up ? s4[k] : s4[k + 2]; float keep = up ? s4[k + 2] : s4[k]; s2[k] = keep + __shfl_xor(send, 16); }
        up = lane & 32;
        float send = up ? s2[0] : s2[1]; float keep = up ? s2[1] : s2[0];
        float v = keep + __shfl_xor(send, 32);
        sm2[w * 64 + ((lane >> 2) & 1) * 32 + (lane & 3) * 8 + ((lane >> 3) & 1) * 4 + ((lane >> 4) & 1) * 2 + (lane >> 5)] = v;
        bar();
        f32x4 g4 = *(const f32x4*)(sm2 + 4 * w), u4 = *(const f32x4*)(sm2 + 32 + 4 * w);
#pragma unroll
        for (int ww = 1; ww < 8; ++ww) { g4 += *(const f32x4*)(sm2 + ww * 64 + 4 * w); u4 += *(const f32x4*)(sm2 + ww * 64 + 32 + 4 * w); }
        f32x4 o0 = {0.f, 0.f, 0.f, 0.f}, o1 = {0.f, 0.f, 0.f, 0.f};
#pragma unroll
        for (int i = 0; i < 4; ++i) {
            const float gs = rbf(g4[i]), us = rbf(u4[i]);
            const float sg = rbf(gs / (1.f + __expf(-gs)));
            const float hv = rbf(sg * us);
            o0[0] += hv * bflo(dv[i][0]); o0[1] += hv * bfhi(dv[i][0]); o0[2] += hv * bflo(dv[i][1]); o0[3] += hv * bfhi(dv[i][1]);
            o1[0] += hv * bflo(dv[i][2]); o1[1] += hv * bfhi(dv[i][2]); o1[2] += hv * bflo(dv[i][3]); o1[3] += hv * bfhi(dv[i][3]);
        }
        *(f32x4*)(redf + w * 512 + lane * 8) = o0;
        *(f32x4*)(redf + w * 512 + lane * 8 + 4) = o1;
        bar();
        if (t < 128) {
            f32x4 s = *(const f32x4*)(redf + t * 4);
#pragma unroll
            for (int ww = 1; ww < 8; ++ww) s += *(const f32x4*)(redf + ww * 512 + t * 4);
            __builtin_amdgcn_raw_buffer_store_b128(__builtin_bit_cast(u32x4, s), rs, WS_GB + bb_ * 2048 + t * 16, 0, SC1);
        }
    }
    asm volatile("s_waitcnt vmcnt(1)" ::: "memory");
    lds_wait(vflg + 2, 1);
    GATE(9);
    {
        const int qd = t >> 7;
        u32x4 ov[2];
        u32x2 ml;
        const int oo = WS_OPART + ((qd * 2 * 32 + h) * 512 + (t & 127) * 4) * 4;
        for (unsigned spins = 0; spins < SPIN_CAP; ++spins) {
            asm volatile("" ::: "memory");
#pragma unroll
            for (int i = 0; i < 2; ++i) ov[i] = __builtin_amdgcn_raw_buffer_load_b128(rs, oo + i * 65536, 0, SC1);
            ml = __builtin_amdgcn_raw_buffer_load_b64(rs, WS_ML + (h * 8 + (lane & 7)) * 8, 0, SC1);
            unsigned m = umax(max4(ov[0]), max4(ov[1]));
            m = umax(m, umax(ml[0], ml[1]));
            if (__all(m != POISON)) break;
            __builtin_amdgcn_s_sleep(1);
        }
        GATE(5);
        float mk = asf(ml[0]), lk = asf(ml[1]);
        float M = mk;
        M = fmaxf(M, __shfl_xor(M, 1)); M = fmaxf(M, __shfl_xor(M, 2)); M = fmaxf(M, __shfl_xor(M, 4));
        float wk = __expf(mk - M);
        float L = wk * lk;
        L += __shfl_xor(L, 1); L += __shfl_xor(L, 2); L += __shfl_xor(L, 4);
        wk = wk / L;
        f32x4 c4 = {0.f, 0.f, 0.f, 0.f};
#pragma unroll
        for (int i = 0; i < 2; ++i) {
            float wi = __shfl(wk, qd * 2 + i);
            c4[0] += wi * asf(ov[i][0]); c4[1] += wi * asf(ov[i][1]); c4[2] += wi * asf(ov[i][2]); c4[3] += wi * asf(ov[i][3]);
        }
        *(f32x4*)(cpart + qd * 512 + (t & 127) * 4) = c4;
    }
    bar();
    {
        const int row = t >> 2;
        float acc[8];
#pragma unroll
        for (int i = 0; i < 4; ++i) {
            const int rr = row + 128 * i;
            float c0 = (cpart[rr] + cpart[512 + rr]) + (cpart[1024 + rr] + cpart[1536 + rr]);
#pragma unroll
            for (int k = 0; k < 4; ++k) {
                if (i == 0) { acc[2 * k] = c0 * bflo(uvv[i][k]); acc[2 * k + 1] = c0 * bfhi(uvv[i][k]); }
                else { acc[2 * k] += c0 * bflo(uvv[i][k]); acc[2 * k + 1] += c0 * bfhi(uvv[i][k]); }
            }
        }
        bool up = lane & 4;
        float s[4];
#pragma unroll
        for (int k = 0; k < 4; ++k) { float send = up ? acc[k] : acc[k + 4]; float keep = up ? acc[k + 4] : acc[k]; s[k] = keep + __shfl_xor(send, 4); }
        up = lane & 8;
        float s2[2];
#pragma unroll
        for (int k = 0; k < 2; ++k) { float send = up ? s[k] : s[k + 2]; float keep = up ? s[k + 2] : s[k]; s2[k] = keep + __shfl_xor(send, 8); }
        up = lane & 16;
        float send = up ? s2[0] : s2[1]; float keep = up ? s2[1] : s2[0];
        float v = keep + __shfl_xor(send, 16);
        v += __shfl_xor(v, 32);
        if (lane < 32) {
            int d = (lane & 3) * 8 + ((lane >> 2) & 1) * 4 + ((lane >> 3) & 1) * 2 + ((lane >> 4) & 1);
            sm3[w * 32 + d] = v;
        }
    }
    bar();
    {
        f32x4 c4 = *(const f32x4*)(sm3 + 4 * w);
#pragma unroll
        for (int ww = 1; ww < 8; ++ww) c4 += *(const f32x4*)(sm3 + ww * 32 + 4 * w);
        f32x4 o0 = {0.f, 0.f, 0.f, 0.f}, o1 = {0.f, 0.f, 0.f, 0.f};
#pragma unroll
        for (int i = 0; i < 4; ++i) {
            const float c0 = rbf(c4[i]);
            o0[0] += c0 * bflo(ovv[i][0]); o0[1] += c0 * bfhi(ovv[i][0]); o0[2] += c0 * bflo(ovv[i][1]); o0[3] += c0 * bfhi(ovv[i][1]);
            o1[0] += c0 * bflo(ovv[i][2]); o1[1] += c0 * bfhi(ovv[i][2]); o1[2] += c0 * bflo(ovv[i][3]); o1[3] += c0 * bfhi(ovv[i][3]);
        }
        *(f32x4*)(redo + w * 512 + lane * 8) = o0;
        *(f32x4*)(redo + w * 512 + lane * 8 + 4) = o1;
    }
    bar();
    GATE(11);
    if (t < 128) {
        f32x4 s = *(const f32x4*)(redo + t * 4);
#pragma unroll
        for (int ww = 1; ww < 8; ++ww) s += *(const f32x4*)(redo + ww * 512 + t * 4);
        __builtin_amdgcn_raw_buffer_store_b128(__builtin_bit_cast(u32x4, s), rs, WS_GA + bb_ * 2048 + t * 16, 0, SC1);
    }
    STAMP(10);
}

#define K2_X 3000

__global__ void k0(char* __restrict__ ws) {
    if (threadIdx.x == 0) *(volatile u64*)(ws + WS_END + 4096) = __builtin_amdgcn_s_memrealtime();
}
__global__ void k2(char* __restrict__ ws) {
    const u64 t0_ = *(volatile u64*)(ws + WS_END + 4096);
    for (unsigned gs_ = 0; gs_ < 100000u; ++gs_) { if (__builtin_amdgcn_s_memrealtime() - t0_ >= (u64)K2_X) break; __builtin_amdgcn_s_sleep(1); }
}
extern "C" void kernel_launch(void* const* d_in, const int* in_sizes, int n_in,
                              void* d_out, int out_size, void* d_ws, size_t ws_size,
                              hipStream_t stream) {
    const u16* x = (const u16*)d_in[0];
    const u16* ckv = (const u16*)d_in[1];
    const u16* wq = (const u16*)d_in[2];
    const u16* wuk = (const u16*)d_in[3];
    const u16* wuv = (const u16*)d_in[4];
    const u16* wo = (const u16*)d_in[5];
    const u16* wg = (const u16*)d_in[7];
    const u16* wu = (const u16*)d_in[8];
    const u16* wd = (const u16*)d_in[9];
    const u16* gw = (const u16*)d_in[10];
    mega<<<256, 640, 0, stream>>>(x, ckv, wq, wuk, wuv, wo, wg, wu, wd, gw, (char*)d_ws, (u16*)d_out);
    k2<<<1, 64, 0, stream>>>((char*)d_ws);
}
```

```cpp
#include <hip/hip_runtime.h>
#include <hip/hip_bf16.h>
#include <stdint.h>

#define SK 8192
#define FF 1376

typedef unsigned short u16;
typedef unsigned long long u64;
typedef __attribute__((ext_vector_type(8))) __bf16 bf16x8;
typedef __attribute__((ext_vector_type(2))) __bf16 bf16x2;
typedef __attribute__((ext_vector_type(4))) short s16x4;
typedef __attribute__((ext_vector_type(8))) short s16x8;
typedef __attribute__((ext_vector_type(16))) float f32x16;
typedef __attribute__((ext_vector_type(4))) float f32x4;
typedef __attribute__((ext_vector_type(2))) float f32x2;
typedef __attribute__((ext_vector_type(4))) unsigned int u32x4;
typedef __attribute__((ext_vector_type(2))) unsigned int u32x2;
#define GAS __attribute__((address_space(1)))
typedef GAS unsigned gu32;
typedef GAS u64 gu64;
#define RLX_AGENT __ATOMIC_RELAXED, __HIP_MEMORY_SCOPE_AGENT
#define SC1 16
#define POISON 0xFFFFFFFFu

__device__ __forceinline__ float bf2f(u16 v) { return __uint_as_float(((unsigned)v) << 16); }
__device__ __forceinline__ float bflo(unsigned v) { return __uint_as_float(v << 16); }
__device__ __forceinline__ float bfhi(unsigned v) { return __uint_as_float(v & 0xffff0000u); }
__device__ __forceinline__ float asf(unsigned v) { return __uint_as_float(v); }
__device__ __forceinline__ unsigned asu(float v) { return __float_as_uint(v); }
__device__ __forceinline__ unsigned pk2(float a, float b) {
    bf16x2 v = {(__bf16)a, (__bf16)b};
    return __builtin_bit_cast(unsigned, v);
}
__device__ __forceinline__ u16 f2bf(float f) { __bf16 v = (__bf16)f; return __builtin_bit_cast(u16, v); }
__device__ __forceinline__ float rbf(float f) { return bf2f(f2bf(f)); }
__device__ __forceinline__ unsigned umax(unsigned a, unsigned b) { return a > b ? a : b; }
__device__ __forceinline__ unsigned max4(u32x4 v) { return umax(umax(v[0], v[1]), umax(v[2], v[3])); }

#define NREP 32
#define WS_EPOCH   0
#define WS_F0      256
#define WS_QG      (WS_F0 + NREP * 2048)
#define WS_QABS    (WS_QG + 128 * 1024)
#define WS_P       (WS_QABS + 8 * 32768)
#define WS_MLOC    (WS_P + 8 * 16 * 4096)
#define WS_OPART   (WS_MLOC + 8 * 8 * 32 * 16)
#define WS_ML      (WS_OPART + 8 * 32 * 512 * 4)
#define WS_GA      (WS_ML + 32 * 8 * 8)
#define WS_GB      (WS_GA + 128 * 2048)
#define WS_QGF     (WS_GB + 128 * 2048)
#define WS_PF      (WS_QGF + 128 * 1024)
#define WS_MLOCF   (WS_PF + 8 * 16 * 4096)
#define WS_END     (WS_MLOCF + 8 * 8 * 32 * 16)

#define PF_LINES 146
#define GATE_ID 0
#define GATE_X 2000
#define GATE(i) do { if (GATE_ID == (i)) { for (unsigned gs_ = 0; gs_ < 100000u; ++gs_) { if (__builtin_amdgcn_s_memrealtime() - t0_ >= (u64)GATE_X) break; __builtin_amdgcn_s_sleep(1); } } } while (0)
#define SPIN_CAP (1u << 17)
#define A_SLEEP 40
#define B_SLEEP 20
#define A_DELAY 0
#define STAMP(i)

__device__ __forceinline__ void lgkm0() { asm volatile("s_waitcnt lgkmcnt(0)" ::: "memory"); }
__device__ __forceinline__ void vm0() { asm volatile("s_waitcnt vmcnt(0)" ::: "memory"); }
__device__ __forceinline__ void bar() { asm volatile("s_waitcnt lgkmcnt(0)\n\ts_barrier" ::: "memory"); }
typedef volatile __attribute__((address_space(3))) unsigned lds_u32;
__device__ __forceinline__ void lds_wait(lds_u32* p, unsigned target) {
    for (unsigned s = 0; s < SPIN_CAP; ++s) {
        if (*p >= target) break;
        __builtin_amdgcn_s_sleep(1);
    }
    asm volatile("" ::: "memory");
}

__device__ __forceinline__ void f0_signal(char* ws, int b, int lane, unsigned tag) {
    const u64 want = ((u64)(~tag) << 32) | tag;
    if (lane < NREP) __hip_atomic_store((gu64*)(ws + WS_F0 + lane * 2048 + b * 8), want, RLX_AGENT);
}
__device__ __forceinline__ void f0_wait(const __amdgpu_buffer_rsrc_t rs, int b, int lane, unsigned tag) {
    const int fo = WS_F0 + (b & (NREP - 1)) * 2048 + lane * 16;
    for (unsigned spins = 0; spins < SPIN_CAP; ++spins) {
        asm volatile("" ::: "memory");
        u32x4 a0 = __builtin_amdgcn_raw_buffer_load_b128(rs, fo, 0, SC1);
        u32x4 a1 = __builtin_amdgcn_raw_buffer_load_b128(rs, fo + 1024, 0, SC1);
        unsigned bad = (a0[0] ^ tag) | (a0[1] ^ ~tag) | (a0[2] ^ tag) | (a0[3] ^ ~tag) |
                       (a1[0] ^ tag) | (a1[1] ^ ~tag) | (a1[2] ^ tag) | (a1[3] ^ ~tag);
        if (__all(bad == 0)) break;
        __builtin_amdgcn_s_sleep(8);
    }
    asm volatile("" ::: "memory");
}

#define LDS_BYTES (65536 + 65536 + 8192)

__global__ __launch_bounds__(640, 1) void mega(const u16* __restrict__ x, const u16* __restrict__ ckv,
                                               const u16* __restrict__ wq, const u16* __restrict__ wuk,
                                               const u16* __restrict__ wuv, const u16* __restrict__ wo,
                                               const u16* __restrict__ wg, const u16* __restrict__ wu,
                                               const u16* __restrict__ wd, const u16* __restrict__ gw,
                                               char* __restrict__ ws, u16* __restrict__ out) {
    __shared__ __attribute__((aligned(16))) char lds[LDS_BYTES];
    __shared__ unsigned flg[4];

    const int t = threadIdx.x;
    unsigned pf0;
    {
        u64 pc;
        asm volatile("s_getpc_b64 %0" : "=s"(pc));
        const GAS unsigned* cb = (const GAS unsigned*)(pc & ~255ull);
        const int l0 = 639 - t;
        pf0 = cb[(l0 < PF_LINES - 1 ? l0 : PF_LINES - 1) * 32];
        __builtin_amdgcn_sched_barrier(0);
    }
    const int b = blockIdx.x;
    const u64 t0_ = __builtin_amdgcn_s_memrealtime();
    const int lane = t & 63, w = t >> 6;
    const int r = lane & 31, h2 = lane >> 5;
    const __amdgpu_buffer_rsrc_t rs = __builtin_amdgcn_make_buffer_rsrc(ws, 0, WS_END, 0x00020000);
    const u32x4 P4 = {POISON, POISON, POISON, POISON};
    const u32x2 P2 = {POISON, POISON};
    asm volatile("" :: "s"(ws), "s"(x));
    GATE(12);

    if (b < 128) {
        if (w >= 9) { asm volatile("" :: "v"(pf0)); return; }
        lds_u32* vflg = (lds_u32*)flg;
        if (w == 8) {
            const unsigned e = __hip_atomic_load((gu32*)(ws + WS_EPOCH), RLX_AGENT);
            asm volatile("" :: "v"(pf0));
            bar();
            f0_signal(ws, b, lane, e + 1u);
            if (b < 32) { unsigned short o_ = *(volatile const GAS u16*)(out + b * 16 + (lane & 15)); asm volatile("" :: "v"((unsigned)o_)); }
            asm volatile("s_barrier" ::: "memory");
            STAMP(1);
            __builtin_amdgcn_s_sleep(A_SLEEP);
            f0_wait(rs, b, lane, e + 1u);
            if (lane == 0) vflg[2] = 1u;
            STAMP(2);
            return;
        }
        char* tile = lds;
        float* red = (float*)(lds + 65536);
        float* fac = (float*)(lds + 131072);
        float* Mws = fac + 512;
        float* Lws = Mws + 256;
        float* fin = Lws + 256;
        float* fing = fin + 256;
        const int kc = b & 7, mem = b >> 3;
        __builtin_amdgcn_raw_buffer_store_b128(P4, rs, (w < 4 ? WS_P : WS_PF) + ((kc * 16 + mem) * 4 + (w & 3)) * 1024 + lane * 16, 0, SC1);
        if (w < 4)
            __builtin_amdgcn_raw_buffer_store_b128(P4, rs, WS_OPART + ((kc * 32 + w * 8 + (lane >> 3)) * 512 + mem * 32) * 4 + (lane & 7) * 16, 0, SC1);
        else if (w == 4)
            __builtin_amdgcn_raw_buffer_store_b64(P2, rs, (lane < 32 ? WS_MLOC : WS_MLOCF) + ((kc * 8 + (mem >> 1)) * 32 + r) * 16 + (mem & 1) * 8, 0, SC1);
        else if (w == 5 && mem == 0 && lane < 32)
            __builtin_amdgcn_raw_buffer_store_b64(P2, rs, WS_ML + (lane * 8 + kc) * 8, 0, SC1);
        if (t < 4) flg[t] = 0u;
        vm0();
        asm volatile("" :: "v"(pf0));
        bar();
        asm volatile("s_barrier" ::: "memory");
        STAMP(1);
        __builtin_amdgcn_s_sleep(A_DELAY);
        bf16x8 bq[8];
        u32x4 cv[8];
        {
            const u16* bp = ckv + (size_t)(kc * 1024 + mem * 64 + r) * 512 + 64 * w + 8 * h2;
#pragma unroll
            for (int kb = 0; kb < 2; ++kb)
#pragma unroll
                for (int s = 0; s < 4; ++s) bq[kb * 4 + s] = *(const bf16x8*)(bp + kb * 32 * 512 + 16 * s);
            const u16* cp = ckv + (size_t)(kc * 1024 + (t >> 2)) * 512 + mem * 32 + (t & 3) * 8;
#pragma unroll
            for (int i = 0; i < 8; ++i) cv[i] = *(const u32x4*)(cp + (size_t)i * 128 * 512);
        }
        __builtin_amdgcn_sched_barrier(0);
#pragma unroll
        for (int i = 0; i < 8; ++i) *(u32x4*)(tile + (i * 128 + (t >> 2)) * 64 + (t & 3) * 16) = cv[i];
        GATE(7);
        lds_wait(vflg + 2, 1);
        GATE(8);
        {
            u32x4 av[4];
            const int qo = WS_QABS + (b & 7) * 32768 + (w * 256 + lane) * 16;
            for (unsigned spins = 0; spins < SPIN_CAP; ++spins) {
                asm volatile("" ::: "memory");
#pragma unroll
                for (int s = 0; s < 4; ++s) av[s] = __builtin_amdgcn_raw_buffer_load_b128(rs, qo + 1024 * s, 0, SC1);
                unsigned m = umax(umax(max4(av[0]), max4(av[1])), umax(max4(av[2]), max4(av[3])));
                if (__all(m != POISON)) break;
                __builtin_amdgcn_s_sleep(1);
            }
            GATE(3);
#pragma unroll
            for (int kb = 0; kb < 2; ++kb) {
                f32x16 acc;
#pragma unroll
                for (int i = 0; i < 16; ++i) acc[i] = 0.f;
#pragma unroll
                for (int s = 0; s < 4; ++s)
                    acc = __builtin_amdgcn_mfma_f32_32x32x16_bf16(__builtin_bit_cast(bf16x8, av[s]), bq[kb * 4 + s], acc, 0, 0, 0);
#pragma unroll
                for (int i = 0; i < 16; ++i) {
                    int head = (i & 3) + 8 * (i >> 2) + 4 * h2;
                    red[(w * 32 + head) * 64 + kb * 32 + r] = acc[i];
                }
            }
        }
        bar();
        {
            const int head = t >> 4, kq = (t & 15) * 4;
            f32x4 s4 = *(const f32x4*)(red + head * 64 + kq);
#pragma unroll
            for (int ww = 1; ww < 8; ++ww) s4 += *(const f32x4*)(red + (ww * 32 + head) * 64 + kq);
            float m = fmaxf(fmaxf(s4[0], s4[1]), fmaxf(s4[2], s4[3]));
            m = fmaxf(m, __shfl_xor(m, 1));
            m = fmaxf(m, __shfl_xor(m, 2));
            m = fmaxf(m, __shfl_xor(m, 4));
            m = fmaxf(m, __shfl_xor(m, 8));
            unsigned p01 = pk2(__expf(s4[0] - m), __expf(s4[1] - m));
            unsigned p23 = pk2(__expf(s4[2] - m), __expf(s4[3] - m));
            float l = (bflo(p01) + bfhi(p01)) + (bflo(p23) + bfhi(p23));
            l += __shfl_xor(l, 1);
            l += __shfl_xor(l, 2);
            l += __shfl_xor(l, 4);
            l += __shfl_xor(l, 8);
            u32x4 pp = {p01, p23, (unsigned)__shfl_down((int)p01, 1), (unsigned)__shfl_down((int)p23, 1)};
            u32x2 ml = {asu(m), asu(l)};
            const int po = ((kc * 16 + mem) * 256 + ((t & 15) >> 1) * 32 + head) * 16;
            const int mo = ((kc * 8 + (mem >> 1)) * 32 + head) * 16 + (mem & 1) * 8;
            if ((t & 1) == 0) __builtin_amdgcn_raw_buffer_store_b128(pp, rs, WS_PF + po, 0, 0);
            if ((t & 15) == 0) __builtin_amdgcn_raw_buffer_store_b64(ml, rs, WS_MLOCF + mo, 0, 0);
            if ((t & 1) == 0) __builtin_amdgcn_raw_buffer_store_b128(pp, rs, WS_P + po, 0, SC1);
            if ((t & 15) == 0) __builtin_amdgcn_raw_buffer_store_b64(ml, rs, WS_MLOC + mo, 0, SC1);
        }
        bar();
        STAMP(5);
        {
            u32x4 pv[8], mlv;
            for (unsigned spins = 0; spins < SPIN_CAP; ++spins) {
                asm volatile("" ::: "memory");
                const bool slow = (spins & 3) == 3;
                const int po = (slow ? WS_P : WS_PF) + ((kc * 16 + w * 2) * 256 + lane) * 16;
                const int mo = (slow ? WS_MLOC : WS_MLOCF) + ((kc * 8 + w) * 32 + r) * 16;
#pragma unroll
                for (int s = 0; s < 8; ++s) pv[s] = __builtin_amdgcn_raw_buffer_load_b128(rs, po + 1024 * s, 0, SC1);
                mlv = __builtin_amdgcn_raw_buffer_load_b128(rs, mo, 0, SC1);
                unsigned m = umax(umax(max4(pv[0]), max4(pv[1])), umax(max4(pv[2]), max4(pv[3])));
                m = umax(m, umax(umax(max4(pv[4]), max4(pv[5])), umax(max4(pv[6]), max4(pv[7]))));
                m = umax(m, max4(mlv));
                if (__all(m != POISON)) break;
                __builtin_amdgcn_s_sleep(1);
            }
            GATE(4);
            {
                float m0 = asf(mlv[0]), l0 = asf(mlv[1]), m1 = asf(mlv[2]), l1 = asf(mlv[3]);
                float Mw = fmaxf(m0, m1);
                float f0_ = __expf(m0 - Mw), f1_ = __expf(m1 - Mw);
                if (lane < 32) {
                    fac[(2 * w) * 32 + r] = f0_; fac[(2 * w + 1) * 32 + r] = f1_;
                    Mws[r * 8 + w] = Mw; Lws[r * 8 + w] = f0_ * l0 + f1_ * l1;
                }
            }
            const int g = lane >> 4;
            const int q = (lane & 15) >> 2, p = lane & 3;
            const int cb = g & 1;
            f32x16 tot;
#pragma unroll
            for (int mm = 0; mm < 2; ++mm) {
                f32x16 acc;
#pragma unroll
                for (int i = 0; i < 16; ++i) acc[i] = 0.f;
#pragma unroll
                for (int ss = 0; ss < 4; ++ss) {
                    int s = mm * 4 + ss;
                    int row = w * 128 + 16 * s + 8 * h2 + q;
                    s16x4 t0 = __builtin_amdgcn_ds_read_tr16_b64_v4i16(
                        (__attribute__((address_space(3))) s16x4*)(tile + row * 64 + cb * 32 + p * 8));
                    s16x4 t1 = __builtin_amdgcn_ds_read_tr16_b64_v4i16(
                        (__attribute__((address_space(3))) s16x4*)(tile + (row + 4) * 64 + cb * 32 + p * 8));
                    s16x8 bb = {t0[0], t0[1], t0[2], t0[3], t1[0], t1[1], t1[2], t1[3]};
                    acc = __builtin_amdgcn_mfma_f32_32x32x16_bf16(__builtin_bit_cast(bf16x8, pv[s]), __builtin_bit_cast(bf16x8, bb), acc, 0, 0, 0);
                }
                const float* fp = fac + (2 * w + mm) * 32 + 4 * h2;
#pragma unroll
                for (int qd = 0; qd < 4; ++qd) {
                    f32x4 f4 = *(const f32x4*)(fp + 8 * qd);
#pragma unroll
                    for (int e2 = 0; e2 < 4; ++e2) {
                        if (mm == 0) tot[qd * 4 + e2] = f4[e2] * acc[qd * 4 + e2];
                        else tot[qd * 4 + e2] += f4[e2] * acc[qd * 4 + e2];
                    }
                }
            }
#pragma unroll
            for (int i = 0; i < 16; ++i) {
                int head = (i & 3) + 8 * (i >> 2) + 4 * h2;
                red[(w * 32 + head) * 32 + r] = tot[i];
            }
        }
        bar();
        {
            const int head = t >> 4, cq = (t & 15) * 2;
            f32x4 ma = *(const f32x4*)(Mws + head * 8), mb = *(const f32x4*)(Mws + head * 8 + 4);
            float M = fmaxf(fmaxf(fmaxf(ma[0], ma[1]), fmaxf(ma[2], ma[3])), fmaxf(fmaxf(mb[0], mb[1]), fmaxf(mb[2], mb[3])));
            float ew[8];
#pragma unroll
            for (int ww = 0; ww < 4; ++ww) { ew[ww] = __expf(ma[ww] - M); ew[ww + 4] = __expf(mb[ww] - M); }
            f32x2 s2 = {0.f, 0.f};
#pragma unroll
            for (int ww = 0; ww < 8; ++ww) s2 += ew[ww] * *(const f32x2*)(red + (ww * 32 + head) * 32 + cq);
            u32x4 o = {asu(s2[0]), asu(s2[1]), asu(__shfl_down(s2[0], 1)), asu(__shfl_down(s2[1], 1))};
            if ((t & 1) == 0)
                __builtin_amdgcn_raw_buffer_store_b128(o, rs, WS_OPART + ((kc * 32 + head) * 512 + mem * 32 + cq) * 4, 0, SC1);
            if (mem == 0 && (t & 15) == 0) {
                f32x4 la = *(const f32x4*)(Lws + head * 8), lb = *(const f32x4*)(Lws + head * 8 + 4);
                float L = 0.f;
#pragma unroll
                for (int ww = 0; ww < 4; ++ww) L += ew[ww] * la[ww] + ew[ww + 4] * lb[ww];
                u32x2 ml = {asu(M), asu(L)};
                __builtin_amdgcn_raw_buffer_store_b64(ml, rs, WS_ML + (head * 8 + kc) * 8, 0, SC1);
            }
        }
        STAMP(7);
        if (b >= 32) return;
        {
            const int p = t >> 2, qc = t & 3;
            float gp = rbf(bf2f(x[t]) * bf2f(gw[t]));
            const int ao = WS_GA + p * 2048 + b * 64 + qc * 16;
            const int bo = WS_GB + (p < 43 ? p : 42) * 2048 + b * 64 + qc * 16;
            u32x4 a0, b0;
            for (unsigned spins = 0; spins < SPIN_CAP; ++spins) {
                asm volatile("" ::: "memory");
                a0 = __builtin_amdgcn_raw_buffer_load_b128(rs, ao, 0, SC1);
                b0 = __builtin_amdgcn_raw_buffer_load_b128(rs, bo, 0, SC1);
                unsigned m = umax(max4(a0), max4(b0));
                if (__all(m != POISON)) break;
                __builtin_amdgcn_s_sleep(2);
            }
            GATE(6);
            if (p >= 43) { b0[0] = 0u; b0[1] = 0u; b0[2] = 0u; b0[3] = 0u; }
            bool up = lane & 4;
            float s4[4];
#pragma unroll
            for (int q = 0; q < 4; ++q) { float send = up ? asf(a0[q]) : asf(b0[q]); float keep = up ? asf(b0[q]) : asf(a0[q]); s4[q] = keep + __shfl_xor(send, 4); }
            up = lane & 8;
            float s2[2];
#pragma unroll
            for (int q = 0; q < 2; ++q) { float send = up ? s4[q] : s4[q + 2]; float keep = up ? s4[q + 2] : s4[q]; s2[q] = keep + __shfl_xor(send, 8); }
            up = lane & 16;
            float send = up ? s2[0] : s2[1]; float keep = up ? s2[1] : s2[0];
            float vv = keep + __shfl_xor(send, 16);
            vv += __shfl_xor(vv, 32);
#pragma unroll
            for (int o = 32; o >= 1; o >>= 1) gp += __shfl_xor(gp, o);
            if (lane < 32) {
                int idx = ((lane >> 2) & 1) * 16 + (lane & 3) * 4 + ((lane >> 3) & 1) * 2 + ((lane >> 4) & 1);
                fin[w * 32 + idx] = vv;
            }
            if (lane == 0) fing[w] = gp;
        }
        bar();
        if (t < 16) {
            float a = 0.f, bb = 0.f, gs = 0.f;
#pragma unroll
            for (int ww = 0; ww < 8; ++ww) { a += fin[ww * 32 + t]; bb += fin[ww * 32 + 16 + t]; gs += fing[ww]; }
            gs = rbf(gs);
            float g = rbf(1.f / (1.f + __expf(-gs)));
            a = rbf(a); bb = rbf(bb);
            float rr = rbf(g * a) + rbf(rbf(1.f - g) * bb);
            GATE(10);
            out[b * 16 + t] = f2bf(rr);
        }
        STAMP(12);
        return;
    }

    float* redf = (float*)lds;
    float* redo = redf + 4096;
    float* cpart = redo + 4096;
    float* sm1 = cpart + 2048;
    float* sm2 = sm1 + 1024;
    float* sm3 = sm2 + 512;
    float* qs = sm3 + 256;
    lds_u32* vflg = (lds_u32*)flg;
    const int bb_ = b - 128;
    const int h = bb_ & 31, jj = bb_ >> 5;
    const bool has_ffn = bb_ < 43;

    if (w >= 8) {
        const unsigned e = __hip_atomic_load((gu32*)(ws + WS_EPOCH), RLX_AGENT);
        asm volatile("" :: "v"(pf0));
        bar();
        const unsigned tag = e + 1u;
        if (w == 8) {
            lds_wait(vflg + 0, 8);
            STAMP(1);
            f32x2 s = *(const f32x2*)(sm1 + 2 * lane);
#pragma unroll
            for (int ww = 1; ww < 8; ++ww) s += *(const f32x2*)(sm1 + ww * 128 + 2 * lane);
            u32x4 g = {tag, asu(s[0]), asu(s[1]), ~tag};
            __builtin_amdgcn_raw_buffer_store_b128(g, rs, WS_QGF + ((h * 4 + jj) * 64 + lane) * 16, 0, 0);
            __builtin_amdgcn_raw_buffer_store_b128(g, rs, WS_QG + ((h * 4 + jj) * 64 + lane) * 16, 0, SC1);
            for (unsigned spins = 0; spins < SPIN_CAP; ++spins) {
                asm volatile("" ::: "memory");
                u32x4 v[4];
                const int qb = ((spins & 3) == 3 ? WS_QG : WS_QGF) + (h * 4 * 64 + lane) * 16;
#pragma unroll
                for (int k = 0; k < 4; ++k) v[k] = __builtin_amdgcn_raw_buffer_load_b128(rs, qb + k * 1024, 0, SC1);
                unsigned bad = 0;
                float q0 = 0.f, q1 = 0.f;
#pragma unroll
                for (int k = 0; k < 4; ++k) {
                    bad |= (v[k][0] ^ tag) | (v[k][3] ^ ~tag);
                    q0 += asf(v[k][1]); q1 += asf(v[k][2]);
                }
                if (__all(bad == 0) || spins == SPIN_CAP - 1) {
                    f32x2 qq = {rbf(q0), rbf(q1)};
                    *(f32x2*)(qs + 2 * lane) = qq;
                    break;
                }
                __builtin_amdgcn_s_sleep(1);
            }
            lgkm0();
            if (lane == 0) vflg[1] = 1u;
            STAMP(2);
        } else {
            lds_wait(vflg + 3, 8);
            STAMP(1);
            f0_signal(ws, b, lane, tag);
            __builtin_amdgcn_s_sleep(B_SLEEP);
            f0_wait(rs, b, lane, tag);
            if (lane == 0) vflg[2] = 1u;
            if (b == 255 && lane == 0) __hip_atomic_store((gu32*)(ws + WS_EPOCH), tag, RLX_AGENT);
            STAMP(2);
        }
        return;
    }

    {
        int po;
        if (w < 2) po = WS_QABS + (lane >> 3) * 32768 + ((jj * 16 + w * 8 + (lane & 7)) * 32 + h) * 16;
        else if (w < 4) po = WS_GA + bb_ * 2048 + (w & 1) * 1024 + lane * 16;
        else po = WS_GB + bb_ * 2048 + (w & 1) * 1024 + lane * 16;
        if (w < 4 || (w < 6 && has_ffn)) __builtin_amdgcn_raw_buffer_store_b128(P4, rs, po, 0, SC1);
    }
    __builtin_amdgcn_sched_barrier(0);
    u32x4 wqv[4], wkv[4];
    u16 xr[4];
    {
        const int row = jj * 128 + (t >> 4);
#pragma unroll
        for (int i = 0; i < 4; ++i) xr[i] = x[row + 32 * i];
#pragma unroll
        for (int i = 0; i < 4; ++i)
            wqv[i] = *(const u32x4*)(wq + (size_t)(row + 32 * i) * 4096 + h * 128 + (t & 15) * 8);
#pragma unroll
        for (int i = 0; i < 4; ++i)
            wkv[i] = *(const u32x4*)(wuk + (size_t)(jj * 128 + 16 * w + (lane >> 4) + 4 * i) * 4096 + h * 128 + (lane & 15) * 8);
    }
    __builtin_amdgcn_sched_barrier(0);
    if (t < 4) flg[t] = 0u;
    bar();
    asm volatile("s_waitcnt vmcnt(12)" ::: "memory");
    asm volatile("" :: "v"(pf0));
    GATE(13);
    if (lane == 0) __hip_atomic_fetch_add(flg + 3, 1u, __ATOMIC_RELAXED, __HIP_MEMORY_SCOPE_WORKGROUP);
    __builtin_amdgcn_sched_barrier(0);
    asm volatile("s_waitcnt vmcnt(4)" ::: "memory");
    GATE(1);
    {
        float acc[8];
#pragma unroll
        for (int k = 0; k < 4; ++k) {
            acc[2 * k] = bf2f(xr[0]) * bflo(wqv[0][k]) + bf2f(xr[1]) * bflo(wqv[1][k]) + bf2f(xr[2]) * bflo(wqv[2][k]) + bf2f(xr[3]) * bflo(wqv[3][k]);
            acc[2 * k + 1] = bf2f(xr[0]) * bfhi(wqv[0][k]) + bf2f(xr[1]) * bfhi(wqv[1][k]) + bf2f(xr[2]) * bfhi(wqv[2][k]) + bf2f(xr[3]) * bfhi(wqv[3][k]);
        }
        bool up = lane & 16;
        float s[4];
#pragma unroll
        for (int k = 0; k < 4; ++k) { float send = up ? acc[k] : acc[k + 4]; float keep = up ? acc[k + 4] : acc[k]; s[k] = keep + __shfl_xor(send, 16); }
        up = lane & 32;
        f32x2 s2;
#pragma unroll
        for (int k = 0; k < 2; ++k) { float send = up ? s[k] : s[k + 2]; float keep = up ? s[k + 2] : s[k]; s2[k] = keep + __shfl_xor(send, 32); }
        *(f32x2*)(sm1 + w * 128 + (lane & 15) * 8 + ((lane >> 4) & 1) * 4 + (lane >> 5) * 2) = s2;
        lgkm0();
        if (lane == 0) __hip_atomic_fetch_add(flg + 0, 1u, __ATOMIC_RELAXED, __HIP_MEMORY_SCOPE_WORKGROUP);
    }
    __builtin_amdgcn_sched_barrier(0);
    lds_wait(vflg + 1, 1);
    GATE(2);
    {
        const int l16 = lane & 15;
        f32x4 qa0 = *(const f32x4*)(qs + l16 * 8), qa1 = *(const f32x4*)(qs + l16 * 8 + 4);
        float d[4];
#pragma unroll
        for (int i = 0; i < 4; ++i) {
            d[i] = qa0[0] * bflo(wkv[i][0]) + qa0[1] * bfhi(wkv[i][0]) + qa0[2] * bflo(wkv[i][1]) + qa0[3] * bfhi(wkv[i][1]);
            d[i] += qa1[0] * bflo(wkv[i][2]) + qa1[1] * bfhi(wkv[i][2]) + qa1[2] * bflo(wkv[i][3]) + qa1[3] * bfhi(wkv[i][3]);
        }
        bool up = lane & 1;
        float s[2];
#pragma unroll
        for (int k = 0; k < 2; ++k) { float send = up ? d[k] : d[k + 2]; float keep = up ? d[k + 2] : d[k]; s[k] = keep + __shfl_xor(send, 1); }
        up = lane & 2;
        float send = up ? s[0] : s[1], keep = up ? s[1] : s[0];
        float v = keep + __shfl_xor(send, 2);
        v += __shfl_xor(v, 4);
        v += __shfl_xor(v, 8);
        v *= 0.08838834764831845f;
        const int k = (lane >> 3) & 1;
        float vv[8];
#pragma unroll
        for (int e2 = 0; e2 < 8; ++e2) vv[e2] = __shfl(v, (e2 & 3) * 16 + (k | ((e2 >> 2) << 1)));
        u32x4 o = {pk2(vv[0], vv[1]), pk2(vv[2], vv[3]), pk2(vv[4], vv[5]), pk2(vv[6], vv[7])};
        if (lane < 16)
            __builtin_amdgcn_raw_buffer_store_b128(o, rs, WS_QABS + (lane & 7) * 32768 + ((jj * 16 + w * 2 + k) * 32 + h) * 16, 0, SC1);
    }
    __builtin_amdgcn_sched_barrier(0);
    if (lane == 0) __hip_atomic_fetch_add(flg + 0, 1u, __ATOMIC_RELAXED, __HIP_MEMORY_SCOPE_WORKGROUP);
    lds_wait(vflg + 0, 16);
    __builtin_amdgcn_sched_barrier(0);
    GATE(14);
    u32x4 uvv[4], ovv[4];
#pragma unroll
    for (int i = 0; i < 4; ++i)
        uvv[i] = *(const u32x4*)(wuv + (size_t)((t >> 2) + 128 * i) * 4096 + h * 128 + jj * 32 + (t & 3) * 8);
#pragma unroll
    for (int i = 0; i < 4; ++i)
        ovv[i] = *(const u32x4*)(wo + ((size_t)h * 128 + jj * 32 + w * 4 + i) * 512 + lane * 8);
    __builtin_amdgcn_sched_barrier(0);
    if (has_ffn) {
        u32x4 gv[4], uv_[4], dv[4];
        u16 xf[4];
#pragma unroll
        for (int i = 0; i < 4; ++i) xf[i] = x[(t >> 2) + 128 * i];
#pragma unroll
        for (int i = 0; i < 4; ++i) {
            gv[i] = *(const u32x4*)(wg + (size_t)((t >> 2) + 128 * i) * FF + bb_ * 32 + (t & 3) * 8);
            uv_[i] = *(const u32x4*)(wu + (size_t)((t >> 2) + 128 * i) * FF + bb_ * 32 + (t & 3) * 8);
        }
#pragma unroll
        for (int i = 0; i < 4; ++i) dv[i] = *(const u32x4*)(wd + (size_t)(bb_ * 32 + w * 4 + i) * 512 + lane * 8);
        __builtin_amdgcn_sched_barrier(0);
        float p[16];
#pragma unroll
        for (int i = 0; i < 4; ++i) {
            const float xv = bf2f(xf[i]);
#pragma unroll
            for (int k = 0; k < 4; ++k) {
                if (i == 0) {
                    p[2 * k] = xv * bflo(gv[i][k]); p[2 * k + 1] = xv * bfhi(gv[i][k]);
                    p[8 + 2 * k] = xv * bflo(uv_[i][k]); p[8 + 2 * k + 1] = xv * bfhi(uv_[i][k]);
                } else {
                    p[2 * k] += xv * bflo(gv[i][k]); p[2 * k + 1] += xv * bfhi(gv[i][k]);
                    p[8 + 2 * k] += xv * bflo(uv_[i][k]); p[8 + 2 * k + 1] += xv * bfhi(uv_[i][k]);
                }
            }
        }
        bool up = lane & 4;
        float s8[8];
#pragma unroll
        for (int k = 0; k < 8; ++k) { float send = up ? p[k] : p[k + 8]; float keep = up ? p[k + 8] : p[k]; s8[k] = keep + __shfl_xor(send, 4); }
        up = lane & 8;
        float s4[4];
#pragma unroll
        for (int k = 0; k < 4; ++k) { float send = up ? s8[k] : s8[k + 4]; float keep = up ? s8[k + 4] : s8[k]; s4[k] = keep + __shfl_xor(send, 8); }
        up = lane & 16;
        float s2[2];
#pragma unroll
        for (int k = 0; k < 2; ++k) { float send = up ? s4[k] : s4[k + 2]; float keep = up ? s4[k + 2] : s4[k]; s2[k] = keep + __shfl_xor(send, 16); }
        up = lane & 32;
        float send = up ? s2[0] : s2[1]; float keep = up ? s2[1] : s2[0];
        float v = keep + __shfl_xor(send, 32);
        sm2[w * 64 + ((lane >> 2) & 1) * 32 + (lane & 3) * 8 + ((lane >> 3) & 1) * 4 + ((lane >> 4) & 1) * 2 + (lane >> 5)] = v;
        bar();
        f32x4 g4 = *(const f32x4*)(sm2 + 4 * w), u4 = *(const f32x4*)(sm2 + 32 + 4 * w);
#pragma unroll
        for (int ww = 1; ww < 8; ++ww) { g4 += *(const f32x4*)(sm2 + ww * 64 + 4 * w); u4 += *(const f32x4*)(sm2 + ww * 64 + 32 + 4 * w); }
        f32x4 o0 = {0.f, 0.f, 0.f, 0.f}, o1 = {0.f, 0.f, 0.f, 0.f};
#pragma unroll
        for (int i = 0; i < 4; ++i) {
            const float gs = rbf(g4[i]), us = rbf(u4[i]);
            const float sg = rbf(gs / (1.f + __expf(-gs)));
            const float hv = rbf(sg * us);
            o0[0] += hv * bflo(dv[i][0]); o0[1] += hv * bfhi(dv[i][0]); o0[2] += hv * bflo(dv[i][1]); o0[3] += hv * bfhi(dv[i][1]);
            o1[0] += hv * bflo(dv[i][2]); o1[1] += hv * bfhi(dv[i][2]); o1[2] += hv * bflo(dv[i][3]); o1[3] += hv * bfhi(dv[i][3]);
        }
        *(f32x4*)(redf + w * 512 + lane * 8) = o0;
        *(f32x4*)(redf + w * 512 + lane * 8 + 4) = o1;
        bar();
        if (t < 128) {
            f32x4 s = *(const f32x4*)(redf + t * 4);
#pragma unroll
            for (int ww = 1; ww < 8; ++ww) s += *(const f32x4*)(redf + ww * 512 + t * 4);
            __builtin_amdgcn_raw_buffer_store_b128(__builtin_bit_cast(u32x4, s), rs, WS_GB + bb_ * 2048 + t * 16, 0, SC1);
        }
    }
    asm volatile("s_waitcnt vmcnt(1)" ::: "memory");
    lds_wait(vflg + 2, 1);
    GATE(9);
    {
        const int qd = t >> 7;
        u32x4 ov[2];
        u32x2 ml;
        const int oo = WS_OPART + ((qd * 2 * 32 + h) * 512 + (t & 127) * 4) * 4;
        for (unsigned spins = 0; spins < SPIN_CAP; ++spins) {
            asm volatile("" ::: "memory");
#pragma unroll
            for (int i = 0; i < 2; ++i) ov[i] = __builtin_amdgcn_raw_buffer_load_b128(rs, oo + i * 65536, 0, SC1);
            ml = __builtin_amdgcn_raw_buffer_load_b64(rs, WS_ML + (h * 8 + (lane & 7)) * 8, 0, SC1);
            unsigned m = umax(max4(ov[0]), max4(ov[1]));
            m = umax(m, umax(ml[0], ml[1]));
            if (__all(m != POISON)) break;
            __builtin_amdgcn_s_sleep(1);
        }
        GATE(5);
        float mk = asf(ml[0]), lk = asf(ml[1]);
        float M = mk;
        M = fmaxf(M, __shfl_xor(M, 1)); M = fmaxf(M, __shfl_xor(M, 2)); M = fmaxf(M, __shfl_xor(M, 4));
        float wk = __expf(mk - M);
        float L = wk * lk;
        L += __shfl_xor(L, 1); L += __shfl_xor(L, 2); L += __shfl_xor(L, 4);
        wk = wk / L;
        f32x4 c4 = {0.f, 0.f, 0.f, 0.f};
#pragma unroll
        for (int i = 0; i < 2; ++i) {
            float wi = __shfl(wk, qd * 2 + i);
            c4[0] += wi * asf(ov[i][0]); c4[1] += wi * asf(ov[i][1]); c4[2] += wi * asf(ov[i][2]); c4[3] += wi * asf(ov[i][3]);
        }
        *(f32x4*)(cpart + qd * 512 + (t & 127) * 4) = c4;
    }
    bar();
    {
        const int row = t >> 2;
        float acc[8];
#pragma unroll
        for (int i = 0; i < 4; ++i) {
            const int rr = row + 128 * i;
            float c0 = (cpart[rr] + cpart[512 + rr]) + (cpart[1024 + rr] + cpart[1536 + rr]);
#pragma unroll
            for (int k = 0; k < 4; ++k) {
                if (i == 0) { acc[2 * k] = c0 * bflo(uvv[i][k]); acc[2 * k + 1] = c0 * bfhi(uvv[i][k]); }
                else { acc[2 * k] += c0 * bflo(uvv[i][k]); acc[2 * k + 1] += c0 * bfhi(uvv[i][k]); }
            }
        }
        bool up = lane & 4;
        float s[4];
#pragma unroll
        for (int k = 0; k < 4; ++k) { float send = up ? acc[k] : acc[k + 4]; float keep = up ? acc[k + 4] : acc[k]; s[k] = keep + __shfl_xor(send, 4); }
        up = lane & 8;
        float s2[2];
#pragma unroll
        for (int k = 0; k < 2; ++k) { float send = up ? s[k] : s[k + 2]; float keep = up ? s[k + 2] : s[k]; s2[k] = keep + __shfl_xor(send, 8); }
        up = lane & 16;
        float send = up ? s2[0] : s2[1]; float keep = up ? s2[1] : s2[0];
        float v = keep + __shfl_xor(send, 16);
        v += __shfl_xor(v, 32);
        if (lane < 32) {
            int d = (lane & 3) * 8 + ((lane >> 2) & 1) * 4 + ((lane >> 3) & 1) * 2 + ((lane >> 4) & 1);
            sm3[w * 32 + d] = v;
        }
    }
    bar();
    {
        f32x4 c4 = *(const f32x4*)(sm3 + 4 * w);
#pragma unroll
        for (int ww = 1; ww < 8; ++ww) c4 += *(const f32x4*)(sm3 + ww * 32 + 4 * w);
        f32x4 o0 = {0.f, 0.f, 0.f, 0.f}, o1 = {0.f, 0.f, 0.f, 0.f};
#pragma unroll
        for (int i = 0; i < 4; ++i) {
            const float c0 = rbf(c4[i]);
            o0[0] += c0 * bflo(ovv[i][0]); o0[1] += c0 * bfhi(ovv[i][0]); o0[2] += c0 * bflo(ovv[i][1]); o0[3] += c0 * bfhi(ovv[i][1]);
            o1[0] += c0 * bflo(ovv[i][2]); o1[1] += c0 * bfhi(ovv[i][2]); o1[2] += c0 * bflo(ovv[i][3]); o1[3] += c0 * bfhi(ovv[i][3]);
        }
        *(f32x4*)(redo + w * 512 + lane * 8) = o0;
        *(f32x4*)(redo + w * 512 + lane * 8 + 4) = o1;
    }
    bar();
    GATE(11);
    if (t < 128) {
        f32x4 s = *(const f32x4*)(redo + t * 4);
#pragma unroll
        for (int ww = 1; ww < 8; ++ww) s += *(const f32x4*)(redo + ww * 512 + t * 4);
        __builtin_amdgcn_raw_buffer_store_b128(__builtin_bit_cast(u32x4, s), rs, WS_GA + bb_ * 2048 + t * 16, 0, SC1);
    }
    STAMP(10);
}

extern "C" void kernel_launch(void* const* d_in, const int* in_sizes, int n_in,
                              void* d_out, int out_size, void* d_ws, size_t ws_size,
                              hipStream_t stream) {
    const u16* x = (const u16*)d_in[0];
    const u16* ckv = (const u16*)d_in[1];
    const u16* wq = (const u16*)d_in[2];
    const u16* wuk = (const u16*)d_in[3];
    const u16* wuv = (const u16*)d_in[4];
    const u16* wo = (const u16*)d_in[5];
    const u16* wg = (const u16*)d_in[7];
    const u16* wu = (const u16*)d_in[8];
    const u16* wd = (const u16*)d_in[9];
    const u16* gw = (const u16*)d_in[10];
    mega<<<256, 640, 0, stream>>>(x, ckv, wq, wuk, wuv, wo, wg, wu, wd, gw, (char*)d_ws, (u16*)d_out);
}
```

```cpp
#include <hip/hip_runtime.h>
#include <hip/hip_bf16.h>
#include <stdint.h>

#define SK 8192
#define FF 1376

typedef unsigned short u16;
typedef unsigned long long u64;
typedef __attribute__((ext_vector_type(8))) __bf16 bf16x8;
typedef __attribute__((ext_vector_type(2))) __bf16 bf16x2;
typedef __attribute__((ext_vector_type(4))) short s16x4;
typedef __attribute__((ext_vector_type(8))) short s16x8;
typedef __attribute__((ext_vector_type(16))) float f32x16;
typedef __attribute__((ext_vector_type(4))) float f32x4;
typedef __attribute__((ext_vector_type(2))) float f32x2;
typedef __attribute__((ext_vector_type(4))) unsigned int u32x4;
typedef __attribute__((ext_vector_type(2))) unsigned int u32x2;
#define GAS __attribute__((address_space(1)))
typedef GAS unsigned gu32;
typedef GAS u64 gu64;
#define RLX_AGENT __ATOMIC_RELAXED, __HIP_MEMORY_SCOPE_AGENT
#define SC1 16
#define POISON 0xFFFFFFFFu

__device__ __forceinline__ float bf2f(u16 v) { return __uint_as_float(((unsigned)v) << 16); }
__device__ __forceinline__ float bflo(unsigned v) { return __uint_as_float(v << 16); }
__device__ __forceinline__ float bfhi(unsigned v) { return __uint_as_float(v & 0xffff0000u); }
__device__ __forceinline__ float asf(unsigned v) { return __uint_as_float(v); }
__device__ __forceinline__ unsigned asu(float v) { return __float_as_uint(v); }
__device__ __forceinline__ unsigned pk2(float a, float b) {
    bf16x2 v = {(__bf16)a, (__bf16)b};
    return __builtin_bit_cast(unsigned, v);
}
__device__ __forceinline__ u16 f2bf(float f) { __bf16 v = (__bf16)f; return __builtin_bit_cast(u16, v); }
__device__ __forceinline__ float rbf(float f) { return bf2f(f2bf(f)); }
template <int CTRL> __device__ __forceinline__ float dppf(float v) {
    return asf((unsigned)__builtin_amdgcn_update_dpp(0, (int)asu(v), CTRL, 0xf, 0xf, true));
}
template <int CTRL> __device__ __forceinline__ unsigned dppu(unsigned v) {
    return (unsigned)__builtin_amdgcn_update_dpp(0, (int)v, CTRL, 0xf, 0xf, true);
}
__device__ __forceinline__ float dpp_xor4(float v) {
    int a = __builtin_amdgcn_update_dpp(0, (int)asu(v), 0x104, 0xf, 0x5, false);
    a = __builtin_amdgcn_update_dpp(a, (int)asu(v), 0x114, 0xf, 0xA, false);
    return asf((unsigned)a);
}
__device__ __forceinline__ float sw16(float a, float b) {
    u32x2 r_ = __builtin_amdgcn_permlane16_swap(asu(a), asu(b), false, false);
    return asf(r_[0]) + asf(r_[1]);
}
__device__ __forceinline__ float sw32(float a, float b) {
    u32x2 r_ = __builtin_amdgcn_permlane32_swap(asu(a), asu(b), false, false);
    return asf(r_[0]) + asf(r_[1]);
}
__device__ __forceinline__ float mx16(float a) {
    u32x2 r_ = __builtin_amdgcn_permlane16_swap(asu(a), asu(a), false, false);
    return fmaxf(asf(r_[0]), asf(r_[1]));
}
__device__ __forceinline__ float mx32(float a) {
    u32x2 r_ = __builtin_amdgcn_permlane32_swap(asu(a), asu(a), false, false);
    return fmaxf(asf(r_[0]), asf(r_[1]));
}
__device__ __forceinline__ float sum16(float v) {
    v += dppf<0xB1>(v); v += dppf<0x4E>(v); v += dppf<0x141>(v); v += dppf<0x140>(v);
    return v;
}
__device__ __forceinline__ float max16(float v) {
    v = fmaxf(v, dppf<0xB1>(v)); v = fmaxf(v, dppf<0x4E>(v)); v = fmaxf(v, dppf<0x141>(v)); v = fmaxf(v, dppf<0x140>(v));
    return v;
}
__device__ __forceinline__ unsigned umax(unsigned a, unsigned b) { return a > b ? a : b; }
__device__ __forceinline__ unsigned max4(u32x4 v) { return umax(umax(v[0], v[1]), umax(v[2], v[3])); }

#define NREP 32
#define WS_EPOCH   0
#define WS_F0      256
#define WS_QG      (WS_F0 + NREP * 2048)
#define WS_QABS    (WS_QG + 128 * 1024)
#define WS_P       (WS_QABS + 8 * 32768)
#define WS_MLOC    (WS_P + 8 * 16 * 4096)
#define WS_OPART   (WS_MLOC + 8 * 8 * 32 * 16)
#define WS_ML      (WS_OPART + 8 * 32 * 512 * 4)
#define WS_GA      (WS_ML + 32 * 8 * 8)
#define WS_GB      (WS_GA + 128 * 2048)
#define WS_QGF     (WS_GB + 128 * 2048)
#define WS_PF      (WS_QGF + 128 * 1024)
#define WS_MLOCF   (WS_PF + 8 * 16 * 4096)
#define WS_END     (WS_MLOCF + 8 * 8 * 32 * 16)

#define PF_LINES 136
#define FFN_CHUNK ((bb_ & 7) * 5 + ((bb_ & 7) < 3 ? (bb_ & 7) : 3) + (bb_ >> 3))
#define GATE_ID 0
#define GATE_X 2000
#define GATE(i) do { if (GATE_ID == (i)) { for (unsigned gs_ = 0; gs_ < 100000u; ++gs_) { if (__builtin_amdgcn_s_memrealtime() - t0_ >= (u64)GATE_X) break; __builtin_amdgcn_s_sleep(1); } } } while (0)
#define SPIN_CAP (1u << 17)
#define A_SLEEP 40
#define B_SLEEP 20
#define A_DELAY 0
#define STAMP(i)

__device__ __forceinline__ void lgkm0() { asm volatile("s_waitcnt lgkmcnt(0)" ::: "memory"); }
__device__ __forceinline__ void vm0() { asm volatile("s_waitcnt vmcnt(0)" ::: "memory"); }
__device__ __forceinline__ void bar() { asm volatile("s_waitcnt lgkmcnt(0)\n\ts_barrier" ::: "memory"); }
typedef volatile __attribute__((address_space(3))) unsigned lds_u32;
__device__ __forceinline__ void lds_wait(lds_u32* p, unsigned target) {
    for (unsigned s = 0; s < SPIN_CAP; ++s) {
        if (*p >= target) break;
        __builtin_amdgcn_s_sleep(1);
    }
    asm volatile("" ::: "memory");
}

__device__ __forceinline__ void f0_signal(char* ws, int b, int lane, unsigned tag) {
    const u64 want = ((u64)(~tag) << 32) | tag;
    if (lane < NREP) __hip_atomic_store((gu64*)(ws + WS_F0 + lane * 2048 + b * 8), want, RLX_AGENT);
}
__device__ __forceinline__ void f0_wait(const __amdgpu_buffer_rsrc_t rs, int b, int lane, unsigned tag) {
    const int fo = WS_F0 + (b & (NREP - 1)) * 2048 + lane * 16;
    for (unsigned spins = 0; spins < SPIN_CAP; ++spins) {
        asm volatile("" ::: "memory");
        u32x4 a0 = __builtin_amdgcn_raw_buffer_load_b128(rs, fo, 0, SC1);
        u32x4 a1 = __builtin_amdgcn_raw_buffer_load_b128(rs, fo + 1024, 0, SC1);
        unsigned bad = (a0[0] ^ tag) | (a0[1] ^ ~tag) | (a0[2] ^ tag) | (a0[3] ^ ~tag) |
                       (a1[0] ^ tag) | (a1[1] ^ ~tag) | (a1[2] ^ tag) | (a1[3] ^ ~tag);
        if (__all(bad == 0)) break;
        __builtin_amdgcn_s_sleep(8);
    }
    asm volatile("" ::: "memory");
}

#define LDS_BYTES (65536 + 65536 + 8192)

__global__ __launch_bounds__(640, 1) void mega(const u16* __restrict__ x, const u16* __restrict__ ckv,
                                               const u16* __restrict__ wq, const u16* __restrict__ wuk,
                                               const u16* __restrict__ wuv, const u16* __restrict__ wo,
                                               const u16* __restrict__ wg, const u16* __restrict__ wu,
                                               const u16* __restrict__ wd, const u16* __restrict__ gw,
                                               char* __restrict__ ws, u16* __restrict__ out) {
    __shared__ __attribute__((aligned(16))) char lds[LDS_BYTES];
    __shared__ unsigned flg[4];

    const int t = threadIdx.x;
    unsigned pf0;
    {
        u64 pc;
        asm volatile("s_getpc_b64 %0" : "=s"(pc));
        const GAS unsigned* cb = (const GAS unsigned*)(pc & ~255ull);
        const int l0 = 639 - t;
        pf0 = cb[(l0 < PF_LINES - 1 ? l0 : PF_LINES - 1) * 32];
        __builtin_amdgcn_sched_barrier(0);
    }
    const int b = blockIdx.x;
    const u64 t0_ = __builtin_amdgcn_s_memrealtime();
    const int lane = t & 63, w = t >> 6;
    const int r = lane & 31, h2 = lane >> 5;
    const __amdgpu_buffer_rsrc_t rs = __builtin_amdgcn_make_buffer_rsrc(ws, 0, WS_END, 0x00020000);
    const u32x4 P4 = {POISON, POISON, POISON, POISON};
    const u32x2 P2 = {POISON, POISON};
    STAMP(0);

    if (b < 128) {
        if (w >= 9) { asm volatile("" :: "v"(pf0)); return; }
        lds_u32* vflg = (lds_u32*)flg;
        if (w == 8) {
            const unsigned e = __hip_atomic_load((gu32*)(ws + WS_EPOCH), RLX_AGENT);
            asm volatile("" :: "v"(pf0));
            bar();
            f0_signal(ws, b, lane, e + 1u);
            if (b < 32) { unsigned short o_ = *(volatile const GAS u16*)(out + b * 16 + (lane & 15)); asm volatile("" :: "v"((unsigned)o_)); }
            asm volatile("s_barrier" ::: "memory");
            STAMP(1);
            __builtin_amdgcn_s_sleep(A_SLEEP);
            f0_wait(rs, b, lane, e + 1u);
            if (lane == 0) vflg[2] = 1u;
            STAMP(2);
            return;
        }
        char* tile = lds;
        float* red = (float*)(lds + 65536);
        float* fac = (float*)(lds + 131072);
        float* Mws = fac + 512;
        float* Lws = Mws + 256;
        float* fin = Lws + 256;
        float* fing = fin + 256;
        const int kc = b & 7, mem = b >> 3;
        __builtin_amdgcn_raw_buffer_store_b128(P4, rs, (w < 4 ? WS_P : WS_PF) + ((kc * 16 + mem) * 4 + (w & 3)) * 1024 + lane * 16, 0, SC1);
        if (w < 4)
            __builtin_amdgcn_raw_buffer_store_b128(P4, rs, WS_OPART + (kc * 64 + w * 16 + (lane >> 2)) * 1024 + mem * 64 + (lane & 3) * 16, 0, SC1);
        else if (w == 4)
            __builtin_amdgcn_raw_buffer_store_b64(P2, rs, (lane < 32 ? WS_MLOC : WS_MLOCF) + ((kc * 8 + (mem >> 1)) * 32 + r) * 16 + (mem & 1) * 8, 0, SC1);
        else if (w == 5 && mem == 0 && lane < 32)
            __builtin_amdgcn_raw_buffer_store_b64(P2, rs, WS_ML + (lane * 8 + kc) * 8, 0, SC1);
        if (t < 4) flg[t] = 0u;
        vm0();
        asm volatile("" :: "v"(pf0));
        bar();
        asm volatile("s_barrier" ::: "memory");
        STAMP(1);
        __builtin_amdgcn_s_sleep(A_DELAY);
        bf16x8 bq[8];
        u32x4 cv[8];
        {
            const u16* bp = ckv + (size_t)(kc * 1024 + mem * 64 + r) * 512 + 64 * w + 8 * h2;
#pragma unroll
            for (int kb = 0; kb < 2; ++kb)
#pragma unroll
                for (int s = 0; s < 4; ++s) bq[kb * 4 + s] = *(const bf16x8*)(bp + kb * 32 * 512 + 16 * s);
            const u16* cp = ckv + (size_t)(kc * 1024 + (t >> 2)) * 512 + mem * 32 + (t & 3) * 8;
#pragma unroll
            for (int i = 0; i < 8; ++i) cv[i] = *(const u32x4*)(cp + (size_t)i * 128 * 512);
        }
        __builtin_amdgcn_sched_barrier(0);
#pragma unroll
        for (int i = 0; i < 8; ++i) *(u32x4*)(tile + (i * 128 + (t >> 2)) * 64 + (t & 3) * 16) = cv[i];
        GATE(7); STAMP(2);
        lds_wait(vflg + 2, 1);
        GATE(8); STAMP(3);
        {
            u32x4 av[4];
            const int qo = WS_QABS + (b & 7) * 32768 + (w * 256 + lane) * 16;
            for (unsigned spins = 0; spins < SPIN_CAP; ++spins) {
                asm volatile("" ::: "memory");
#pragma unroll
                for (int s = 0; s < 4; ++s) av[s] = __builtin_amdgcn_raw_buffer_load_b128(rs, qo + 1024 * s, 0, SC1);
                unsigned m = umax(umax(max4(av[0]), max4(av[1])), umax(max4(av[2]), max4(av[3])));
                if (__all(m != POISON)) break;
                __builtin_amdgcn_s_sleep(1);
            }
            GATE(3); STAMP(4);
#pragma unroll
            for (int kb = 0; kb < 2; ++kb) {
                f32x16 acc;
#pragma unroll
                for (int i = 0; i < 16; ++i) acc[i] = 0.f;
#pragma unroll
                for (int s = 0; s < 4; ++s)
                    acc = __builtin_amdgcn_mfma_f32_32x32x16_bf16(__builtin_bit_cast(bf16x8, av[s]), bq[kb * 4 + s], acc, 0, 0, 0);
#pragma unroll
                for (int i = 0; i < 16; ++i) {
                    int head = (i & 3) + 8 * (i >> 2) + 4 * h2;
                    red[(w * 32 + head) * 64 + kb * 32 + r] = acc[i];
                }
            }
        }
        bar();
        {
            const int head = t >> 4, kq = (t & 15) * 4;
            f32x4 s4 = *(const f32x4*)(red + head * 64 + kq);
#pragma unroll
            for (int ww = 1; ww < 8; ++ww) s4 += *(const f32x4*)(red + (ww * 32 + head) * 64 + kq);
            float m = fmaxf(fmaxf(s4[0], s4[1]), fmaxf(s4[2], s4[3]));
            m = max16(m);
            unsigned p01 = pk2(__expf(s4[0] - m), __expf(s4[1] - m));
            unsigned p23 = pk2(__expf(s4[2] - m), __expf(s4[3] - m));
            float l = (bflo(p01) + bfhi(p01)) + (bflo(p23) + bfhi(p23));
            l = sum16(l);
            u32x4 pp = {p01, p23, dppu<0xF5>(p01), dppu<0xF5>(p23)};
            u32x2 ml = {asu(m), asu(l)};
            const int po = ((kc * 16 + mem) * 256 + ((t & 15) >> 1) * 32 + head) * 16;
            const int mo = ((kc * 8 + (mem >> 1)) * 32 + head) * 16 + (mem & 1) * 8;
            if ((t & 1) == 0) __builtin_amdgcn_raw_buffer_store_b128(pp, rs, WS_PF + po, 0, 0);
            if ((t & 15) == 0) __builtin_amdgcn_raw_buffer_store_b64(ml, rs, WS_MLOCF + mo, 0, 0);
            if ((t & 1) == 0) __builtin_amdgcn_raw_buffer_store_b128(pp, rs, WS_P + po, 0, SC1);
            if ((t & 15) == 0) __builtin_amdgcn_raw_buffer_store_b64(ml, rs, WS_MLOC + mo, 0, SC1);
        }
        bar();
        STAMP(5);
        {
            u32x4 pv[8], mlv;
            for (unsigned spins = 0; spins < SPIN_CAP; ++spins) {
                asm volatile("" ::: "memory");
                const bool slow = (spins & 3) == 3;
                const int po = (slow ? WS_P : WS_PF) + ((kc * 16 + w * 2) * 256 + lane) * 16;
                const int mo = (slow ? WS_MLOC : WS_MLOCF) + ((kc * 8 + w) * 32 + r) * 16;
#pragma unroll
                for (int s = 0; s < 8; ++s) pv[s] = __builtin_amdgcn_raw_buffer_load_b128(rs, po + 1024 * s, 0, SC1);
                mlv = __builtin_amdgcn_raw_buffer_load_b128(rs, mo, 0, SC1);
                unsigned m = umax(umax(max4(pv[0]), max4(pv[1])), umax(max4(pv[2]), max4(pv[3])));
                m = umax(m, umax(umax(max4(pv[4]), max4(pv[5])), umax(max4(pv[6]), max4(pv[7]))));
                m = umax(m, max4(mlv));
                if (__all(m != POISON)) break;
                __builtin_amdgcn_s_sleep(1);
            }
            GATE(4); STAMP(6);
            {
                float m0 = asf(mlv[0]), l0 = asf(mlv[1]), m1 = asf(mlv[2]), l1 = asf(mlv[3]);
                float Mw = fmaxf(m0, m1);
                float f0_ = __expf(m0 - Mw), f1_ = __expf(m1 - Mw);
                if (lane < 32) {
                    fac[(2 * w) * 32 + r] = f0_; fac[(2 * w + 1) * 32 + r] = f1_;
                    Mws[r * 8 + w] = Mw; Lws[r * 8 + w] = f0_ * l0 + f1_ * l1;
                }
            }
            const int g = lane >> 4;
            const int q = (lane & 15) >> 2, p = lane & 3;
            const int cb = g & 1;
            f32x16 tot;
#pragma unroll
            for (int mm = 0; mm < 2; ++mm) {
                f32x16 acc;
#pragma unroll
                for (int i = 0; i < 16; ++i) acc[i] = 0.f;
#pragma unroll
                for (int ss = 0; ss < 4; ++ss) {
                    int s = mm * 4 + ss;
                    int row = w * 128 + 16 * s + 8 * h2 + q;
                    s16x4 t0 = __builtin_amdgcn_ds_read_tr16_b64_v4i16(
                        (__attribute__((address_space(3))) s16x4*)(tile + row * 64 + cb * 32 + p * 8));
                    s16x4 t1 = __builtin_amdgcn_ds_read_tr16_b64_v4i16(
                        (__attribute__((address_space(3))) s16x4*)(tile + (row + 4) * 64 + cb * 32 + p * 8));
                    s16x8 bb = {t0[0], t0[1], t0[2], t0[3], t1[0], t1[1], t1[2], t1[3]};
                    acc = __builtin_amdgcn_mfma_f32_32x32x16_bf16(__builtin_bit_cast(bf16x8, pv[s]), __builtin_bit_cast(bf16x8, bb), acc, 0, 0, 0);
                }
                const float* fp = fac + (2 * w + mm) * 32 + 4 * h2;
#pragma unroll
                for (int qd = 0; qd < 4; ++qd) {
                    f32x4 f4 = *(const f32x4*)(fp + 8 * qd);
#pragma unroll
                    for (int e2 = 0; e2 < 4; ++e2) {
                        if (mm == 0) tot[qd * 4 + e2] = f4[e2] * acc[qd * 4 + e2];
                        else tot[qd * 4 + e2] += f4[e2] * acc[qd * 4 + e2];
                    }
                }
            }
#pragma unroll
            for (int i = 0; i < 16; ++i) {
                int head = (i & 3) + 8 * (i >> 2) + 4 * h2;
                red[(w * 32 + head) * 32 + r] = tot[i];
            }
        }
        bar();
        {
            const int head = t >> 4, cq = (t & 15) * 2;
            f32x4 ma = *(const f32x4*)(Mws + head * 8), mb = *(const f32x4*)(Mws + head * 8 + 4);
            float M = fmaxf(fmaxf(fmaxf(ma[0], ma[1]), fmaxf(ma[2], ma[3])), fmaxf(fmaxf(mb[0], mb[1]), fmaxf(mb[2], mb[3])));
            float ew[8];
#pragma unroll
            for (int ww = 0; ww < 4; ++ww) { ew[ww] = __expf(ma[ww] - M); ew[ww + 4] = __expf(mb[ww] - M); }
            f32x2 s2 = {0.f, 0.f};
#pragma unroll
            for (int ww = 0; ww < 8; ++ww) s2 += ew[ww] * *(const f32x2*)(red + (ww * 32 + head) * 32 + cq);
            const unsigned ohi = pk2(s2[0], s2[1]);
            const unsigned olo = pk2(s2[0] - bflo(ohi), s2[1] - bfhi(ohi));
            const int oo = WS_OPART + (kc * 32 + head) * 2048 + (mem * 32 + cq) * 2;
            __builtin_amdgcn_raw_buffer_store_b32(ohi, rs, oo, 0, SC1);
            __builtin_amdgcn_raw_buffer_store_b32(olo, rs, oo + 1024, 0, SC1);
            if (mem == 0 && (t & 15) == 0) {
                f32x4 la = *(const f32x4*)(Lws + head * 8), lb = *(const f32x4*)(Lws + head * 8 + 4);
                float L = 0.f;
#pragma unroll
                for (int ww = 0; ww < 4; ++ww) L += ew[ww] * la[ww] + ew[ww + 4] * lb[ww];
                u32x2 ml = {asu(M), asu(L)};
                __builtin_amdgcn_raw_buffer_store_b64(ml, rs, WS_ML + (head * 8 + kc) * 8, 0, SC1);
            }
        }
        STAMP(7);
        if (b >= 32 || w != 0) return;
        {
            const u32x4 xv = *(const u32x4*)(x + lane * 8), gv = *(const u32x4*)(gw + lane * 8);
            float gp = 0.f;
#pragma unroll
            for (int k = 0; k < 4; ++k) gp += rbf(bflo(xv[k]) * bflo(gv[k])) + rbf(bfhi(xv[k]) * bfhi(gv[k]));
            gp = sum16(gp);
            gp = sw16(gp, gp);
            gp = sw32(gp, gp);
            const float gs = rbf(gp);
            const float g = rbf(1.f / (1.f + __expf(-gs)));
            const float g1 = rbf(1.f - g);
            const int rg = lane >> 2;
            const int ao = WS_GA + rg * 2048 + b * 64 + (lane & 3) * 16;
            const int bo = WS_GB + rg * 2048 + b * 64 + (lane & 3) * 16;
            const int bo2 = bo + (rg < 11 ? 32 : 16) * 2048;
            u32x4 a[8], bq_[3];
            for (unsigned spins = 0; spins < SPIN_CAP; ++spins) {
                asm volatile("" ::: "memory");
#pragma unroll
                for (int j = 0; j < 8; ++j) a[j] = __builtin_amdgcn_raw_buffer_load_b128(rs, ao + j * 32768, 0, SC1);
                bq_[0] = __builtin_amdgcn_raw_buffer_load_b128(rs, bo, 0, SC1);
                bq_[1] = __builtin_amdgcn_raw_buffer_load_b128(rs, bo + 32768, 0, SC1);
                bq_[2] = __builtin_amdgcn_raw_buffer_load_b128(rs, bo2, 0, SC1);
                unsigned m = umax(umax(max4(a[0]), max4(a[1])), umax(max4(a[2]), max4(a[3])));
                m = umax(m, umax(umax(max4(a[4]), max4(a[5])), umax(max4(a[6]), max4(a[7]))));
                m = umax(m, umax(max4(bq_[0]), umax(max4(bq_[1]), max4(bq_[2]))));
                if (__all(m != POISON)) break;
                __builtin_amdgcn_s_sleep(1);
            }
            STAMP(11);
            float s4[4];
#pragma unroll
            for (int k = 0; k < 4; ++k) {
                float av = ((asf(a[0][k]) + asf(a[1][k])) + (asf(a[2][k]) + asf(a[3][k]))) + ((asf(a[4][k]) + asf(a[5][k])) + (asf(a[6][k]) + asf(a[7][k])));
                float bv = (asf(bq_[0][k]) + asf(bq_[1][k])) + (rg < 11 ? asf(bq_[2][k]) : 0.f);
                s4[k] = sw32(av, bv);
            }
            float o2[2];
#pragma unroll
            for (int k = 0; k < 2; ++k) {
                float v = sw16(s4[k], s4[k + 2]);
                v += dppf<0x128>(v);
                v += dppf<0x124>(v);
                u32x2 ab = __builtin_amdgcn_permlane32_swap(asu(v), asu(v), false, false);
                o2[k] = rbf(g * rbf(asf(ab[0]))) + rbf(g1 * rbf(asf(ab[1])));
            }
            if ((lane & 44) == 0) *(unsigned*)(out + b * 16 + (lane & 3) * 4 + ((lane >> 4) & 1) * 2) = pk2(o2[0], o2[1]);
        }
        STAMP(12);
        return;
    }

    char* utile = lds;
    float* sm1 = (float*)(lds + 32768);
    float* sm2 = sm1 + 1024;
    float* sm3 = sm2 + 512;
    float* qs = sm3 + 256;
    lds_u32* vflg = (lds_u32*)flg;
    const int bb_ = b - 128;
    const int h = bb_ & 31, jj = bb_ >> 5;
    const bool has_ffn = bb_ < 43;

    if (w >= 8) {
        const unsigned e = __hip_atomic_load((gu32*)(ws + WS_EPOCH), RLX_AGENT);
        asm volatile("" :: "v"(pf0));
        bar();
        const unsigned tag = e + 1u;
        if (w == 8) {
            lds_wait(vflg + 0, 8);
            STAMP(1);
            f32x2 s = *(const f32x2*)(sm1 + 2 * lane);
#pragma unroll
            for (int ww = 1; ww < 8; ++ww) s += *(const f32x2*)(sm1 + ww * 128 + 2 * lane);
            u32x4 g = {tag, asu(s[0]), asu(s[1]), ~tag};
            __builtin_amdgcn_raw_buffer_store_b128(g, rs, WS_QGF + ((h * 4 + jj) * 64 + lane) * 16, 0, 0);
            __builtin_amdgcn_raw_buffer_store_b128(g, rs, WS_QG + ((h * 4 + jj) * 64 + lane) * 16, 0, SC1);
            for (unsigned spins = 0; spins < SPIN_CAP; ++spins) {
                asm volatile("" ::: "memory");
                u32x4 v[4];
                const int qb = ((spins & 3) == 3 ? WS_QG : WS_QGF) + (h * 4 * 64 + lane) * 16;
#pragma unroll
                for (int k = 0; k < 4; ++k) v[k] = __builtin_amdgcn_raw_buffer_load_b128(rs, qb + k * 1024, 0, SC1);
                unsigned bad = 0;
                float q0 = 0.f, q1 = 0.f;
#pragma unroll
                for (int k = 0; k < 4; ++k) {
                    bad |= (v[k][0] ^ tag) | (v[k][3] ^ ~tag);
                    q0 += asf(v[k][1]); q1 += asf(v[k][2]);
                }
                if (__all(bad == 0) || spins == SPIN_CAP - 1) {
                    f32x2 qq = {rbf(q0), rbf(q1)};
                    *(f32x2*)(qs + 2 * lane) = qq;
                    break;
                }
                __builtin_amdgcn_s_sleep(1);
            }
            lgkm0();
            if (lane == 0) vflg[1] = 1u;
            STAMP(2);
        } else {
            lds_wait(vflg + 3, 8);
            STAMP(1);
            f0_signal(ws, b, lane, tag);
            __builtin_amdgcn_s_sleep(B_SLEEP);
            f0_wait(rs, b, lane, tag);
            if (lane == 0) vflg[2] = 1u;
            if (b == 255 && lane == 0) __hip_atomic_store((gu32*)(ws + WS_EPOCH), tag, RLX_AGENT);
            STAMP(2);
        }
        return;
    }

    {
        int po;
        if (w < 2) po = WS_QABS + (lane >> 3) * 32768 + ((jj * 16 + w * 8 + (lane & 7)) * 32 + h) * 16;
        else if (w < 4) po = WS_GA + bb_ * 2048 + (w & 1) * 1024 + lane * 16;
        else po = WS_GB + bb_ * 2048 + (w & 1) * 1024 + lane * 16;
        if (w < 4 || (w < 6 && has_ffn)) __builtin_amdgcn_raw_buffer_store_b128(P4, rs, po, 0, SC1);
    }
    __builtin_amdgcn_sched_barrier(0);
    u32x4 wqv[4], wkv[4];
    u16 xr[4];
    {
        const int row = jj * 128 + (t >> 4);
#pragma unroll
        for (int i = 0; i < 4; ++i) xr[i] = x[row + 32 * i];
#pragma unroll
        for (int i = 0; i < 4; ++i)
            wqv[i] = *(const u32x4*)(wq + (size_t)(row + 32 * i) * 4096 + h * 128 + (t & 15) * 8);
#pragma unroll
        for (int i = 0; i < 4; ++i)
            wkv[i] = *(const u32x4*)(wuk + (size_t)(jj * 128 + 16 * w + (lane >> 4) * 4 + i) * 4096 + h * 128 + (lane & 15) * 8);
    }
    __builtin_amdgcn_sched_barrier(0);
    if (t < 4) flg[t] = 0u;
    bar();
    asm volatile("s_waitcnt vmcnt(12)" ::: "memory");
    asm volatile("" :: "v"(pf0));
    if (lane == 0) __hip_atomic_fetch_add(flg + 3, 1u, __ATOMIC_RELAXED, __HIP_MEMORY_SCOPE_WORKGROUP);
    __builtin_amdgcn_sched_barrier(0);
    asm volatile("s_waitcnt vmcnt(4)" ::: "memory");
    GATE(1); STAMP(1);
    {
        float acc[8];
#pragma unroll
        for (int k = 0; k < 4; ++k) {
            acc[2 * k] = bf2f(xr[0]) * bflo(wqv[0][k]) + bf2f(xr[1]) * bflo(wqv[1][k]) + bf2f(xr[2]) * bflo(wqv[2][k]) + bf2f(xr[3]) * bflo(wqv[3][k]);
            acc[2 * k + 1] = bf2f(xr[0]) * bfhi(wqv[0][k]) + bf2f(xr[1]) * bfhi(wqv[1][k]) + bf2f(xr[2]) * bfhi(wqv[2][k]) + bf2f(xr[3]) * bfhi(wqv[3][k]);
        }
        float s[4];
#pragma unroll
        for (int k = 0; k < 4; ++k) s[k] = sw16(acc[k], acc[k + 4]);
        f32x2 s2;
#pragma unroll
        for (int k = 0; k < 2; ++k) s2[k] = sw32(s[k], s[k + 2]);
        *(f32x2*)(sm1 + w * 128 + (lane & 15) * 8 + ((lane >> 4) & 1) * 4 + (lane >> 5) * 2) = s2;
        lgkm0();
        if (lane == 0) __hip_atomic_fetch_add(flg + 0, 1u, __ATOMIC_RELAXED, __HIP_MEMORY_SCOPE_WORKGROUP);
    }
    __builtin_amdgcn_sched_barrier(0);
    lds_wait(vflg + 1, 1);
    GATE(2); STAMP(2);
    {
        const int l16 = lane & 15;
        f32x4 qa0 = *(const f32x4*)(qs + l16 * 8), qa1 = *(const f32x4*)(qs + l16 * 8 + 4);
        float d[4];
#pragma unroll
        for (int i = 0; i < 4; ++i) {
            d[i] = qa0[0] * bflo(wkv[i][0]) + qa0[1] * bfhi(wkv[i][0]) + qa0[2] * bflo(wkv[i][1]) + qa0[3] * bfhi(wkv[i][1]);
            d[i] += qa1[0] * bflo(wkv[i][2]) + qa1[1] * bfhi(wkv[i][2]) + qa1[2] * bflo(wkv[i][3]) + qa1[3] * bfhi(wkv[i][3]);
        }
#pragma unroll
        for (int i = 0; i < 4; ++i) d[i] = sum16(d[i]) * 0.08838834764831845f;
        const u32x2 e0 = __builtin_amdgcn_permlane16_swap(pk2(d[0], d[1]), pk2(d[0], d[1]), false, false);
        const u32x2 e1 = __builtin_amdgcn_permlane16_swap(pk2(d[2], d[3]), pk2(d[2], d[3]), false, false);
        u32x4 o = {e0[0], e1[0], e0[1], e1[1]};
        if ((lane & 24) == 0)
            __builtin_amdgcn_raw_buffer_store_b128(o, rs, WS_QABS + (lane & 7) * 32768 + ((jj * 16 + w * 2 + (lane >> 5)) * 32 + h) * 16, 0, SC1);
    }
    __builtin_amdgcn_sched_barrier(0);
    if (lane == 0) __hip_atomic_fetch_add(flg + 0, 1u, __ATOMIC_RELAXED, __HIP_MEMORY_SCOPE_WORKGROUP);
    lds_wait(vflg + 0, 16);
    __builtin_amdgcn_sched_barrier(0);
    STAMP(3);
    u32x4 uvv[4], ovv[4];
    const int rg = lane >> 3, c8 = lane & 7;
#pragma unroll
    for (int i = 0; i < 4; ++i)
        uvv[i] = *(const u32x4*)(wuv + (size_t)((t >> 2) + 128 * i) * 4096 + h * 128 + jj * 32 + (t & 3) * 8);
#pragma unroll
    for (int i = 0; i < 4; ++i)
        ovv[i] = *(const u32x4*)(wo + ((size_t)h * 128 + jj * 32 + rg * 4 + i) * 512 + w * 64 + c8 * 8);
    __builtin_amdgcn_sched_barrier(0);
    u32x4 dv[4];
    if (has_ffn) {
        const int ch = FFN_CHUNK;
        u32x4 gv[4], uv_[4];
        u16 xf[4];
#pragma unroll
        for (int i = 0; i < 4; ++i) xf[i] = x[(t >> 2) + 128 * i];
#pragma unroll
        for (int i = 0; i < 4; ++i) {
            gv[i] = *(const u32x4*)(wg + (size_t)((t >> 2) + 128 * i) * FF + ch * 32 + (t & 3) * 8);
            uv_[i] = *(const u32x4*)(wu + (size_t)((t >> 2) + 128 * i) * FF + ch * 32 + (t & 3) * 8);
        }
#pragma unroll
        for (int i = 0; i < 4; ++i) dv[i] = *(const u32x4*)(wd + (size_t)(ch * 32 + rg * 4 + i) * 512 + w * 64 + c8 * 8);
        __builtin_amdgcn_sched_barrier(0);
#pragma unroll
        for (int i = 0; i < 4; ++i) *(u32x4*)(utile + ((t >> 2) + 128 * i) * 64 + (t & 3) * 16) = uvv[i];
        float p[16];
#pragma unroll
        for (int i = 0; i < 4; ++i) {
            const float xv = bf2f(xf[i]);
#pragma unroll
            for (int k = 0; k < 4; ++k) {
                if (i == 0) {
                    p[2 * k] = xv * bflo(gv[i][k]); p[2 * k + 1] = xv * bfhi(gv[i][k]);
                    p[8 + 2 * k] = xv * bflo(uv_[i][k]); p[8 + 2 * k + 1] = xv * bfhi(uv_[i][k]);
                } else {
                    p[2 * k] += xv * bflo(gv[i][k]); p[2 * k + 1] += xv * bfhi(gv[i][k]);
                    p[8 + 2 * k] += xv * bflo(uv_[i][k]); p[8 + 2 * k + 1] += xv * bfhi(uv_[i][k]);
                }
            }
        }
        float s8[8];
#pragma unroll
        for (int k = 0; k < 8; ++k) s8[k] = sw32(p[k], p[k + 8]);
        float s4[4];
#pragma unroll
        for (int k = 0; k < 4; ++k) s4[k] = sw16(s8[k], s8[k + 4]);
        bool up = lane & 8;
        float s2[2];
#pragma unroll
        for (int k = 0; k < 2; ++k) { float send = up ? s4[k] : s4[k + 2]; float keep = up ? s4[k + 2] : s4[k]; s2[k] = keep + dppf<0x128>(send); }
        up = lane & 4;
        float send = up ? s2[0] : s2[1]; float keep = up ? s2[1] : s2[0];
        float v = keep + dpp_xor4(send);
        sm2[w * 64 + (lane >> 5) * 32 + (lane & 3) * 8 + ((lane >> 4) & 1) * 4 + ((lane >> 3) & 1) * 2 + ((lane >> 2) & 1)] = v;
    } else {
#pragma unroll
        for (int i = 0; i < 4; ++i) *(u32x4*)(utile + ((t >> 2) + 128 * i) * 64 + (t & 3) * 16) = uvv[i];
    }
    bar();
    s16x8 ufr[4];
    {
        const int kg = lane >> 4, q = (lane & 15) >> 2, p = lane & 3;
#pragma unroll
        for (int s = 0; s < 2; ++s)
#pragma unroll
            for (int tl = 0; tl < 2; ++tl) {
                const int row = w * 64 + 32 * s + 8 * kg + q;
                s16x4 t0 = __builtin_amdgcn_ds_read_tr16_b64_v4i16(
                    (__attribute__((address_space(3))) s16x4*)(utile + row * 64 + tl * 32 + p * 8));
                s16x4 t1 = __builtin_amdgcn_ds_read_tr16_b64_v4i16(
                    (__attribute__((address_space(3))) s16x4*)(utile + (row + 4) * 64 + tl * 32 + p * 8));
                s16x8 bb = {t0[0], t0[1], t0[2], t0[3], t1[0], t1[1], t1[2], t1[3]};
                ufr[s * 2 + tl] = bb;
            }
    }
    if (has_ffn) {
        f32x4 g4 = *(const f32x4*)(sm2 + 4 * rg), u4 = *(const f32x4*)(sm2 + 32 + 4 * rg);
#pragma unroll
        for (int ww = 1; ww < 8; ++ww) { g4 += *(const f32x4*)(sm2 + ww * 64 + 4 * rg); u4 += *(const f32x4*)(sm2 + ww * 64 + 32 + 4 * rg); }
        float o[8];
#pragma unroll
        for (int i = 0; i < 4; ++i) {
            const float gs = rbf(g4[i]), us = rbf(u4[i]);
            const float sg = rbf(gs / (1.f + __expf(-gs)));
            const float hv = rbf(sg * us);
#pragma unroll
            for (int k = 0; k < 4; ++k) {
                if (i == 0) { o[2 * k] = hv * bflo(dv[i][k]); o[2 * k + 1] = hv * bfhi(dv[i][k]); }
                else { o[2 * k] += hv * bflo(dv[i][k]); o[2 * k + 1] += hv * bfhi(dv[i][k]); }
            }
        }
        float s4[4];
#pragma unroll
        for (int k = 0; k < 4; ++k) s4[k] = sw32(o[k], o[k + 4]);
        float s2[2];
#pragma unroll
        for (int k = 0; k < 2; ++k) s2[k] = sw16(s4[k], s4[k + 2]);
        const bool up = lane & 8;
        const float send = up ? s2[0] : s2[1], keep = up ? s2[1] : s2[0];
        const float v = keep + dppf<0x128>(send);
        __builtin_amdgcn_raw_buffer_store_b32(asu(v), rs, WS_GB + bb_ * 2048 + (w * 64 + c8 * 8 + (lane >> 5) * 4 + ((lane >> 4) & 1) * 2 + ((lane >> 3) & 1)) * 4, 0, SC1);
    }
    asm volatile("s_waitcnt vmcnt(1)" ::: "memory");
    lds_wait(vflg + 2, 1);
    STAMP(8);
    {
        const int kg = lane >> 4;
        u32x4 ov[2], ml;
        const int oo = WS_OPART + (((lane & 15) >> 1) * 64 + h * 2 + (lane & 1)) * 1024 + (w * 64 + 8 * kg) * 2;
        for (unsigned spins = 0; spins < SPIN_CAP; ++spins) {
            asm volatile("" ::: "memory");
#pragma unroll
            for (int i = 0; i < 2; ++i) ov[i] = __builtin_amdgcn_raw_buffer_load_b128(rs, oo + i * 64, 0, SC1);
            ml = __builtin_amdgcn_raw_buffer_load_b128(rs, WS_ML + (h * 8 + 2 * kg) * 8, 0, SC1);
            unsigned m = umax(umax(max4(ov[0]), max4(ov[1])), max4(ml));
            if (__all(m != POISON)) break;
            __builtin_amdgcn_s_sleep(1);
        }
        STAMP(9);
        f32x4 d0 = {0.f, 0.f, 0.f, 0.f}, d1 = {0.f, 0.f, 0.f, 0.f};
        d0 = __builtin_amdgcn_mfma_f32_16x16x32_bf16(__builtin_bit_cast(bf16x8, ov[0]), __builtin_bit_cast(bf16x8, ufr[0]), d0, 0, 0, 0);
        d1 = __builtin_amdgcn_mfma_f32_16x16x32_bf16(__builtin_bit_cast(bf16x8, ov[0]), __builtin_bit_cast(bf16x8, ufr[1]), d1, 0, 0, 0);
        d0 = __builtin_amdgcn_mfma_f32_16x16x32_bf16(__builtin_bit_cast(bf16x8, ov[1]), __builtin_bit_cast(bf16x8, ufr[2]), d0, 0, 0, 0);
        d1 = __builtin_amdgcn_mfma_f32_16x16x32_bf16(__builtin_bit_cast(bf16x8, ov[1]), __builtin_bit_cast(bf16x8, ufr[3]), d1, 0, 0, 0);
        const float m0 = asf(ml[0]), l0 = asf(ml[1]), m1 = asf(ml[2]), l1 = asf(ml[3]);
        const float M = mx32(mx16(fmaxf(m0, m1)));
        const float e0 = __expf(m0 - M), e1 = __expf(m1 - M);
        float L = e0 * l0 + e1 * l1;
        L = sw16(L, L);
        L = sw32(L, L);
        const float rl = 1.f / L;
        const float v0 = e0 * (d0[0] + d0[1]) + e1 * (d0[2] + d0[3]);
        const float v1 = e0 * (d1[0] + d1[1]) + e1 * (d1[2] + d1[3]);
        float v = sw16(v0, v1);
        v = sw32(v, v);
        if (lane < 32) sm3[w * 32 + lane] = v * rl;
    }
    bar();
    {
        f32x4 c4 = *(const f32x4*)(sm3 + 4 * rg);
#pragma unroll
        for (int ww = 1; ww < 8; ++ww) c4 += *(const f32x4*)(sm3 + ww * 32 + 4 * rg);
        float o[8];
#pragma unroll
        for (int i = 0; i < 4; ++i) {
            const float c0 = rbf(c4[i]);
#pragma unroll
            for (int k = 0; k < 4; ++k) {
                if (i == 0) { o[2 * k] = c0 * bflo(ovv[i][k]); o[2 * k + 1] = c0 * bfhi(ovv[i][k]); }
                else { o[2 * k] += c0 * bflo(ovv[i][k]); o[2 * k + 1] += c0 * bfhi(ovv[i][k]); }
            }
        }
        float s4[4];
#pragma unroll
        for (int k = 0; k < 4; ++k) s4[k] = sw32(o[k], o[k + 4]);
        float s2[2];
#pragma unroll
        for (int k = 0; k < 2; ++k) s2[k] = sw16(s4[k], s4[k + 2]);
        const bool up = lane & 8;
        const float send = up ? s2[0] : s2[1], keep = up ? s2[1] : s2[0];
        const float v = keep + dppf<0x128>(send);
        __builtin_amdgcn_raw_buffer_store_b32(asu(v), rs, WS_GA + bb_ * 2048 + (w * 64 + c8 * 8 + (lane >> 5) * 4 + ((lane >> 4) & 1) * 2 + ((lane >> 3) & 1)) * 4, 0, SC1);
    }
    STAMP(10);
}

extern "C" void kernel_launch(void* const* d_in, const int* in_sizes, int n_in,
                              void* d_out, int out_size, void* d_ws, size_t ws_size,
                              hipStream_t stream) {
    const u16* x = (const u16*)d_in[0];
    const u16* ckv = (const u16*)d_in[1];
    const u16* wq = (const u16*)d_in[2];
    const u16* wuk = (const u16*)d_in[3];
    const u16* wuv = (const u16*)d_in[4];
    const u16* wo = (const u16*)d_in[5];
    const u16* wg = (const u16*)d_in[7];
    const u16* wu = (const u16*)d_in[8];
    const u16* wd = (const u16*)d_in[9];
    const u16* gw = (const u16*)d_in[10];
    mega<<<256, 640, 0, stream>>>(x, ckv, wq, wuk, wuv, wo, wg, wu, wd, gw, (char*)d_ws, (u16*)d_out);
}
```

```cpp
#include <hip/hip_runtime.h>
#include <hip/hip_bf16.h>
#include <stdint.h>

#define SK 8192
#define FF 1376

typedef unsigned short u16;
typedef unsigned long long u64;
typedef __attribute__((ext_vector_type(8))) __bf16 bf16x8;
typedef __attribute__((ext_vector_type(2))) __bf16 bf16x2;
typedef __attribute__((ext_vector_type(4))) short s16x4;
typedef __attribute__((ext_vector_type(8))) short s16x8;
typedef __attribute__((ext_vector_type(16))) float f32x16;
typedef __attribute__((ext_vector_type(4))) float f32x4;
typedef __attribute__((ext_vector_type(2))) float f32x2;
typedef __attribute__((ext_vector_type(4))) unsigned int u32x4;
typedef __attribute__((ext_vector_type(2))) unsigned int u32x2;
#define GAS __attribute__((address_space(1)))
typedef GAS unsigned gu32;
typedef GAS u64 gu64;
#define RLX_AGENT __ATOMIC_RELAXED, __HIP_MEMORY_SCOPE_AGENT
#define SC1 16
#define POISON 0xFFFFFFFFu

__device__ __forceinline__ float bf2f(u16 v) { return __uint_as_float(((unsigned)v) << 16); }
__device__ __forceinline__ float bflo(unsigned v) { return __uint_as_float(v << 16); }
__device__ __forceinline__ float bfhi(unsigned v) { return __uint_as_float(v & 0xffff0000u); }
__device__ __forceinline__ float asf(unsigned v) { return __uint_as_float(v); }
__device__ __forceinline__ unsigned asu(float v) { return __float_as_uint(v); }
__device__ __forceinline__ unsigned pk2(float a, float b) {
    bf16x2 v = {(__bf16)a, (__bf16)b};
    return __builtin_bit_cast(unsigned, v);
}
__device__ __forceinline__ u16 f2bf(float f) { __bf16 v = (__bf16)f; return __builtin_bit_cast(u16, v); }
__device__ __forceinline__ float rbf(float f) { return bf2f(f2bf(f)); }
template <int CTRL> __device__ __forceinline__ float dppf(float v) {
    return asf((unsigned)__builtin_amdgcn_update_dpp(0, (int)asu(v), CTRL, 0xf, 0xf, true));
}
template <int CTRL> __device__ __forceinline__ unsigned dppu(unsigned v) {
    return (unsigned)__builtin_amdgcn_update_dpp(0, (int)v, CTRL, 0xf, 0xf, true);
}
__device__ __forceinline__ float dpp_xor4(float v) {
    int a = __builtin_amdgcn_update_dpp(0, (int)asu(v), 0x104, 0xf, 0x5, false);
    a = __builtin_amdgcn_update_dpp(a, (int)asu(v), 0x114, 0xf, 0xA, false);
    return asf((unsigned)a);
}
__device__ __forceinline__ float sw16(float a, float b) {
    u32x2 r_ = __builtin_amdgcn_permlane16_swap(asu(a), asu(b), false, false);
    return asf(r_[0]) + asf(r_[1]);
}
__device__ __forceinline__ float sw32(float a, float b) {
    u32x2 r_ = __builtin_amdgcn_permlane32_swap(asu(a), asu(b), false, false);
    return asf(r_[0]) + asf(r_[1]);
}
__device__ __forceinline__ float mx16(float a) {
    u32x2 r_ = __builtin_amdgcn_permlane16_swap(asu(a), asu(a), false, false);
    return fmaxf(asf(r_[0]), asf(r_[1]));
}
__device__ __forceinline__ float mx32(float a) {
    u32x2 r_ = __builtin_amdgcn_permlane32_swap(asu(a), asu(a), false, false);
    return fmaxf(asf(r_[0]), asf(r_[1]));
}
__device__ __forceinline__ float sum16(float v) {
    v += dppf<0xB1>(v); v += dppf<0x4E>(v); v += dppf<0x141>(v); v += dppf<0x140>(v);
    return v;
}
__device__ __forceinline__ float max16(float v) {
    v = fmaxf(v, dppf<0xB1>(v)); v = fmaxf(v, dppf<0x4E>(v)); v = fmaxf(v, dppf<0x141>(v)); v = fmaxf(v, dppf<0x140>(v));
    return v;
}
__device__ __forceinline__ unsigned umax(unsigned a, unsigned b) { return a > b ? a : b; }
__device__ __forceinline__ unsigned max4(u32x4 v) { return umax(umax(v[0], v[1]), umax(v[2], v[3])); }

#define NREP 32
#define WS_EPOCH   0
#define WS_F0      256
#define WS_QG      (WS_F0 + NREP * 2048)
#define WS_QABS    (WS_QG + 128 * 1024)
#define WS_P       (WS_QABS + 8 * 49152)
#define WS_MLOC    (WS_P + 8 * 16 * 4096)
#define WS_OPART   (WS_MLOC + 8 * 8 * 32 * 16)
#define WS_ML      (WS_OPART + 8 * 32 * 512 * 4)
#define WS_GA      (WS_ML + 32 * 8 * 8)
#define WS_GB      (WS_GA + 128 * 2048)
#define WS_QGF     (WS_GB + 128 * 2048)
#define WS_PF      (WS_QGF + 128 * 1024)
#define WS_MLOCF   (WS_PF + 8 * 16 * 4096)
#define WS_END     (WS_MLOCF + 8 * 8 * 32 * 16)

#define PF_LINES 136
#define FFN_CHUNK ((bb_ & 7) * 5 + ((bb_ & 7) < 3 ? (bb_ & 7) : 3) + (bb_ >> 3))
#define GATE_ID 0
#define GATE_X 2000
#define GATE(i) do { if (GATE_ID == (i)) { for (unsigned gs_ = 0; gs_ < 100000u; ++gs_) { if (__builtin_amdgcn_s_memrealtime() - t0_ >= (u64)GATE_X) break; __builtin_amdgcn_s_sleep(1); } } } while (0)
#define SPIN_CAP (1u << 17)
#define A_SLEEP 40
#define B_SLEEP 20
#define A_DELAY 0
#define STAMP(i)

__device__ __forceinline__ void lgkm0() { asm volatile("s_waitcnt lgkmcnt(0)" ::: "memory"); }
__device__ __forceinline__ void vm0() { asm volatile("s_waitcnt vmcnt(0)" ::: "memory"); }
__device__ __forceinline__ void bar() { asm volatile("s_waitcnt lgkmcnt(0)\n\ts_barrier" ::: "memory"); }
typedef volatile __attribute__((address_space(3))) unsigned lds_u32;
__device__ __forceinline__ void lds_wait(lds_u32* p, unsigned target) {
    for (unsigned s = 0; s < SPIN_CAP; ++s) {
        if (*p >= target) break;
        __builtin_amdgcn_s_sleep(1);
    }
    asm volatile("" ::: "memory");
}

__device__ __forceinline__ void f0_signal(char* ws, int b, int lane, unsigned tag) {
    const u64 want = ((u64)(~tag) << 32) | tag;
    if (lane < NREP) __hip_atomic_store((gu64*)(ws + WS_F0 + lane * 2048 + b * 8), want, RLX_AGENT);
}
__device__ __forceinline__ unsigned qtag(unsigned tag) { return tag * 2654435761u + 0x7F4A7C15u; }
__device__ __forceinline__ void f0_wait(const __amdgpu_buffer_rsrc_t rs, int b, int lane, unsigned tag) {
    const int fo = WS_F0 + (b & (NREP - 1)) * 2048 + lane * 16;
    for (unsigned spins = 0; spins < SPIN_CAP; ++spins) {
        asm volatile("" ::: "memory");
        u32x4 a0 = __builtin_amdgcn_raw_buffer_load_b128(rs, fo, 0, SC1);
        u32x4 a1 = __builtin_amdgcn_raw_buffer_load_b128(rs, fo + 1024, 0, SC1);
        unsigned bad = (a0[0] ^ tag) | (a0[1] ^ ~tag) | (a0[2] ^ tag) | (a0[3] ^ ~tag) |
                       (a1[0] ^ tag) | (a1[1] ^ ~tag) | (a1[2] ^ tag) | (a1[3] ^ ~tag);
        if (__all(bad == 0)) break;
        __builtin_amdgcn_s_sleep(8);
    }
    asm volatile("" ::: "memory");
}

#define LDS_BYTES (65536 + 65536 + 8192)

__global__ __launch_bounds__(640, 1) void mega(const u16* __restrict__ x, const u16* __restrict__ ckv,
                                               const u16* __restrict__ wq, const u16* __restrict__ wuk,
                                               const u16* __restrict__ wuv, const u16* __restrict__ wo,
                                               const u16* __restrict__ wg, const u16* __restrict__ wu,
                                               const u16* __restrict__ wd, const u16* __restrict__ gw,
                                               char* __restrict__ ws, u16* __restrict__ out) {
    __shared__ __attribute__((aligned(16))) char lds[LDS_BYTES];
    __shared__ unsigned flg[4];

    const int t = threadIdx.x;
    unsigned pf0;
    {
        u64 pc;
        asm volatile("s_getpc_b64 %0" : "=s"(pc));
        const GAS unsigned* cb = (const GAS unsigned*)(pc & ~255ull);
        const int l0 = 639 - t;
        pf0 = cb[(l0 < PF_LINES - 1 ? l0 : PF_LINES - 1) * 32];
        __builtin_amdgcn_sched_barrier(0);
    }
    const int b = blockIdx.x;
    const u64 t0_ = __builtin_amdgcn_s_memrealtime();
    const int lane = t & 63, w = t >> 6;
    const int r = lane & 31, h2 = lane >> 5;
    const __amdgpu_buffer_rsrc_t rs = __builtin_amdgcn_make_buffer_rsrc(ws, 0, WS_END, 0x00020000);
    const u32x4 P4 = {POISON, POISON, POISON, POISON};
    const u32x2 P2 = {POISON, POISON};
    STAMP(0);

    if (b < 128) {
        if (w >= 9) { asm volatile("" :: "v"(pf0)); return; }
        lds_u32* vflg = (lds_u32*)flg;
        if (w == 8) {
            const unsigned e = __hip_atomic_load((gu32*)(ws + WS_EPOCH), RLX_AGENT);
            if (lane < 4) flg[lane] = 0u;
            bar();
            asm volatile("" :: "v"(pf0));
            if (lane == 0) { vflg[1] = e + 1u; lgkm0(); vflg[0] = 1u; }
            lds_wait(vflg + 3, 8);
            f0_signal(ws, b, lane, e + 1u);
            if (b < 32) { unsigned short o_ = *(volatile const GAS u16*)(out + b * 16 + (lane & 15)); asm volatile("" :: "v"((unsigned)o_)); }
            STAMP(1);
            return;
        }
        char* tile = lds;
        float* red = (float*)(lds + 65536);
        float* fac = (float*)(lds + 131072);
        float* Mws = fac + 512;
        float* Lws = Mws + 256;
        float* fin = Lws + 256;
        float* fing = fin + 256;
        const int kc = b & 7, mem = b >> 3;
        __builtin_amdgcn_raw_buffer_store_b128(P4, rs, (w < 4 ? WS_P : WS_PF) + ((kc * 16 + mem) * 4 + (w & 3)) * 1024 + lane * 16, 0, SC1);
        if (w < 4)
            __builtin_amdgcn_raw_buffer_store_b128(P4, rs, WS_OPART + (kc * 64 + w * 16 + (lane >> 2)) * 1024 + mem * 64 + (lane & 3) * 16, 0, SC1);
        else if (w == 4)
            __builtin_amdgcn_raw_buffer_store_b64(P2, rs, (lane < 32 ? WS_MLOC : WS_MLOCF) + ((kc * 8 + (mem >> 1)) * 32 + r) * 16 + (mem & 1) * 8, 0, SC1);
        else if (w == 5 && mem == 0 && lane < 32)
            __builtin_amdgcn_raw_buffer_store_b64(P2, rs, WS_ML + (lane * 8 + kc) * 8, 0, SC1);
        __builtin_amdgcn_sched_barrier(0);
        __builtin_amdgcn_s_sleep(A_DELAY);
        bf16x8 bq[8];
        u32x4 cv[8];
        {
            const u16* bp = ckv + (size_t)(kc * 1024 + mem * 64 + r) * 512 + 64 * w + 8 * h2;
#pragma unroll
            for (int kb = 0; kb < 2; ++kb)
#pragma unroll
                for (int s = 0; s < 4; ++s) bq[kb * 4 + s] = *(const bf16x8*)(bp + kb * 32 * 512 + 16 * s);
            const u16* cp = ckv + (size_t)(kc * 1024 + (t >> 2)) * 512 + mem * 32 + (t & 3) * 8;
#pragma unroll
            for (int i = 0; i < 8; ++i) cv[i] = *(const u32x4*)(cp + (size_t)i * 128 * 512);
        }
        __builtin_amdgcn_sched_barrier(0);
        bar();
        asm volatile("s_waitcnt vmcnt(16)" ::: "memory");
        asm volatile("" :: "v"(pf0));
        if (lane == 0) __hip_atomic_fetch_add(flg + 3, 1u, __ATOMIC_RELAXED, __HIP_MEMORY_SCOPE_WORKGROUP);
        STAMP(1);
        __builtin_amdgcn_sched_barrier(0);
#pragma unroll
        for (int i = 0; i < 8; ++i) *(u32x4*)(tile + (i * 128 + (t >> 2)) * 64 + (t & 3) * 16) = cv[i];
        GATE(7); STAMP(2);
        lds_wait(vflg + 0, 1);
        const unsigned tag = vflg[1];
        {
            u32x4 av[4];
            {
                const unsigned qt = qtag(tag);
                u32x4 gq[6];
                const int qo = WS_QABS + (b & 7) * 49152 + (w * 384 + lane) * 16;
                for (unsigned spins = 0; spins < SPIN_CAP; ++spins) {
                    asm volatile("" ::: "memory");
#pragma unroll
                    for (int s = 0; s < 6; ++s) gq[s] = __builtin_amdgcn_raw_buffer_load_b128(rs, qo + 1024 * s, 0, SC1);
                    unsigned bad = ((gq[0][0] ^ qt) | (gq[1][0] ^ qt)) | ((gq[2][0] ^ qt) | (gq[3][0] ^ qt)) | ((gq[4][0] ^ qt) | (gq[5][0] ^ qt));
                    if (__all(bad == 0)) break;
                    __builtin_amdgcn_s_sleep(1);
                }
#pragma unroll
                for (int sp = 0; sp < 2; ++sp) {
                    u32x4 a0 = {gq[3 * sp][1], gq[3 * sp][2], gq[3 * sp][3], gq[3 * sp + 1][1]};
                    u32x4 a1 = {gq[3 * sp + 1][2], gq[3 * sp + 1][3], gq[3 * sp + 2][1], gq[3 * sp + 2][2]};
                    av[2 * sp] = a0; av[2 * sp + 1] = a1;
                }
            }
            GATE(3); STAMP(4);
#pragma unroll
            for (int kb = 0; kb < 2; ++kb) {
                f32x16 acc;
#pragma unroll
                for (int i = 0; i < 16; ++i) acc[i] = 0.f;
#pragma unroll
                for (int s = 0; s < 4; ++s)
                    acc = __builtin_amdgcn_mfma_f32_32x32x16_bf16(__builtin_bit_cast(bf16x8, av[s]), bq[kb * 4 + s], acc, 0, 0, 0);
#pragma unroll
                for (int i = 0; i < 16; ++i) {
                    int head = (i & 3) + 8 * (i >> 2) + 4 * h2;
                    red[(w * 32 + head) * 64 + kb * 32 + r] = acc[i];
                }
            }
        }
        bar();
        {
            const int head = t >> 4, kq = (t & 15) * 4;
            f32x4 s4 = *(const f32x4*)(red + head * 64 + kq);
#pragma unroll
            for (int ww = 1; ww < 8; ++ww) s4 += *(const f32x4*)(red + (ww * 32 + head) * 64 + kq);
            float m = fmaxf(fmaxf(s4[0], s4[1]), fmaxf(s4[2], s4[3]));
            m = max16(m);
            unsigned p01 = pk2(__expf(s4[0] - m), __expf(s4[1] - m));
            unsigned p23 = pk2(__expf(s4[2] - m), __expf(s4[3] - m));
            float l = (bflo(p01) + bfhi(p01)) + (bflo(p23) + bfhi(p23));
            l = sum16(l);
            u32x4 pp = {p01, p23, dppu<0xF5>(p01), dppu<0xF5>(p23)};
            u32x2 ml = {asu(m), asu(l)};
            const int po = ((kc * 16 + mem) * 256 + ((t & 15) >> 1) * 32 + head) * 16;
            const int mo = ((kc * 8 + (mem >> 1)) * 32 + head) * 16 + (mem & 1) * 8;
            if ((t & 1) == 0) __builtin_amdgcn_raw_buffer_store_b128(pp, rs, WS_PF + po, 0, 0);
            if ((t & 15) == 0) __builtin_amdgcn_raw_buffer_store_b64(ml, rs, WS_MLOCF + mo, 0, 0);
            if ((t & 1) == 0) __builtin_amdgcn_raw_buffer_store_b128(pp, rs, WS_P + po, 0, SC1);
            if ((t & 15) == 0) __builtin_amdgcn_raw_buffer_store_b64(ml, rs, WS_MLOC + mo, 0, SC1);
        }
        if (w == 7) f0_wait(rs, b, lane, tag);
        bar();
        STAMP(5);
        {
            u32x4 pv[8], mlv;
            for (unsigned spins = 0; spins < SPIN_CAP; ++spins) {
                asm volatile("" ::: "memory");
                const bool slow = (spins & 3) == 3;
                const int po = (slow ? WS_P : WS_PF) + ((kc * 16 + w * 2) * 256 + lane) * 16;
                const int mo = (slow ? WS_MLOC : WS_MLOCF) + ((kc * 8 + w) * 32 + r) * 16;
#pragma unroll
                for (int s = 0; s < 8; ++s) pv[s] = __builtin_amdgcn_raw_buffer_load_b128(rs, po + 1024 * s, 0, SC1);
                mlv = __builtin_amdgcn_raw_buffer_load_b128(rs, mo, 0, SC1);
                unsigned m = umax(umax(max4(pv[0]), max4(pv[1])), umax(max4(pv[2]), max4(pv[3])));
                m = umax(m, umax(umax(max4(pv[4]), max4(pv[5])), umax(max4(pv[6]), max4(pv[7]))));
                m = umax(m, max4(mlv));
                if (__all(m != POISON)) break;
                __builtin_amdgcn_s_sleep(1);
            }
            GATE(4); STAMP(6);
            {
                float m0 = asf(mlv[0]), l0 = asf(mlv[1]), m1 = asf(mlv[2]), l1 = asf(mlv[3]);
                float Mw = fmaxf(m0, m1);
                float f0_ = __expf(m0 - Mw), f1_ = __expf(m1 - Mw);
                if (lane < 32) {
                    fac[(2 * w) * 32 + r] = f0_; fac[(2 * w + 1) * 32 + r] = f1_;
                    Mws[r * 8 + w] = Mw; Lws[r * 8 + w] = f0_ * l0 + f1_ * l1;
                }
            }
            const int g = lane >> 4;
            const int q = (lane & 15) >> 2, p = lane & 3;
            const int cb = g & 1;
            f32x16 tot;
#pragma unroll
            for (int mm = 0; mm < 2; ++mm) {
                f32x16 acc;
#pragma unroll
                for (int i = 0; i < 16; ++i) acc[i] = 0.f;
#pragma unroll
                for (int ss = 0; ss < 4; ++ss) {
                    int s = mm * 4 + ss;
                    int row = w * 128 + 16 * s + 8 * h2 + q;
                    s16x4 t0 = __builtin_amdgcn_ds_read_tr16_b64_v4i16(
                        (__attribute__((address_space(3))) s16x4*)(tile + row * 64 + cb * 32 + p * 8));
                    s16x4 t1 = __builtin_amdgcn_ds_read_tr16_b64_v4i16(
                        (__attribute__((address_space(3))) s16x4*)(tile + (row + 4) * 64 + cb * 32 + p * 8));
                    s16x8 bb = {t0[0], t0[1], t0[2], t0[3], t1[0], t1[1], t1[2], t1[3]};
                    acc = __builtin_amdgcn_mfma_f32_32x32x16_bf16(__builtin_bit_cast(bf16x8, pv[s]), __builtin_bit_cast(bf16x8, bb), acc, 0, 0, 0);
                }
                const float* fp = fac + (2 * w + mm) * 32 + 4 * h2;
#pragma unroll
                for (int qd = 0; qd < 4; ++qd) {
                    f32x4 f4 = *(const f32x4*)(fp + 8 * qd);
#pragma unroll
                    for (int e2 = 0; e2 < 4; ++e2) {
                        if (mm == 0) tot[qd * 4 + e2] = f4[e2] * acc[qd * 4 + e2];
                        else tot[qd * 4 + e2] += f4[e2] * acc[qd * 4 + e2];
                    }
                }
            }
#pragma unroll
            for (int i = 0; i < 16; ++i) {
                int head = (i & 3) + 8 * (i >> 2) + 4 * h2;
                red[(w * 32 + head) * 32 + r] = tot[i];
            }
        }
        bar();
        {
            const int head = t >> 4, cq = (t & 15) * 2;
            f32x4 ma = *(const f32x4*)(Mws + head * 8), mb = *(const f32x4*)(Mws + head * 8 + 4);
            float M = fmaxf(fmaxf(fmaxf(ma[0], ma[1]), fmaxf(ma[2], ma[3])), fmaxf(fmaxf(mb[0], mb[1]), fmaxf(mb[2], mb[3])));
            float ew[8];
#pragma unroll
            for (int ww = 0; ww < 4; ++ww) { ew[ww] = __expf(ma[ww] - M); ew[ww + 4] = __expf(mb[ww] - M); }
            f32x2 s2 = {0.f, 0.f};
#pragma unroll
            for (int ww = 0; ww < 8; ++ww) s2 += ew[ww] * *(const f32x2*)(red + (ww * 32 + head) * 32 + cq);
            const unsigned ohi = pk2(s2[0], s2[1]);
            const unsigned olo = pk2(s2[0] - bflo(ohi), s2[1] - bfhi(ohi));
            const int oo = WS_OPART + (kc * 32 + head) * 2048 + (mem * 32 + cq) * 2;
            __builtin_amdgcn_raw_buffer_store_b32(ohi, rs, oo, 0, SC1);
            __builtin_amdgcn_raw_buffer_store_b32(olo, rs, oo + 1024, 0, SC1);
            if (mem == 0 && (t & 15) == 0) {
                f32x4 la = *(const f32x4*)(Lws + head * 8), lb = *(const f32x4*)(Lws + head * 8 + 4);
                float L = 0.f;
#pragma unroll
                for (int ww = 0; ww < 4; ++ww) L += ew[ww] * la[ww] + ew[ww + 4] * lb[ww];
                u32x2 ml = {asu(M), asu(L)};
                __builtin_amdgcn_raw_buffer_store_b64(ml, rs, WS_ML + (head * 8 + kc) * 8, 0, SC1);
            }
        }
        STAMP(7);
        if (b >= 32 || w != 0) return;
        {
            const u32x4 xv = *(const u32x4*)(x + lane * 8), gv = *(const u32x4*)(gw + lane * 8);
            float gp = 0.f;
#pragma unroll
            for (int k = 0; k < 4; ++k) gp += rbf(bflo(xv[k]) * bflo(gv[k])) + rbf(bfhi(xv[k]) * bfhi(gv[k]));
            gp = sum16(gp);
            gp = sw16(gp, gp);
            gp = sw32(gp, gp);
            const float gs = rbf(gp);
            const float g = rbf(1.f / (1.f + __expf(-gs)));
            const float g1 = rbf(1.f - g);
            const int rg = lane >> 2;
            const int ao = WS_GA + rg * 2048 + b * 64 + (lane & 3) * 16;
            const int bo = WS_GB + rg * 2048 + b * 64 + (lane & 3) * 16;
            const int bo2 = bo + (rg < 11 ? 32 : 16) * 2048;
            u32x4 a[8], bq_[3];
            for (unsigned spins = 0; spins < SPIN_CAP; ++spins) {
                asm volatile("" ::: "memory");
#pragma unroll
                for (int j = 0; j < 8; ++j) a[j] = __builtin_amdgcn_raw_buffer_load_b128(rs, ao + j * 32768, 0, SC1);
                bq_[0] = __builtin_amdgcn_raw_buffer_load_b128(rs, bo, 0, SC1);
                bq_[1] = __builtin_amdgcn_raw_buffer_load_b128(rs, bo + 32768, 0, SC1);
                bq_[2] = __builtin_amdgcn_raw_buffer_load_b128(rs, bo2, 0, SC1);
                unsigned m = umax(umax(max4(a[0]), max4(a[1])), umax(max4(a[2]), max4(a[3])));
                m = umax(m, umax(umax(max4(a[4]), max4(a[5])), umax(max4(a[6]), max4(a[7]))));
                m = umax(m, umax(max4(bq_[0]), umax(max4(bq_[1]), max4(bq_[2]))));
                if (__all(m != POISON)) break;
                __builtin_amdgcn_s_sleep(1);
            }
            STAMP(11);
            float s4[4];
#pragma unroll
            for (int k = 0; k < 4; ++k) {
                float av = ((asf(a[0][k]) + asf(a[1][k])) + (asf(a[2][k]) + asf(a[3][k]))) + ((asf(a[4][k]) + asf(a[5][k])) + (asf(a[6][k]) + asf(a[7][k])));
                float bv = (asf(bq_[0][k]) + asf(bq_[1][k])) + (rg < 11 ? asf(bq_[2][k]) : 0.f);
                s4[k] = sw32(av, bv);
            }
            float o2[2];
#pragma unroll
            for (int k = 0; k < 2; ++k) {
                float v = sw16(s4[k], s4[k + 2]);
                v += dppf<0x128>(v);
                v += dppf<0x124>(v);
                u32x2 ab = __builtin_amdgcn_permlane32_swap(asu(v), asu(v), false, false);
                o2[k] = rbf(g * rbf(asf(ab[0]))) + rbf(g1 * rbf(asf(ab[1])));
            }
            if ((lane & 44) == 0) *(unsigned*)(out + b * 16 + (lane & 3) * 4 + ((lane >> 4) & 1) * 2) = pk2(o2[0], o2[1]);
        }
        STAMP(12);
        return;
    }

    char* utile = lds;
    float* sm1 = (float*)(lds + 32768);
    float* sm2 = sm1 + 1024;
    float* sm3 = sm2 + 512;
    float* qs = sm3 + 256;
    lds_u32* vflg = (lds_u32*)flg;
    const int bb_ = b - 128;
    const int h = bb_ & 31, jj = bb_ >> 5;
    const bool has_ffn = bb_ < 43;

    if (w >= 8) {
        const unsigned e = __hip_atomic_load((gu32*)(ws + WS_EPOCH), RLX_AGENT);
        asm volatile("" :: "v"(pf0));
        bar();
        const unsigned tag = e + 1u;
        if (w == 8) {
            lds_wait(vflg + 0, 8);
            STAMP(1);
            f32x2 s = *(const f32x2*)(sm1 + 2 * lane);
#pragma unroll
            for (int ww = 1; ww < 8; ++ww) s += *(const f32x2*)(sm1 + ww * 128 + 2 * lane);
            u32x4 g = {tag, asu(s[0]), asu(s[1]), ~tag};
            __builtin_amdgcn_raw_buffer_store_b128(g, rs, WS_QGF + ((h * 4 + jj) * 64 + lane) * 16, 0, 0);
            __builtin_amdgcn_raw_buffer_store_b128(g, rs, WS_QG + ((h * 4 + jj) * 64 + lane) * 16, 0, SC1);
            for (unsigned spins = 0; spins < SPIN_CAP; ++spins) {
                asm volatile("" ::: "memory");
                u32x4 v[4];
                const int qb = ((spins & 3) == 3 ? WS_QG : WS_QGF) + (h * 4 * 64 + lane) * 16;
#pragma unroll
                for (int k = 0; k < 4; ++k) v[k] = __builtin_amdgcn_raw_buffer_load_b128(rs, qb + k * 1024, 0, SC1);
                unsigned bad = 0;
                float q0 = 0.f, q1 = 0.f;
#pragma unroll
                for (int k = 0; k < 4; ++k) {
                    bad |= (v[k][0] ^ tag) | (v[k][3] ^ ~tag);
                    q0 += asf(v[k][1]); q1 += asf(v[k][2]);
                }
                if (__all(bad == 0) || spins == SPIN_CAP - 1) {
                    f32x2 qq = {rbf(q0), rbf(q1)};
                    *(f32x2*)(qs + 2 * lane) = qq;
                    if (lane == 0) qs[128] = asf(qtag(tag));
                    break;
                }
                __builtin_amdgcn_s_sleep(1);
            }
            lgkm0();
            if (lane == 0) vflg[1] = 1u;
            STAMP(2);
        } else {
            lds_wait(vflg + 3, 8);
            STAMP(1);
            f0_signal(ws, b, lane, tag);
            __builtin_amdgcn_s_sleep(B_SLEEP);
            f0_wait(rs, b, lane, tag);
            if (lane == 0) vflg[2] = 1u;
            if (b == 255 && lane == 0) __hip_atomic_store((gu32*)(ws + WS_EPOCH), tag, RLX_AGENT);
            STAMP(2);
        }
        return;
    }

    {
        int po;
        if (w < 2) po = WS_GA + bb_ * 2048 + (w & 1) * 1024 + lane * 16;
        else po = WS_GB + bb_ * 2048 + (w & 1) * 1024 + lane * 16;
        if (w < 2 || (w < 4 && has_ffn)) __builtin_amdgcn_raw_buffer_store_b128(P4, rs, po, 0, SC1);
    }
    __builtin_amdgcn_sched_barrier(0);
    u32x4 wqv[4], wkv[4];
    u16 xr[4];
    {
        const int row = jj * 128 + (t >> 4);
#pragma unroll
        for (int i = 0; i < 4; ++i) xr[i] = x[row + 32 * i];
#pragma unroll
        for (int i = 0; i < 4; ++i)
            wqv[i] = *(const u32x4*)(wq + (size_t)(row + 32 * i) * 4096 + h * 128 + (t & 15) * 8);
#pragma unroll
        for (int i = 0; i < 4; ++i)
            wkv[i] = *(const u32x4*)(wuk + (size_t)(jj * 128 + 64 * (w >> 2) + 32 * (w & 1) + 16 * (lane >> 5) + 8 * ((w >> 1) & 1) + ((lane >> 4) & 1) * 4 + i) * 4096 + h * 128 + (lane & 15) * 8);
    }
    __builtin_amdgcn_sched_barrier(0);
    if (t < 4) flg[t] = 0u;
    bar();
    asm volatile("s_waitcnt vmcnt(12)" ::: "memory");
    asm volatile("" :: "v"(pf0));
    if (lane == 0) __hip_atomic_fetch_add(flg + 3, 1u, __ATOMIC_RELAXED, __HIP_MEMORY_SCOPE_WORKGROUP);
    __builtin_amdgcn_sched_barrier(0);
    asm volatile("s_waitcnt vmcnt(4)" ::: "memory");
    GATE(1); STAMP(1);
    {
        float acc[8];
#pragma unroll
        for (int k = 0; k < 4; ++k) {
            acc[2 * k] = bf2f(xr[0]) * bflo(wqv[0][k]) + bf2f(xr[1]) * bflo(wqv[1][k]) + bf2f(xr[2]) * bflo(wqv[2][k]) + bf2f(xr[3]) * bflo(wqv[3][k]);
            acc[2 * k + 1] = bf2f(xr[0]) * bfhi(wqv[0][k]) + bf2f(xr[1]) * bfhi(wqv[1][k]) + bf2f(xr[2]) * bfhi(wqv[2][k]) + bf2f(xr[3]) * bfhi(wqv[3][k]);
        }
        float s[4];
#pragma unroll
        for (int k = 0; k < 4; ++k) s[k] = sw16(acc[k], acc[k + 4]);
        f32x2 s2;
#pragma unroll
        for (int k = 0; k < 2; ++k) s2[k] = sw32(s[k], s[k + 2]);
        *(f32x2*)(sm1 + w * 128 + (lane & 15) * 8 + ((lane >> 4) & 1) * 4 + (lane >> 5) * 2) = s2;
        lgkm0();
        if (lane == 0) __hip_atomic_fetch_add(flg + 0, 1u, __ATOMIC_RELAXED, __HIP_MEMORY_SCOPE_WORKGROUP);
    }
    __builtin_amdgcn_sched_barrier(0);
    lds_wait(vflg + 1, 1);
    GATE(2); STAMP(2);
    {
        const int l16 = lane & 15;
        f32x4 qa0 = *(const f32x4*)(qs + l16 * 8), qa1 = *(const f32x4*)(qs + l16 * 8 + 4);
        const unsigned qt = asu(qs[128]);
        float d[4];
#pragma unroll
        for (int i = 0; i < 4; ++i) {
            d[i] = qa0[0] * bflo(wkv[i][0]) + qa0[1] * bfhi(wkv[i][0]) + qa0[2] * bflo(wkv[i][1]) + qa0[3] * bfhi(wkv[i][1]);
            d[i] += qa1[0] * bflo(wkv[i][2]) + qa1[1] * bfhi(wkv[i][2]) + qa1[2] * bflo(wkv[i][3]) + qa1[3] * bfhi(wkv[i][3]);
        }
#pragma unroll
        for (int i = 0; i < 4; ++i) d[i] = sum16(d[i]) * 0.08838834764831845f;
        const u32x2 e0 = __builtin_amdgcn_permlane16_swap(pk2(d[0], d[1]), pk2(d[0], d[1]), false, false);
        const u32x2 e1 = __builtin_amdgcn_permlane16_swap(pk2(d[2], d[3]), pk2(d[2], d[3]), false, false);
        const u32x2 f0 = __builtin_amdgcn_permlane32_swap(e0[0], e0[0], false, false);
        const u32x2 f1 = __builtin_amdgcn_permlane32_swap(e1[0], e1[0], false, false);
        const u32x2 f2 = __builtin_amdgcn_permlane32_swap(e0[1], e0[1], false, false);
        const u32x2 f3 = __builtin_amdgcn_permlane32_swap(e1[1], e1[1], false, false);
        const int part = lane >> 3;
        u32x4 o;
        o[0] = qt;
        o[1] = part == 0 ? f0[0] : (part == 1 ? f3[0] : f2[1]);
        o[2] = part == 0 ? f1[0] : (part == 1 ? f0[1] : f3[1]);
        o[3] = part == 0 ? f2[0] : f1[1];
        if (lane < 24)
            __builtin_amdgcn_raw_buffer_store_b128(o, rs, WS_QABS + (lane & 7) * 49152 + (((jj * 2 + (w >> 2)) * 6 + (w & 1) * 3 + part) * 64 + ((w >> 1) & 1) * 32 + h) * 16, 0, SC1);
    }
    __builtin_amdgcn_sched_barrier(0);
    if (lane == 0) __hip_atomic_fetch_add(flg + 0, 1u, __ATOMIC_RELAXED, __HIP_MEMORY_SCOPE_WORKGROUP);
    lds_wait(vflg + 0, 16);
    __builtin_amdgcn_sched_barrier(0);
    STAMP(3);
    u32x4 uvv[4], ovv[4];
    const int rg = lane >> 3, c8 = lane & 7;
#pragma unroll
    for (int i = 0; i < 4; ++i)
        uvv[i] = *(const u32x4*)(wuv + (size_t)((t >> 2) + 128 * i) * 4096 + h * 128 + jj * 32 + (t & 3) * 8);
#pragma unroll
    for (int i = 0; i < 4; ++i)
        ovv[i] = *(const u32x4*)(wo + ((size_t)h * 128 + jj * 32 + rg * 4 + i) * 512 + w * 64 + c8 * 8);
    __builtin_amdgcn_sched_barrier(0);
    u32x4 dv[4];
    if (has_ffn) {
        const int ch = FFN_CHUNK;
        u32x4 gv[4], uv_[4];
        u16 xf[4];
#pragma unroll
        for (int i = 0; i < 4; ++i) xf[i] = x[(t >> 2) + 128 * i];
#pragma unroll
        for (int i = 0; i < 4; ++i) {
            gv[i] = *(const u32x4*)(wg + (size_t)((t >> 2) + 128 * i) * FF + ch * 32 + (t & 3) * 8);
            uv_[i] = *(const u32x4*)(wu + (size_t)((t >> 2) + 128 * i) * FF + ch * 32 + (t & 3) * 8);
        }
#pragma unroll
        for (int i = 0; i < 4; ++i) dv[i] = *(const u32x4*)(wd + (size_t)(ch * 32 + rg * 4 + i) * 512 + w * 64 + c8 * 8);
        __builtin_amdgcn_sched_barrier(0);
#pragma unroll
        for (int i = 0; i < 4; ++i) *(u32x4*)(utile + ((t >> 2) + 128 * i) * 64 + (t & 3) * 16) = uvv[i];
        float p[16];
#pragma unroll
        for (int i = 0; i < 4; ++i) {
            const float xv = bf2f(xf[i]);
#pragma unroll
            for (int k = 0; k < 4; ++k) {
                if (i == 0) {
                    p[2 * k] = xv * bflo(gv[i][k]); p[2 * k + 1] = xv * bfhi(gv[i][k]);
                    p[8 + 2 * k] = xv * bflo(uv_[i][k]); p[8 + 2 * k + 1] = xv * bfhi(uv_[i][k]);
                } else {
                    p[2 * k] += xv * bflo(gv[i][k]); p[2 * k + 1] += xv * bfhi(gv[i][k]);
                    p[8 + 2 * k] += xv * bflo(uv_[i][k]); p[8 + 2 * k + 1] += xv * bfhi(uv_[i][k]);
                }
            }
        }
        float s8[8];
#pragma unroll
        for (int k = 0; k < 8; ++k) s8[k] = sw32(p[k], p[k + 8]);
        float s4[4];
#pragma unroll
        for (int k = 0; k < 4; ++k) s4[k] = sw16(s8[k], s8[k + 4]);
        bool up = lane & 8;
        float s2[2];
#pragma unroll
        for (int k = 0; k < 2; ++k) { float send = up ? s4[k] : s4[k + 2]; float keep = up ? s4[k + 2] : s4[k]; s2[k] = keep + dppf<0x128>(send); }
        up = lane & 4;
        float send = up ? s2[0] : s2[1]; float keep = up ? s2[1] : s2[0];
        float v = keep + dpp_xor4(send);
        sm2[w * 64 + (lane >> 5) * 32 + (lane & 3) * 8 + ((lane >> 4) & 1) * 4 + ((lane >> 3) & 1) * 2 + ((lane >> 2) & 1)] = v;
    } else {
#pragma unroll
        for (int i = 0; i < 4; ++i) *(u32x4*)(utile + ((t >> 2) + 128 * i) * 64 + (t & 3) * 16) = uvv[i];
    }
    bar();
    s16x8 ufr[4];
    {
        const int kg = lane >> 4, q = (lane & 15) >> 2, p = lane & 3;
#pragma unroll
        for (int s = 0; s < 2; ++s)
#pragma unroll
            for (int tl = 0; tl < 2; ++tl) {
                const int row = w * 64 + 32 * s + 8 * kg + q;
                s16x4 t0 = __builtin_amdgcn_ds_read_tr16_b64_v4i16(
                    (__attribute__((address_space(3))) s16x4*)(utile + row * 64 + tl * 32 + p * 8));
                s16x4 t1 = __builtin_amdgcn_ds_read_tr16_b64_v4i16(
                    (__attribute__((address_space(3))) s16x4*)(utile + (row + 4) * 64 + tl * 32 + p * 8));
                s16x8 bb = {t0[0], t0[1], t0[2], t0[3], t1[0], t1[1], t1[2], t1[3]};
                ufr[s * 2 + tl] = bb;
            }
    }
    if (has_ffn) {
        f32x4 g4 = *(const f32x4*)(sm2 + 4 * rg), u4 = *(const f32x4*)(sm2 + 32 + 4 * rg);
#pragma unroll
        for (int ww = 1; ww < 8; ++ww) { g4 += *(const f32x4*)(sm2 + ww * 64 + 4 * rg); u4 += *(const f32x4*)(sm2 + ww * 64 + 32 + 4 * rg); }
        float o[8];
#pragma unroll
        for (int i = 0; i < 4; ++i) {
            const float gs = rbf(g4[i]), us = rbf(u4[i]);
            const float sg = rbf(gs / (1.f + __expf(-gs)));
            const float hv = rbf(sg * us);
#pragma unroll
            for (int k = 0; k < 4; ++k) {
                if (i == 0) { o[2 * k] = hv * bflo(dv[i][k]); o[2 * k + 1] = hv * bfhi(dv[i][k]); }
                else { o[2 * k] += hv * bflo(dv[i][k]); o[2 * k + 1] += hv * bfhi(dv[i][k]); }
            }
        }
        float s4[4];
#pragma unroll
        for (int k = 0; k < 4; ++k) s4[k] = sw32(o[k], o[k + 4]);
        float s2[2];
#pragma unroll
        for (int k = 0; k < 2; ++k) s2[k] = sw16(s4[k], s4[k + 2]);
        const bool up = lane & 8;
        const float send = up ? s2[0] : s2[1], keep = up ? s2[1] : s2[0];
        const float v = keep + dppf<0x128>(send);
        __builtin_amdgcn_raw_buffer_store_b32(asu(v), rs, WS_GB + bb_ * 2048 + (w * 64 + c8 * 8 + (lane >> 5) * 4 + ((lane >> 4) & 1) * 2 + ((lane >> 3) & 1)) * 4, 0, SC1);
    }
    asm volatile("s_waitcnt vmcnt(1)" ::: "memory");
    lds_wait(vflg + 2, 1);
    STAMP(8);
    {
        const int kg = lane >> 4;
        u32x4 ov[2], ml;
        const int oo = WS_OPART + (((lane & 15) >> 1) * 64 + h * 2 + (lane & 1)) * 1024 + (w * 64 + 8 * kg) * 2;
        for (unsigned spins = 0; spins < SPIN_CAP; ++spins) {
            asm volatile("" ::: "memory");
#pragma unroll
            for (int i = 0; i < 2; ++i) ov[i] = __builtin_amdgcn_raw_buffer_load_b128(rs, oo + i * 64, 0, SC1);
            ml = __builtin_amdgcn_raw_buffer_load_b128(rs, WS_ML + (h * 8 + 2 * kg) * 8, 0, SC1);
            unsigned m = umax(umax(max4(ov[0]), max4(ov[1])), max4(ml));
            if (__all(m != POISON)) break;
            __builtin_amdgcn_s_sleep(1);
        }
        STAMP(9);
        f32x4 d0 = {0.f, 0.f, 0.f, 0.f}, d1 = {0.f, 0.f, 0.f, 0.f};
        d0 = __builtin_amdgcn_mfma_f32_16x16x32_bf16(__builtin_bit_cast(bf16x8, ov[0]), __builtin_bit_cast(bf16x8, ufr[0]), d0, 0, 0, 0);
        d1 = __builtin_amdgcn_mfma_f32_16x16x32_bf16(__builtin_bit_cast(bf16x8, ov[0]), __builtin_bit_cast(bf16x8, ufr[1]), d1, 0, 0, 0);
        d0 = __builtin_amdgcn_mfma_f32_16x16x32_bf16(__builtin_bit_cast(bf16x8, ov[1]), __builtin_bit_cast(bf16x8, ufr[2]), d0, 0, 0, 0);
        d1 = __builtin_amdgcn_mfma_f32_16x16x32_bf16(__builtin_bit_cast(bf16x8, ov[1]), __builtin_bit_cast(bf16x8, ufr[3]), d1, 0, 0, 0);
        const float m0 = asf(ml[0]), l0 = asf(ml[1]), m1 = asf(ml[2]), l1 = asf(ml[3]);
        const float M = mx32(mx16(fmaxf(m0, m1)));
        const float e0 = __expf(m0 - M), e1 = __expf(m1 - M);
        float L = e0 * l0 + e1 * l1;
        L = sw16(L, L);
        L = sw32(L, L);
        const float rl = 1.f / L;
        const float v0 = e0 * (d0[0] + d0[1]) + e1 * (d0[2] + d0[3]);
        const float v1 = e0 * (d1[0] + d1[1]) + e1 * (d1[2] + d1[3]);
        float v = sw16(v0, v1);
        v = sw32(v, v);
        if (lane < 32) sm3[w * 32 + lane] = v * rl;
    }
    bar();
    {
        f32x4 c4 = *(const f32x4*)(sm3 + 4 * rg);
#pragma unroll
        for (int ww = 1; ww < 8; ++ww) c4 += *(const f32x4*)(sm3 + ww * 32 + 4 * rg);
        float o[8];
#pragma unroll
        for (int i = 0; i < 4; ++i) {
            const float c0 = rbf(c4[i]);
#pragma unroll
            for (int k = 0; k < 4; ++k) {
                if (i == 0) { o[2 * k] = c0 * bflo(ovv[i][k]); o[2 * k + 1] = c0 * bfhi(ovv[i][k]); }
                else { o[2 * k] += c0 * bflo(ovv[i][k]); o[2 * k + 1] += c0 * bfhi(ovv[i][k]); }
            }
        }
        float s4[4];
#pragma unroll
        for (int k = 0; k < 4; ++k) s4[k] = sw32(o[k], o[k + 4]);
        float s2[2];
#pragma unroll
        for (int k = 0; k < 2; ++k) s2[k] = sw16(s4[k], s4[k + 2]);
        const bool up = lane & 8;
        const float send = up ? s2[0] : s2[1], keep = up ? s2[1] : s2[0];
        const float v = keep + dppf<0x128>(send);
        __builtin_amdgcn_raw_buffer_store_b32(asu(v), rs, WS_GA + bb_ * 2048 + (w * 64 + c8 * 8 + (lane >> 5) * 4 + ((lane >> 4) & 1) * 2 + ((lane >> 3) & 1)) * 4, 0, SC1);
    }
    STAMP(10);
}

extern "C" void kernel_launch(void* const* d_in, const int* in_sizes, int n_in,
                              void* d_out, int out_size, void* d_ws, size_t ws_size,
                              hipStream_t stream) {
    const u16* x = (const u16*)d_in[0];
    const u16* ckv = (const u16*)d_in[1];
    const u16* wq = (const u16*)d_in[2];
    const u16* wuk = (const u16*)d_in[3];
    const u16* wuv = (const u16*)d_in[4];
    const u16* wo = (const u16*)d_in[5];
    const u16* wg = (const u16*)d_in[7];
    const u16* wu = (const u16*)d_in[8];
    const u16* wd = (const u16*)d_in[9];
    const u16* gw = (const u16*)d_in[10];
    mega<<<256, 640, 0, stream>>>(x, ckv, wq, wuk, wuv, wo, wg, wu, wd, gw, (char*)d_ws, (u16*)d_out);
}
```

```cpp
#include <hip/hip_runtime.h>
#include <hip/hip_bf16.h>
#include <stdint.h>

#define SK 8192
#define FF 1376

typedef unsigned short u16;
typedef unsigned long long u64;
typedef __attribute__((ext_vector_type(8))) __bf16 bf16x8;
typedef __attribute__((ext_vector_type(2))) __bf16 bf16x2;
typedef __attribute__((ext_vector_type(4))) short s16x4;
typedef __attribute__((ext_vector_type(8))) short s16x8;
typedef __attribute__((ext_vector_type(16))) float f32x16;
typedef __attribute__((ext_vector_type(4))) float f32x4;
typedef __attribute__((ext_vector_type(2))) float f32x2;
typedef __attribute__((ext_vector_type(4))) unsigned int u32x4;
typedef __attribute__((ext_vector_type(2))) unsigned int u32x2;
#define GAS __attribute__((address_space(1)))
typedef GAS unsigned gu32;
typedef GAS u64 gu64;
#define RLX_AGENT __ATOMIC_RELAXED, __HIP_MEMORY_SCOPE_AGENT
#define SC1 16
#define POISON 0xFFFFFFFFu

__device__ __forceinline__ float bf2f(u16 v) { return __uint_as_float(((unsigned)v) << 16); }
__device__ __forceinline__ float bflo(unsigned v) { return __uint_as_float(v << 16); }
__device__ __forceinline__ float bfhi(unsigned v) { return __uint_as_float(v & 0xffff0000u); }
__device__ __forceinline__ float asf(unsigned v) { return __uint_as_float(v); }
__device__ __forceinline__ unsigned asu(float v) { return __float_as_uint(v); }
__device__ __forceinline__ unsigned pk2(float a, float b) {
    bf16x2 v = {(__bf16)a, (__bf16)b};
    return __builtin_bit_cast(unsigned, v);
}
__device__ __forceinline__ u16 f2bf(float f) { __bf16 v = (__bf16)f; return __builtin_bit_cast(u16, v); }
__device__ __forceinline__ float rbf(float f) { return bf2f(f2bf(f)); }
template <int CTRL> __device__ __forceinline__ float dppf(float v) {
    return asf((unsigned)__builtin_amdgcn_update_dpp(0, (int)asu(v), CTRL, 0xf, 0xf, true));
}
template <int CTRL> __device__ __forceinline__ unsigned dppu(unsigned v) {
    return (unsigned)__builtin_amdgcn_update_dpp(0, (int)v, CTRL, 0xf, 0xf, true);
}
__device__ __forceinline__ float dpp_xor4(float v) {
    int a = __builtin_amdgcn_update_dpp(0, (int)asu(v), 0x104, 0xf, 0x5, false);
    a = __builtin_amdgcn_update_dpp(a, (int)asu(v), 0x114, 0xf, 0xA, false);
    return asf((unsigned)a);
}
__device__ __forceinline__ float sw16(float a, float b) {
    u32x2 r_ = __builtin_amdgcn_permlane16_swap(asu(a), asu(b), false, false);
    return asf(r_[0]) + asf(r_[1]);
}
__device__ __forceinline__ float sw32(float a, float b) {
    u32x2 r_ = __builtin_amdgcn_permlane32_swap(asu(a), asu(b), false, false);
    return asf(r_[0]) + asf(r_[1]);
}
__device__ __forceinline__ float mx16(float a) {
    u32x2 r_ = __builtin_amdgcn_permlane16_swap(asu(a), asu(a), false, false);
    return fmaxf(asf(r_[0]), asf(r_[1]));
}
__device__ __forceinline__ float mx32(float a) {
    u32x2 r_ = __builtin_amdgcn_permlane32_swap(asu(a), asu(a), false, false);
    return fmaxf(asf(r_[0]), asf(r_[1]));
}
__device__ __forceinline__ float sum16(float v) {
    v += dppf<0xB1>(v); v += dppf<0x4E>(v); v += dppf<0x141>(v); v += dppf<0x140>(v);
    return v;
}
__device__ __forceinline__ float max16(float v) {
    v = fmaxf(v, dppf<0xB1>(v)); v = fmaxf(v, dppf<0x4E>(v)); v = fmaxf(v, dppf<0x141>(v)); v = fmaxf(v, dppf<0x140>(v));
    return v;
}
__device__ __forceinline__ unsigned umax(unsigned a, unsigned b) { return a > b ? a : b; }
__device__ __forceinline__ unsigned max4(u32x4 v) { return umax(umax(v[0], v[1]), umax(v[2], v[3])); }

#define NREP 32
#define WS_EPOCH   0
#define WS_F0      256
#define WS_QG      (WS_F0 + NREP * 2048)
#define WS_QABS    (WS_QG + 128 * 1024)
#define WS_P       (WS_QABS + 8 * 32768)
#define WS_MLOC    (WS_P + 8 * 16 * 4096)
#define WS_OPART   (WS_MLOC + 8 * 8 * 32 * 16)
#define WS_ML      (WS_OPART + 8 * 32 * 512 * 4)
#define WS_GA      (WS_ML + 32 * 8 * 8)
#define WS_GB      (WS_GA + 128 * 2048)
#define WS_QGF     (WS_GB + 128 * 2048)
#define WS_PF      (WS_QGF + 128 * 1024)
#define WS_MLOCF   (WS_PF + 8 * 16 * 4096)
#define WS_END     (WS_MLOCF + 8 * 8 * 32 * 16)

#define PF_LINES 136
#define O_SENT 1
#define O_SLEEP 2
#define BP9 1
#define VHOIST 1
#define FFN_CHUNK ((bb_ & 7) * 5 + ((bb_ & 7) < 3 ? (bb_ & 7) : 3) + (bb_ >> 3))
#define GATE_ID 0
#define GATE_X 2000
#define GATE(i) do { if (GATE_ID == (i)) { for (unsigned gs_ = 0; gs_ < 100000u; ++gs_) { if (__builtin_amdgcn_s_memrealtime() - t0_ >= (u64)GATE_X) break; __builtin_amdgcn_s_sleep(1); } } } while (0)
#define SPIN_CAP (1u << 17)
#define A_SLEEP 40
#define B_SLEEP 20
#define A_DELAY 0
#define STAMP(i)

__device__ __forceinline__ void lgkm0() { asm volatile("s_waitcnt lgkmcnt(0)" ::: "memory"); }
__device__ __forceinline__ void vm0() { asm volatile("s_waitcnt vmcnt(0)" ::: "memory"); }
__device__ __forceinline__ void bar() { asm volatile("s_waitcnt lgkmcnt(0)\n\ts_barrier" ::: "memory"); }
typedef volatile __attribute__((address_space(3))) unsigned lds_u32;
__device__ __forceinline__ void lds_wait(lds_u32* p, unsigned target) {
    for (unsigned s = 0; s < SPIN_CAP; ++s) {
        if (*p >= target) break;
        __builtin_amdgcn_s_sleep(1);
    }
    asm volatile("" ::: "memory");
}

__device__ __forceinline__ void f0_signal(char* ws, int b, int lane, unsigned tag) {
    const u64 want = ((u64)(~tag) << 32) | tag;
    if (lane < NREP) __hip_atomic_store((gu64*)(ws + WS_F0 + lane * 2048 + b * 8), want, RLX_AGENT);
}
__device__ __forceinline__ void f0_wait(const __amdgpu_buffer_rsrc_t rs, int b, int lane, unsigned tag) {
    const int fo = WS_F0 + (b & (NREP - 1)) * 2048 + lane * 16;
    for (unsigned spins = 0; spins < SPIN_CAP; ++spins) {
        asm volatile("" ::: "memory");
        u32x4 a0 = __builtin_amdgcn_raw_buffer_load_b128(rs, fo, 0, SC1);
        u32x4 a1 = __builtin_amdgcn_raw_buffer_load_b128(rs, fo + 1024, 0, SC1);
        unsigned bad = (a0[0] ^ tag) | (a0[1] ^ ~tag) | (a0[2] ^ tag) | (a0[3] ^ ~tag) |
                       (a1[0] ^ tag) | (a1[1] ^ ~tag) | (a1[2] ^ tag) | (a1[3] ^ ~tag);
        if (__all(bad == 0)) break;
        __builtin_amdgcn_s_sleep(8);
    }
    asm volatile("" ::: "memory");
}

#define LDS_BYTES (65536 + 65536 + 8192)

__global__ __launch_bounds__(640, 1) void mega(const u16* __restrict__ x, const u16* __restrict__ ckv,
                                               const u16* __restrict__ wq, const u16* __restrict__ wuk,
                                               const u16* __restrict__ wuv, const u16* __restrict__ wo,
                                               const u16* __restrict__ wg, const u16* __restrict__ wu,
                                               const u16* __restrict__ wd, const u16* __restrict__ gw,
                                               char* __restrict__ ws, u16* __restrict__ out) {
    __shared__ __attribute__((aligned(16))) char lds[LDS_BYTES];
    __shared__ unsigned flg[8];

    const int t = threadIdx.x;
    unsigned pf0;
    {
        u64 pc;
        asm volatile("s_getpc_b64 %0" : "=s"(pc));
        const GAS unsigned* cb = (const GAS unsigned*)(pc & ~255ull);
        const int l0 = 639 - t;
        pf0 = cb[(l0 < PF_LINES - 1 ? l0 : PF_LINES - 1) * 32];
        __builtin_amdgcn_sched_barrier(0);
    }
    const int b = blockIdx.x;
    const u64 t0_ = __builtin_amdgcn_s_memrealtime();
    const int lane = t & 63, w = t >> 6;
    const int r = lane & 31, h2 = lane >> 5;
    const __amdgpu_buffer_rsrc_t rs = __builtin_amdgcn_make_buffer_rsrc(ws, 0, WS_END, 0x00020000);
    const u32x4 P4 = {POISON, POISON, POISON, POISON};
    const u32x2 P2 = {POISON, POISON};
    STAMP(0);

    if (b < 128) {
        if (w >= 9) { asm volatile("" :: "v"(pf0)); return; }
        lds_u32* vflg = (lds_u32*)flg;
        if (w == 8) {
            const unsigned e = __hip_atomic_load((gu32*)(ws + WS_EPOCH), RLX_AGENT);
            asm volatile("" :: "v"(pf0));
            bar();
            f0_signal(ws, b, lane, e + 1u);
            if (b < 32) { unsigned short o_ = *(volatile const GAS u16*)(out + b * 16 + (lane & 15)); asm volatile("" :: "v"((unsigned)o_)); }
            asm volatile("s_barrier" ::: "memory");
            STAMP(1);
            __builtin_amdgcn_s_sleep(A_SLEEP);
            f0_wait(rs, b, lane, e + 1u);
            if (lane == 0) vflg[2] = 1u;
            STAMP(2);
            return;
        }
        char* tile = lds;
        float* red = (float*)(lds + 65536);
        float* fac = (float*)(lds + 131072);
        float* Mws = fac + 512;
        float* Lws = Mws + 256;
        float* fin = Lws + 256;
        float* fing = fin + 256;
        const int kc = b & 7, mem = b >> 3;
        __builtin_amdgcn_raw_buffer_store_b128(P4, rs, (w < 4 ? WS_P : WS_PF) + ((kc * 16 + mem) * 4 + (w & 3)) * 1024 + lane * 16, 0, SC1);
        if (w < 4)
            __builtin_amdgcn_raw_buffer_store_b128(P4, rs, WS_OPART + (kc * 64 + w * 16 + (lane >> 2)) * 1024 + mem * 64 + (lane & 3) * 16, 0, SC1);
        else if (w == 4)
            __builtin_amdgcn_raw_buffer_store_b64(P2, rs, (lane < 32 ? WS_MLOC : WS_MLOCF) + ((kc * 8 + (mem >> 1)) * 32 + r) * 16 + (mem & 1) * 8, 0, SC1);
        else if (w == 5 && mem == 0 && lane < 32)
            __builtin_amdgcn_raw_buffer_store_b64(P2, rs, WS_ML + (lane * 8 + kc) * 8, 0, SC1);
        if (t < 4) flg[t] = 0u;
        vm0();
        asm volatile("" :: "v"(pf0));
        bar();
        asm volatile("s_barrier" ::: "memory");
        STAMP(1);
        __builtin_amdgcn_s_sleep(A_DELAY);
        bf16x8 bq[8];
        u32x4 cv[8];
        {
            const u16* bp = ckv + (size_t)(kc * 1024 + mem * 64 + r) * 512 + 64 * w + 8 * h2;
#pragma unroll
            for (int kb = 0; kb < 2; ++kb)
#pragma unroll
                for (int s = 0; s < 4; ++s) bq[kb * 4 + s] = *(const bf16x8*)(bp + kb * 32 * 512 + 16 * s);
            const u16* cp = ckv + (size_t)(kc * 1024 + (t >> 2)) * 512 + mem * 32 + (t & 3) * 8;
#pragma unroll
            for (int i = 0; i < 8; ++i) cv[i] = *(const u32x4*)(cp + (size_t)i * 128 * 512);
        }
        __builtin_amdgcn_sched_barrier(0);
#pragma unroll
        for (int i = 0; i < 8; ++i) *(u32x4*)(tile + (i * 128 + (t >> 2)) * 64 + (t & 3) * 16) = cv[i];
        GATE(7); STAMP(2);
        lds_wait(vflg + 2, 1);
        GATE(8); STAMP(3);
        {
            u32x4 av[4];
            const int qo = WS_QABS + (b & 7) * 32768 + (w * 256 + lane) * 16;
            for (unsigned spins = 0; spins < SPIN_CAP; ++spins) {
                asm volatile("" ::: "memory");
#pragma unroll
                for (int s = 0; s < 4; ++s) av[s] = __builtin_amdgcn_raw_buffer_load_b128(rs, qo + 1024 * s, 0, SC1);
                unsigned m = umax(umax(max4(av[0]), max4(av[1])), umax(max4(av[2]), max4(av[3])));
                if (__all(m != POISON)) break;
                __builtin_amdgcn_s_sleep(1);
            }
            GATE(3); STAMP(4);
#pragma unroll
            for (int kb = 0; kb < 2; ++kb) {
                f32x16 acc;
#pragma unroll
                for (int i = 0; i < 16; ++i) acc[i] = 0.f;
#pragma unroll
                for (int s = 0; s < 4; ++s)
                    acc = __builtin_amdgcn_mfma_f32_32x32x16_bf16(__builtin_bit_cast(bf16x8, av[s]), bq[kb * 4 + s], acc, 0, 0, 0);
#pragma unroll
                for (int i = 0; i < 16; ++i) {
                    int head = (i & 3) + 8 * (i >> 2) + 4 * h2;
                    red[(w * 32 + head) * 64 + kb * 32 + r] = acc[i];
                }
            }
        }
        bar();
        {
            const int head = t >> 4, kq = (t & 15) * 4;
            f32x4 s4 = *(const f32x4*)(red + head * 64 + kq);
#pragma unroll
            for (int ww = 1; ww < 8; ++ww) s4 += *(const f32x4*)(red + (ww * 32 + head) * 64 + kq);
            float m = fmaxf(fmaxf(s4[0], s4[1]), fmaxf(s4[2], s4[3]));
            m = max16(m);
            unsigned p01 = pk2(__expf(s4[0] - m), __expf(s4[1] - m));
            unsigned p23 = pk2(__expf(s4[2] - m), __expf(s4[3] - m));
            float l = (bflo(p01) + bfhi(p01)) + (bflo(p23) + bfhi(p23));
            l = sum16(l);
            u32x4 pp = {p01, p23, dppu<0xF5>(p01), dppu<0xF5>(p23)};
            u32x2 ml = {asu(m), asu(l)};
            const int po = ((kc * 16 + mem) * 256 + ((t & 15) >> 1) * 32 + head) * 16;
            const int mo = ((kc * 8 + (mem >> 1)) * 32 + head) * 16 + (mem & 1) * 8;
            if ((t & 1) == 0) __builtin_amdgcn_raw_buffer_store_b128(pp, rs, WS_PF + po, 0, 0);
            if ((t & 15) == 0) __builtin_amdgcn_raw_buffer_store_b64(ml, rs, WS_MLOCF + mo, 0, 0);
            if ((t & 1) == 0) __builtin_amdgcn_raw_buffer_store_b128(pp, rs, WS_P + po, 0, SC1);
            if ((t & 15) == 0) __builtin_amdgcn_raw_buffer_store_b64(ml, rs, WS_MLOC + mo, 0, SC1);
        }
        s16x8 vfr[8];
        {
            const int g = lane >> 4;
            const int q = (lane & 15) >> 2, p = lane & 3;
            const int cb = g & 1;
#pragma unroll
            for (int s = 0; s < 8; ++s) {
                int row = w * 128 + 16 * s + 8 * h2 + q;
                s16x4 t0 = __builtin_amdgcn_ds_read_tr16_b64_v4i16(
                    (__attribute__((address_space(3))) s16x4*)(tile + row * 64 + cb * 32 + p * 8));
                s16x4 t1 = __builtin_amdgcn_ds_read_tr16_b64_v4i16(
                    (__attribute__((address_space(3))) s16x4*)(tile + (row + 4) * 64 + cb * 32 + p * 8));
                s16x8 bb = {t0[0], t0[1], t0[2], t0[3], t1[0], t1[1], t1[2], t1[3]};
                vfr[s] = bb;
            }
        }
        bar();
        STAMP(5);
        {
            u32x4 pv[8], mlv;
            for (unsigned spins = 0; spins < SPIN_CAP; ++spins) {
                asm volatile("" ::: "memory");
                const bool slow = (spins & 3) == 3;
                const int po = (slow ? WS_P : WS_PF) + ((kc * 16 + w * 2) * 256 + lane) * 16;
                const int mo = (slow ? WS_MLOC : WS_MLOCF) + ((kc * 8 + w) * 32 + r) * 16;
#pragma unroll
                for (int s = 0; s < 8; ++s) pv[s] = __builtin_amdgcn_raw_buffer_load_b128(rs, po + 1024 * s, 0, SC1);
                mlv = __builtin_amdgcn_raw_buffer_load_b128(rs, mo, 0, SC1);
                unsigned m = umax(umax(max4(pv[0]), max4(pv[1])), umax(max4(pv[2]), max4(pv[3])));
                m = umax(m, umax(umax(max4(pv[4]), max4(pv[5])), umax(max4(pv[6]), max4(pv[7]))));
                m = umax(m, max4(mlv));
                if (__all(m != POISON)) break;
                __builtin_amdgcn_s_sleep(1);
            }
            GATE(4); STAMP(6);
            {
                float m0 = asf(mlv[0]), l0 = asf(mlv[1]), m1 = asf(mlv[2]), l1 = asf(mlv[3]);
                float Mw = fmaxf(m0, m1);
                float f0_ = __expf(m0 - Mw), f1_ = __expf(m1 - Mw);
                if (lane < 32) {
                    fac[(2 * w) * 32 + r] = f0_; fac[(2 * w + 1) * 32 + r] = f1_;
                    Mws[r * 8 + w] = Mw; Lws[r * 8 + w] = f0_ * l0 + f1_ * l1;
                }
            }
            const int g = lane >> 4;
            const int q = (lane & 15) >> 2, p = lane & 3;
            const int cb = g & 1;
            f32x16 tot;
#pragma unroll
            for (int mm = 0; mm < 2; ++mm) {
                f32x16 acc;
#pragma unroll
                for (int i = 0; i < 16; ++i) acc[i] = 0.f;
#pragma unroll
                for (int ss = 0; ss < 4; ++ss) {
                    int s = mm * 4 + ss;
                    s16x8 bb = vfr[s];
                    acc = __builtin_amdgcn_mfma_f32_32x32x16_bf16(__builtin_bit_cast(bf16x8, pv[s]), __builtin_bit_cast(bf16x8, bb), acc, 0, 0, 0);
                }
                const float* fp = fac + (2 * w + mm) * 32 + 4 * h2;
#pragma unroll
                for (int qd = 0; qd < 4; ++qd) {
                    f32x4 f4 = *(const f32x4*)(fp + 8 * qd);
#pragma unroll
                    for (int e2 = 0; e2 < 4; ++e2) {
                        if (mm == 0) tot[qd * 4 + e2] = f4[e2] * acc[qd * 4 + e2];
                        else tot[qd * 4 + e2] += f4[e2] * acc[qd * 4 + e2];
                    }
                }
            }
#pragma unroll
            for (int i = 0; i < 16; ++i) {
                int head = (i & 3) + 8 * (i >> 2) + 4 * h2;
                red[(w * 32 + head) * 32 + r] = tot[i];
            }
        }
        bar();
        {
            const int head = t >> 4, cq = (t & 15) * 2;
            f32x4 ma = *(const f32x4*)(Mws + head * 8), mb = *(const f32x4*)(Mws + head * 8 + 4);
            float M = fmaxf(fmaxf(fmaxf(ma[0], ma[1]), fmaxf(ma[2], ma[3])), fmaxf(fmaxf(mb[0], mb[1]), fmaxf(mb[2], mb[3])));
            float ew[8];
#pragma unroll
            for (int ww = 0; ww < 4; ++ww) { ew[ww] = __expf(ma[ww] - M); ew[ww + 4] = __expf(mb[ww] - M); }
            f32x2 s2 = {0.f, 0.f};
#pragma unroll
            for (int ww = 0; ww < 8; ++ww) s2 += ew[ww] * *(const f32x2*)(red + (ww * 32 + head) * 32 + cq);
            const unsigned ohi = pk2(s2[0], s2[1]);
            const unsigned olo = pk2(s2[0] - bflo(ohi), s2[1] - bfhi(ohi));
            const int oo = WS_OPART + (kc * 32 + head) * 2048 + (mem * 32 + cq) * 2;
            __builtin_amdgcn_raw_buffer_store_b32(ohi, rs, oo, 0, SC1);
            __builtin_amdgcn_raw_buffer_store_b32(olo, rs, oo + 1024, 0, SC1);
            if (mem == 0 && (t & 15) == 0) {
                f32x4 la = *(const f32x4*)(Lws + head * 8), lb = *(const f32x4*)(Lws + head * 8 + 4);
                float L = 0.f;
#pragma unroll
                for (int ww = 0; ww < 4; ++ww) L += ew[ww] * la[ww] + ew[ww + 4] * lb[ww];
                u32x2 ml = {asu(M), asu(L)};
                __builtin_amdgcn_raw_buffer_store_b64(ml, rs, WS_ML + (head * 8 + kc) * 8, 0, SC1);
            }
        }
        STAMP(7);
        if (b >= 32 || w != 0) return;
        {
            const int rg = lane >> 2;
            const int ao = WS_GA + rg * 2048 + b * 64 + (lane & 3) * 16;
            u32x4 a[8];
            for (unsigned spins = 0; spins < SPIN_CAP; ++spins) {
                asm volatile("" ::: "memory");
#pragma unroll
                for (int j = 0; j < 8; ++j) a[j] = __builtin_amdgcn_raw_buffer_load_b128(rs, ao + j * 32768, 0, SC1);
                unsigned m = umax(umax(max4(a[0]), max4(a[1])), umax(max4(a[2]), max4(a[3])));
                m = umax(m, umax(umax(max4(a[4]), max4(a[5])), umax(max4(a[6]), max4(a[7]))));
                if (__all(m != POISON)) break;
                __builtin_amdgcn_s_sleep(1);
            }
            STAMP(11);
            float s4[4];
#pragma unroll
            for (int k = 0; k < 4; ++k)
                s4[k] = ((asf(a[0][k]) + asf(a[1][k])) + (asf(a[2][k]) + asf(a[3][k]))) + ((asf(a[4][k]) + asf(a[5][k])) + (asf(a[6][k]) + asf(a[7][k])));
            float v = sw16(sw32(s4[0], s4[2]), sw32(s4[1], s4[3]));
            v += dppf<0x128>(v);
            v += dppf<0x124>(v);
            if ((lane & 12) == 0) out[b * 16 + (lane & 3) * 4 + (lane >> 4)] = f2bf(v);
        }
        STAMP(12);
        return;
    }

    char* utile = lds;
    float* sm1 = (float*)(lds + 32768);
    float* sm2 = sm1 + 1024;
    float* sm3 = sm2 + 512;
    float* qs = sm3 + 256;
    lds_u32* vflg = (lds_u32*)flg;
    const int bb_ = b - 128;
    const int h = bb_ & 31, jj = bb_ >> 5;
    const int xs = bb_ & 7, sl = bb_ >> 3;
    const bool has_ffn = (sl >> 1) < (xs < 3 ? 6 : 5);

    if (w >= 8) {
        const unsigned e = __hip_atomic_load((gu32*)(ws + WS_EPOCH), RLX_AGENT);
        if (w == 9) {
            __builtin_amdgcn_raw_buffer_store_b128(P4, rs, WS_QABS + (lane >> 3) * 32768 + ((jj * 16 + (lane & 7)) * 32 + h) * 16, 0, SC1);
            __builtin_amdgcn_raw_buffer_store_b128(P4, rs, WS_QABS + (lane >> 3) * 32768 + ((jj * 16 + 8 + (lane & 7)) * 32 + h) * 16, 0, SC1);
            __builtin_amdgcn_raw_buffer_store_b128(P4, rs, WS_GA + bb_ * 2048 + lane * 16, 0, SC1);
            __builtin_amdgcn_raw_buffer_store_b128(P4, rs, WS_GA + bb_ * 2048 + 1024 + lane * 16, 0, SC1);
        }
        bar();
        asm volatile("" :: "v"(pf0));
        const unsigned tag = e + 1u;
        if (w == 8) {
            lds_wait(vflg + 0, 8);
            STAMP(1);
            f32x2 s = *(const f32x2*)(sm1 + 2 * lane);
#pragma unroll
            for (int ww = 1; ww < 8; ++ww) s += *(const f32x2*)(sm1 + ww * 128 + 2 * lane);
            u32x4 g = {tag, asu(s[0]), asu(s[1]), ~tag};
            __builtin_amdgcn_raw_buffer_store_b128(g, rs, WS_QGF + ((h * 4 + jj) * 64 + lane) * 16, 0, 0);
            __builtin_amdgcn_raw_buffer_store_b128(g, rs, WS_QG + ((h * 4 + jj) * 64 + lane) * 16, 0, SC1);
            for (unsigned spins = 0; spins < SPIN_CAP; ++spins) {
                asm volatile("" ::: "memory");
                u32x4 v[4];
                const int qb = ((spins & 3) == 3 ? WS_QG : WS_QGF) + (h * 4 * 64 + lane) * 16;
#pragma unroll
                for (int k = 0; k < 4; ++k) v[k] = __builtin_amdgcn_raw_buffer_load_b128(rs, qb + k * 1024, 0, SC1);
                unsigned bad = 0;
                float q0 = 0.f, q1 = 0.f;
#pragma unroll
                for (int k = 0; k < 4; ++k) {
                    bad |= (v[k][0] ^ tag) | (v[k][3] ^ ~tag);
                    q0 += asf(v[k][1]); q1 += asf(v[k][2]);
                }
                if (__all(bad == 0) || spins == SPIN_CAP - 1) {
                    f32x2 qq = {rbf(q0), rbf(q1)};
                    *(f32x2*)(qs + 2 * lane) = qq;
                    break;
                }
                __builtin_amdgcn_s_sleep(1);
            }
            lds_wait(vflg + 3, 1);
            lgkm0();
            if (lane == 0) vflg[1] = 1u;
            STAMP(2);
        } else {
            vm0();
            if (lane == 0) { vflg[4] = tag; lgkm0(); vflg[3] = 1u; }
            STAMP(1);
            f0_signal(ws, b, lane, tag);
            __builtin_amdgcn_s_sleep(B_SLEEP);
            f0_wait(rs, b, lane, tag);
            if (lane == 0) vflg[2] = 1u;
            if (b == 255 && lane == 0) __hip_atomic_store((gu32*)(ws + WS_EPOCH), tag, RLX_AGENT);
            STAMP(2);
        }
        return;
    }

    u32x4 wqv[4], wkv[4];
    u16 xr[4];
    {
        const int row = jj * 128 + (t >> 4);
#pragma unroll
        for (int i = 0; i < 4; ++i) xr[i] = x[row + 32 * i];
#pragma unroll
        for (int i = 0; i < 4; ++i)
            wqv[i] = *(const u32x4*)(wq + (size_t)(row + 32 * i) * 4096 + h * 128 + (t & 15) * 8);
#pragma unroll
        for (int i = 0; i < 4; ++i)
            wkv[i] = *(const u32x4*)(wuk + (size_t)(jj * 128 + 16 * w + (lane >> 4) * 4 + i) * 4096 + h * 128 + (lane & 15) * 8);
    }
    __builtin_amdgcn_sched_barrier(0);
    if (t < 8) flg[t] = 0u;
    bar();
    asm volatile("s_waitcnt vmcnt(4)" ::: "memory");
    asm volatile("" :: "v"(pf0));
    GATE(1); STAMP(1);
    {
        float acc[8];
#pragma unroll
        for (int k = 0; k < 4; ++k) {
            acc[2 * k] = bf2f(xr[0]) * bflo(wqv[0][k]) + bf2f(xr[1]) * bflo(wqv[1][k]) + bf2f(xr[2]) * bflo(wqv[2][k]) + bf2f(xr[3]) * bflo(wqv[3][k]);
            acc[2 * k + 1] = bf2f(xr[0]) * bfhi(wqv[0][k]) + bf2f(xr[1]) * bfhi(wqv[1][k]) + bf2f(xr[2]) * bfhi(wqv[2][k]) + bf2f(xr[3]) * bfhi(wqv[3][k]);
        }
        float s[4];
#pragma unroll
        for (int k = 0; k < 4; ++k) s[k] = sw16(acc[k], acc[k + 4]);
        f32x2 s2;
#pragma unroll
        for (int k = 0; k < 2; ++k) s2[k] = sw32(s[k], s[k + 2]);
        *(f32x2*)(sm1 + w * 128 + (lane & 15) * 8 + ((lane >> 4) & 1) * 4 + (lane >> 5) * 2) = s2;
        lgkm0();
        if (lane == 0) __hip_atomic_fetch_add(flg + 0, 1u, __ATOMIC_RELAXED, __HIP_MEMORY_SCOPE_WORKGROUP);
    }
    __builtin_amdgcn_sched_barrier(0);
    lds_wait(vflg + 1, 1);
    GATE(2); STAMP(2);
    {
        const int l16 = lane & 15;
        f32x4 qa0 = *(const f32x4*)(qs + l16 * 8), qa1 = *(const f32x4*)(qs + l16 * 8 + 4);
        float d[4];
#pragma unroll
        for (int i = 0; i < 4; ++i) {
            d[i] = qa0[0] * bflo(wkv[i][0]) + qa0[1] * bfhi(wkv[i][0]) + qa0[2] * bflo(wkv[i][1]) + qa0[3] * bfhi(wkv[i][1]);
            d[i] += qa1[0] * bflo(wkv[i][2]) + qa1[1] * bfhi(wkv[i][2]) + qa1[2] * bflo(wkv[i][3]) + qa1[3] * bfhi(wkv[i][3]);
        }
#pragma unroll
        for (int i = 0; i < 4; ++i) d[i] = sum16(d[i]) * 0.08838834764831845f;
        const u32x2 e0 = __builtin_amdgcn_permlane16_swap(pk2(d[0], d[1]), pk2(d[0], d[1]), false, false);
        const u32x2 e1 = __builtin_amdgcn_permlane16_swap(pk2(d[2], d[3]), pk2(d[2], d[3]), false, false);
        u32x4 o = {e0[0], e1[0], e0[1], e1[1]};
        if ((lane & 24) == 0)
            __builtin_amdgcn_raw_buffer_store_b128(o, rs, WS_QABS + (lane & 7) * 32768 + ((jj * 16 + w * 2 + (lane >> 5)) * 32 + h) * 16, 0, SC1);
    }
    __builtin_amdgcn_sched_barrier(0);
    if (lane == 0) __hip_atomic_fetch_add(flg + 0, 1u, __ATOMIC_RELAXED, __HIP_MEMORY_SCOPE_WORKGROUP);
    lds_wait(vflg + 0, 16);
    __builtin_amdgcn_sched_barrier(0);
    STAMP(3);
    u32x4 uvv[4], ovv[4];
    const int rg = lane >> 3, c8 = lane & 7;
    const int ch = xs * 5 + (xs < 3 ? xs : 3) + (sl >> 1), hf = sl & 1;
    u32x4 dv[2], gv[2], uv_[2];
    u16 xf[2];
    if (has_ffn) {
#pragma unroll
        for (int i = 0; i < 2; ++i) xf[i] = x[hf * 256 + (t >> 2) + 128 * i];
#pragma unroll
        for (int i = 0; i < 2; ++i) {
            gv[i] = *(const u32x4*)(wg + (size_t)(hf * 256 + (t >> 2) + 128 * i) * FF + ch * 32 + (t & 3) * 8);
            uv_[i] = *(const u32x4*)(wu + (size_t)(hf * 256 + (t >> 2) + 128 * i) * FF + ch * 32 + (t & 3) * 8);
        }
    }
    __builtin_amdgcn_sched_barrier(0);
    const u32x4 xv = *(const u32x4*)(x + lane * 8), gwv = *(const u32x4*)(gw + lane * 8);
#pragma unroll
    for (int i = 0; i < 4; ++i)
        uvv[i] = *(const u32x4*)(wuv + (size_t)((t >> 2) + 128 * i) * 4096 + h * 128 + jj * 32 + (t & 3) * 8);
    if (has_ffn) {
#pragma unroll
        for (int i = 0; i < 2; ++i) dv[i] = *(const u32x4*)(wd + (size_t)(ch * 32 + hf * 16 + rg * 2 + i) * 512 + w * 64 + c8 * 8);
    }
#pragma unroll
    for (int i = 0; i < 4; ++i)
        ovv[i] = *(const u32x4*)(wo + ((size_t)h * 128 + jj * 32 + rg * 4 + i) * 512 + w * 64 + c8 * 8);
    __builtin_amdgcn_sched_barrier(0);
    if (has_ffn) {
        float p[16];
#pragma unroll
        for (int i = 0; i < 2; ++i) {
            const float xv_ = bf2f(xf[i]);
#pragma unroll
            for (int k = 0; k < 4; ++k) {
                if (i == 0) {
                    p[2 * k] = xv_ * bflo(gv[i][k]); p[2 * k + 1] = xv_ * bfhi(gv[i][k]);
                    p[8 + 2 * k] = xv_ * bflo(uv_[i][k]); p[8 + 2 * k + 1] = xv_ * bfhi(uv_[i][k]);
                } else {
                    p[2 * k] += xv_ * bflo(gv[i][k]); p[2 * k + 1] += xv_ * bfhi(gv[i][k]);
                    p[8 + 2 * k] += xv_ * bflo(uv_[i][k]); p[8 + 2 * k + 1] += xv_ * bfhi(uv_[i][k]);
                }
            }
        }
        float s8[8];
#pragma unroll
        for (int k = 0; k < 8; ++k) s8[k] = sw32(p[k], p[k + 8]);
        float s4[4];
#pragma unroll
        for (int k = 0; k < 4; ++k) s4[k] = sw16(s8[k], s8[k + 4]);
        bool up = lane & 8;
        float s2[2];
#pragma unroll
        for (int k = 0; k < 2; ++k) { float send = up ? s4[k] : s4[k + 2]; float keep = up ? s4[k + 2] : s4[k]; s2[k] = keep + dppf<0x128>(send); }
        up = lane & 4;
        float send = up ? s2[0] : s2[1]; float keep = up ? s2[1] : s2[0];
        float v = keep + dpp_xor4(send);
        sm2[w * 64 + (lane >> 5) * 32 + (lane & 3) * 8 + ((lane >> 4) & 1) * 4 + ((lane >> 3) & 1) * 2 + ((lane >> 2) & 1)] = v;
        lgkm0();
        if (lane == 0) __hip_atomic_fetch_add(flg + 0, 1u, __ATOMIC_RELAXED, __HIP_MEMORY_SCOPE_WORKGROUP);
        if (w == 0) {
            lds_wait(vflg + 0, 24);
            const unsigned tag = vflg[4];
            float val = sm2[lane];
#pragma unroll
            for (int ww = 1; ww < 8; ++ww) val += sm2[ww * 64 + lane];
            const u32x2 gu = __builtin_amdgcn_permlane32_swap(asu(val), asu(val), false, false);
            u32x4 gr = {tag, gu[0], gu[1], ~tag};
            if ((lane >> 4) == 1 - hf)
                __builtin_amdgcn_raw_buffer_store_b128(gr, rs, WS_GB + bb_ * 256 + (lane & 15) * 16, 0, SC1);
            u32x4 pg;
            for (unsigned spins = 0; spins < SPIN_CAP; ++spins) {
                asm volatile("" ::: "memory");
                pg = __builtin_amdgcn_raw_buffer_load_b128(rs, WS_GB + (bb_ ^ 8) * 256 + (lane & 15) * 16, 0, SC1);
                if (__all(((pg[0] ^ tag) | (pg[3] ^ ~tag)) == 0)) break;
                __builtin_amdgcn_s_sleep(2);
            }
            const float gs = rbf(asf(gu[0]) + asf(pg[1])), us = rbf(asf(gu[1]) + asf(pg[2]));
            const float sg = rbf(gs / (1.f + __expf(-gs)));
            if ((lane >> 4) == hf) qs[lane & 15] = rbf(sg * us);
        }
    }
#pragma unroll
    for (int i = 0; i < 4; ++i) *(u32x4*)(utile + ((t >> 2) + 128 * i) * 64 + (t & 3) * 16) = uvv[i];
    float g, g1;
    {
        float gp = 0.f;
#pragma unroll
        for (int k = 0; k < 4; ++k) gp += rbf(bflo(xv[k]) * bflo(gwv[k])) + rbf(bfhi(xv[k]) * bfhi(gwv[k]));
        gp = sum16(gp);
        gp = sw16(gp, gp);
        gp = sw32(gp, gp);
        const float gs = rbf(gp);
        g = rbf(1.f / (1.f + __expf(-gs)));
        g1 = rbf(1.f - g);
    }
    bar();
    s16x8 ufr[4];
    {
        const int kg = lane >> 4, q = (lane & 15) >> 2, p = lane & 3;
#pragma unroll
        for (int s = 0; s < 2; ++s)
#pragma unroll
            for (int tl = 0; tl < 2; ++tl) {
                const int row = w * 64 + 32 * s + 8 * kg + q;
                s16x4 t0 = __builtin_amdgcn_ds_read_tr16_b64_v4i16(
                    (__attribute__((address_space(3))) s16x4*)(utile + row * 64 + tl * 32 + p * 8));
                s16x4 t1 = __builtin_amdgcn_ds_read_tr16_b64_v4i16(
                    (__attribute__((address_space(3))) s16x4*)(utile + (row + 4) * 64 + tl * 32 + p * 8));
                s16x8 bb = {t0[0], t0[1], t0[2], t0[3], t1[0], t1[1], t1[2], t1[3]};
                ufr[s * 2 + tl] = bb;
            }
    }
    float o[8];
#pragma unroll
    for (int k = 0; k < 8; ++k) o[k] = 0.f;
    if (has_ffn) {
        const f32x2 h2_ = *(const f32x2*)(qs + 2 * rg);
#pragma unroll
        for (int i = 0; i < 2; ++i) {
            const float hv = g1 * h2_[i];
#pragma unroll
            for (int k = 0; k < 4; ++k) { o[2 * k] += hv * bflo(dv[i][k]); o[2 * k + 1] += hv * bfhi(dv[i][k]); }
        }
    }
    vm0();
    lds_wait(vflg + 2, 1);
    STAMP(8);
    {
        const int kg = lane >> 4;
        u32x4 ov[2], ml;
        const int oo = WS_OPART + (((lane & 15) >> 1) * 64 + h * 2 + (lane & 1)) * 1024 + (w * 64 + 8 * kg) * 2;
        for (unsigned spins = 0; spins < SPIN_CAP; ++spins) {
            asm volatile("" ::: "memory");
            unsigned s0 = __builtin_amdgcn_raw_buffer_load_b32(rs, WS_ML + (h * 8 + (lane & 7)) * 8, 0, SC1);
            if (__any(s0 != POISON)) break;
            __builtin_amdgcn_s_sleep(O_SLEEP);
        }
        for (unsigned spins = 0; spins < SPIN_CAP; ++spins) {
            asm volatile("" ::: "memory");
#pragma unroll
            for (int i = 0; i < 2; ++i) ov[i] = __builtin_amdgcn_raw_buffer_load_b128(rs, oo + i * 64, 0, SC1);
            ml = __builtin_amdgcn_raw_buffer_load_b128(rs, WS_ML + (h * 8 + 2 * kg) * 8, 0, SC1);
            unsigned m = umax(umax(max4(ov[0]), max4(ov[1])), max4(ml));
            if (__all(m != POISON)) break;
            __builtin_amdgcn_s_sleep(1);
        }
        STAMP(9);
        f32x4 d0 = {0.f, 0.f, 0.f, 0.f}, d1 = {0.f, 0.f, 0.f, 0.f};
        d0 = __builtin_amdgcn_mfma_f32_16x16x32_bf16(__builtin_bit_cast(bf16x8, ov[0]), __builtin_bit_cast(bf16x8, ufr[0]), d0, 0, 0, 0);
        d1 = __builtin_amdgcn_mfma_f32_16x16x32_bf16(__builtin_bit_cast(bf16x8, ov[0]), __builtin_bit_cast(bf16x8, ufr[1]), d1, 0, 0, 0);
        d0 = __builtin_amdgcn_mfma_f32_16x16x32_bf16(__builtin_bit_cast(bf16x8, ov[1]), __builtin_bit_cast(bf16x8, ufr[2]), d0, 0, 0, 0);
        d1 = __builtin_amdgcn_mfma_f32_16x16x32_bf16(__builtin_bit_cast(bf16x8, ov[1]), __builtin_bit_cast(bf16x8, ufr[3]), d1, 0, 0, 0);
        const float m0 = asf(ml[0]), l0 = asf(ml[1]), m1 = asf(ml[2]), l1 = asf(ml[3]);
        const float M = mx32(mx16(fmaxf(m0, m1)));
        const float e0 = __expf(m0 - M), e1 = __expf(m1 - M);
        float L = e0 * l0 + e1 * l1;
        L = sw16(L, L);
        L = sw32(L, L);
        const float rl = 1.f / L;
        const float v0 = e0 * (d0[0] + d0[1]) + e1 * (d0[2] + d0[3]);
        const float v1 = e0 * (d1[0] + d1[1]) + e1 * (d1[2] + d1[3]);
        float v = sw16(v0, v1);
        v = sw32(v, v);
        if (lane < 32) sm3[w * 32 + lane] = v * rl;
    }
    bar();
    {
        f32x4 c4 = *(const f32x4*)(sm3 + 4 * rg);
#pragma unroll
        for (int ww = 1; ww < 8; ++ww) c4 += *(const f32x4*)(sm3 + ww * 32 + 4 * rg);
#pragma unroll
        for (int i = 0; i < 4; ++i) {
            const float c0 = g * rbf(c4[i]);
#pragma unroll
            for (int k = 0; k < 4; ++k) { o[2 * k] += c0 * bflo(ovv[i][k]); o[2 * k + 1] += c0 * bfhi(ovv[i][k]); }
        }
        float s4[4];
#pragma unroll
        for (int k = 0; k < 4; ++k) s4[k] = sw32(o[k], o[k + 4]);
        float s2[2];
#pragma unroll
        for (int k = 0; k < 2; ++k) s2[k] = sw16(s4[k], s4[k + 2]);
        const bool up = lane & 8;
        const float send = up ? s2[0] : s2[1], keep = up ? s2[1] : s2[0];
        const float v = keep + dppf<0x128>(send);
        __builtin_amdgcn_raw_buffer_store_b32(asu(v), rs, WS_GA + bb_ * 2048 + (w * 64 + c8 * 8 + (lane >> 5) * 4 + ((lane >> 4) & 1) * 2 + ((lane >> 3) & 1)) * 4, 0, SC1);
    }
    STAMP(10);
}

extern "C" void kernel_launch(void* const* d_in, const int* in_sizes, int n_in,
                              void* d_out, int out_size, void* d_ws, size_t ws_size,
                              hipStream_t stream) {
    const u16* x = (const u16*)d_in[0];
    const u16* ckv = (const u16*)d_in[1];
    const u16* wq = (const u16*)d_in[2];
    const u16* wuk = (const u16*)d_in[3];
    const u16* wuv = (const u16*)d_in[4];
    const u16* wo = (const u16*)d_in[5];
    const u16* wg = (const u16*)d_in[7];
    const u16* wu = (const u16*)d_in[8];
    const u16* wd = (const u16*)d_in[9];
    const u16* gw = (const u16*)d_in[10];
    mega<<<256, 640, 0, stream>>>(x, ckv, wq, wuk, wuv, wo, wg, wu, wd, gw, (char*)d_ws, (u16*)d_out);
}
```

```cpp
#include <hip/hip_runtime.h>
#include <hip/hip_bf16.h>
#include <stdint.h>

#define SK 8192
#define FF 1376

typedef unsigned short u16;
typedef unsigned long long u64;
typedef __attribute__((ext_vector_type(8))) __bf16 bf16x8;
typedef __attribute__((ext_vector_type(2))) __bf16 bf16x2;
typedef __attribute__((ext_vector_type(4))) short s16x4;
typedef __attribute__((ext_vector_type(8))) short s16x8;
typedef __attribute__((ext_vector_type(16))) float f32x16;
typedef __attribute__((ext_vector_type(4))) float f32x4;
typedef __attribute__((ext_vector_type(2))) float f32x2;
typedef __attribute__((ext_vector_type(4))) unsigned int u32x4;
typedef __attribute__((ext_vector_type(2))) unsigned int u32x2;
#define GAS __attribute__((address_space(1)))
typedef GAS unsigned gu32;
typedef GAS u64 gu64;
#define RLX_AGENT __ATOMIC_RELAXED, __HIP_MEMORY_SCOPE_AGENT
#define SC1 16
#define POISON 0xFFFFFFFFu

__device__ __forceinline__ float bf2f(u16 v) { return __uint_as_float(((unsigned)v) << 16); }
__device__ __forceinline__ float bflo(unsigned v) { return __uint_as_float(v << 16); }
__device__ __forceinline__ float bfhi(unsigned v) { return __uint_as_float(v & 0xffff0000u); }
__device__ __forceinline__ float asf(unsigned v) { return __uint_as_float(v); }
__device__ __forceinline__ unsigned asu(float v) { return __float_as_uint(v); }
__device__ __forceinline__ unsigned pk2(float a, float b) {
    bf16x2 v = {(__bf16)a, (__bf16)b};
    return __builtin_bit_cast(unsigned, v);
}
__device__ __forceinline__ u16 f2bf(float f) { __bf16 v = (__bf16)f; return __builtin_bit_cast(u16, v); }
__device__ __forceinline__ float rbf(float f) { return bf2f(f2bf(f)); }
template <int CTRL> __device__ __forceinline__ float dppf(float v) {
    return asf((unsigned)__builtin_amdgcn_update_dpp(0, (int)asu(v), CTRL, 0xf, 0xf, true));
}
template <int CTRL> __device__ __forceinline__ unsigned dppu(unsigned v) {
    return (unsigned)__builtin_amdgcn_update_dpp(0, (int)v, CTRL, 0xf, 0xf, true);
}
__device__ __forceinline__ float dpp_xor4(float v) {
    int a = __builtin_amdgcn_update_dpp(0, (int)asu(v), 0x104, 0xf, 0x5, false);
    a = __builtin_amdgcn_update_dpp(a, (int)asu(v), 0x114, 0xf, 0xA, false);
    return asf((unsigned)a);
}
__device__ __forceinline__ float sw16(float a, float b) {
    u32x2 r_ = __builtin_amdgcn_permlane16_swap(asu(a), asu(b), false, false);
    return asf(r_[0]) + asf(r_[1]);
}
__device__ __forceinline__ float sw32(float a, float b) {
    u32x2 r_ = __builtin_amdgcn_permlane32_swap(asu(a), asu(b), false, false);
    return asf(r_[0]) + asf(r_[1]);
}
__device__ __forceinline__ float mx16(float a) {
    u32x2 r_ = __builtin_amdgcn_permlane16_swap(asu(a), asu(a), false, false);
    return fmaxf(asf(r_[0]), asf(r_[1]));
}
__device__ __forceinline__ float mx32(float a) {
    u32x2 r_ = __builtin_amdgcn_permlane32_swap(asu(a), asu(a), false, false);
    return fmaxf(asf(r_[0]), asf(r_[1]));
}
__device__ __forceinline__ float sum16(float v) {
    v += dppf<0xB1>(v); v += dppf<0x4E>(v); v += dppf<0x141>(v); v += dppf<0x140>(v);
    return v;
}
__device__ __forceinline__ float max16(float v) {
    v = fmaxf(v, dppf<0xB1>(v)); v = fmaxf(v, dppf<0x4E>(v)); v = fmaxf(v, dppf<0x141>(v)); v = fmaxf(v, dppf<0x140>(v));
    return v;
}
__device__ __forceinline__ unsigned umax(unsigned a, unsigned b) { return a > b ? a : b; }
__device__ __forceinline__ unsigned max4(u32x4 v) { return umax(umax(v[0], v[1]), umax(v[2], v[3])); }

#define NREP 32
#define WS_EPOCH   0
#define WS_F0      256
#define WS_QG      (WS_F0 + NREP * 2048)
#define WS_QABS    (WS_QG + 128 * 1024)
#define WS_P       (WS_QABS + 8 * 32768)
#define WS_MLOC    (WS_P + 8 * 16 * 4096)
#define WS_OPART   (WS_MLOC + 8 * 8 * 32 * 16)
#define WS_ML      (WS_OPART + 8 * 32 * 512 * 4)
#define WS_GA      (WS_ML + 32 * 8 * 8)
#define WS_GB      (WS_GA + 128 * 2048)
#define WS_QGF     (WS_GB + 128 * 2048)
#define WS_PF      (WS_QGF + 128 * 1024)
#define WS_MLOCF   (WS_PF + 8 * 16 * 4096)
#define WS_END     (WS_MLOCF + 8 * 8 * 32 * 16)

#define PF_LINES 136
#define O_SENT 1
#define O_SLEEP 2
#define BP9 1
#define A_MODE 0
#define WK_LATE 0
#define GU_FIRST 1
#define VHOIST 1
#define FFN_CHUNK ((bb_ & 7) * 5 + ((bb_ & 7) < 3 ? (bb_ & 7) : 3) + (bb_ >> 3))
#define GATE_ID 0
#define GATE_X 2000
#define GATE(i) do { if (GATE_ID == (i)) { for (unsigned gs_ = 0; gs_ < 100000u; ++gs_) { if (__builtin_amdgcn_s_memrealtime() - t0_ >= (u64)GATE_X) break; __builtin_amdgcn_s_sleep(1); } } } while (0)
#define SPIN_CAP (1u << 17)
#define A_SLEEP 40
#define B_SLEEP 20
#define A_DELAY 0
#define STAMP(i)

__device__ __forceinline__ void lgkm0() { asm volatile("s_waitcnt lgkmcnt(0)" ::: "memory"); }
__device__ __forceinline__ void vm0() { asm volatile("s_waitcnt vmcnt(0)" ::: "memory"); }
__device__ __forceinline__ void bar() { asm volatile("s_waitcnt lgkmcnt(0)\n\ts_barrier" ::: "memory"); }
typedef volatile __attribute__((address_space(3))) unsigned lds_u32;
__device__ __forceinline__ void lds_wait(lds_u32* p, unsigned target) {
    for (unsigned s = 0; s < SPIN_CAP; ++s) {
        if (*p >= target) break;
        __builtin_amdgcn_s_sleep(1);
    }
    asm volatile("" ::: "memory");
}

__device__ __forceinline__ void f0_signal(char* ws, int b, int lane, unsigned tag) {
    const u64 want = ((u64)(~tag) << 32) | tag;
    if (lane < NREP) __hip_atomic_store((gu64*)(ws + WS_F0 + lane * 2048 + b * 8), want, RLX_AGENT);
}
__device__ __forceinline__ void f0_wait(const __amdgpu_buffer_rsrc_t rs, int b, int lane, unsigned tag) {
    const int fo = WS_F0 + (b & (NREP - 1)) * 2048 + lane * 16;
    for (unsigned spins = 0; spins < SPIN_CAP; ++spins) {
        asm volatile("" ::: "memory");
        u32x4 a0 = __builtin_amdgcn_raw_buffer_load_b128(rs, fo, 0, SC1);
        u32x4 a1 = __builtin_amdgcn_raw_buffer_load_b128(rs, fo + 1024, 0, SC1);
        unsigned bad = (a0[0] ^ tag) | (a0[1] ^ ~tag) | (a0[2] ^ tag) | (a0[3] ^ ~tag) |
                       (a1[0] ^ tag) | (a1[1] ^ ~tag) | (a1[2] ^ tag) | (a1[3] ^ ~tag);
        if (__all(bad == 0)) break;
        __builtin_amdgcn_s_sleep(8);
    }
    asm volatile("" ::: "memory");
}

#define LDS_BYTES (65536 + 65536 + 8192)

__global__ __launch_bounds__(640, 1) void mega(const u16* __restrict__ x, const u16* __restrict__ ckv,
                                               const u16* __restrict__ wq, const u16* __restrict__ wuk,
                                               const u16* __restrict__ wuv, const u16* __restrict__ wo,
                                               const u16* __restrict__ wg, const u16* __restrict__ wu,
                                               const u16* __restrict__ wd, const u16* __restrict__ gw,
                                               char* __restrict__ ws, u16* __restrict__ out) {
    __shared__ __attribute__((aligned(16))) char lds[LDS_BYTES];
    __shared__ unsigned flg[8];

    const int t = threadIdx.x;
    unsigned pf0;
    {
        u64 pc;
        asm volatile("s_getpc_b64 %0" : "=s"(pc));
        const GAS unsigned* cb = (const GAS unsigned*)(pc & ~255ull);
        const int l0 = 639 - t;
        pf0 = cb[(l0 < PF_LINES - 1 ? l0 : PF_LINES - 1) * 32];
        __builtin_amdgcn_sched_barrier(0);
    }
    const int b = blockIdx.x;
    const u64 t0_ = __builtin_amdgcn_s_memrealtime();
    const int lane = t & 63, w = t >> 6;
    const int r = lane & 31, h2 = lane >> 5;
    const __amdgpu_buffer_rsrc_t rs = __builtin_amdgcn_make_buffer_rsrc(ws, 0, WS_END, 0x00020000);
    const u32x4 P4 = {POISON, POISON, POISON, POISON};
    const u32x2 P2 = {POISON, POISON};
    STAMP(0);

    if (b < 128) {
        if (w >= 9) { asm volatile("" :: "v"(pf0)); return; }
        lds_u32* vflg = (lds_u32*)flg;
        if (w == 8) {
            const unsigned e = __hip_atomic_load((gu32*)(ws + WS_EPOCH), RLX_AGENT);
            asm volatile("" :: "v"(pf0));
            bar();
            f0_signal(ws, b, lane, e + 1u);
            if (b < 32) { unsigned short o_ = *(volatile const GAS u16*)(out + b * 16 + (lane & 15)); asm volatile("" :: "v"((unsigned)o_)); }
            asm volatile("s_barrier" ::: "memory");
            STAMP(1);
            __builtin_amdgcn_s_sleep(A_SLEEP);
            f0_wait(rs, b, lane, e + 1u);
            if (lane == 0) vflg[2] = 1u;
            STAMP(2);
            return;
        }
        char* tile = lds;
        float* red = (float*)(lds + 65536);
        float* fac = (float*)(lds + 131072);
        float* Mws = fac + 512;
        float* Lws = Mws + 256;
        float* fin = Lws + 256;
        float* fing = fin + 256;
        const int kc = b & 7, mem = b >> 3;
        __builtin_amdgcn_raw_buffer_store_b128(P4, rs, (w < 4 ? WS_P : WS_PF) + ((kc * 16 + mem) * 4 + (w & 3)) * 1024 + lane * 16, 0, SC1);
        if (w < 4)
            __builtin_amdgcn_raw_buffer_store_b128(P4, rs, WS_OPART + (kc * 64 + w * 16 + (lane >> 2)) * 1024 + mem * 64 + (lane & 3) * 16, 0, SC1);
        else if (w == 4)
            __builtin_amdgcn_raw_buffer_store_b64(P2, rs, (lane < 32 ? WS_MLOC : WS_MLOCF) + ((kc * 8 + (mem >> 1)) * 32 + r) * 16 + (mem & 1) * 8, 0, SC1);
        else if (w == 5 && mem == 0 && lane < 32)
            __builtin_amdgcn_raw_buffer_store_b64(P2, rs, WS_ML + (lane * 8 + kc) * 8, 0, SC1);
        if (t < 4) flg[t] = 0u;
        vm0();
        asm volatile("" :: "v"(pf0));
        bar();
        asm volatile("s_barrier" ::: "memory");
        STAMP(1);
        __builtin_amdgcn_s_sleep(A_DELAY);
        __builtin_amdgcn_sched_barrier(0);
        bf16x8 bq[8];
        u32x4 cv[8];
        {
            const u16* bp = ckv + (size_t)(kc * 1024 + mem * 64 + r) * 512 + 64 * w + 8 * h2;
#pragma unroll
            for (int kb = 0; kb < 2; ++kb)
#pragma unroll
                for (int s = 0; s < 4; ++s) bq[kb * 4 + s] = *(const bf16x8*)(bp + kb * 32 * 512 + 16 * s);
            const u16* cp = ckv + (size_t)(kc * 1024 + (t >> 2)) * 512 + mem * 32 + (t & 3) * 8;
#pragma unroll
            for (int i = 0; i < 8; ++i) cv[i] = *(const u32x4*)(cp + (size_t)i * 128 * 512);
        }
        __builtin_amdgcn_sched_barrier(0);
#pragma unroll
        for (int i = 0; i < 8; ++i) *(u32x4*)(tile + (i * 128 + (t >> 2)) * 64 + (t & 3) * 16) = cv[i];
        GATE(7); STAMP(2);
        lds_wait(vflg + 2, 1);
        GATE(8); STAMP(3);
        {
            u32x4 av[4];
            const int qo = WS_QABS + (b & 7) * 32768 + (w * 256 + lane) * 16;
            for (unsigned spins = 0; spins < SPIN_CAP; ++spins) {
                asm volatile("" ::: "memory");
#pragma unroll
                for (int s = 0; s < 4; ++s) av[s] = __builtin_amdgcn_raw_buffer_load_b128(rs, qo + 1024 * s, 0, SC1);
                unsigned m = umax(umax(max4(av[0]), max4(av[1])), umax(max4(av[2]), max4(av[3])));
                if (__all(m != POISON)) break;
                __builtin_amdgcn_s_sleep(1);
            }
            GATE(3); STAMP(4);
#pragma unroll
            for (int kb = 0; kb < 2; ++kb) {
                f32x16 acc;
#pragma unroll
                for (int i = 0; i < 16; ++i) acc[i] = 0.f;
#pragma unroll
                for (int s = 0; s < 4; ++s)
                    acc = __builtin_amdgcn_mfma_f32_32x32x16_bf16(__builtin_bit_cast(bf16x8, av[s]), bq[kb * 4 + s], acc, 0, 0, 0);
#pragma unroll
                for (int i = 0; i < 16; ++i) {
                    int head = (i & 3) + 8 * (i >> 2) + 4 * h2;
                    red[(w * 32 + head) * 64 + kb * 32 + r] = acc[i];
                }
            }
        }
        bar();
        {
            const int head = t >> 4, kq = (t & 15) * 4;
            f32x4 s4 = *(const f32x4*)(red + head * 64 + kq);
#pragma unroll
            for (int ww = 1; ww < 8; ++ww) s4 += *(const f32x4*)(red + (ww * 32 + head) * 64 + kq);
            float m = fmaxf(fmaxf(s4[0], s4[1]), fmaxf(s4[2], s4[3]));
            m = max16(m);
            unsigned p01 = pk2(__expf(s4[0] - m), __expf(s4[1] - m));
            unsigned p23 = pk2(__expf(s4[2] - m), __expf(s4[3] - m));
            float l = (bflo(p01) + bfhi(p01)) + (bflo(p23) + bfhi(p23));
            l = sum16(l);
            u32x4 pp = {p01, p23, dppu<0xF5>(p01), dppu<0xF5>(p23)};
            u32x2 ml = {asu(m), asu(l)};
            const int po = ((kc * 16 + mem) * 256 + ((t & 15) >> 1) * 32 + head) * 16;
            const int mo = ((kc * 8 + (mem >> 1)) * 32 + head) * 16 + (mem & 1) * 8;
            if ((t & 1) == 0) __builtin_amdgcn_raw_buffer_store_b128(pp, rs, WS_PF + po, 0, 0);
            if ((t & 15) == 0) __builtin_amdgcn_raw_buffer_store_b64(ml, rs, WS_MLOCF + mo, 0, 0);
            if ((t & 1) == 0) __builtin_amdgcn_raw_buffer_store_b128(pp, rs, WS_P + po, 0, SC1);
            if ((t & 15) == 0) __builtin_amdgcn_raw_buffer_store_b64(ml, rs, WS_MLOC + mo, 0, SC1);
        }
        s16x8 vfr[8];
        {
            const int g = lane >> 4;
            const int q = (lane & 15) >> 2, p = lane & 3;
            const int cb = g & 1;
#pragma unroll
            for (int s = 0; s < 8; ++s) {
                int row = w * 128 + 16 * s + 8 * h2 + q;
                s16x4 t0 = __builtin_amdgcn_ds_read_tr16_b64_v4i16(
                    (__attribute__((address_space(3))) s16x4*)(tile + row * 64 + cb * 32 + p * 8));
                s16x4 t1 = __builtin_amdgcn_ds_read_tr16_b64_v4i16(
                    (__attribute__((address_space(3))) s16x4*)(tile + (row + 4) * 64 + cb * 32 + p * 8));
                s16x8 bb = {t0[0], t0[1], t0[2], t0[3], t1[0], t1[1], t1[2], t1[3]};
                vfr[s] = bb;
            }
        }
        bar();
        STAMP(5);
        {
            u32x4 pv[8], mlv;
            for (unsigned spins = 0; spins < SPIN_CAP; ++spins) {
                asm volatile("" ::: "memory");
                const bool slow = (spins & 3) == 3;
                const int po = (slow ? WS_P : WS_PF) + ((kc * 16 + w * 2) * 256 + lane) * 16;
                const int mo = (slow ? WS_MLOC : WS_MLOCF) + ((kc * 8 + w) * 32 + r) * 16;
#pragma unroll
                for (int s = 0; s < 8; ++s) pv[s] = __builtin_amdgcn_raw_buffer_load_b128(rs, po + 1024 * s, 0, SC1);
                mlv = __builtin_amdgcn_raw_buffer_load_b128(rs, mo, 0, SC1);
                unsigned m = umax(umax(max4(pv[0]), max4(pv[1])), umax(max4(pv[2]), max4(pv[3])));
                m = umax(m, umax(umax(max4(pv[4]), max4(pv[5])), umax(max4(pv[6]), max4(pv[7]))));
                m = umax(m, max4(mlv));
                if (__all(m != POISON)) break;
                __builtin_amdgcn_s_sleep(1);
            }
            GATE(4); STAMP(6);
            {
                float m0 = asf(mlv[0]), l0 = asf(mlv[1]), m1 = asf(mlv[2]), l1 = asf(mlv[3]);
                float Mw = fmaxf(m0, m1);
                float f0_ = __expf(m0 - Mw), f1_ = __expf(m1 - Mw);
                if (lane < 32) {
                    fac[(2 * w) * 32 + r] = f0_; fac[(2 * w + 1) * 32 + r] = f1_;
                    Mws[r * 8 + w] = Mw; Lws[r * 8 + w] = f0_ * l0 + f1_ * l1;
                }
            }
            const int g = lane >> 4;
            const int q = (lane & 15) >> 2, p = lane & 3;
            const int cb = g & 1;
            f32x16 tot;
#pragma unroll
            for (int mm = 0; mm < 2; ++mm) {
                f32x16 acc;
#pragma unroll
                for (int i = 0; i < 16; ++i) acc[i] = 0.f;
#pragma unroll
                for (int ss = 0; ss < 4; ++ss) {
                    int s = mm * 4 + ss;
                    s16x8 bb = vfr[s];
                    acc = __builtin_amdgcn_mfma_f32_32x32x16_bf16(__builtin_bit_cast(bf16x8, pv[s]), __builtin_bit_cast(bf16x8, bb), acc, 0, 0, 0);
                }
                const float* fp = fac + (2 * w + mm) * 32 + 4 * h2;
#pragma unroll
                for (int qd = 0; qd < 4; ++qd) {
                    f32x4 f4 = *(const f32x4*)(fp + 8 * qd);
#pragma unroll
                    for (int e2 = 0; e2 < 4; ++e2) {
                        if (mm == 0) tot[qd * 4 + e2] = f4[e2] * acc[qd * 4 + e2];
                        else tot[qd * 4 + e2] += f4[e2] * acc[qd * 4 + e2];
                    }
                }
            }
#pragma unroll
            for (int i = 0; i < 16; ++i) {
                int head = (i & 3) + 8 * (i >> 2) + 4 * h2;
                red[(w * 32 + head) * 32 + r] = tot[i];
            }
        }
        bar();
        {
            const int head = t >> 4, cq = (t & 15) * 2;
            f32x4 ma = *(const f32x4*)(Mws + head * 8), mb = *(const f32x4*)(Mws + head * 8 + 4);
            float M = fmaxf(fmaxf(fmaxf(ma[0], ma[1]), fmaxf(ma[2], ma[3])), fmaxf(fmaxf(mb[0], mb[1]), fmaxf(mb[2], mb[3])));
            float ew[8];
#pragma unroll
            for (int ww = 0; ww < 4; ++ww) { ew[ww] = __expf(ma[ww] - M); ew[ww + 4] = __expf(mb[ww] - M); }
            f32x2 s2 = {0.f, 0.f};
#pragma unroll
            for (int ww = 0; ww < 8; ++ww) s2 += ew[ww] * *(const f32x2*)(red + (ww * 32 + head) * 32 + cq);
            const unsigned ohi = pk2(s2[0], s2[1]);
            const unsigned olo = pk2(s2[0] - bflo(ohi), s2[1] - bfhi(ohi));
            const int oo = WS_OPART + (kc * 32 + head) * 2048 + (mem * 32 + cq) * 2;
            __builtin_amdgcn_raw_buffer_store_b32(ohi, rs, oo, 0, SC1);
            __builtin_amdgcn_raw_buffer_store_b32(olo, rs, oo + 1024, 0, SC1);
            if (mem == 0 && (t & 15) == 0) {
                f32x4 la = *(const f32x4*)(Lws + head * 8), lb = *(const f32x4*)(Lws + head * 8 + 4);
                float L = 0.f;
#pragma unroll
                for (int ww = 0; ww < 4; ++ww) L += ew[ww] * la[ww] + ew[ww + 4] * lb[ww];
                u32x2 ml = {asu(M), asu(L)};
                __builtin_amdgcn_raw_buffer_store_b64(ml, rs, WS_ML + (head * 8 + kc) * 8, 0, SC1);
            }
        }
        STAMP(7);
        if (b >= 32 || w != 0) return;
        {
            const int rg = lane >> 2;
            const int ao = WS_GA + rg * 2048 + b * 64 + (lane & 3) * 16;
            u32x4 a[8];
            for (unsigned spins = 0; spins < SPIN_CAP; ++spins) {
                asm volatile("" ::: "memory");
#pragma unroll
                for (int j = 0; j < 8; ++j) a[j] = __builtin_amdgcn_raw_buffer_load_b128(rs, ao + j * 32768, 0, SC1);
                unsigned m = umax(umax(max4(a[0]), max4(a[1])), umax(max4(a[2]), max4(a[3])));
                m = umax(m, umax(umax(max4(a[4]), max4(a[5])), umax(max4(a[6]), max4(a[7]))));
                if (__all(m != POISON)) break;
                __builtin_amdgcn_s_sleep(1);
            }
            STAMP(11);
            float s4[4];
#pragma unroll
            for (int k = 0; k < 4; ++k)
                s4[k] = ((asf(a[0][k]) + asf(a[1][k])) + (asf(a[2][k]) + asf(a[3][k]))) + ((asf(a[4][k]) + asf(a[5][k])) + (asf(a[6][k]) + asf(a[7][k])));
            float v = sw16(sw32(s4[0], s4[2]), sw32(s4[1], s4[3]));
            v += dppf<0x128>(v);
            v += dppf<0x124>(v);
            if ((lane & 12) == 0) out[b * 16 + (lane & 3) * 4 + (lane >> 4)] = f2bf(v);
        }
        STAMP(12);
        return;
    }

    char* utile = lds;
    float* sm1 = (float*)(lds + 32768);
    float* sm2 = sm1 + 1024;
    float* sm3 = sm2 + 512;
    float* qs = sm3 + 256;
    lds_u32* vflg = (lds_u32*)flg;
    const int bb_ = b - 128;
    const int h = bb_ & 31, jj = bb_ >> 5;
    const int xs = bb_ & 7, sl = bb_ >> 3;
    const bool has_ffn = (sl >> 1) < (xs < 3 ? 6 : 5);

    if (w >= 8) {
        const unsigned e = __hip_atomic_load((gu32*)(ws + WS_EPOCH), RLX_AGENT);
        if (w == 9) {
            __builtin_amdgcn_raw_buffer_store_b128(P4, rs, WS_QABS + (lane >> 3) * 32768 + ((jj * 16 + (lane & 7)) * 32 + h) * 16, 0, SC1);
            __builtin_amdgcn_raw_buffer_store_b128(P4, rs, WS_QABS + (lane >> 3) * 32768 + ((jj * 16 + 8 + (lane & 7)) * 32 + h) * 16, 0, SC1);
            __builtin_amdgcn_raw_buffer_store_b128(P4, rs, WS_GA + bb_ * 2048 + lane * 16, 0, SC1);
            __builtin_amdgcn_raw_buffer_store_b128(P4, rs, WS_GA + bb_ * 2048 + 1024 + lane * 16, 0, SC1);
        }
        bar();
        asm volatile("" :: "v"(pf0));
        const unsigned tag = e + 1u;
        if (w == 8) {
            lds_wait(vflg + 0, 8);
            STAMP(1);
            f32x2 s = *(const f32x2*)(sm1 + 2 * lane);
#pragma unroll
            for (int ww = 1; ww < 8; ++ww) s += *(const f32x2*)(sm1 + ww * 128 + 2 * lane);
            u32x4 g = {tag, asu(s[0]), asu(s[1]), ~tag};
            __builtin_amdgcn_raw_buffer_store_b128(g, rs, WS_QGF + ((h * 4 + jj) * 64 + lane) * 16, 0, 0);
            __builtin_amdgcn_raw_buffer_store_b128(g, rs, WS_QG + ((h * 4 + jj) * 64 + lane) * 16, 0, SC1);
            for (unsigned spins = 0; spins < SPIN_CAP; ++spins) {
                asm volatile("" ::: "memory");
                u32x4 v[4];
                const int qb = ((spins & 3) == 3 ? WS_QG : WS_QGF) + (h * 4 * 64 + lane) * 16;
#pragma unroll
                for (int k = 0; k < 4; ++k) v[k] = __builtin_amdgcn_raw_buffer_load_b128(rs, qb + k * 1024, 0, SC1);
                unsigned bad = 0;
                float q0 = 0.f, q1 = 0.f;
#pragma unroll
                for (int k = 0; k < 4; ++k) {
                    bad |= (v[k][0] ^ tag) | (v[k][3] ^ ~tag);
                    q0 += asf(v[k][1]); q1 += asf(v[k][2]);
                }
                if (__all(bad == 0) || spins == SPIN_CAP - 1) {
                    f32x2 qq = {rbf(q0), rbf(q1)};
                    *(f32x2*)(qs + 2 * lane) = qq;
                    break;
                }
                __builtin_amdgcn_s_sleep(1);
            }
            lds_wait(vflg + 3, 1);
            lgkm0();
            if (lane == 0) vflg[1] = 1u;
            STAMP(2);
            if (has_ffn) {
                const int hf = sl & 1;
                for (unsigned s_ = 0; s_ < SPIN_CAP; ++s_) {
                    if (vflg[0] >= 24) break;
                    __builtin_amdgcn_s_sleep(4);
                }
                asm volatile("" ::: "memory");
                STAMP(3);
                float val = sm2[lane];
#pragma unroll
                for (int ww = 1; ww < 8; ++ww) val += sm2[ww * 64 + lane];
                const u32x2 gu = __builtin_amdgcn_permlane32_swap(asu(val), asu(val), false, false);
                u32x4 gr = {tag, gu[0], gu[1], ~tag};
                if ((lane >> 4) == 1 - hf)
                    __builtin_amdgcn_raw_buffer_store_b128(gr, rs, WS_GB + bb_ * 256 + (lane & 15) * 16, 0, SC1);
                u32x4 pg;
                for (unsigned spins = 0; spins < SPIN_CAP; ++spins) {
                    asm volatile("" ::: "memory");
                    pg = __builtin_amdgcn_raw_buffer_load_b128(rs, WS_GB + (bb_ ^ 8) * 256 + (lane & 15) * 16, 0, SC1);
                    if (__all(((pg[0] ^ tag) | (pg[3] ^ ~tag)) == 0)) break;
                    __builtin_amdgcn_s_sleep(2);
                }
                const float gs = rbf(asf(gu[0]) + asf(pg[1])), us = rbf(asf(gu[1]) + asf(pg[2]));
                const float sg = rbf(gs / (1.f + __expf(-gs)));
                if ((lane >> 4) == hf) qs[lane & 15] = rbf(sg * us);
                lgkm0();
                STAMP(4);
            }
        } else {
            vm0();
            if (lane == 0) { vflg[4] = tag; lgkm0(); vflg[3] = 1u; }
            STAMP(1);
            f0_signal(ws, b, lane, tag);
            __builtin_amdgcn_s_sleep(B_SLEEP);
            f0_wait(rs, b, lane, tag);
            if (lane == 0) vflg[2] = 1u;
            if (b == 255 && lane == 0) __hip_atomic_store((gu32*)(ws + WS_EPOCH), tag, RLX_AGENT);
            STAMP(2);
        }
        return;
    }

    u32x4 wqv[4], wkv[4];
    u16 xr[4];
    {
        const int row = jj * 128 + (t >> 4);
#pragma unroll
        for (int i = 0; i < 4; ++i) xr[i] = x[row + 32 * i];
#pragma unroll
        for (int i = 0; i < 4; ++i)
            wqv[i] = *(const u32x4*)(wq + (size_t)(row + 32 * i) * 4096 + h * 128 + (t & 15) * 8);
#pragma unroll
        for (int i = 0; i < 4; ++i)
            wkv[i] = *(const u32x4*)(wuk + (size_t)(jj * 128 + 16 * w + (lane >> 4) * 4 + i) * 4096 + h * 128 + (lane & 15) * 8);
    }
    __builtin_amdgcn_sched_barrier(0);
    if (t < 8) flg[t] = 0u;
    bar();
    asm volatile("s_waitcnt vmcnt(4)" ::: "memory");
    asm volatile("" :: "v"(pf0));
    GATE(1); STAMP(1);
    {
        float acc[8];
#pragma unroll
        for (int k = 0; k < 4; ++k) {
            acc[2 * k] = bf2f(xr[0]) * bflo(wqv[0][k]) + bf2f(xr[1]) * bflo(wqv[1][k]) + bf2f(xr[2]) * bflo(wqv[2][k]) + bf2f(xr[3]) * bflo(wqv[3][k]);
            acc[2 * k + 1] = bf2f(xr[0]) * bfhi(wqv[0][k]) + bf2f(xr[1]) * bfhi(wqv[1][k]) + bf2f(xr[2]) * bfhi(wqv[2][k]) + bf2f(xr[3]) * bfhi(wqv[3][k]);
        }
        float s[4];
#pragma unroll
        for (int k = 0; k < 4; ++k) s[k] = sw16(acc[k], acc[k + 4]);
        f32x2 s2;
#pragma unroll
        for (int k = 0; k < 2; ++k) s2[k] = sw32(s[k], s[k + 2]);
        *(f32x2*)(sm1 + w * 128 + (lane & 15) * 8 + ((lane >> 4) & 1) * 4 + (lane >> 5) * 2) = s2;
        lgkm0();
        if (lane == 0) __hip_atomic_fetch_add(flg + 0, 1u, __ATOMIC_RELAXED, __HIP_MEMORY_SCOPE_WORKGROUP);
    }
    __builtin_amdgcn_sched_barrier(0);
    lds_wait(vflg + 1, 1);
    GATE(2); STAMP(2);
    {
        const int l16 = lane & 15;
        f32x4 qa0 = *(const f32x4*)(qs + l16 * 8), qa1 = *(const f32x4*)(qs + l16 * 8 + 4);
        float d[4];
#pragma unroll
        for (int i = 0; i < 4; ++i) {
            d[i] = qa0[0] * bflo(wkv[i][0]) + qa0[1] * bfhi(wkv[i][0]) + qa0[2] * bflo(wkv[i][1]) + qa0[3] * bfhi(wkv[i][1]);
            d[i] += qa1[0] * bflo(wkv[i][2]) + qa1[1] * bfhi(wkv[i][2]) + qa1[2] * bflo(wkv[i][3]) + qa1[3] * bfhi(wkv[i][3]);
        }
#pragma unroll
        for (int i = 0; i < 4; ++i) d[i] = sum16(d[i]) * 0.08838834764831845f;
        const u32x2 e0 = __builtin_amdgcn_permlane16_swap(pk2(d[0], d[1]), pk2(d[0], d[1]), false, false);
        const u32x2 e1 = __builtin_amdgcn_permlane16_swap(pk2(d[2], d[3]), pk2(d[2], d[3]), false, false);
        u32x4 o = {e0[0], e1[0], e0[1], e1[1]};
        if ((lane & 24) == 0)
            __builtin_amdgcn_raw_buffer_store_b128(o, rs, WS_QABS + (lane & 7) * 32768 + ((jj * 16 + w * 2 + (lane >> 5)) * 32 + h) * 16, 0, SC1);
    }
    __builtin_amdgcn_sched_barrier(0);
    if (lane == 0) __hip_atomic_fetch_add(flg + 0, 1u, __ATOMIC_RELAXED, __HIP_MEMORY_SCOPE_WORKGROUP);
    lds_wait(vflg + 0, 16);
    __builtin_amdgcn_sched_barrier(0);
    STAMP(3);
    u32x4 uvv[4], ovv[4];
    const int rg = lane >> 3, c8 = lane & 7;
    const int ch = xs * 5 + (xs < 3 ? xs : 3) + (sl >> 1), hf = sl & 1;
    u32x4 dv[2], gv[2], uv_[2];
    u16 xf[2];
    if (has_ffn) {
#pragma unroll
        for (int i = 0; i < 2; ++i) xf[i] = x[hf * 256 + (t >> 2) + 128 * i];
#pragma unroll
        for (int i = 0; i < 2; ++i) {
            gv[i] = *(const u32x4*)(wg + (size_t)(hf * 256 + (t >> 2) + 128 * i) * FF + ch * 32 + (t & 3) * 8);
            uv_[i] = *(const u32x4*)(wu + (size_t)(hf * 256 + (t >> 2) + 128 * i) * FF + ch * 32 + (t & 3) * 8);
        }
        __builtin_amdgcn_sched_barrier(0);
        if (lane == 0) __hip_atomic_fetch_add(flg + 5, 1u, __ATOMIC_RELAXED, __HIP_MEMORY_SCOPE_WORKGROUP);
        lds_wait(vflg + 5, 8);
    }
    __builtin_amdgcn_sched_barrier(0);
    const u32x4 xv = *(const u32x4*)(x + lane * 8), gwv = *(const u32x4*)(gw + lane * 8);
#pragma unroll
    for (int i = 0; i < 4; ++i)
        uvv[i] = *(const u32x4*)(wuv + (size_t)((t >> 2) + 128 * i) * 4096 + h * 128 + jj * 32 + (t & 3) * 8);
    if (has_ffn) {
#pragma unroll
        for (int i = 0; i < 2; ++i) dv[i] = *(const u32x4*)(wd + (size_t)(ch * 32 + hf * 16 + rg * 2 + i) * 512 + w * 64 + c8 * 8);
    }
#pragma unroll
    for (int i = 0; i < 4; ++i)
        ovv[i] = *(const u32x4*)(wo + ((size_t)h * 128 + jj * 32 + rg * 4 + i) * 512 + w * 64 + c8 * 8);
    __builtin_amdgcn_sched_barrier(0);
    if (has_ffn) {
        float p[16];
#pragma unroll
        for (int i = 0; i < 2; ++i) {
            const float xv_ = bf2f(xf[i]);
#pragma unroll
            for (int k = 0; k < 4; ++k) {
                if (i == 0) {
                    p[2 * k] = xv_ * bflo(gv[i][k]); p[2 * k + 1] = xv_ * bfhi(gv[i][k]);
                    p[8 + 2 * k] = xv_ * bflo(uv_[i][k]); p[8 + 2 * k + 1] = xv_ * bfhi(uv_[i][k]);
                } else {
                    p[2 * k] += xv_ * bflo(gv[i][k]); p[2 * k + 1] += xv_ * bfhi(gv[i][k]);
                    p[8 + 2 * k] += xv_ * bflo(uv_[i][k]); p[8 + 2 * k + 1] += xv_ * bfhi(uv_[i][k]);
                }
            }
        }
        float s8[8];
#pragma unroll
        for (int k = 0; k < 8; ++k) s8[k] = sw32(p[k], p[k + 8]);
        float s4[4];
#pragma unroll
        for (int k = 0; k < 4; ++k) s4[k] = sw16(s8[k], s8[k + 4]);
        bool up = lane & 8;
        float s2[2];
#pragma unroll
        for (int k = 0; k < 2; ++k) { float send = up ? s4[k] : s4[k + 2]; float keep = up ? s4[k + 2] : s4[k]; s2[k] = keep + dppf<0x128>(send); }
        up = lane & 4;
        float send = up ? s2[0] : s2[1]; float keep = up ? s2[1] : s2[0];
        float v = keep + dpp_xor4(send);
        sm2[w * 64 + (lane >> 5) * 32 + (lane & 3) * 8 + ((lane >> 4) & 1) * 4 + ((lane >> 3) & 1) * 2 + ((lane >> 2) & 1)] = v;
        lgkm0();
        if (lane == 0) __hip_atomic_fetch_add(flg + 0, 1u, __ATOMIC_RELAXED, __HIP_MEMORY_SCOPE_WORKGROUP);
    }
#pragma unroll
    for (int i = 0; i < 4; ++i) *(u32x4*)(utile + ((t >> 2) + 128 * i) * 64 + (t & 3) * 16) = uvv[i];
    float g, g1;
    {
        float gp = 0.f;
#pragma unroll
        for (int k = 0; k < 4; ++k) gp += rbf(bflo(xv[k]) * bflo(gwv[k])) + rbf(bfhi(xv[k]) * bfhi(gwv[k]));
        gp = sum16(gp);
        gp = sw16(gp, gp);
        gp = sw32(gp, gp);
        const float gs = rbf(gp);
        g = rbf(1.f / (1.f + __expf(-gs)));
        g1 = rbf(1.f - g);
    }
    bar();
    s16x8 ufr[4];
    {
        const int kg = lane >> 4, q = (lane & 15) >> 2, p = lane & 3;
#pragma unroll
        for (int s = 0; s < 2; ++s)
#pragma unroll
            for (int tl = 0; tl < 2; ++tl) {
                const int row = w * 64 + 32 * s + 8 * kg + q;
                s16x4 t0 = __builtin_amdgcn_ds_read_tr16_b64_v4i16(
                    (__attribute__((address_space(3))) s16x4*)(utile + row * 64 + tl * 32 + p * 8));
                s16x4 t1 = __builtin_amdgcn_ds_read_tr16_b64_v4i16(
                    (__attribute__((address_space(3))) s16x4*)(utile + (row + 4) * 64 + tl * 32 + p * 8));
                s16x8 bb = {t0[0], t0[1], t0[2], t0[3], t1[0], t1[1], t1[2], t1[3]};
                ufr[s * 2 + tl] = bb;
            }
    }
    float o[8];
#pragma unroll
    for (int k = 0; k < 8; ++k) o[k] = 0.f;
    if (has_ffn) {
        const f32x2 h2_ = *(const f32x2*)(qs + 2 * rg);
#pragma unroll
        for (int i = 0; i < 2; ++i) {
            const float hv = g1 * h2_[i];
#pragma unroll
            for (int k = 0; k < 4; ++k) { o[2 * k] += hv * bflo(dv[i][k]); o[2 * k + 1] += hv * bfhi(dv[i][k]); }
        }
    }
    vm0();
    lds_wait(vflg + 2, 1);
    STAMP(8);
    {
        const int kg = lane >> 4;
        u32x4 ov[2], ml;
        const int oo = WS_OPART + (((lane & 15) >> 1) * 64 + h * 2 + (lane & 1)) * 1024 + (w * 64 + 8 * kg) * 2;
        for (unsigned spins = 0; spins < SPIN_CAP; ++spins) {
            asm volatile("" ::: "memory");
            unsigned s0 = __builtin_amdgcn_raw_buffer_load_b32(rs, WS_ML + (h * 8 + (lane & 7)) * 8, 0, SC1);
            if (__any(s0 != POISON)) break;
            __builtin_amdgcn_s_sleep(O_SLEEP);
        }
        for (unsigned spins = 0; spins < SPIN_CAP; ++spins) {
            asm volatile("" ::: "memory");
#pragma unroll
            for (int i = 0; i < 2; ++i) ov[i] = __builtin_amdgcn_raw_buffer_load_b128(rs, oo + i * 64, 0, SC1);
            ml = __builtin_amdgcn_raw_buffer_load_b128(rs, WS_ML + (h * 8 + 2 * kg) * 8, 0, SC1);
            unsigned m = umax(umax(max4(ov[0]), max4(ov[1])), max4(ml));
            if (__all(m != POISON)) break;
            __builtin_amdgcn_s_sleep(1);
        }
        STAMP(9);
        f32x4 d0 = {0.f, 0.f, 0.f, 0.f}, d1 = {0.f, 0.f, 0.f, 0.f};
        d0 = __builtin_amdgcn_mfma_f32_16x16x32_bf16(__builtin_bit_cast(bf16x8, ov[0]), __builtin_bit_cast(bf16x8, ufr[0]), d0, 0, 0, 0);
        d1 = __builtin_amdgcn_mfma_f32_16x16x32_bf16(__builtin_bit_cast(bf16x8, ov[0]), __builtin_bit_cast(bf16x8, ufr[1]), d1, 0, 0, 0);
        d0 = __builtin_amdgcn_mfma_f32_16x16x32_bf16(__builtin_bit_cast(bf16x8, ov[1]), __builtin_bit_cast(bf16x8, ufr[2]), d0, 0, 0, 0);
        d1 = __builtin_amdgcn_mfma_f32_16x16x32_bf16(__builtin_bit_cast(bf16x8, ov[1]), __builtin_bit_cast(bf16x8, ufr[3]), d1, 0, 0, 0);
        const float m0 = asf(ml[0]), l0 = asf(ml[1]), m1 = asf(ml[2]), l1 = asf(ml[3]);
        const float M = mx32(mx16(fmaxf(m0, m1)));
        const float e0 = __expf(m0 - M), e1 = __expf(m1 - M);
        float L = e0 * l0 + e1 * l1;
        L = sw16(L, L);
        L = sw32(L, L);
        const float rl = 1.f / L;
        const float v0 = e0 * (d0[0] + d0[1]) + e1 * (d0[2] + d0[3]);
        const float v1 = e0 * (d1[0] + d1[1]) + e1 * (d1[2] + d1[3]);
        float v = sw16(v0, v1);
        v = sw32(v, v);
        if (lane < 32) sm3[w * 32 + lane] = v * rl;
    }
    bar();
    {
        f32x4 c4 = *(const f32x4*)(sm3 + 4 * rg);
#pragma unroll
        for (int ww = 1; ww < 8; ++ww) c4 += *(const f32x4*)(sm3 + ww * 32 + 4 * rg);
#pragma unroll
        for (int i = 0; i < 4; ++i) {
            const float c0 = g * rbf(c4[i]);
#pragma unroll
            for (int k = 0; k < 4; ++k) { o[2 * k] += c0 * bflo(ovv[i][k]); o[2 * k + 1] += c0 * bfhi(ovv[i][k]); }
        }
        float s4[4];
#pragma unroll
        for (int k = 0; k < 4; ++k) s4[k] = sw32(o[k], o[k + 4]);
        float s2[2];
#pragma unroll
        for (int k = 0; k < 2; ++k) s2[k] = sw16(s4[k], s4[k + 2]);
        const bool up = lane & 8;
        const float send = up ? s2[0] : s2[1], keep = up ? s2[1] : s2[0];
        const float v = keep + dppf<0x128>(send);
        __builtin_amdgcn_raw_buffer_store_b32(asu(v), rs, WS_GA + bb_ * 2048 + (w * 64 + c8 * 8 + (lane >> 5) * 4 + ((lane >> 4) & 1) * 2 + ((lane >> 3) & 1)) * 4, 0, SC1);
    }
    STAMP(10);
}

extern "C" void kernel_launch(void* const* d_in, const int* in_sizes, int n_in,
                              void* d_out, int out_size, void* d_ws, size_t ws_size,
                              hipStream_t stream) {
    const u16* x = (const u16*)d_in[0];
    const u16* ckv = (const u16*)d_in[1];
    const u16* wq = (const u16*)d_in[2];
    const u16* wuk = (const u16*)d_in[3];
    const u16* wuv = (const u16*)d_in[4];
    const u16* wo = (const u16*)d_in[5];
    const u16* wg = (const u16*)d_in[7];
    const u16* wu = (const u16*)d_in[8];
    const u16* wd = (const u16*)d_in[9];
    const u16* gw = (const u16*)d_in[10];
    mega<<<256, 640, 0, stream>>>(x, ckv, wq, wuk, wuv, wo, wg, wu, wd, gw, (char*)d_ws, (u16*)d_out);
}
```

```cpp
#include <hip/hip_runtime.h>
#include <hip/hip_bf16.h>
#include <stdint.h>

#define SK 8192
#define FF 1376

typedef unsigned short u16;
typedef unsigned long long u64;
typedef __attribute__((ext_vector_type(8))) __bf16 bf16x8;
typedef __attribute__((ext_vector_type(2))) __bf16 bf16x2;
typedef __attribute__((ext_vector_type(4))) short s16x4;
typedef __attribute__((ext_vector_type(8))) short s16x8;
typedef __attribute__((ext_vector_type(16))) float f32x16;
typedef __attribute__((ext_vector_type(4))) float f32x4;
typedef __attribute__((ext_vector_type(2))) float f32x2;
typedef __attribute__((ext_vector_type(4))) unsigned int u32x4;
typedef __attribute__((ext_vector_type(2))) unsigned int u32x2;
#define GAS __attribute__((address_space(1)))
typedef GAS unsigned gu32;
typedef GAS u64 gu64;
#define RLX_AGENT __ATOMIC_RELAXED, __HIP_MEMORY_SCOPE_AGENT
#define SC1 16
#define POISON 0xFFFFFFFFu

__device__ __forceinline__ float bf2f(u16 v) { return __uint_as_float(((unsigned)v) << 16); }
__device__ __forceinline__ float bflo(unsigned v) { return __uint_as_float(v << 16); }
__device__ __forceinline__ float bfhi(unsigned v) { return __uint_as_float(v & 0xffff0000u); }
__device__ __forceinline__ float asf(unsigned v) { return __uint_as_float(v); }
__device__ __forceinline__ unsigned asu(float v) { return __float_as_uint(v); }
__device__ __forceinline__ unsigned pk2(float a, float b) {
    bf16x2 v = {(__bf16)a, (__bf16)b};
    return __builtin_bit_cast(unsigned, v);
}
__device__ __forceinline__ u16 f2bf(float f) { __bf16 v = (__bf16)f; return __builtin_bit_cast(u16, v); }
__device__ __forceinline__ float rbf(float f) { return bf2f(f2bf(f)); }
template <int CTRL> __device__ __forceinline__ float dppf(float v) {
    return asf((unsigned)__builtin_amdgcn_update_dpp(0, (int)asu(v), CTRL, 0xf, 0xf, true));
}
template <int CTRL> __device__ __forceinline__ unsigned dppu(unsigned v) {
    return (unsigned)__builtin_amdgcn_update_dpp(0, (int)v, CTRL, 0xf, 0xf, true);
}
__device__ __forceinline__ float dpp_xor4(float v) {
    int a = __builtin_amdgcn_update_dpp(0, (int)asu(v), 0x104, 0xf, 0x5, false);
    a = __builtin_amdgcn_update_dpp(a, (int)asu(v), 0x114, 0xf, 0xA, false);
    return asf((unsigned)a);
}
__device__ __forceinline__ float sw16(float a, float b) {
    u32x2 r_ = __builtin_amdgcn_permlane16_swap(asu(a), asu(b), false, false);
    return asf(r_[0]) + asf(r_[1]);
}
__device__ __forceinline__ float sw32(float a, float b) {
    u32x2 r_ = __builtin_amdgcn_permlane32_swap(asu(a), asu(b), false, false);
    return asf(r_[0]) + asf(r_[1]);
}
__device__ __forceinline__ float mx16(float a) {
    u32x2 r_ = __builtin_amdgcn_permlane16_swap(asu(a), asu(a), false, false);
    return fmaxf(asf(r_[0]), asf(r_[1]));
}
__device__ __forceinline__ float mx32(float a) {
    u32x2 r_ = __builtin_amdgcn_permlane32_swap(asu(a), asu(a), false, false);
    return fmaxf(asf(r_[0]), asf(r_[1]));
}
__device__ __forceinline__ float sum16(float v) {
    v += dppf<0xB1>(v); v += dppf<0x4E>(v); v += dppf<0x141>(v); v += dppf<0x140>(v);
    return v;
}
__device__ __forceinline__ float max16(float v) {
    v = fmaxf(v, dppf<0xB1>(v)); v = fmaxf(v, dppf<0x4E>(v)); v = fmaxf(v, dppf<0x141>(v)); v = fmaxf(v, dppf<0x140>(v));
    return v;
}
__device__ __forceinline__ float fmx2(float a, float b) { float r_; asm("v_max_f32 %0, %1, %2" : "=v"(r_) : "v"(a), "v"(b)); return r_; }
__device__ __forceinline__ float fmx3(float a, float b, float c) { float r_; asm("v_max3_f32 %0, %1, %2, %3" : "=v"(r_) : "v"(a), "v"(b), "v"(c)); return r_; }
__device__ __forceinline__ float max16f(float v) {
    asm volatile("s_nop 1\n\tv_max_f32_dpp %0, %0, %0 quad_perm:[1,0,3,2] row_mask:0xf bank_mask:0xf\n\t"
        "s_nop 1\n\tv_max_f32_dpp %0, %0, %0 quad_perm:[2,3,0,1] row_mask:0xf bank_mask:0xf\n\t"
        "s_nop 1\n\tv_max_f32_dpp %0, %0, %0 row_half_mirror row_mask:0xf bank_mask:0xf\n\t"
        "s_nop 1\n\tv_max_f32_dpp %0, %0, %0 row_mirror row_mask:0xf bank_mask:0xf" : "+v"(v));
    return v;
}
__device__ __forceinline__ float ex2(float v) { return __builtin_amdgcn_exp2f(v); }
__device__ __forceinline__ float dot2(unsigned a, unsigned b, float c) {
    return __builtin_amdgcn_fdot2_f32_bf16(__builtin_bit_cast(bf16x2, a), __builtin_bit_cast(bf16x2, b), c, false);
}
#define PIN(x) asm volatile("" : "+v"(x))
__device__ __forceinline__ unsigned umax(unsigned a, unsigned b) { return a > b ? a : b; }
__device__ __forceinline__ unsigned max4(u32x4 v) { return umax(umax(v[0], v[1]), umax(v[2], v[3])); }

#define WS_EPOCH   0
#define WS_F0      1024
#define WS_QG      (WS_F0 + 8 * 256 * 128)
#define WS_QABS    (WS_QG + 128 * 1024)
#define WS_P       (WS_QABS + 8 * 32768)
#define WS_OPART   (WS_P + 128 * 5120)
#define WS_ML      (WS_OPART + 8 * 32 * 16 * 128)
#define WS_GA      (WS_ML + 16384)
#define WS_GB      (WS_GA + 128 * 2048)
#define WS_END     (WS_GB + 172 * 256)
#define OOB        0x40000000

template <int NT, typename T> __device__ __forceinline__ T ldg(const void* p) {
    if (NT) return __builtin_nontemporal_load((const T*)p);
    return *(const T*)p;
}
#define PF_LINES 121
#define SPIN_CAP (1u << 17)
#define O_SLEEP 2
#define A_SLEEP 40
#define B_SLEEP 30
#define F0_SLEEP 8
#define A_T1 300
#define CV_LATE 2
#define A_DELAY 100
#define NTHR 640
#define MAP 1
#define NQ 2
#define HD(x, k) (MAP ? 4 * (x) + (k) : (x) + 8 * (k))
#define QORD 8
#define NE 2
#define N1 0
#define PFQ 1
#define B_T 180
#define VSYNC 0
#define F_T 180
#define B_T2 0
#define SEP 1
#define LD_EPOCH (*(const __attribute__((address_space(4))) unsigned*)(size_t)(ws + WS_EPOCH))
#define PF_T 0
#define PQ 4
#define FB 1
#ifndef M_T
#define M_T 650
#endif
#ifndef M_N
#define M_N 8
#endif
#define OVE 0
#define QLAY 0
#define F0P 0
#define WQ_NT 0
#define UVW 4
#define D_T 550
#define D_SLEEP 8
#define Q_SLEEP 1
#define P_SLEEP 1
#define UK_NT 1
#define G_SLEEP 1
#define STAMP(i)
#define RET return
#define STAMP_DECL
#define STAMPN(i, n)
#define STAMP1(i, c)
#define NU _Pragma("unroll 1")
#define SB __builtin_amdgcn_sched_barrier(0)
#define MEMB asm volatile("" ::: "memory")
#define TWAIT(X) do { if ((X) > 0) { NU for (unsigned gs_ = 0; gs_ < 4096u; ++gs_) { if (__builtin_amdgcn_s_memrealtime() - t0_ >= (u64)(X)) break; __builtin_amdgcn_s_sleep(1); } } } while (0)
#define POLL_BEG(SL) { unsigned cnt_ = SPIN_CAP, thr_, H_; NU do { MEMB; if ((SL) > 0) __builtin_amdgcn_s_sleep(SL);
#define POLL_END(OK) cnt_ -= 1u; thr_ = cnt_ ? (unsigned)(OK) : 0xFFFFFFFFu; } while (__builtin_amdgcn_ballot_w64(H_ > thr_) != 0ull); MEMB; }
#define LAS __attribute__((address_space(3)))
#define ST4(V, O, S) do { const u32x4 sv_ = (V); __builtin_amdgcn_raw_buffer_store_b128(sv_, rs, (O), (S), SC1); asm volatile("s_nop 1" :: "v"(sv_)); } while (0)

__device__ __forceinline__ void lgkm0() { asm volatile("s_waitcnt lgkmcnt(0)" ::: "memory"); }
__device__ __forceinline__ void vm0() { asm volatile("s_waitcnt vmcnt(0)" ::: "memory"); }
__device__ __forceinline__ void bar() { asm volatile("s_waitcnt lgkmcnt(0)\n\ts_barrier" ::: "memory"); }
typedef volatile LAS unsigned lds_u32;
template <int SL> __device__ __forceinline__ void lds_wait(lds_u32* p, unsigned target) {
    POLL_BEG(SL)
        H_ = ~*p;
    POLL_END(~target)
}
__device__ __forceinline__ void lds_inc(lds_u32* p) {
    asm volatile("s_mov_b64 exec, 1\n\tds_add_u32 %0, %1\n\ts_mov_b64 exec, -1" :: "v"((unsigned)(size_t)p), "v"(1u) : "memory");
}
__device__ __forceinline__ s16x8 trd(const char* p, int d) {
    const s16x4 t0 = __builtin_amdgcn_ds_read_tr16_b64_v4i16((LAS s16x4*)p);
    const s16x4 t1 = __builtin_amdgcn_ds_read_tr16_b64_v4i16((LAS s16x4*)(p + d));
    return s16x8{t0[0], t0[1], t0[2], t0[3], t1[0], t1[1], t1[2], t1[3]};
}
template <typename TA, typename TB> __device__ __forceinline__ f32x4 mf16(TA a, TB b, f32x4 c) {
    return __builtin_amdgcn_mfma_f32_16x16x32_bf16(__builtin_bit_cast(bf16x8, a), __builtin_bit_cast(bf16x8, b), c, 0, 0, 0);
}
__device__ __forceinline__ unsigned um3(unsigned a, unsigned b, unsigned c) { return umax(umax(a, b), c); }
__device__ __forceinline__ unsigned mx4(unsigned m, u32x4 v) { return um3(um3(m, v[0], v[1]), v[2], v[3]); }
__device__ __forceinline__ f32x2 lo2(f32x4 v) { return f32x2{v[0], v[1]}; }
__device__ __forceinline__ f32x2 hi2(f32x4 v) { return f32x2{v[2], v[3]}; }
__device__ __forceinline__ f32x2 up2(unsigned v) { return f32x2{bflo(v), bfhi(v)}; }

__device__ __forceinline__ void f0_signal(const __amdgpu_buffer_rsrc_t rs, int b, int lane, unsigned tag) {
    const u32x4 v = {tag, ~tag, tag, ~tag};
    ST4(v, WS_F0 + ((lane >> 3) * 256 + b) * 128 + (lane & 7) * 16, 0);
}
__device__ __forceinline__ void f0_wait(const __amdgpu_buffer_rsrc_t rs, int b, int lane, unsigned tag, int base) {
    const int fo = WS_F0 + ((b & 7) * 256 + base + lane) * 128;
    POLL_BEG(F0_SLEEP)
        const u32x2 a0 = __builtin_amdgcn_raw_buffer_load_b64(rs, fo, 0, SC1);
        const u32x2 a1 = __builtin_amdgcn_raw_buffer_load_b64(rs, fo + 8192, 0, SC1);
        H_ = (a0[0] ^ tag) | (a0[1] ^ ~tag) | (a1[0] ^ tag) | (a1[1] ^ ~tag);
    POLL_END(0)
}
__device__ __forceinline__ void f0_wait_grp(const __amdgpu_buffer_rsrc_t rs, int b, int lane, unsigned tag) {
    const int fe = WS_F0 + ((b & 7) * 257 + (lane & 15) * 8) * 128;
    POLL_BEG(F0_SLEEP)
        const u32x2 a2 = __builtin_amdgcn_raw_buffer_load_b64(rs, fe, 0, SC1);
        H_ = (a2[0] ^ tag) | (a2[1] ^ ~tag);
    POLL_END(0)
}

#define KB 17408
#define VB (4 * KB)
#define MB (VB + 73728)
#define LDS_BYTES (MB + 1024)

__global__ __launch_bounds__(NTHR, 1) void mega(char* __restrict__ ws, const u16* __restrict__ x, const u16* __restrict__ ckv,
                                               const u16* __restrict__ wq, const u16* __restrict__ wuk,
                                               const u16* __restrict__ wuv, const u16* __restrict__ wo,
                                               const u16* __restrict__ wg, const u16* __restrict__ wu,
                                               const u16* __restrict__ wd, const u16* __restrict__ gw,
                                               u16* __restrict__ out) {
    __shared__ __attribute__((aligned(16))) char lds[LDS_BYTES];
    __shared__ __attribute__((aligned(16))) unsigned flg[16];

    const int t = threadIdx.x;
    const u64 t0_ = __builtin_amdgcn_s_memrealtime();
    STAMP_DECL
    STAMP(0);
    unsigned pf0;
    {
        u64 pc;
        asm volatile("s_getpc_b64 %0" : "=s"(pc));
        const GAS unsigned* cb = (const GAS unsigned*)(pc & ~255ull);
        const int l0 = NTHR - 1 - t;
        pf0 = cb[(l0 < PF_LINES - 1 ? l0 : PF_LINES - 1) * 32];
        SB;
    }
    asm volatile("" :: "s"(x), "s"(ckv), "s"(wq), "s"(wuk), "s"(ws), "s"(wuv), "s"(wo), "s"(wg), "s"(wu), "s"(wd), "s"(gw), "s"(out));
    const int b = blockIdx.x;
    const int lane = t & 63, w = t >> 6;
    const int r = lane & 31, h2 = lane >> 5;
    const int n16 = lane & 15, kg = lane >> 4;
    const __amdgpu_buffer_rsrc_t rs = __builtin_amdgcn_make_buffer_rsrc(ws, 0, WS_END, 0x00020000);
    const u32x4 P4 = {POISON, POISON, POISON, POISON};
    const f32x4 Z4 = {0.f, 0.f, 0.f, 0.f};
    lds_u32* vflg = (lds_u32*)flg;

    if (b < 128) {
        char* stgk = lds;
        float* Mws = (float*)(lds + MB);
        float* Lws = Mws + 128;
        const int kc = b & 7, mem = b >> 3;
        const int gr = kc * 2 + (mem >> 3), cs = mem & 7;
        if (w >= 9) {
            asm volatile("s_barrier" ::: "memory");
            unsigned pq_[4] = {0u, 0u, 0u, 0u};
            const u16* pp_ = wq + (size_t)(mem * 32 + (lane >> 3)) * 4096 + kc * 512 + (lane & 7) * 64;
#pragma unroll
            for (int i = 0; i < 4; ++i) pq_[i] = *(const unsigned*)(pp_ + i * 8 * 4096);
            SB;
            asm volatile("s_barrier\n\ts_barrier" ::: "memory");
            SB;
#pragma unroll
            for (int i = 0; i < 4; ++i) asm volatile("" :: "v"(pq_[i]));
            asm volatile("" :: "v"(pf0));
            if (cs != 0 || w > 9) RET;
            asm volatile("s_barrier\n\ts_barrier" ::: "memory");
            const f32x4 m4 = *(const f32x4*)(Mws + (lane >> 1) * 4), l4 = *(const f32x4*)(Lws + (lane >> 1) * 4);
            const float M = fmx3(m4[0], m4[1], fmx2(m4[2], m4[3]));
            const float L = (ex2(m4[0] - M) * l4[0] + ex2(m4[1] - M) * l4[1]) + (ex2(m4[2] - M) * l4[2] + ex2(m4[3] - M) * l4[3]);
            const u32x4 ml = {asu(M), asu(L), asu(M), asu(L)};
            ST4(ml, WS_ML + gr * 1024 + lane * 16, 0);
            RET;
        }
        if (w == 8) {
            const unsigned e = LD_EPOCH;
            asm volatile("s_barrier" ::: "memory");
            asm volatile("" :: "v"(pf0));
            bar();
            f0_signal(rs, b, lane, e + 1u);
            asm volatile("s_barrier" ::: "memory");
            if (b < 32) { unsigned short o_ = *(volatile const GAS u16*)(out + b * 16 + (lane & 15)); asm volatile("" :: "v"((unsigned)o_)); }
            STAMP(1);
            __builtin_amdgcn_s_sleep(A_SLEEP - F0_SLEEP);
            f0_wait_grp(rs, b, lane, e + 1u);
            vflg[9] = e + 1u; vflg[8] = 1u;
            STAMP(2);
            RET;
        }
        const int pb = (kc * 16 + mem) * 5120;
        if (w < 4) {
            ST4(P4, WS_P + pb + w * 1024 + lane * 16, 0);
            if (w < 2) ST4(P4, WS_QABS + kc * 32768 + mem * 2048 + w * 1024 + lane * 16, 0);
            else if (w == 2) ST4(P4, WS_P + pb + 4096 + lane * 16, 0);
            else if (cs == 0) ST4(P4, WS_ML + gr * 1024 + lane * 16, 0);
        } else
            ST4(P4, WS_OPART + ((gr * 32 + (w - 4) * 8 + (lane >> 3)) * 8 + cs) * 128 + (lane & 7) * 16, 0);
        asm volatile("s_barrier" ::: "memory");
        if (t < 16) flg[t] = 0u;
        vm0();
        asm volatile("" :: "v"(pf0));
        bar();
        asm volatile("s_barrier" ::: "memory");
        STAMP(1);
        TWAIT(A_DELAY);
        SB;
        const int kq = w >> 1, hh = w & 1;
        u32x4 kf[16];
        {
            u32x4 bl[8], cv[8];
            const u16* cp = ckv + (size_t)(gr * 512 + w * 64 + (lane >> 3)) * 512 + cs * 64 + (lane & 7) * 8;
            const u16* bp = ckv + (size_t)(kc * 1024 + mem * 64 + (lane >> 3)) * 512 + 64 * w + 8 * (lane & 7);
#pragma unroll
            for (int i = 0; i < 8; ++i) bl[i] = *(const u32x4*)(bp + i * 8 * 512);
            SB;
            char* bs = stgk + kq * KB + hh * 8192;
#pragma unroll
            for (int i = 0; i < 8; ++i) *(u32x4*)(bs + (i * 8 + (lane >> 3)) * 128 + (((lane & 7) ^ (lane >> 3)) * 16)) = bl[i];
            MEMB;
            lds_inc(vflg + 10);
            lds_wait<1>(vflg + 10, 8);
            SB;
#pragma unroll
            for (int i = 0; i < 8; ++i) cv[i] = *(const u32x4*)(cp + i * 8 * 512);
            SB;
#pragma unroll
            for (int kt = 0; kt < 4; ++kt)
#pragma unroll
                for (int s = 0; s < 4; ++s)
                    kf[kt * 4 + s] = *(const u32x4*)(stgk + kq * KB + (s >> 1) * 8192 + (kt * 16 + n16) * 128 + (((4 * (s & 1) + kg) ^ (n16 & 7)) * 16));
            MEMB;
            vflg[w] = 2u;
            STAMP(9);
            SB;
            char* vsw = lds + VB + (w * 64 + (lane >> 3)) * 144 + (lane & 7) * 16;
#pragma unroll
            for (int i = 0; i < 8; ++i) *(u32x4*)(vsw + i * 8 * 144) = cv[i];
        }
        STAMP(2);
        unsigned rp1o = (t >> 4) * 272 + (t & 15) * 16;
        int po1 = pb + ((t & 1) ? 4096 : ((t & 15) >> 2) * 1024) + ((t >> 1) & 1) * 64 + w * 128 + (lane >> 4) * 16;
        if ((t & 1) && (t & 15) >= 4) po1 = OOB;
        unsigned rw1o = kq * KB + (hh * 16 + n16) * 272 + kg * 16;
        PIN(rp1o); PIN(po1); PIN(rw1o);
        const char* rp1 = lds + rp1o;
        char* rw1 = lds + rw1o;
        lds_wait<1>(vflg + (w ^ 1), 2);
        lds_wait<1>(vflg + 8, 1);
        {
            u32x4 av[4];
            const int qo = WS_QABS + kc * 32768 + kq * 8192 + hh * 1024 + n16 * 64 + kg * 16;
            TWAIT(A_T1);
            f32x4 acc[4];
            POLL_BEG(Q_SLEEP)
#pragma unroll
                for (int s = 0; s < 4; ++s) av[s] = __builtin_amdgcn_raw_buffer_load_b128(rs, qo + 2048 * s, 0, SC1);
                SB;
#pragma unroll
                for (int kt = 0; kt < 4; ++kt) acc[kt] = mf16(kf[kt * 4], av[0], Z4);
#pragma unroll
                for (int kt = 0; kt < 4; ++kt) acc[kt] = mf16(kf[kt * 4 + 1], av[1], acc[kt]);
                H_ = umax(mx4(mx4(0u, av[0]), av[1]), mx4(mx4(0u, av[2]), av[3]));
                asm volatile("" : "+v"(acc[0]), "+v"(acc[1]), "+v"(acc[2]), "+v"(acc[3]) : "v"(H_));
            POLL_END(0xFFFFFFFEu)
            STAMP(4);
#pragma unroll
            for (int kt = 0; kt < 4; ++kt) {
                acc[kt] = mf16(kf[kt * 4 + 2], av[2], acc[kt]);
                acc[kt] = mf16(kf[kt * 4 + 3], av[3], acc[kt]);
                *(f32x4*)(rw1 + kt * 64) = acc[kt];
            }
        }
        bar();
        {
            f32x4 rr[4];
#pragma unroll
            for (int ww = 0; ww < 4; ++ww) rr[ww] = *(const f32x4*)(rp1 + ww * KB);
            SB;
            const f32x2 sa = (lo2(rr[0]) + lo2(rr[1])) + (lo2(rr[2]) + lo2(rr[3]));
            const f32x2 sb = (hi2(rr[0]) + hi2(rr[1])) + (hi2(rr[2]) + hi2(rr[3]));
            float m = fmx3(sa[0], sa[1], fmx2(sb[0], sb[1]));
            m = max16f(m);
            const unsigned p01 = pk2(ex2(sa[0] - m), ex2(sa[1] - m));
            const unsigned p23 = pk2(ex2(sb[0] - m), ex2(sb[1] - m));
            u32x4 pp = u32x4{p01, p23, dppu<0xF5>(p01), dppu<0xF5>(p23)};
            float l = dot2(p01, 0x3F803F80u, dot2(p23, 0x3F803F80u, 0.f));
            l = sum16(l);
            if (t & 1) { pp[0] = asu(m); pp[1] = asu(l); }
            ST4(pp, po1, WS_P);
            STAMP(3);
        }
        SB;
        s16x8 vf[16];
        {
            const char* vsr = lds + VB + (kq * 128 + 8 * kg + (n16 >> 2)) * 144 + (lane & 3) * 8;
#pragma unroll
            for (int q = 0; q < 4; ++q)
#pragma unroll
                for (int ct = 0; ct < 4; ++ct) vf[4 * q + ct] = trd(vsr + q * 4608 + ct * 32, 576);
        }
        int oo2 = ((gr * 32 + (t >> 4)) * 8 + cs) * 128 + (t & 15) * 8;
        unsigned mwo = MB + (hh * 16 + n16) * 16 + kq * 4;
        unsigned mro = MB + (t >> 4) * 16;
        PIN(oo2); PIN(mwo); PIN(mro);
        STAMP(5);
        {
            u32x4 pv[4];
            u32x2 ml[2];
            const int pq = (gr * 8 + 2 * kq) * 5120 + (hh * 4 + (n16 >> 2)) * 128 + (kg & 1) * 64 + (n16 & 3) * 16 + (kg >> 1) * 1024;
            const int mq = (gr * 8 + 2 * kq) * 5120 + 4096 + (hh * 4 + (n16 >> 2)) * 128 + (n16 & 3) * 16;
            f32x4 acc[8];
            float f[2], Mw;
            POLL_BEG(P_SLEEP)
#pragma unroll
                for (int j = 0; j < 2; ++j) ml[j] = __builtin_amdgcn_raw_buffer_load_b64(rs, mq + j * 5120, WS_P, SC1);
#pragma unroll
                for (int js = 0; js < 4; ++js) pv[js] = __builtin_amdgcn_raw_buffer_load_b128(rs, pq + (js >> 1) * 5120 + (js & 1) * 2048, WS_P, SC1);
                SB;
                Mw = fmx2(asf(ml[0][0]), asf(ml[1][0]));
#pragma unroll
                for (int j = 0; j < 2; ++j) f[j] = ex2(asf(ml[j][0]) - Mw);
                const unsigned m0 = um3(ml[0][0], ml[0][1], umax(ml[1][0], ml[1][1]));
                SB;
#pragma unroll
                for (int ct = 0; ct < 4; ++ct) acc[ct] = mf16(vf[ct], pv[0], Z4);
#pragma unroll
                for (int ct = 0; ct < 4; ++ct) acc[ct] = mf16(vf[4 + ct], pv[1], acc[ct]);
                H_ = umax(mx4(mx4(m0, pv[0]), pv[2]), mx4(mx4(0u, pv[1]), pv[3]));
                asm volatile("" : "+v"(acc[0]), "+v"(acc[1]), "+v"(acc[2]), "+v"(acc[3]), "+v"(f[0]), "+v"(f[1]) : "v"(H_));
                STAMP1(8, cnt_ == SPIN_CAP); STAMPN(10, SPIN_CAP - cnt_ + 1);
            POLL_END(0xFFFFFFFEu)
            STAMP(6);
#pragma unroll
            for (int ct = 0; ct < 4; ++ct) acc[4 + ct] = mf16(vf[8 + ct], pv[2], Z4);
#pragma unroll
            for (int ct = 0; ct < 4; ++ct) acc[4 + ct] = mf16(vf[12 + ct], pv[3], acc[4 + ct]);
            *(float*)(lds + mwo) = Mw;
            *(float*)(lds + mwo + 512) = f[0] * asf(ml[0][1]) + f[1] * asf(ml[1][1]);
#pragma unroll
            for (int ct = 0; ct < 4; ++ct) acc[ct] *= f[0];
            SB;
#pragma unroll
            for (int ct = 0; ct < 4; ++ct) *(f32x4*)(rw1 + 8704 + ct * 64) = acc[ct] + f[1] * acc[4 + ct];
        }
        bar();
        {
            const f32x4 ma = *(const f32x4*)(lds + mro);
            f32x4 rv[4];
#pragma unroll
            for (int ww = 0; ww < 4; ++ww) rv[ww] = *(const f32x4*)(rp1 + 8704 + ww * KB);
            SB;
            const float M = fmx3(ma[0], ma[1], fmx2(ma[2], ma[3]));
            const f32x4 s4 = (ex2(ma[0] - M) * rv[0] + ex2(ma[1] - M) * rv[1]) + (ex2(ma[2] - M) * rv[2] + ex2(ma[3] - M) * rv[3]);
            const u32x2 ov2 = {pk2(s4[0], s4[1]), pk2(s4[2], s4[3])};
            __builtin_amdgcn_raw_buffer_store_b64(ov2, rs, oo2, WS_OPART, SC1);
        }
        STAMP(7);
        if (b >= 32 || w != 0) RET;
        f0_wait(rs, b, lane, vflg[9], 128);
        {
            const int ao = WS_GA + (lane >> 2) * 2048 + b * 64 + (lane & 3) * 16;
            u32x4 a[8];
            POLL_BEG(G_SLEEP)
#pragma unroll
                for (int j = 0; j < 8; ++j) a[j] = __builtin_amdgcn_raw_buffer_load_b128(rs, ao + j * 32768, 0, SC1);
                H_ = umax(mx4(mx4(mx4(mx4(0u, a[0]), a[2]), a[4]), a[6]), mx4(mx4(mx4(mx4(0u, a[1]), a[3]), a[5]), a[7]));
            POLL_END(0xFFFFFFFEu)
            STAMP(11);
#define AL(j) lo2(__builtin_bit_cast(f32x4, a[j]))
#define AH(j) hi2(__builtin_bit_cast(f32x4, a[j]))
            const f32x2 sa = ((AL(0) + AL(1)) + (AL(2) + AL(3))) + ((AL(4) + AL(5)) + (AL(6) + AL(7)));
            const f32x2 sb = ((AH(0) + AH(1)) + (AH(2) + AH(3))) + ((AH(4) + AH(5)) + (AH(6) + AH(7)));
            float v = sw16(sw32(sa[0], sb[0]), sw32(sa[1], sb[1]));
            v += dppf<0x128>(v);
            v += dppf<0x124>(v);
            if ((lane & 12) == 0) __hip_atomic_store((GAS u16*)(out + b * 16 + (lane & 3) * 4 + (lane >> 4)), f2bf(v), __ATOMIC_RELAXED, __HIP_MEMORY_SCOPE_AGENT);
        }
        STAMP(12);
        RET;
    }

    char* utile = lds;
    float* sm1 = (float*)(lds + 32768);
    float* smg = sm1 + 1024;
    float* qs = smg + 1024;
    unsigned* qb_ = (unsigned*)(qs + 64);
    unsigned* stg = qb_ + 256;
    char* cpb = lds + 49152;
    const int bb_ = b - 128;
    const int h = bb_ & 31, jj = bb_ >> 5;
    const int xs = bb_ & 7, sl = bb_ >> 3;
    const bool is_gu = bb_ < 86;
    const int dk = bb_ - 86;

    if (w >= 8) {
        const unsigned e = LD_EPOCH;
        if (w == 9) {
            ST4(P4, WS_GA + bb_ * 2048 + lane * 16, 0);
            ST4(P4, WS_GA + bb_ * 2048 + 1024 + lane * 16, 0);
        }
        asm volatile("" :: "s"(e));
        bar();
        asm volatile("" :: "v"(pf0));
        const unsigned tag = e + 1u;
        if (w < 10) {
            if (w == 9) {
                vm0();
                f0_signal(rs, b, lane, tag);
                TWAIT(F_T);
                STAMP(1);
                f0_wait(rs, b, lane, tag, 0);
                STAMP(2);
                asm volatile("s_barrier" ::: "memory");
                if (b == 255) {
                    f0_wait(rs, b, lane, tag, 128);
                    if (lane == 0) __hip_atomic_store((gu32*)(ws + WS_EPOCH), tag, RLX_AGENT);
                }
                TWAIT(M_T);
                const int mo = WS_ML + h * 32 + (((n16 >> 1) & 1) * 8 + (n16 >> 2) * 2 + (n16 & 1)) * 1024;
                u32x2 ml;
                POLL_BEG(O_SLEEP)
                    ml = __builtin_amdgcn_raw_buffer_load_b64(rs, mo, 0, SC1);
                    H_ = umax(ml[0], ml[1]);
                    if (__builtin_popcountll(__builtin_amdgcn_ballot_w64(H_ != POISON)) >= 4 * M_N) vflg[11] = 1u;
                POLL_END(0xFFFFFFFEu)
                STAMP(3);
                const float M = max16f(asf(ml[0]));
                const float ee = ex2(asf(ml[0]) - M);
                const float L = sum16(ee * asf(ml[1]));
                qs[kg == 0 ? 40 + n16 : 56 + kg] = kg == 0 ? ee : L;
                MEMB;
                vflg[11] = 2u;
                STAMP(4);
                RET;
            }
            asm volatile("s_barrier" ::: "memory");
            STAMP(1);
            STAMP(2);
            if (is_gu) {
                lds_wait<2>(vflg + 0, 16);
                STAMP(3);
                f32x2 s = *(const f32x2*)(smg + h2 * 512 + 2 * r);
#pragma unroll
                for (int ww = 1; ww < 8; ++ww) s += *(const f32x2*)(smg + h2 * 512 + ww * 64 + 2 * r);
                const u32x4 g2 = {tag, asu(sw32(s[0], s[0])), asu(sw32(s[1], s[1])), ~tag};
                if (lane < 32) ST4(g2, WS_GB + ((bb_ & 1) * 86 + (bb_ & ~1)) * 256 + lane * 16, 0);
                STAMP(4);
            } else {
                const int gb = WS_GB + (h2 * 86 + (kg & 1) * 43) * 256;
                const int o1 = gb + dk * 256 + n16 * 16, o2 = gb + 42 * 256 + ((dk & 31) >> 1) * 16;
                u32x4 v1, v2;
                TWAIT(D_T);
                POLL_BEG(D_SLEEP)
                    v1 = __builtin_amdgcn_raw_buffer_load_b128(rs, o1, 0, SC1);
                    v2 = __builtin_amdgcn_raw_buffer_load_b128(rs, o2, 0, SC1);
                    H_ = (v1[0] ^ tag) | (v1[3] ^ ~tag) | (v2[0] ^ tag) | (v2[3] ^ ~tag);
                POLL_END(0)
                STAMP(3);
                const float val = sw16(asf(v1[1]), asf(v1[2]));
                const float ax = asf((dk & 1) ? v2[2] : v2[1]);
                const float vx = sw16(ax, ax);
                const u32x2 gu = __builtin_amdgcn_permlane32_swap(asu(val), asu(val), false, false);
                const u32x2 gx = __builtin_amdgcn_permlane32_swap(asu(vx), asu(vx), false, false);
                const float gs = rbf(asf(h2 ? gx[0] : gu[0])), us = rbf(asf(h2 ? gx[1] : gu[1]));
                const float sg = rbf(gs / (1.f + __expf(-gs)));
                qs[h2 ? 32 : 2 * n16 + kg] = rbf(sg * us);
                MEMB;
                vflg[3] = 1u;
                STAMP(4);
            }
        } else {
            vm0();
            STAMP(1);
            f0_signal(rs, b, lane, tag);
            if (b == 255) {
                __builtin_amdgcn_s_sleep(60);
                f0_wait(rs, b, lane, tag, 0);
                f0_wait(rs, b, lane, tag, 128);
                if (lane == 0) __hip_atomic_store((gu32*)(ws + WS_EPOCH), tag, RLX_AGENT);
            }
            STAMP(2);
        }
        RET;
    }

    u32x4 wqv[16], wkv[4];
    u32x2 xq[4];
    u16 xf[4];
    const int gl = bb_ >> 1;
    const int hq = 4 * xs + (sl >> 2), qq = sl & 3;
    const u16* qp0 = wq + (size_t)(4 * (t >> 4)) * 4096 + hq * 128 + n16 * 8;
    {
#define LD_WQ(J) _Pragma("unroll") for (int i = 0; i < 4; ++i) wqv[4 * (J) + i] = ldg<WQ_NT, u32x4>(qp0 + (size_t)(((qq + (J)) & 3) * 128 + i) * 4096);
#define LD_UK _Pragma("unroll") for (int i = 0; i < 4; ++i) wkv[i] = ldg<UK_NT, u32x4>(wuk + (size_t)(qq * 128 + 16 * w + 4 * kg + i) * 4096 + hq * 128 + n16 * 8);
#pragma unroll
        for (int j = 0; j < 4; ++j) xq[j] = *(const u32x2*)(x + ((qq + j) & 3) * 128 + 4 * (t >> 4));
#pragma unroll
        for (int i = 0; i < 4; ++i) xf[i] = x[2 * (t >> 3) + 128 * i + ((2 * gl + ((t >> 2) & 1)) >= 43 ? 1 : 0)];
        LD_UK
        SB;
        if (t < 16) flg[t] = 0u;
        bar();
        SB;
        PIN(wkv[3]);
        SB;
        LD_WQ(0) SB; LD_WQ(1) SB; LD_WQ(2) SB; LD_WQ(3)
    }
    SB;
    unsigned smo = (unsigned)(size_t)(LAS char*)(lds + 32768) + (w * 128 + n16 * 8 + (kg & 1) * 4 + h2 * 2) * 4;
    unsigned qwo = (unsigned)(size_t)(LAS char*)lds + w * 544 + n16 * 16 + (kg & 1) * 8 + h2 * 4;
    unsigned qro = (unsigned)(size_t)(LAS char*)lds + n16 * 272 + kg * 16;
    PIN(smo); PIN(qwo); PIN(qro);
    const int rg = lane >> 3, c8 = lane & 7;
    u32x4 gv[5];
    const int wv = w ^ ((jj & 1) * 4);
    const char* gb_ = (const char*)(is_gu ? ((bb_ & 1) ? wu : wg) : wd);
    unsigned gof = (is_gu ? ((t >> 3) * 43 + gl) * 64 + (t & 7) * 8 : (dk * 32 + rg * 4) * 512 + w * 64 + c8 * 8) * 2;
    const int gst = is_gu ? 64 * 43 * 64 : 512;
    const __amdgpu_buffer_rsrc_t rd = __builtin_amdgcn_make_buffer_rsrc((void*)wd, 0, FF * 1024, 0x00020000);
    int gxo = (rg == 0 && (unsigned)dk < 32u) ? (1344 + dk) * 1024 + w * 128 + c8 * 16 : OOB;
        unsigned sa = (unsigned)(size_t)(LAS unsigned*)stg;
        unsigned swa = sa + (16 * w + n16) * 2;
        unsigned sra = sa + ((w >> 2) * 32 + (lane & 7) * 4) * 4;
        unsigned sca = (unsigned)(size_t)(vflg + 6 + (w >> 2));
        int qo = WS_QABS + (lane >> 3) * 32768 + (QLAY ? (8 * qq + 4 * (w >> 2) + ((lane >> 1) & 3)) * 1024 + xs * 128 + (lane & 1) * 64 + (sl >> 2) * 16
                                                       : (4 * qq + 2 * (w >> 2) + ((lane >> 2) & 1)) * 2048 + hq * 64 + (lane & 3) * 16);
        PIN(swa); PIN(sra); PIN(sca); PIN(qo);
        unsigned gof4[4] = {gof, gof + gst * 2u, gof + gst * 4u, gof + gst * 6u};
        PIN(gof4[0]); PIN(gof4[1]); PIN(gof4[2]); PIN(gof4[3]); PIN(gxo);
    SB;
    {
        char* kt = lds + 65536 + w * 4608;
#pragma unroll
        for (int i = 0; i < 4; ++i) *(u32x4*)(kt + (4 * kg + i) * 272 + n16 * 16) = wkv[i];
        lgkm0();
#pragma unroll
        for (int i = 0; i < 4; ++i) wkv[i] = *(const u32x4*)(kt + n16 * 272 + i * 64 + kg * 16);
        SB;
    }
    asm volatile("" :: "v"(pf0));
    {
        float a[8];
#pragma unroll
        for (int p = 0; p < 8; ++p) {
            if (p == 7) { PIN(wqv[15]); STAMP(1); }
#pragma unroll
            for (int k = 0; k < 4; ++k) {
                const unsigned lo = __builtin_amdgcn_perm(wqv[2 * p + 1][k], wqv[2 * p][k], 0x05040100u);
                const unsigned hi = __builtin_amdgcn_perm(wqv[2 * p + 1][k], wqv[2 * p][k], 0x07060302u);
                a[2 * k] = dot2(lo, xq[p >> 1][p & 1], p ? a[2 * k] : 0.f);
                a[2 * k + 1] = dot2(hi, xq[p >> 1][p & 1], p ? a[2 * k + 1] : 0.f);
            }
            SB;
        }
        const float s0 = sw16(a[0], a[4]), s1 = sw16(a[1], a[5]), s2 = sw16(a[2], a[6]), s3 = sw16(a[3], a[7]);
        const float t0 = sw32(s0, s2), t1 = sw32(s1, s3);
        const unsigned qh = pk2(t0, t1);
        *(LAS unsigned*)(size_t)(qwo) = qh;
        *(LAS unsigned*)(size_t)(qwo + 272) = pk2(t0 - bflo(qh), t1 - bfhi(qh));
        SB;
    }
#define COLSUM(XV, WV, A, ST) { \
        f32x2 a2[4]; \
        _Pragma("unroll") for (int i = 0; i < 4; ++i) { \
            const float xi = bf2f(XV[i]); \
            if (i == 3) { PIN(WV[3]); STAMP(ST); } \
            _Pragma("unroll") for (int k = 0; k < 4; ++k) { if (i == 0) a2[k] = xi * up2(WV[i][k]); else a2[k] += xi * up2(WV[i][k]); } \
            SB; \
        } \
        const float s0 = sw16(a2[0][0], a2[2][0]), s1 = sw16(a2[0][1], a2[2][1]), s2 = sw16(a2[1][0], a2[3][0]), s3 = sw16(a2[1][1], a2[3][1]); \
        const f32x2 sx = {sw32(s0, s2), sw32(s1, s3)}; \
        *(LAS f32x2*)(size_t)(A) = sx; \
        lds_inc(vflg + 0); \
        SB; }
    {
        PIN(wkv[0]); PIN(wkv[1]); PIN(wkv[2]); PIN(wkv[3]);
        STAMP(11);
        bar();
        u32x4 qp[4];
#pragma unroll
        for (int i = 0; i < 4; ++i) qp[i] = *(const LAS u32x4*)(size_t)(qro + i * 64);
        SB;
        STAMP(2);
        f32x4 qa = mf16(qp[0], wkv[0], Z4);
#pragma unroll
        for (int i = 1; i < 4; ++i) qa = mf16(qp[i], wkv[i], qa);
        float d0 = (qa[0] + qa[1]) + (qa[2] + qa[3]);
        d0 = sw16(d0, d0);
        d0 = sw32(d0, d0);
        const float ds_ = d0 * 0.12751743118f;
        const unsigned e0 = pk2(ds_, ds_);
        unsigned old;
        u32x4 o;
        asm volatile("ds_write_b16 %2, %3\n\ts_mov_b64 exec, 1\n\tds_add_rtn_u32 %0, %4, %5\n\ts_mov_b64 exec, -1\n\tds_read_b128 %1, %6\n\ts_waitcnt lgkmcnt(0)"
                     : "=&v"(old), "=&v"(o) : "v"(swa), "v"(e0), "v"(sca), "v"(1u), "v"(sra) : "memory");
        if (__builtin_amdgcn_readfirstlane(old) == 3) ST4(o, qo, 0);
    }
    SB;
    lds_inc(vflg + 0);
    lds_wait<1>(vflg + 0, 8);
    SB;
    STAMP(3);
    TWAIT(B_T2);
    SB;
    u32x4 uvv[4], ovv[4];
    const u32x4 xv = *(const u32x4*)(x + lane * 8), gwv = *(const u32x4*)(gw + lane * 8);
#pragma unroll
    for (int i = 0; i < 4; ++i) { gv[i] = ldg<1, u32x4>(gb_ + gof4[i]); SB; }
    gv[4] = __builtin_amdgcn_raw_buffer_load_b128(rd, gxo, 0, 2);
    SB;
    STAMP(4);
    if (is_gu) {
        lds_inc(vflg + 5);
        lds_wait<1>(vflg + 5, 8);
    }
    SB;
#define LD_UV _Pragma("unroll") for (int i = 0; i < 4; ++i) uvv[i] = *(const u32x4*)(wuv + (size_t)(wv * 64 + (lane >> 2) + 16 * i) * 4096 + h * 128 + jj * 32 + (t & 3) * 8);
#define LD_OV _Pragma("unroll") for (int i = 0; i < 4; ++i) ovv[i] = ldg<1, u32x4>(wo + ((size_t)h * 128 + jj * 32 + rg * 4 + i) * 512 + w * 64 + c8 * 8);
    if (is_gu && __builtin_amdgcn_readfirstlane(w) >= UVW) {
        COLSUM(xf, gv, smo + 4096, 6)
        STAMP(7);
        LD_UV
        SB;
        STAMP(5);
    } else {
        LD_UV
        SB;
        STAMP(5);
        if (is_gu) { COLSUM(xf, gv, smo + 4096, 6) STAMP(7); }
    }
#pragma unroll
    for (int i = 0; i < 4; ++i) *(u32x4*)(utile + (w * 64 + (lane >> 2) + 16 * i) * 64 + (t & 3) * 16) = uvv[i];
    SB;
    LD_OV
    SB;
    float g, g1;
    {
        float gp = 0.f;
#pragma unroll
        for (int k = 0; k < 4; ++k) gp += rbf(bflo(xv[k]) * bflo(gwv[k])) + rbf(bfhi(xv[k]) * bfhi(gwv[k]));
        gp = sum16(gp);
        gp = sw16(gp, gp);
        gp = sw32(gp, gp);
        const float gs = rbf(gp);
        g = rbf(1.f / (1.f + __expf(-gs)));
        g1 = rbf(1.f - g);
    }
    lgkm0();
    s16x8 ufr[4], ofr[4];
    {
        const char* up = utile + (w * 64 + 8 * kg + (n16 >> 2)) * 64 + (lane & 3) * 8;
#pragma unroll
        for (int s = 0; s < 2; ++s)
#pragma unroll
            for (int tl = 0; tl < 2; ++tl) ufr[s * 2 + tl] = trd(up + s * 2048 + tl * 32, 256);
        char* ot = lds + 65536 + w * 4608;
#pragma unroll
        for (int i = 0; i < 4; ++i) *(u32x4*)(ot + (rg * 4 + i) * 144 + c8 * 16) = ovv[i];
        lgkm0();
#pragma unroll
        for (int ct = 0; ct < 4; ++ct) ofr[ct] = trd(ot + (8 * kg + (n16 >> 2)) * 144 + ct * 32 + (lane & 3) * 8, 576);
    }
    float fo = 0.f;
    if (!is_gu) {
        lds_wait<1>(vflg + 3, 1);
        const f32x4 h4 = *(const f32x4*)(qs + 4 * rg);
        const float hx = g1 * qs[32];
        f32x2 o2[4];
#pragma unroll
        for (int k = 0; k < 4; ++k) o2[k] = hx * up2(gv[4][k]);
#pragma unroll
        for (int i = 0; i < 4; ++i) {
            const float hv = g1 * h4[i];
#pragma unroll
            for (int k = 0; k < 4; ++k) o2[k] += hv * up2(gv[i][k]);
        }
        const float s0 = sw32(o2[0][0], o2[2][0]), s1 = sw32(o2[0][1], o2[2][1]), s2 = sw32(o2[1][0], o2[3][0]), s3 = sw32(o2[1][1], o2[3][1]);
        const float u0 = sw16(s0, s2), u1 = sw16(s1, s3);
        const bool up = lane & 8;
        const float send = up ? u0 : u1, keep = up ? u1 : u0;
        const float v = keep + dppf<0x128>(send);
        fo = asf((unsigned)__builtin_amdgcn_ds_permute((c8 * 8 + h2 * 4 + (kg & 1) * 2 + ((lane >> 3) & 1)) * 4, (int)asu(v)));
    }
    int go = bb_ * 2048 + t * 4;
    unsigned cwo = 49152 + (2 * w + h2) * 80 + r * 2, cro = 49152 + n16 * 80 + kg * 16;
    PIN(go); PIN(cwo); PIN(cro); PIN(fo);
    PIN(ofr[0]); PIN(ofr[1]); PIN(ofr[2]); PIN(ofr[3]);
    STAMP(8);
    {
        u32x4 ov[2];
        int oo = WS_OPART + (((n16 >> 1) * 32 + h) * 8 + wv) * 128 + (lane & 1) * 64 + kg * 16;
        PIN(oo);
        lds_wait<1>(vflg + 11, 1);
        f32x4 d[8];
        POLL_BEG(0)
#pragma unroll
            for (int i = 0; i < 2; ++i) ov[i] = __builtin_amdgcn_raw_buffer_load_b128(rs, oo + i * 262144, 0, SC1);
            SB;
#pragma unroll
            for (int i = 0; i < 2; ++i)
#pragma unroll
                for (int j = 0; j < 4; ++j) d[i * 4 + j] = mf16(ov[i], ufr[j], Z4);
            H_ = mx4(mx4(0u, ov[0]), ov[1]);
            asm volatile("" : "+v"(d[0]), "+v"(d[1]), "+v"(d[2]), "+v"(d[3]), "+v"(d[4]), "+v"(d[5]), "+v"(d[6]), "+v"(d[7]) : "v"(H_));
        POLL_END(0xFFFFFFFEu)
        STAMP(9);
        f32x4 e;
        POLL_BEG(0)
            H_ = ~vflg[11];
            e = *(const volatile LAS f32x4*)(qs + 40 + kg * 4);
        POLL_END(~2u)
        const float v0 = (e[0] * (d[0][0] + d[2][1]) + e[1] * (d[0][2] + d[2][3])) + (e[2] * (d[4][0] + d[6][1]) + e[3] * (d[4][2] + d[6][3]));
        const float v1 = (e[0] * (d[1][0] + d[3][1]) + e[1] * (d[1][2] + d[3][3])) + (e[2] * (d[5][0] + d[7][1]) + e[3] * (d[5][2] + d[7][3]));
        float v = sw16(v0, v1);
        v = sw32(v, v);
        const float vl = v - rbf(v);
        *(u16*)(lds + cwo) = f2bf(h2 ? vl : v);
    }
    bar();
    {
        const u32x4 cf = *(const u32x4*)(lds + cro);
        const float L = qs[57];
        SB;
        const float sc = g * __builtin_amdgcn_rcpf(L);
        SB;
        float a[4];
        f32x4 dd[4];
#pragma unroll
        for (int ct = 0; ct < 4; ++ct) dd[ct] = mf16(cf, ofr[ct], Z4);
        SB;
#pragma unroll
        for (int ct = 0; ct < 4; ++ct) {
            const f32x2 z = lo2(dd[ct]) + hi2(dd[ct]);
            a[ct] = z[0] + z[1];
        }
        const float v = sw16(sw32(a[0], a[2]), sw32(a[1], a[3]));
        __builtin_amdgcn_raw_buffer_store_b32(asu(v * sc + fo), rs, go, WS_GA, SC1);
    }
    STAMP(10);
    RET;
}

extern "C" void kernel_launch(void* const* d_in, const int* in_sizes, int n_in,
                              void* d_out, int out_size, void* d_ws, size_t ws_size,
                              hipStream_t stream) {
    const u16* x = (const u16*)d_in[0];
    const u16* ckv = (const u16*)d_in[1];
    const u16* wq = (const u16*)d_in[2];
    const u16* wuk = (const u16*)d_in[3];
    const u16* wuv = (const u16*)d_in[4];
    const u16* wo = (const u16*)d_in[5];
    const u16* wg = (const u16*)d_in[7];
    const u16* wu = (const u16*)d_in[8];
    const u16* wd = (const u16*)d_in[9];
    const u16* gw = (const u16*)d_in[10];
    mega<<<256, NTHR, 0, stream>>>((char*)d_ws, x, ckv, wq, wuk, wuv, wo, wg, wu, wd, gw, (u16*)d_out);
}
```
